```python
import torch

def _softmax(x, axis=-1):
    return torch.softmax(x, dim=axis)
def _gelu(x):
    return torch.nn.functional.gelu(x, approximate="tanh")
def _silu(x):
    return torch.nn.functional.silu(x)
def _sigmoid(x):
    return torch.sigmoid(x)
def _layernorm(x, axis=-1, eps=1e-5):
    m = x.mean(dim=axis, keepdim=True)
    v = x.var(dim=axis, keepdim=True, unbiased=False)
    return (x - m) / torch.sqrt(v + eps)
def _scatter_add(tgt, idx, src, axis=0):
    return tgt.clone().index_add_(axis, idx, src)
def _segment_sum(src, seg, n):
    out = torch.zeros((n,) + src.shape[1:], dtype=src.dtype)
    return out.index_add_(0, seg, src)
def _pad(x, pw):
    flat = tuple(v for pair in reversed(pw) for v in pair)
    return torch.nn.functional.pad(x, flat)

kernel_name = 'recurrent_scan__linear_attn_block__swiglu_ffn__prompt'

def setup_inputs():

    _w = (lambda a: (lambda t: t.to(torch.bfloat16) if t.is_floating_point() else t)(torch.as_tensor(a)))
    def _A_setup():
        B, S, D = 1, 2048, 1024
        return {
            "x": _w((lambda *s: torch.randn(*s, dtype=torch.bfloat16) * 0.05)(B, S, D)),
            "wqkv": _w((lambda *s: torch.randn(*s, dtype=torch.bfloat16) * 0.1)(D, 3 * D)),
            "wo": _w((lambda *s: torch.randn(*s, dtype=torch.bfloat16) * 0.05)(D, D)),
        }
    _A_in = _A_setup()

    def _B_setup():
        B, S, D, F = 1, 2048, 1024, 3584
        return {
            "x": _w((lambda *s: torch.randn(*s, dtype=torch.bfloat16) * 0.05)(B, S, D)),
            "w_gate": _w((lambda *s: torch.randn(*s, dtype=torch.bfloat16) * 0.05)(D, F)),
            "w_up": _w((lambda *s: torch.randn(*s, dtype=torch.bfloat16) * 0.05)(D, F)),
            "w_down": _w((lambda *s: torch.randn(*s, dtype=torch.bfloat16) * 0.05)(F, D)),
        }
    _B_in = _B_setup()

    return {
        "A_x": _A_in["x"],
        "A_wqkv": _A_in["wqkv"],
        "A_wo": _A_in["wo"],
        "B_x": _B_in["x"],
        "B_w_gate": _B_in["w_gate"],
        "B_w_up": _B_in["w_up"],
        "B_w_down": _B_in["w_down"],
    }

def reference(A_x, A_wqkv, A_wo, B_x, B_w_gate, B_w_up, B_w_down):
    def _A_ref(x, wqkv, wo):
        B, S, D = x.shape
        H = 32; Dh = D // H
        qkv = (lambda x, *d: x.permute(*d))((x @ wqkv).reshape(B, S, 3, H, Dh), 2, 0, 3, 1, 4)
        q, k, v = qkv[0], qkv[1], qkv[2]
        phi = lambda t: torch.where(t > 0, t + 1, torch.exp(t)) + 1e-6
        pq, pk = phi(q), phi(k)
        kv = torch.einsum("bhsd,bhse->bhde", pk, v)
        z = torch.einsum("bhsd->bhd", pk)
        num = torch.einsum("bhqd,bhde->bhqe", pq, kv)
        den = torch.einsum("bhqd,bhd->bhq", pq, z)[..., None] + 1e-6
        o = num / den
        return (lambda x, *d: x.permute(*d))(o, 0, 2, 1, 3).reshape(B, S, D) @ wo

    def _B_ref(x, w_gate, w_up, w_down):
        g = _silu(x @ w_gate)
        u = x @ w_up
        return (g * u) @ w_down

    x = A_x
    xn = _layernorm(x, axis=-1)
    s = _A_ref(xn, A_wqkv, A_wo)
    g = _silu(_B_ref(xn, B_w_gate, B_w_up, B_w_down))
    return x + s * g
```

```cpp
#include <hip/hip_runtime.h>
#include <hip/hip_bf16.h>
typedef unsigned short u16;
__device__ __forceinline__ float b2f(u16 v){ return __uint_as_float(((unsigned)v)<<16); }
__device__ __forceinline__ u16 f2b(float f){ unsigned u=__float_as_uint(f); u+=0x7fffu+((u>>16)&1u); return (u16)(u>>16);}
__global__ void ln_k(const u16* x, u16* xn){
  int row=blockIdx.x; __shared__ float red[256];
  float s=0; for(int i=threadIdx.x;i<1024;i+=256) s+=b2f(x[row*1024+i]);
  red[threadIdx.x]=s; __syncthreads();
  for(int o=128;o>0;o>>=1){ if(threadIdx.x<o) red[threadIdx.x]+=red[threadIdx.x+o]; __syncthreads(); }
  float m=red[0]/1024.f; __syncthreads();
  float q=0; for(int i=threadIdx.x;i<1024;i+=256){ float d=b2f(x[row*1024+i])-m; q+=d*d; }
  red[threadIdx.x]=q; __syncthreads();
  for(int o=128;o>0;o>>=1){ if(threadIdx.x<o) red[threadIdx.x]+=red[threadIdx.x+o]; __syncthreads(); }
  float r=rsqrtf(red[0]/1024.f+1e-5f);
  for(int i=threadIdx.x;i<1024;i+=256) xn[row*1024+i]=f2b((b2f(x[row*1024+i])-m)*r);
}
__global__ void gemm_k(const u16* A,const u16* B,float* C,int M,int N,int K){
  __shared__ float As[32][33], Bs[32][33];
  int tx=threadIdx.x&31, ty=threadIdx.x>>5;
  int r0=blockIdx.y*32, c0=blockIdx.x*32; float acc[4]={0,0,0,0};
  for(int k0=0;k0<K;k0+=32){
    for(int i=0;i<4;i++){ As[ty+8*i][tx]=b2f(A[(size_t)(r0+ty+8*i)*K+k0+tx]); Bs[ty+8*i][tx]=b2f(B[(size_t)(k0+ty+8*i)*N+c0+tx]); }
    __syncthreads();
    for(int k=0;k<32;k++){ float b=Bs[k][tx]; for(int i=0;i<4;i++) acc[i]+=As[ty+8*i][k]*b; }
    __syncthreads();
  }
  for(int i=0;i<4;i++) C[(size_t)(r0+ty+8*i)*N+c0+tx]=acc[i];
}
__device__ __forceinline__ float phi(float t){ return (t>0.f? t+1.f: __expf(t))+1e-6f; }
__global__ void kv_k(const float* qkv, float* kv, float* z){
  int h=blockIdx.x, d=threadIdx.x>>5, e=threadIdx.x&31; float a=0, zz=0;
  for(int s=0;s<2048;s++){ float pk=phi(qkv[(size_t)s*3072+1024+h*32+d]); a+=pk*qkv[(size_t)s*3072+2048+h*32+e]; zz+=pk; }
  kv[h*1024+d*32+e]=a; if(e==0) z[h*32+d]=zz;
}
__global__ void o_k(const float* qkv,const float* kv,const float* z,u16* o){
  int s=blockIdx.x, h=threadIdx.x>>5, e=threadIdx.x&31; float n=0, dn=0;
  for(int d=0;d<32;d++){ float pq=phi(qkv[(size_t)s*3072+h*32+d]); n+=pq*kv[h*1024+d*32+e]; dn+=pq*z[h*32+d]; }
  o[(size_t)s*1024+h*32+e]=f2b(n/(dn+1e-6f));
}
__global__ void h_k(const float* g,const float* u,u16* h,int n){ int i=blockIdx.x*256+threadIdx.x; if(i<n){ float x=g[i]; h[i]=f2b(x/(1.f+__expf(-x))*u[i]); } }
__global__ void fin_k(const u16* x,const float* s,const float* f,u16* out,int n){ int i=blockIdx.x*256+threadIdx.x; if(i<n){ float y=f[i]; out[i]=f2b(b2f(x[i])+s[i]*(y/(1.f+__expf(-y)))); } }
extern "C" void kernel_launch(void* const* d_in, const int* in_sizes, int n_in, void* d_out, int out_size, void* d_ws, size_t ws_size, hipStream_t stream){
  const u16 *x=(const u16*)d_in[0], *wqkv=(const u16*)d_in[1], *wo=(const u16*)d_in[2], *wg=(const u16*)d_in[4], *wu=(const u16*)d_in[5], *wd=(const u16*)d_in[6];
  char* ws=(char*)d_ws;
  u16* xn=(u16*)ws; ws+=2048*1024*2;
  float* qkv=(float*)ws; ws+=(size_t)2048*3072*4;
  float* g=(float*)ws; ws+=(size_t)2048*3584*4;
  float* u=(float*)ws; ws+=(size_t)2048*3584*4;
  float* kv=(float*)ws; ws+=32*1024*4; float* z=(float*)ws; ws+=4096;
  u16* o=(u16*)ws; ws+=2048*1024*2; u16* h=(u16*)ws; ws+=(size_t)2048*3584*2;
  float* s=(float*)ws; ws+=2048*1024*4; float* f=(float*)ws;
  ln_k<<<2048,256,0,stream>>>(x,xn);
  gemm_k<<<dim3(3072/32,64),256,0,stream>>>(xn,wqkv,qkv,2048,3072,1024);
  gemm_k<<<dim3(3584/32,64),256,0,stream>>>(xn,wg,g,2048,3584,1024);
  gemm_k<<<dim3(3584/32,64),256,0,stream>>>(xn,wu,u,2048,3584,1024);
  kv_k<<<32,1024,0,stream>>>(qkv,kv,z);
  o_k<<<2048,1024,0,stream>>>(qkv,kv,z,o);
  h_k<<<2048*3584/256,256,0,stream>>>(g,u,h,2048*3584);
  gemm_k<<<dim3(32,64),256,0,stream>>>(o,wo,s,2048,1024,1024);
  gemm_k<<<dim3(32,64),256,0,stream>>>(h,wd,f,2048,1024,3584);
  fin_k<<<2048*1024/256,256,0,stream>>>(x,s,f,(u16*)d_out,2048*1024);
}
```

```cpp
#include <hip/hip_runtime.h>
typedef unsigned short u16;
typedef unsigned long long u64;
typedef float f32x2 __attribute__((ext_vector_type(2)));
typedef float f32x4 __attribute__((ext_vector_type(4)));
typedef float f32x16 __attribute__((ext_vector_type(16)));
typedef __bf16 bf16x2 __attribute__((ext_vector_type(2)));
typedef unsigned u32x2 __attribute__((ext_vector_type(2)));
typedef unsigned u32x4 __attribute__((ext_vector_type(4)));
typedef unsigned u32x8 __attribute__((ext_vector_type(8)));
typedef unsigned u32x16 __attribute__((ext_vector_type(16)));
#define DI __device__ __forceinline__
#define RING 4
constexpr int DEF = 0;
constexpr int PFE = 1;
constexpr int PFF = 1;
constexpr int PFM = 1;
constexpr int MIXED = 0;
constexpr int ROT = 1;

constexpr size_t WS_ST = 0;
constexpr size_t WS_CS = 65536;
constexpr size_t WS_AO = 4u << 20;
constexpr size_t WS_KV = WS_AO + 128u * 147456u;
constexpr size_t WS_PART = WS_KV + 32u * 8u * 8192u;
constexpr size_t WS_FLAG = WS_PART + 64u * 196608u;
constexpr size_t FL_P1 = 0, FL_CS = 8 * 10240, FL_KV = FL_CS + 32 * 64, FL_FP = FL_KV + 16 * 1024, FL_EPOCH = FL_FP + 64 * 8192, FL_END = FL_EPOCH + 256;
constexpr unsigned OOB = 0x40000000u;
constexpr unsigned LDS_BYTES = 163840, LDS_X = 147456, T_NM = 4096, T_CS = 6144, T_X = 7680;

struct Params { const u16 *x, *wqkv, *wo, *wg, *wu, *wd; u16* out; char* ws; };

DI __amdgpu_buffer_rsrc_t mk(const void* p, unsigned bytes) { return __builtin_amdgcn_make_buffer_rsrc((void*)p, 0, bytes, 0x00020000); }
DI unsigned pk(float a, float b) { f32x2 v = {a, b}; return __builtin_bit_cast(unsigned, __builtin_convertvector(v, bf16x2)); }
DI float bflo(unsigned u) { return __uint_as_float(u << 16); }
DI float bfhi(unsigned u) { return __uint_as_float(u & 0xffff0000u); }
DI float rbf(float f) { return bflo(pk(f, f) & 0xffffu); }
DI u64 mix(u64 z) { z += 0x9e3779b97f4a7c15ull; z = (z ^ (z >> 30)) * 0xbf58476d1ce4e5b9ull; z = (z ^ (z >> 27)) * 0x94d049bb133111ebull; return z ^ (z >> 31); }
DI int lane_id() { int l; asm volatile("v_mbcnt_lo_u32_b32 %0, -1, 0\n\tv_mbcnt_hi_u32_b32 %0, -1, %0" : "=v"(l)); return l; }

DI float wave_sum(float v) {
  v += __builtin_bit_cast(float, __builtin_amdgcn_update_dpp(0, __builtin_bit_cast(int, v), 0xB1, 0xf, 0xf, true));
  v += __builtin_bit_cast(float, __builtin_amdgcn_update_dpp(0, __builtin_bit_cast(int, v), 0x4E, 0xf, 0xf, true));
  v += __builtin_bit_cast(float, __builtin_amdgcn_update_dpp(0, __builtin_bit_cast(int, v), 0x141, 0xf, 0xf, true));
  v += __builtin_bit_cast(float, __builtin_amdgcn_update_dpp(0, __builtin_bit_cast(int, v), 0x140, 0xf, 0xf, true));
  { const auto r = __builtin_amdgcn_permlane16_swap(__float_as_uint(v), __float_as_uint(v), false, false); v = __uint_as_float(r[0]) + __uint_as_float(r[1]); }
  { const auto r = __builtin_amdgcn_permlane32_swap(__float_as_uint(v), __float_as_uint(v), false, false); v = __uint_as_float(r[0]) + __uint_as_float(r[1]); }
  return v; }
DI unsigned ln_row(const u32x4 a, const u32x4 b) { f32x2 v[8];
#pragma unroll
  for (int i = 0; i < 4; ++i) { v[i] = (f32x2){bflo(a[i]), bfhi(a[i])}; v[4 + i] = (f32x2){bflo(b[i]), bfhi(b[i])}; }
  const f32x2 s = ((v[0] + v[1]) + (v[2] + v[3])) + ((v[4] + v[5]) + (v[6] + v[7]));
  const float m = wave_sum(s.x + s.y) * (1.f / 1024.f); f32x2 q = {0.f, 0.f}, r = {0.f, 0.f};
#pragma unroll
  for (int i = 0; i < 8; i += 2) { const f32x2 d = v[i] - m, e = v[i + 1] - m; q = __builtin_elementwise_fma(d, d, q); r = __builtin_elementwise_fma(e, e, r); }
  q += r;
  return pk(m, __builtin_amdgcn_sqrtf(rbf(rbf(wave_sum(q.x + q.y) * (1.f / 1024.f)) + 1e-5f))); }
DI bool map_ta(int b) { return MIXED ? b < 128 : !(b & 4); }
DI int map_col(int b) { return MIXED ? (b & 7) * 2 + ((b >> 6) & 1) : (b & 3) * 4 + (b >> 6); }
DI int map_role(int b) { return (map_ta(b) ? 2 : 0) + (b & 1); }
DI int map_pm2(int b) { return ((b >> 1) & 1) * 4 + ((b >> 3) & 3); }
DI int map_pn2(int b) { return MIXED ? ((b >> 2) & 1) * 4 + ((b >> 5) & 3) : b >> 5; }
typedef const __attribute__((address_space(4))) Params* KP;
DI Params fresh() { KP k = (KP)__builtin_amdgcn_kernarg_segment_ptr(); asm volatile("" : "+s"(k));
  Params P; P.x = k->x; P.wqkv = k->wqkv; P.wo = k->wo; P.wg = k->wg; P.wu = k->wu; P.wd = k->wd; P.out = k->out; P.ws = k->ws; return P; }
template <bool TA> DI void run(const unsigned ldsb, int w) {
  Params P = fresh(); int bid = blockIdx.x;
  int col = map_col(bid), p = (bid >> 3) & 7, wr = w >> 1, wc = w & 1, odd = col & 1;
  f32x16 c[13]; u64 tag; __amdgpu_buffer_rsrc_t srdw, srda2, srdb; unsigned l16;
  {
    const int lane = lane_id(), g = lane >> 4, fr = lane & 15, q = fr >> 2, pp = lane & 3;
    const __amdgpu_buffer_rsrc_t srda = mk((const char*)P.x + (size_t)p * 256 * 2048, 256 * 2048);
    const unsigned gcol = TA ? (2560 + 64 * col) * 2 : 320 * col;
    const __amdgpu_buffer_rsrc_t srd0 = mk((const char*)P.wqkv + 128 * col, 1024 * 6144 - 128 * col), srd1 = mk((const char*)P.wg + gcol, 1024 * 7168 - gcol), srd2 = mk((const char*)P.wu + gcol, 1024 * 7168 - gcol);
    u32x16 a0;
    __amdgpu_buffer_rsrc_t srdp; unsigned pstep; const int prow = 64 * (lane >> 5) + 8 * p + ((lane >> 2) & 7);
    { const bool q = TA && w == 1, g1 = TA ? w == 2 : w == 1; const int n = lane & 3;
      srdp = w == 0 ? srda : q ? srd0 : g1 ? srd1 : srd2; pstep = w == 0 ? 128 : q ? 2 * 393216 : 2 * 458752;
      const unsigned vw = prow * (q ? 6144 : 7168) + n * (q ? 2048 : 128), va = (64 * (bid >> 6) + lane) * 2048; const int nl = TA ? (q ? 3 : 1) : (w < 3 ? 3 : 0);
      a0[13] = w == 0 ? va : n < nl ? vw : OOB; }
    const unsigned pf0 = w == 0 ? a0[13] : OOB;
    asm volatile("s_mov_b32 m0, %3\n\ts_nop 4\n\tbuffer_load_dword %0, %2, 0 offen lds\n\tbuffer_load_dword %1, %2, 0 offen offset:128 lds" :: "v"(a0[13]), "v"(pf0), "s"(srdp), "s"(ldsb + LDS_X + T_X + 1024 + w * 1024) : "memory");
  { u32x4 d; const int l = lane_id(); const unsigned vo = MIXED ? (l < 5 ? ((((bid >> 3) & 15) * 4 + w) * 5 + l) * 128 : OOB) : (l < 3 ? (((bid >> 3) * 4 + w) * 3 + l) * 128 : OOB);
    if constexpr (TA) asm volatile("s_getpc_b64 s[84:85]\n0:\n\ts_mov_b32 s86, blk_end_A-0b\n\ts_mov_b32 s87, 0x20000\n\ts_mov_b32 m0, %2\n\ts_nop 0\n\tbuffer_load_dword %1, s[84:87], 0 offen lds" : "=&{s[84:87]}"(d) : "v"(vo), "s"(ldsb + LDS_X + T_X + w * 256) : "memory");
    else asm volatile("s_getpc_b64 s[84:85]\n0:\n\ts_mov_b32 s86, blk_end_B-0b\n\ts_mov_b32 s87, 0x20000\n\ts_mov_b32 m0, %2\n\ts_nop 0\n\tbuffer_load_dword %1, s[84:87], 0 offen lds" : "=&{s[84:87]}"(d) : "v"(vo), "s"(ldsb + LDS_X + T_X + w * 256) : "memory"); }
    const int ci = (TA ? 0 : 16) + col; u32x4 x0, x1, x2, x3; u64 ep;
    { const __amdgpu_buffer_rsrc_t xs = mk((const char*)P.x + (size_t)(p * 256 + ci * 8 + 2 * w) * 2048, 4096);
      asm volatile("s_nop 4\n\tbuffer_load_dwordx4 %0, %5, %6, 0 offen\n\tbuffer_load_dwordx4 %1, %5, %6, 0 offen offset:1024\n\tbuffer_load_dwordx4 %2, %5, %6, 0 offen offset:2048\n\tbuffer_load_dwordx4 %3, %5, %6, 0 offen offset:3072\n\tglobal_load_dwordx2 %4, %7, %8 sc1"
        : "=&v"(x0), "=&v"(x1), "=&v"(x2), "=&v"(x3), "=&v"(ep) : "v"(lane * 16), "s"(xs), "v"(0), "s"(P.ws + WS_FLAG + FL_EPOCH) : "memory"); }
#pragma unroll
    for (int j = 0; j < 2; ++j) { const int ra = 64 * w + 8 * j + (lane >> 3); a0[j] = ra * 2048 + (((lane & 7) ^ ((ra >> 1) & 7)) << 4) - j * 1024;
      const int row = 8 * (2 * w + j) + (lane >> 3), sw = (((row >> 1) & 1) << 2) | (((row >> 3) & 1) << 1), ch = (((lane & 7) ^ sw) << 4) - j * 1024;
      if (TA) { a0[2 + j] = row * 6144 + ch; a0[4 + j] = row * 7168 + ch; } else a0[2 + j] = row * 7168 + ch + (odd ? 64 : 0); }
    if (!TA) { const int row = 16 * w + (lane >> 2); a0[4] = row * 7168 + (((lane & 3) ^ (((row >> 3) & 1) << 1)) << 4) + (odd ? 0 : 256); a0[5] = lane; }
    unsigned sa = 0, sq = 0, sg = 0, sp = w ? pstep : 256, sp2 = 0, tmp, cnt; const unsigned m0a = ldsb + w * 8192, m0b = ldsb + w * 2048, m0c = ldsb + w * 1024;
#pragma unroll
    for (int ks = 0; ks < 2; ++ks) a0[6 + ks] = ldsb + (wr * 128 + fr) * 128 + (((4 * ks + g) ^ (fr >> 1)) << 4);
    const int brow = 8 * g + q, bsw = ((q >> 1) << 2) | ((g & 1) << 1);
#pragma unroll
    for (int f = 0; f < 4; ++f) a0[8 + f] = ldsb + 65536 + (f >> 1) * 40960 + brow * 128 + (((2 * (2 * wc + (f & 1)) + (pp >> 1)) ^ bsw) << 4) + 8 * (pp & 1);
    a0[12] = ldsb + 65536 + 32768 + brow * 64 + (((2 * wc + (pp >> 1)) ^ ((g & 1) << 1)) << 4) + 8 * (pp & 1);
    __amdgpu_buffer_rsrc_t srdq = w < 3 ? mk(P.wd, 3584 * 2048) : mk(P.wo, 1024 * 2048); const unsigned p2step = w < 3 ? 768 : 640;
    a0[14] = lane < (w < 3 ? 6 : 5) ? ((w < 3 ? (bid * 3 + w) * 84 : bid * 70) + lane) * 128 : OOB;
    a0[15] = lane; srdw = mk(P.ws, WS_FLAG + FL_END);
    const unsigned lowmask = (1u << (4 * p)) - 1u; unsigned ord = p > 3 ? (0x7654000u & ~lowmask) | ((0x7654300u & (lowmask >> 4)) << 4) | (p << 8) | 0x21u : 0x7654321u;
    const int mine = 7 - (p < 3 ? p : 3), lx = ldsb + LDS_X;
    const unsigned cso = WS_CS + ci * 10240, fco = WS_FLAG + FL_CS + ci * 64;
    { const int pm2 = map_pm2(bid), pn2 = map_pn2(bid); srda2 = mk(P.ws + WS_AO + (size_t)pm2 * 16 * 147456, 16 * 147456);
      srdb = map_role(bid) == 3 ? mk((const char*)P.wo + pn2 * 256 - 56 * 131072, 72 * 131072 - pn2 * 256) : mk((const char*)P.wd + pn2 * 256, 56 * 131072 - pn2 * 256);
      asm volatile("" : "+s"(srda2), "+s"(srdb)); l16 = lane * 16; }
    unsigned spec = 2 | (3 << (mine - 2)), csst = cso + p * 1280 + (wc * 10 + wr * 5) * 64, fcsp = fco + p * 8, stld = WS_ST + (p * 256 + 64 * w) * 8, csld = cso + w * 512, csn = w < 3 ? 1280 - 512 * w : 0,
      lrow = lx + w * 1024, lnm = lx + T_NM + w * 256, lcs = lx + T_CS + w * 512, lcsr = lx + T_CS + wc * 640, lrowr = lx + wr * 2048;
    asm volatile("" : "+s"(ord), "+s"(spec), "+s"(csst), "+s"(fcsp), "+s"(stld), "+s"(csld), "+s"(csn), "+s"(lrow), "+s"(lnm), "+s"(lcs), "+s"(lcsr), "+s"(lrowr), "+s"(srdq), "+s"(srdw));
#define G1_PIO "+{v[208:223]}"(a0), [sa] "+s"(sa), [sq] "+s"(sq), [sg] "+s"(sg), [sp] "+s"(sp), [tmp] "=&s"(tmp)
#define G1_PIN [pfl] "v"(((PFM & 1) && w == 0) ? OOB : a0[13]), [srda] "s"(srda), [srd0] "s"(srd0), [srd1] "s"(srd1), [srd2] "s"(srd2), [srdp] "s"(srdp), [pstep] "s"(pstep), [m0a] "s"(m0a), [m0b] "s"(m0b), [m0c] "s"(m0c)
    if constexpr (TA) asm volatile(
    "s_nop 4\n\t"
    "s_add_u32 m0, %[m0a], 0\n\t"
    "s_nop 0\n\t"
    "buffer_load_dwordx4 v208, %[srda], %[sa] offen offset:0 lds\n\t"
    "buffer_load_dwordx4 v209, %[srda], %[sa] offen offset:1024 lds\n\t"
    "s_add_u32 %[tmp], %[sa], 30720\n\t"
    "buffer_load_dwordx4 v208, %[srda], %[tmp] offen offset:2048 lds\n\t"
    "buffer_load_dwordx4 v209, %[srda], %[tmp] offen offset:3072 lds\n\t"
    "s_add_u32 m0, %[m0a], 4096\n\t"
    "s_add_u32 %[tmp], %[sa], 65536\n\t"
    "buffer_load_dwordx4 v208, %[srda], %[tmp] offen offset:0 lds\n\t"
    "buffer_load_dwordx4 v209, %[srda], %[tmp] offen offset:1024 lds\n\t"
    "s_add_u32 %[tmp], %[sa], 96256\n\t"
    "buffer_load_dwordx4 v208, %[srda], %[tmp] offen offset:2048 lds\n\t"
    "buffer_load_dwordx4 v209, %[srda], %[tmp] offen offset:3072 lds\n\t"
    "s_add_u32 m0, %[m0b], 65536\n\t"
    "s_nop 0\n\t"
    "buffer_load_dwordx4 v210, %[srd0], %[sq] offen offset:0 lds\n\t"
    "buffer_load_dwordx4 v211, %[srd0], %[sq] offen offset:1024 lds\n\t"
    "s_add_u32 m0, %[m0b], 73728\n\t"
    "s_add_u32 %[tmp], %[sq], 2048\n\t"
    "buffer_load_dwordx4 v210, %[srd0], %[tmp] offen offset:0 lds\n\t"
    "buffer_load_dwordx4 v211, %[srd0], %[tmp] offen offset:1024 lds\n\t"
    "s_add_u32 m0, %[m0b], 81920\n\t"
    "s_add_u32 %[tmp], %[sq], 4096\n\t"
    "buffer_load_dwordx4 v210, %[srd0], %[tmp] offen offset:0 lds\n\t"
    "buffer_load_dwordx4 v211, %[srd0], %[tmp] offen offset:1024 lds\n\t"
    "s_add_u32 m0, %[m0b], 90112\n\t"
    "s_nop 0\n\t"
    "buffer_load_dwordx4 v212, %[srd1], %[sg] offen offset:0 lds\n\t"
    "buffer_load_dwordx4 v213, %[srd1], %[sg] offen offset:1024 lds\n\t"
    "s_add_u32 m0, %[m0b], 98304\n\t"
    "s_nop 0\n\t"
    "buffer_load_dwordx4 v212, %[srd2], %[sg] offen offset:0 lds\n\t"
    "buffer_load_dwordx4 v213, %[srd2], %[sg] offen offset:1024 lds\n\t"
    "s_add_u32 %[sa], %[sa], 128\n\t"
    "s_add_u32 %[sq], %[sq], 393216\n\t"
    "s_add_u32 %[sg], %[sg], 458752\n\t"
    "s_add_u32 m0, %[m0c], 156160\n\t"
    "s_nop 0\n\t"
    "buffer_load_dword v221, %[srdp], %[sp] offen lds\n\t"
    "s_add_u32 %[sp], %[sp], %[pstep]\n\t"
    "v_mov_b32 v221, %[pfl]\n\t"
        : G1_PIO : G1_PIN, [pf0] "v"(pf0) : "memory", "scc");
    else asm volatile(
    "s_nop 4\n\t"
    "s_add_u32 m0, %[m0a], 0\n\t"
    "s_nop 0\n\t"
    "buffer_load_dwordx4 v208, %[srda], %[sa] offen offset:0 lds\n\t"
    "buffer_load_dwordx4 v209, %[srda], %[sa] offen offset:1024 lds\n\t"
    "s_add_u32 %[tmp], %[sa], 30720\n\t"
    "buffer_load_dwordx4 v208, %[srda], %[tmp] offen offset:2048 lds\n\t"
    "buffer_load_dwordx4 v209, %[srda], %[tmp] offen offset:3072 lds\n\t"
    "s_add_u32 m0, %[m0a], 4096\n\t"
    "s_add_u32 %[tmp], %[sa], 65536\n\t"
    "buffer_load_dwordx4 v208, %[srda], %[tmp] offen offset:0 lds\n\t"
    "buffer_load_dwordx4 v209, %[srda], %[tmp] offen offset:1024 lds\n\t"
    "s_add_u32 %[tmp], %[sa], 96256\n\t"
    "buffer_load_dwordx4 v208, %[srda], %[tmp] offen offset:2048 lds\n\t"
    "buffer_load_dwordx4 v209, %[srda], %[tmp] offen offset:3072 lds\n\t"
    "s_add_u32 m0, %[m0b], 65536\n\t"
    "s_nop 0\n\t"
    "buffer_load_dwordx4 v210, %[srd1], %[sg] offen offset:0 lds\n\t"
    "buffer_load_dwordx4 v211, %[srd1], %[sg] offen offset:1024 lds\n\t"
    "s_add_u32 m0, %[m0b], 73728\n\t"
    "s_add_u32 %[tmp], %[sg], 128\n\t"
    "buffer_load_dwordx4 v210, %[srd1], %[tmp] offen offset:0 lds\n\t"
    "buffer_load_dwordx4 v211, %[srd1], %[tmp] offen offset:1024 lds\n\t"
    "s_add_u32 m0, %[m0b], 81920\n\t"
    "s_nop 0\n\t"
    "buffer_load_dwordx4 v210, %[srd2], %[sg] offen offset:0 lds\n\t"
    "buffer_load_dwordx4 v211, %[srd2], %[sg] offen offset:1024 lds\n\t"
    "s_add_u32 m0, %[m0b], 90112\n\t"
    "s_nop 0\n\t"
    "buffer_load_dwordx4 v210, %[srd2], %[tmp] offen offset:0 lds\n\t"
    "buffer_load_dwordx4 v211, %[srd2], %[tmp] offen offset:1024 lds\n\t"
    "s_add_u32 m0, %[m0c], 98304\n\t"
    "s_nop 0\n\t"
    "buffer_load_dwordx4 v212, %[srd1], %[sg] offen lds\n\t"
    "s_add_u32 m0, %[m0c], 102400\n\t"
    "s_nop 0\n\t"
    "buffer_load_dwordx4 v212, %[srd2], %[sg] offen lds\n\t"
    "s_add_u32 %[sa], %[sa], 128\n\t"
    "s_add_u32 %[sg], %[sg], 458752\n\t"
    "s_add_u32 m0, %[m0c], 156160\n\t"
    "s_nop 0\n\t"
    "buffer_load_dword v221, %[srdp], %[sp] offen lds\n\t"
    "s_add_u32 %[sp], %[sp], %[pstep]\n\t"
    "v_mov_b32 v221, %[pfl]\n\t"
        : G1_PIO : G1_PIN, [pf0] "v"(pf0) : "memory", "scc");

    asm volatile("s_waitcnt vmcnt(%5)" : "+v"(x0), "+v"(x1), "+v"(x2), "+v"(x3), "+v"(ep) : "i"((PFF ? 18 : 20) + PFE) : "memory");
    tag = mix(((u64)__builtin_amdgcn_readfirstlane((unsigned)(ep >> 32)) << 32) | __builtin_amdgcn_readfirstlane((unsigned)ep));
    { const unsigned s0 = ln_row(x0, x1), s1 = ln_row(x2, x3);
      __builtin_amdgcn_raw_buffer_store_b64((u32x2){lane ? s1 : s0, (unsigned)tag}, srdw, lane < 2 ? (unsigned)WS_ST + (p * 256 + ci * 8 + 2 * w + lane) * 8 : OOB, 0, 16); }
#define G1_IO [sa] "+s"(sa), [sq] "+s"(sq), [sg] "+s"(sg), [sp] "+s"(sp), [sp2] "+s"(sp2), [ord] "+s"(ord), [tmp] "=&s"(tmp), [cnt] "=&s"(cnt)
#define G1_IN0 [srda] "s"(srda), [srd0] "s"(srd0), [srd1] "s"(srd1), [srd2] "s"(srd2), [srdp] "s"(srdp), [pstep] "s"(pstep), [m0a] "s"(m0a), [m0b] "s"(m0b), [m0c] "s"(m0c), "{v248}"(l16), "{s[90:91]}"(tag), "{s[92:95]}"((f32x4){1.4426950408889634f, 0.f, 1e-6f, 0.f}), [srdq] "s"(srdq), [p2step] "s"(p2step), [srdw] "s"(srdw), [w] "s"(w), [wr] "s"(wr), \
      [mine] "s"(mine), [spec] "s"(spec), [csst] "s"(csst), [fcsp] "s"(fcsp), [fcs] "s"(fco), [stld] "s"(stld), [csld] "s"(csld), [csn] "s"(csn), [lrow] "s"(lrow), [lnm] "s"(lnm), [lcs] "s"(lcs), [lcsr] "s"(lcsr), [lrowr] "s"(lrowr)
#define G1_CLOB "s96", "s97", "memory", "scc", "vcc", "v224", "v225", "v226", "v227", "v228", "v229", "v230", "v231", "v232", "v233", "v234", "v235", "v236", "v237", "v238", "v239", "v240", "v241", "v242", "v243", "v244", "v245", "v246", "v247", "v249", "v250", "v251", "v252", "v253", "v254", "v255", "a0", "a1", "a2", "a3", "a4", "a5", "a6", "a7", "a8", "a9", "a10", "a11", "a12", "a13", "a14", "a15", "a16", "a17", "a18", "a19", "a20", "a21", "a22", "a23", "a24", "a25", "a26", "a27", "a28", "a29", "a30", "a31", "a32", "a33", "a34", "a35", "a36", "a37", "a38", "a39", "a40", "a41", "a42", "a43", "a44", "a45", "a46", "a47", "a48", "a49", "a50", "a51", "a52", "a53", "a54", "a55", "a56", "a57", "a58", "a59", "a60", "a61", "a62", "a63", "a64", "a65", "a66", "a67", "a68", "a69", "a70", "a71", "a72", "a73", "a74", "a75", "a76", "a77", "a78", "a79", "a80", "a81", "a82", "a83", "a84", "a85", "a86", "a87", "a88", "a89", "a90", "a91", "a92", "a93", "a94", "a95", "a96", "a97", "a98", "a99", "a100", "a101", "a102", "a103", "a104", "a105", "a106", "a107", "a108", "a109", "a110", "a111", "a112", "a113", "a114", "a115", "a116", "a117", "a118", "a119", "a120", "a121", "a122", "a123", "a124", "a125", "a126", "a127", "a128", "a129", "a130", "a131", "a132", "a133", "a134", "a135", "a136", "a137", "a138", "a139", "a140", "a141", "a142", "a143", "a144", "a145", "a146", "a147", "a148", "a149", "a150", "a151", "a152", "a153", "a154", "a155", "a156", "a157", "a158", "a159", "a160", "a161", "a162", "a163", "a164", "a165", "a166", "a167", "a168", "a169", "a170", "a171", "a172", "a173", "a174", "a175", "a176", "a177", "a178", "a179", "a180", "a181", "a182", "a183", "a184", "a185", "a186", "a187", "a188", "a189", "a190", "a191", "a192", "a193", "a194", "a195", "a196", "a197", "a198", "a199", "a200", "a201", "a202", "a203", "a204", "a205", "a206", "a207", "a208", "a209", "a210", "a211", "a212", "a213", "a214", "a215", "a216", "a217", "a218", "a219", "a220", "a221", "a222", "a223", "a224", "a225", "a226", "a227", "a228", "a229", "a230", "a231", "a232", "a233", "a234", "a235", "a236", "a237", "a238", "a239", "a240", "a241", "a242", "a243", "a244", "a245", "a246", "a247", "a248", "a249", "a250", "a251", "a252", "a253", "a254", "a255"
    if constexpr (TA) asm volatile(
    "v_mov_b32 v244, 0x3f803f80\n\t"
    "v_mov_b32 v245, 0x3f803f80\n\t"
    "v_mov_b32 v246, 0x3f803f80\n\t"
    "v_mov_b32 v247, 0x3f803f80\n\t"
    "v_mov_b64 v[224:225], 0\n\t"
    "v_mov_b64 v[226:227], 0\n\t"
    "v_mov_b64 v[228:229], 0\n\t"
    "v_mov_b64 v[230:231], 0\n\t"
    "v_mov_b64 v[232:233], 0\n\t"
    "v_mov_b64 v[234:235], 0\n\t"
    "v_mov_b64 v[236:237], 0\n\t"
    "v_mov_b64 v[238:239], 0\n\t"
    "v_mov_b64 v[240:241], 0\n\t"
    "v_mov_b64 v[242:243], 0\n\t"
    "s_mov_b32 %[cnt], 7\n\t"
    "s_cmp_eq_u32 %[mine], 7\n\t"
    "s_cselect_b64 vcc, -1, 0\n\t"
    "s_waitcnt vmcnt(2)\n\t"
    "s_barrier\n\t"
    "ds_read_b64_tr_b16 a[32:33], v216 offset:0\n\t"
    "ds_read_b64_tr_b16 a[34:35], v216 offset:512\n\t"
    "ds_read_b64_tr_b16 a[36:37], v217 offset:0\n\t"
    "ds_read_b64_tr_b16 a[38:39], v217 offset:512\n\t"
    "ds_read_b128 a[0:3], v214 offset:0\n\t"
    "ds_read_b128 a[4:7], v214 offset:2048\n\t"
    "ds_read_b128 a[8:11], v214 offset:4096\n\t"
    "ds_read_b128 a[12:15], v214 offset:6144\n\t"
    "ds_read_b128 a[16:19], v214 offset:8192\n\t"
    "ds_read_b128 a[20:23], v214 offset:10240\n\t"
    "ds_read_b128 a[24:27], v214 offset:12288\n\t"
    "ds_read_b128 a[28:31], v214 offset:14336\n\t"
    "ds_read_b64_tr_b16 a[40:41], v216 offset:8192\n\t"
    "ds_read_b64_tr_b16 a[42:43], v216 offset:8704\n\t"
    "ds_read_b64_tr_b16 a[44:45], v217 offset:8192\n\t"
    "ds_read_b64_tr_b16 a[46:47], v217 offset:8704\n\t"
    "ds_read_b64_tr_b16 a[48:49], v216 offset:16384\n\t"
    "ds_read_b64_tr_b16 a[50:51], v216 offset:16896\n\t"
    "ds_read_b64_tr_b16 a[52:53], v217 offset:16384\n\t"
    "ds_read_b64_tr_b16 a[54:55], v217 offset:16896\n\t"
    "ds_read_b64_tr_b16 a[56:57], v216 offset:24576\n\t"
    "ds_read_b64_tr_b16 a[58:59], v216 offset:25088\n\t"
    "ds_read_b64_tr_b16 a[60:61], v217 offset:24576\n\t"
    "ds_read_b64_tr_b16 a[62:63], v217 offset:25088\n\t"
    "ds_read_b64_tr_b16 a[64:65], v216 offset:32768\n\t"
    "ds_read_b64_tr_b16 a[66:67], v216 offset:33280\n\t"
    "ds_read_b64_tr_b16 a[68:69], v217 offset:32768\n\t"
    "ds_read_b64_tr_b16 a[70:71], v217 offset:33280\n\t"
    "s_waitcnt lgkmcnt(0)\n\t"
    "v_mfma_f32_16x16x32_bf16 v[0:3], a[32:35], a[0:3], 0\n\t"
    "ds_read_b64_tr_b16 a[104:105], v216 offset:4096\n\t"
    "v_mfma_f32_16x16x32_bf16 v[32:35], a[36:39], a[0:3], 0\n\t"
    "s_add_u32 m0, %[m0a], 32768\n\t"
    "ds_read_b64_tr_b16 a[106:107], v216 offset:4608\n\t"
    "v_mfma_f32_16x16x32_bf16 v[36:39], a[36:39], a[4:7], 0\n\t"
    "buffer_load_dwordx4 v208, %[srda], %[sa] offen offset:0 lds\n\t"
    "v_mfma_f32_16x16x32_bf16 v[4:7], a[32:35], a[4:7], 0\n\t"
    "v_mfma_f32_16x16x32_bf16 v[8:11], a[32:35], a[8:11], 0\n\t"
    "ds_read_b64_tr_b16 a[108:109], v217 offset:4096\n\t"
    "v_mfma_f32_16x16x32_bf16 v[40:43], a[36:39], a[8:11], 0\n\t"
    "buffer_load_dwordx4 v209, %[srda], %[sa] offen offset:1024 lds\n\t"
    "v_mfma_f32_16x16x32_bf16 v[44:47], a[36:39], a[12:15], 0\n\t"
    "ds_read_b64_tr_b16 a[110:111], v217 offset:4608\n\t"
    "v_mfma_f32_16x16x32_bf16 v[12:15], a[32:35], a[12:15], 0\n\t"
    "s_add_u32 %[tmp], %[sa], 30720\n\t"
    "v_mfma_f32_16x16x32_bf16 v[16:19], a[32:35], a[16:19], 0\n\t"
    "buffer_load_dwordx4 v208, %[srda], %[tmp] offen offset:2048 lds\n\t"
    "v_mfma_f32_16x16x32_bf16 v[48:51], a[36:39], a[16:19], 0\n\t"
    "ds_read_b128 a[72:75], v215 offset:0\n\t"
    "v_mfma_f32_16x16x32_bf16 v[52:55], a[36:39], a[20:23], 0\n\t"
    "ds_read_b128 a[76:79], v215 offset:2048\n\t"
    "v_mfma_f32_16x16x32_bf16 v[20:23], a[32:35], a[20:23], 0\n\t"
    "buffer_load_dwordx4 v209, %[srda], %[tmp] offen offset:3072 lds\n\t"
    "v_mfma_f32_16x16x32_bf16 v[24:27], a[32:35], a[24:27], 0\n\t"
    "s_add_u32 m0, %[m0a], 36864\n\t"
    "v_mfma_f32_16x16x32_bf16 v[56:59], a[36:39], a[24:27], 0\n\t"
    "s_add_u32 %[tmp], %[sa], 65536\n\t"
    "ds_read_b128 a[80:83], v215 offset:4096\n\t"
    "v_mfma_f32_16x16x32_bf16 v[60:63], a[36:39], a[28:31], 0\n\t"
    "buffer_load_dwordx4 v208, %[srda], %[tmp] offen offset:0 lds\n\t"
    "v_mfma_f32_16x16x32_bf16 v[28:31], a[32:35], a[28:31], 0\n\t"
    "ds_read_b128 a[84:87], v215 offset:6144\n\t"
    "v_mfma_f32_16x16x32_bf16 v[92:95], a[28:31], a[40:43], 0\n\t"
    "v_mfma_f32_16x16x32_bf16 v[124:127], a[28:31], a[44:47], 0\n\t"
    "buffer_load_dwordx4 v209, %[srda], %[tmp] offen offset:1024 lds\n\t"
    "v_mfma_f32_16x16x32_bf16 v[120:123], a[24:27], a[44:47], 0\n\t"
    "ds_read_b128 a[88:91], v215 offset:8192\n\t"
    "v_mfma_f32_16x16x32_bf16 v[88:91], a[24:27], a[40:43], 0\n\t"
    "s_add_u32 %[tmp], %[sa], 96256\n\t"
    "ds_read_b128 a[92:95], v215 offset:10240\n\t"
    "v_mfma_f32_16x16x32_bf16 v[84:87], a[20:23], a[40:43], 0\n\t"
    "buffer_load_dwordx4 v208, %[srda], %[tmp] offen offset:2048 lds\n\t"
    "v_mfma_f32_16x16x32_bf16 v[116:119], a[20:23], a[44:47], 0\n\t"
    "v_mfma_f32_16x16x32_bf16 v[112:115], a[16:19], a[44:47], 0\n\t"
    "ds_read_b128 a[96:99], v215 offset:12288\n\t"
    "v_mfma_f32_16x16x32_bf16 v[80:83], a[16:19], a[40:43], 0\n\t"
    "buffer_load_dwordx4 v209, %[srda], %[tmp] offen offset:3072 lds\n\t"
    "v_mfma_f32_16x16x32_bf16 v[76:79], a[12:15], a[40:43], 0\n\t"
    "ds_read_b128 a[100:103], v215 offset:14336\n\t"
    "v_mfma_f32_16x16x32_bf16 v[108:111], a[12:15], a[44:47], 0\n\t"
    "s_add_u32 m0, %[m0b], 106496\n\t"
    "v_mfma_f32_16x16x32_bf16 v[104:107], a[8:11], a[44:47], 0\n\t"
    "buffer_load_dwordx4 v210, %[srd0], %[sq] offen offset:0 lds\n\t"
    "v_mfma_f32_16x16x32_bf16 v[72:75], a[8:11], a[40:43], 0\n\t"
    "ds_read_b64_tr_b16 a[112:113], v216 offset:12288\n\t"
    "v_mfma_f32_16x16x32_bf16 v[68:71], a[4:7], a[40:43], 0\n\t"
    "ds_read_b64_tr_b16 a[114:115], v216 offset:12800\n\t"
    "v_mfma_f32_16x16x32_bf16 v[100:103], a[4:7], a[44:47], 0\n\t"
    "buffer_load_dwordx4 v211, %[srd0], %[sq] offen offset:1024 lds\n\t"
    "v_mfma_f32_16x16x32_bf16 v[96:99], a[0:3], a[44:47], 0\n\t"
    "s_add_u32 m0, %[m0b], 114688\n\t"
    "v_mfma_f32_16x16x32_bf16 v[64:67], a[0:3], a[40:43], 0\n\t"
    "s_add_u32 %[tmp], %[sq], 2048\n\t"
    "ds_read_b64_tr_b16 a[116:117], v217 offset:12288\n\t"
    "v_mfma_f32_16x16x32_bf16 v[128:131], a[0:3], a[48:51], 0\n\t"
    "buffer_load_dwordx4 v210, %[srd0], %[tmp] offen offset:0 lds\n\t"
    "v_mfma_f32_16x16x32_bf16 v[160:163], a[0:3], a[52:55], 0\n\t"
    "ds_read_b64_tr_b16 a[118:119], v217 offset:12800\n\t"
    "v_mfma_f32_16x16x32_bf16 v[164:167], a[4:7], a[52:55], 0\n\t"
    "v_mfma_f32_16x16x32_bf16 v[132:135], a[4:7], a[48:51], 0\n\t"
    "buffer_load_dwordx4 v211, %[srd0], %[tmp] offen offset:1024 lds\n\t"
    "v_mfma_f32_16x16x32_bf16 v[136:139], a[8:11], a[48:51], 0\n\t"
    "s_add_u32 m0, %[m0b], 122880\n\t"
    "ds_read_b64_tr_b16 a[120:121], v216 offset:20480\n\t"
    "v_mfma_f32_16x16x32_bf16 v[168:171], a[8:11], a[52:55], 0\n\t"
    "s_add_u32 %[tmp], %[sq], 4096\n\t"
    "ds_read_b64_tr_b16 a[122:123], v216 offset:20992\n\t"
    "v_mfma_f32_16x16x32_bf16 v[172:175], a[12:15], a[52:55], 0\n\t"
    "buffer_load_dwordx4 v210, %[srd0], %[tmp] offen offset:0 lds\n\t"
    "v_mfma_f32_16x16x32_bf16 v[140:143], a[12:15], a[48:51], 0\n\t"
    "v_mfma_f32_16x16x32_bf16 v[144:147], a[16:19], a[48:51], 0\n\t"
    "ds_read_b64_tr_b16 a[124:125], v217 offset:20480\n\t"
    "v_mfma_f32_16x16x32_bf16 v[176:179], a[16:19], a[52:55], 0\n\t"
    "buffer_load_dwordx4 v211, %[srd0], %[tmp] offen offset:1024 lds\n\t"
    "v_mfma_f32_16x16x32_bf16 v[180:183], a[20:23], a[52:55], 0\n\t"
    "ds_read_b64_tr_b16 a[126:127], v217 offset:20992\n\t"
    "v_mfma_f32_16x16x32_bf16 v[148:151], a[20:23], a[48:51], 0\n\t"
    "s_add_u32 m0, %[m0b], 131072\n\t"
    "v_mfma_f32_16x16x32_bf16 v[152:155], a[24:27], a[48:51], 0\n\t"
    "buffer_load_dwordx4 v212, %[srd1], %[sg] offen offset:0 lds\n\t"
    "v_mfma_f32_16x16x32_bf16 v[184:187], a[24:27], a[52:55], 0\n\t"
    "ds_read_b64_tr_b16 a[128:129], v216 offset:28672\n\t"
    "v_mfma_f32_16x16x32_bf16 v[188:191], a[28:31], a[52:55], 0\n\t"
    "ds_read_b64_tr_b16 a[130:131], v216 offset:29184\n\t"
    "v_mfma_f32_16x16x32_bf16 v[156:159], a[28:31], a[48:51], 0\n\t"
    "buffer_load_dwordx4 v213, %[srd1], %[sg] offen offset:1024 lds\n\t"
    "v_mfma_f32_16x16x32_bf16 a[156:159], a[56:59], a[28:31], 0\n\t"
    "v_mfma_f32_16x16x32_bf16 a[188:191], a[60:63], a[28:31], 0\n\t"
    "s_add_u32 m0, %[m0b], 139264\n\t"
    "ds_read_b64_tr_b16 a[132:133], v217 offset:28672\n\t"
    "v_mfma_f32_16x16x32_bf16 a[184:187], a[60:63], a[24:27], 0\n\t"
    "buffer_load_dwordx4 v212, %[srd2], %[sg] offen offset:0 lds\n\t"
    "v_mfma_f32_16x16x32_bf16 a[152:155], a[56:59], a[24:27], 0\n\t"
    "ds_read_b64_tr_b16 a[134:135], v217 offset:29184\n\t"
    "v_mfma_f32_16x16x32_bf16 a[148:151], a[56:59], a[20:23], 0\n\t"
    "v_mfma_f32_16x16x32_bf16 a[180:183], a[60:63], a[20:23], 0\n\t"
    "buffer_load_dwordx4 v213, %[srd2], %[sg] offen offset:1024 lds\n\t"
    "v_mfma_f32_16x16x32_bf16 a[176:179], a[60:63], a[16:19], 0\n\t"
    "ds_read_b64_tr_b16 a[136:137], v216 offset:36864\n\t"
    "v_mfma_f32_16x16x32_bf16 a[144:147], a[56:59], a[16:19], 0\n\t"
    "s_and_b32 %[tmp], %[ord], 15\n\t"
    "ds_read_b64_tr_b16 a[138:139], v216 offset:37376\n\t"
    "v_mfma_f32_16x16x32_bf16 v[204:207], a[56:59], a[12:15], 0\n\t"
    "buffer_load_dword v223, v221, %[srdp], %[sp] offen \n\t"
    "s_lshr_b32 %[ord], %[ord], 4\n\t"
    "v_mfma_f32_16x16x32_bf16 a[172:175], a[60:63], a[12:15], 0\n\t"
    "s_add_u32 %[sp], %[sp], %[pstep]\n\t"
    "s_lshl_b32 %[sa], %[tmp], 8\n\t"
    "v_mfma_f32_16x16x32_bf16 a[168:171], a[60:63], a[8:11], 0\n\t"
    "s_mul_i32 %[sq], %[tmp], 786432\n\t"
    "ds_read_b64_tr_b16 a[140:141], v217 offset:36864\n\t"
    "v_mfma_f32_16x16x32_bf16 v[200:203], a[56:59], a[8:11], 0\n\t"
    "s_mul_i32 %[sg], %[tmp], 917504\n\t"
    "ds_read_b64_tr_b16 a[142:143], v217 offset:37376\n\t"
    "v_mfma_f32_16x16x32_bf16 v[196:199], a[56:59], a[4:7], 0\n\t"
    "buffer_load_dword v223, v221, %[srdp], %[sp] offen \n\t"
    "v_mfma_f32_16x16x32_bf16 a[164:167], a[60:63], a[4:7], 0\n\t"
    "s_add_u32 %[sp], %[sp], %[pstep]\n\t"
    "v_mfma_f32_16x16x32_bf16 a[160:163], a[60:63], a[0:3], 0\n\t"
    "v_mfma_f32_16x16x32_bf16 v[192:195], a[56:59], a[0:3], 0\n\t"
    "v_mfma_f32_16x16x32_bf16 a[192:195], a[64:67], a[0:3], 0\n\t"
    "v_mfma_f32_16x16x32_bf16 a[224:227], a[68:71], a[0:3], 0\n\t"
    "v_mfma_f32_16x16x32_bf16 a[228:231], a[68:71], a[4:7], 0\n\t"
    "s_cbranch_vccnz 140f\n\t"
    "24:\n\t"
    "v_mfma_f32_16x16x32_bf16 a[196:199], a[64:67], a[4:7], 0\n\t"
    "v_mfma_f32_16x16x32_bf16 a[200:203], a[64:67], a[8:11], 0\n\t"
    "v_mfma_f32_16x16x32_bf16 a[232:235], a[68:71], a[8:11], 0\n\t"
    "v_mfma_f32_16x16x32_bf16 a[236:239], a[68:71], a[12:15], 0\n\t"
    "v_mfma_f32_16x16x32_bf16 a[204:207], a[64:67], a[12:15], 0\n\t"
    "v_mfma_f32_16x16x32_bf16 a[208:211], a[64:67], a[16:19], 0\n\t"
    "v_mfma_f32_16x16x32_bf16 a[240:243], a[68:71], a[16:19], 0\n\t"
    "v_mfma_f32_16x16x32_bf16 a[244:247], a[68:71], a[20:23], 0\n\t"
    "v_mfma_f32_16x16x32_bf16 a[212:215], a[64:67], a[20:23], 0\n\t"
    "v_mfma_f32_16x16x32_bf16 a[216:219], a[64:67], a[24:27], 0\n\t"
    "v_mfma_f32_16x16x32_bf16 a[248:251], a[68:71], a[24:27], 0\n\t"
    "v_mfma_f32_16x16x32_bf16 a[252:255], a[68:71], a[28:31], 0\n\t"
    "v_mfma_f32_16x16x32_bf16 a[220:223], a[64:67], a[28:31], 0\n\t"
    "s_branch 8f\n\t"
    "1:\n\t"
    "s_cmp_eq_u32 %[cnt], %[mine]\n\t"
    "s_cselect_b64 vcc, -1, 0\n\t"
    "v_mfma_f32_16x16x32_bf16 v[0:3], a[32:35], a[0:3], v[0:3]\n\t"
    "ds_read_b64_tr_b16 a[104:105], v216 offset:4096\n\t"
    "v_mfma_f32_16x16x32_bf16 v[32:35], a[36:39], a[0:3], v[32:35]\n\t"
    "v_mfma_f32_16x16x32_bf16 v[36:39], a[36:39], a[4:7], v[36:39]\n\t"
    "ds_read_b64_tr_b16 a[106:107], v216 offset:4608\n\t"
    "v_mfma_f32_16x16x32_bf16 v[4:7], a[32:35], a[4:7], v[4:7]\n\t"
    "v_mfma_f32_16x16x32_bf16 v[8:11], a[32:35], a[8:11], v[8:11]\n\t"
    "ds_read_b64_tr_b16 a[108:109], v217 offset:4096\n\t"
    "v_mfma_f32_16x16x32_bf16 v[40:43], a[36:39], a[8:11], v[40:43]\n\t"
    "v_mfma_f32_16x16x32_bf16 v[44:47], a[36:39], a[12:15], v[44:47]\n\t"
    "ds_read_b64_tr_b16 a[110:111], v217 offset:4608\n\t"
    "v_mfma_f32_16x16x32_bf16 v[12:15], a[32:35], a[12:15], v[12:15]\n\t"
    "v_mfma_f32_16x16x32_bf16 v[16:19], a[32:35], a[16:19], v[16:19]\n\t"
    "ds_read_b128 a[72:75], v215 offset:0\n\t"
    "v_mfma_f32_16x16x32_bf16 v[48:51], a[36:39], a[16:19], v[48:51]\n\t"
    "v_mfma_f32_16x16x32_bf16 v[52:55], a[36:39], a[20:23], v[52:55]\n\t"
    "v_mfma_f32_16x16x32_bf16 v[20:23], a[32:35], a[20:23], v[20:23]\n\t"
    "ds_read_b128 a[76:79], v215 offset:2048\n\t"
    "v_mfma_f32_16x16x32_bf16 v[24:27], a[32:35], a[24:27], v[24:27]\n\t"
    "v_mfma_f32_16x16x32_bf16 v[56:59], a[36:39], a[24:27], v[56:59]\n\t"
    "ds_read_b128 a[80:83], v215 offset:4096\n\t"
    "v_mfma_f32_16x16x32_bf16 v[60:63], a[36:39], a[28:31], v[60:63]\n\t"
    "v_mfma_f32_16x16x32_bf16 v[28:31], a[32:35], a[28:31], v[28:31]\n\t"
    "ds_read_b128 a[84:87], v215 offset:6144\n\t"
    "v_mfma_f32_16x16x32_bf16 v[92:95], a[28:31], a[40:43], v[92:95]\n\t"
    "v_mfma_f32_16x16x32_bf16 v[124:127], a[28:31], a[44:47], v[124:127]\n\t"
    "ds_read_b128 a[88:91], v215 offset:8192\n\t"
    "v_mfma_f32_16x16x32_bf16 v[120:123], a[24:27], a[44:47], v[120:123]\n\t"
    "v_mfma_f32_16x16x32_bf16 v[88:91], a[24:27], a[40:43], v[88:91]\n\t"
    "ds_read_b128 a[92:95], v215 offset:10240\n\t"
    "v_mfma_f32_16x16x32_bf16 v[84:87], a[20:23], a[40:43], v[84:87]\n\t"
    "v_mfma_f32_16x16x32_bf16 v[116:119], a[20:23], a[44:47], v[116:119]\n\t"
    "v_mfma_f32_16x16x32_bf16 v[112:115], a[16:19], a[44:47], v[112:115]\n\t"
    "ds_read_b128 a[96:99], v215 offset:12288\n\t"
    "v_mfma_f32_16x16x32_bf16 v[80:83], a[16:19], a[40:43], v[80:83]\n\t"
    "v_mfma_f32_16x16x32_bf16 v[76:79], a[12:15], a[40:43], v[76:79]\n\t"
    "ds_read_b128 a[100:103], v215 offset:14336\n\t"
    "v_mfma_f32_16x16x32_bf16 v[108:111], a[12:15], a[44:47], v[108:111]\n\t"
    "v_mfma_f32_16x16x32_bf16 v[104:107], a[8:11], a[44:47], v[104:107]\n\t"
    "ds_read_b64_tr_b16 a[112:113], v216 offset:12288\n\t"
    "v_mfma_f32_16x16x32_bf16 v[72:75], a[8:11], a[40:43], v[72:75]\n\t"
    "v_mfma_f32_16x16x32_bf16 v[68:71], a[4:7], a[40:43], v[68:71]\n\t"
    "ds_read_b64_tr_b16 a[114:115], v216 offset:12800\n\t"
    "v_mfma_f32_16x16x32_bf16 v[100:103], a[4:7], a[44:47], v[100:103]\n\t"
    "v_mfma_f32_16x16x32_bf16 v[96:99], a[0:3], a[44:47], v[96:99]\n\t"
    "v_mfma_f32_16x16x32_bf16 v[64:67], a[0:3], a[40:43], v[64:67]\n\t"
    "ds_read_b64_tr_b16 a[116:117], v217 offset:12288\n\t"
    "v_mfma_f32_16x16x32_bf16 v[128:131], a[0:3], a[48:51], v[128:131]\n\t"
    "v_mfma_f32_16x16x32_bf16 v[160:163], a[0:3], a[52:55], v[160:163]\n\t"
    "ds_read_b64_tr_b16 a[118:119], v217 offset:12800\n\t"
    "v_mfma_f32_16x16x32_bf16 v[164:167], a[4:7], a[52:55], v[164:167]\n\t"
    "v_mfma_f32_16x16x32_bf16 v[132:135], a[4:7], a[48:51], v[132:135]\n\t"
    "ds_read_b64_tr_b16 a[120:121], v216 offset:20480\n\t"
    "v_mfma_f32_16x16x32_bf16 v[136:139], a[8:11], a[48:51], v[136:139]\n\t"
    "v_mfma_f32_16x16x32_bf16 v[168:171], a[8:11], a[52:55], v[168:171]\n\t"
    "ds_read_b64_tr_b16 a[122:123], v216 offset:20992\n\t"
    "v_mfma_f32_16x16x32_bf16 v[172:175], a[12:15], a[52:55], v[172:175]\n\t"
    "v_mfma_f32_16x16x32_bf16 v[140:143], a[12:15], a[48:51], v[140:143]\n\t"
    "ds_read_b64_tr_b16 a[124:125], v217 offset:20480\n\t"
    "v_mfma_f32_16x16x32_bf16 v[144:147], a[16:19], a[48:51], v[144:147]\n\t"
    "v_mfma_f32_16x16x32_bf16 v[176:179], a[16:19], a[52:55], v[176:179]\n\t"
    "v_mfma_f32_16x16x32_bf16 v[180:183], a[20:23], a[52:55], v[180:183]\n\t"
    "ds_read_b64_tr_b16 a[126:127], v217 offset:20992\n\t"
    "v_mfma_f32_16x16x32_bf16 v[148:151], a[20:23], a[48:51], v[148:151]\n\t"
    "v_mfma_f32_16x16x32_bf16 v[152:155], a[24:27], a[48:51], v[152:155]\n\t"
    "ds_read_b64_tr_b16 a[128:129], v216 offset:28672\n\t"
    "v_mfma_f32_16x16x32_bf16 v[184:187], a[24:27], a[52:55], v[184:187]\n\t"
    "v_mfma_f32_16x16x32_bf16 v[188:191], a[28:31], a[52:55], v[188:191]\n\t"
    "ds_read_b64_tr_b16 a[130:131], v216 offset:29184\n\t"
    "v_mfma_f32_16x16x32_bf16 v[156:159], a[28:31], a[48:51], v[156:159]\n\t"
    "v_mfma_f32_16x16x32_bf16 a[156:159], a[56:59], a[28:31], a[156:159]\n\t"
    "ds_read_b64_tr_b16 a[132:133], v217 offset:28672\n\t"
    "v_mfma_f32_16x16x32_bf16 a[188:191], a[60:63], a[28:31], a[188:191]\n\t"
    "v_mfma_f32_16x16x32_bf16 a[184:187], a[60:63], a[24:27], a[184:187]\n\t"
    "ds_read_b64_tr_b16 a[134:135], v217 offset:29184\n\t"
    "v_mfma_f32_16x16x32_bf16 a[152:155], a[56:59], a[24:27], a[152:155]\n\t"
    "v_mfma_f32_16x16x32_bf16 a[148:151], a[56:59], a[20:23], a[148:151]\n\t"
    "v_mfma_f32_16x16x32_bf16 a[180:183], a[60:63], a[20:23], a[180:183]\n\t"
    "ds_read_b64_tr_b16 a[136:137], v216 offset:36864\n\t"
    "v_mfma_f32_16x16x32_bf16 a[176:179], a[60:63], a[16:19], a[176:179]\n\t"
    "v_mfma_f32_16x16x32_bf16 a[144:147], a[56:59], a[16:19], a[144:147]\n\t"
    "ds_read_b64_tr_b16 a[138:139], v216 offset:37376\n\t"
    "v_mfma_f32_16x16x32_bf16 v[204:207], a[56:59], a[12:15], v[204:207]\n\t"
    "v_mfma_f32_16x16x32_bf16 a[172:175], a[60:63], a[12:15], a[172:175]\n\t"
    "ds_read_b64_tr_b16 a[140:141], v217 offset:36864\n\t"
    "v_mfma_f32_16x16x32_bf16 a[168:171], a[60:63], a[8:11], a[168:171]\n\t"
    "v_mfma_f32_16x16x32_bf16 v[200:203], a[56:59], a[8:11], v[200:203]\n\t"
    "ds_read_b64_tr_b16 a[142:143], v217 offset:37376\n\t"
    "v_mfma_f32_16x16x32_bf16 v[196:199], a[56:59], a[4:7], v[196:199]\n\t"
    "v_mfma_f32_16x16x32_bf16 a[164:167], a[60:63], a[4:7], a[164:167]\n\t"
    "v_mfma_f32_16x16x32_bf16 a[160:163], a[60:63], a[0:3], a[160:163]\n\t"
    "v_mfma_f32_16x16x32_bf16 v[192:195], a[56:59], a[0:3], v[192:195]\n\t"
    "v_mfma_f32_16x16x32_bf16 a[192:195], a[64:67], a[0:3], a[192:195]\n\t"
    "v_mfma_f32_16x16x32_bf16 a[224:227], a[68:71], a[0:3], a[224:227]\n\t"
    "v_mfma_f32_16x16x32_bf16 a[228:231], a[68:71], a[4:7], a[228:231]\n\t"
    "s_cbranch_vccnz 100f\n\t"
    "20:\n\t"
    "v_mfma_f32_16x16x32_bf16 a[196:199], a[64:67], a[4:7], a[196:199]\n\t"
    "v_mfma_f32_16x16x32_bf16 a[200:203], a[64:67], a[8:11], a[200:203]\n\t"
    "v_mfma_f32_16x16x32_bf16 a[232:235], a[68:71], a[8:11], a[232:235]\n\t"
    "v_mfma_f32_16x16x32_bf16 a[236:239], a[68:71], a[12:15], a[236:239]\n\t"
    "v_mfma_f32_16x16x32_bf16 a[204:207], a[64:67], a[12:15], a[204:207]\n\t"
    "v_mfma_f32_16x16x32_bf16 a[208:211], a[64:67], a[16:19], a[208:211]\n\t"
    "v_mfma_f32_16x16x32_bf16 a[240:243], a[68:71], a[16:19], a[240:243]\n\t"
    "v_mfma_f32_16x16x32_bf16 a[244:247], a[68:71], a[20:23], a[244:247]\n\t"
    "v_mfma_f32_16x16x32_bf16 a[212:215], a[64:67], a[20:23], a[212:215]\n\t"
    "v_mfma_f32_16x16x32_bf16 a[216:219], a[64:67], a[24:27], a[216:219]\n\t"
    "v_mfma_f32_16x16x32_bf16 a[248:251], a[68:71], a[24:27], a[248:251]\n\t"
    "v_mfma_f32_16x16x32_bf16 a[252:255], a[68:71], a[28:31], a[252:255]\n\t"
    "v_mfma_f32_16x16x32_bf16 a[220:223], a[64:67], a[28:31], a[220:223]\n\t"
    "8:\n\t"
    "s_waitcnt vmcnt(2) lgkmcnt(0)\n\t"
    "s_barrier\n\t"
    "v_mfma_f32_16x16x32_bf16 v[0:3], a[104:107], a[72:75], v[0:3]\n\t"
    "v_mfma_f32_16x16x32_bf16 v[32:35], a[108:111], a[72:75], v[32:35]\n\t"
    "s_add_u32 m0, %[m0a], 0\n\t"
    "ds_read_b64_tr_b16 a[32:33], v218 offset:0\n\t"
    "v_mfma_f32_16x16x32_bf16 v[36:39], a[108:111], a[76:79], v[36:39]\n\t"
    "buffer_load_dwordx4 v208, %[srda], %[sa] offen offset:0 lds\n\t"
    "v_mfma_f32_16x16x32_bf16 v[4:7], a[104:107], a[76:79], v[4:7]\n\t"
    "ds_read_b64_tr_b16 a[34:35], v218 offset:512\n\t"
    "v_mfma_f32_16x16x32_bf16 v[8:11], a[104:107], a[80:83], v[8:11]\n\t"
    "v_mfma_f32_16x16x32_bf16 v[40:43], a[108:111], a[80:83], v[40:43]\n\t"
    "ds_read_b64_tr_b16 a[36:37], v219 offset:0\n\t"
    "v_mfma_f32_16x16x32_bf16 v[44:47], a[108:111], a[84:87], v[44:47]\n\t"
    "buffer_load_dwordx4 v209, %[srda], %[sa] offen offset:1024 lds\n\t"
    "v_mfma_f32_16x16x32_bf16 v[12:15], a[104:107], a[84:87], v[12:15]\n\t"
    "ds_read_b64_tr_b16 a[38:39], v219 offset:512\n\t"
    "v_mfma_f32_16x16x32_bf16 v[16:19], a[104:107], a[88:91], v[16:19]\n\t"
    "v_mfma_f32_16x16x32_bf16 v[48:51], a[108:111], a[88:91], v[48:51]\n\t"
    "s_add_u32 %[tmp], %[sa], 30720\n\t"
    "ds_read_b128 a[0:3], v214 offset:32768\n\t"
    "v_mfma_f32_16x16x32_bf16 v[52:55], a[108:111], a[92:95], v[52:55]\n\t"
    "buffer_load_dwordx4 v208, %[srda], %[tmp] offen offset:2048 lds\n\t"
    "v_mfma_f32_16x16x32_bf16 v[20:23], a[104:107], a[92:95], v[20:23]\n\t"
    "v_mfma_f32_16x16x32_bf16 v[24:27], a[104:107], a[96:99], v[24:27]\n\t"
    "ds_read_b128 a[4:7], v214 offset:34816\n\t"
    "v_mfma_f32_16x16x32_bf16 v[56:59], a[108:111], a[96:99], v[56:59]\n\t"
    "ds_read_b128 a[8:11], v214 offset:36864\n\t"
    "v_mfma_f32_16x16x32_bf16 v[60:63], a[108:111], a[100:103], v[60:63]\n\t"
    "buffer_load_dwordx4 v209, %[srda], %[tmp] offen offset:3072 lds\n\t"
    "v_mfma_f32_16x16x32_bf16 v[28:31], a[104:107], a[100:103], v[28:31]\n\t"
    "v_mfma_f32_16x16x32_bf16 v[92:95], a[100:103], a[112:115], v[92:95]\n\t"
    "s_add_u32 m0, %[m0a], 4096\n\t"
    "ds_read_b128 a[12:15], v214 offset:38912\n\t"
    "v_mfma_f32_16x16x32_bf16 v[124:127], a[100:103], a[116:119], v[124:127]\n\t"
    "s_add_u32 %[tmp], %[sa], 65536\n\t"
    "v_mfma_f32_16x16x32_bf16 v[120:123], a[96:99], a[116:119], v[120:123]\n\t"
    "buffer_load_dwordx4 v208, %[srda], %[tmp] offen offset:0 lds\n\t"
    "v_mfma_f32_16x16x32_bf16 v[88:91], a[96:99], a[112:115], v[88:91]\n\t"
    "ds_read_b128 a[16:19], v214 offset:40960\n\t"
    "v_mfma_f32_16x16x32_bf16 v[84:87], a[92:95], a[112:115], v[84:87]\n\t"
    "ds_read_b128 a[20:23], v214 offset:43008\n\t"
    "v_mfma_f32_16x16x32_bf16 v[116:119], a[92:95], a[116:119], v[116:119]\n\t"
    "v_mfma_f32_16x16x32_bf16 v[112:115], a[88:91], a[116:119], v[112:115]\n\t"
    "buffer_load_dwordx4 v209, %[srda], %[tmp] offen offset:1024 lds\n\t"
    "v_mfma_f32_16x16x32_bf16 v[80:83], a[88:91], a[112:115], v[80:83]\n\t"
    "ds_read_b128 a[24:27], v214 offset:45056\n\t"
    "v_mfma_f32_16x16x32_bf16 v[76:79], a[84:87], a[112:115], v[76:79]\n\t"
    "v_mfma_f32_16x16x32_bf16 v[108:111], a[84:87], a[116:119], v[108:111]\n\t"
    "s_add_u32 %[tmp], %[sa], 96256\n\t"
    "ds_read_b128 a[28:31], v214 offset:47104\n\t"
    "v_mfma_f32_16x16x32_bf16 v[104:107], a[80:83], a[116:119], v[104:107]\n\t"
    "buffer_load_dwordx4 v208, %[srda], %[tmp] offen offset:2048 lds\n\t"
    "v_mfma_f32_16x16x32_bf16 v[72:75], a[80:83], a[112:115], v[72:75]\n\t"
    "ds_read_b64_tr_b16 a[40:41], v218 offset:8192\n\t"
    "v_mfma_f32_16x16x32_bf16 v[68:71], a[76:79], a[112:115], v[68:71]\n\t"
    "v_mfma_f32_16x16x32_bf16 v[100:103], a[76:79], a[116:119], v[100:103]\n\t"
    "ds_read_b64_tr_b16 a[42:43], v218 offset:8704\n\t"
    "v_mfma_f32_16x16x32_bf16 v[96:99], a[72:75], a[116:119], v[96:99]\n\t"
    "buffer_load_dwordx4 v209, %[srda], %[tmp] offen offset:3072 lds\n\t"
    "v_mfma_f32_16x16x32_bf16 v[64:67], a[72:75], a[112:115], v[64:67]\n\t"
    "v_mfma_f32_16x16x32_bf16 v[128:131], a[72:75], a[120:123], v[128:131]\n\t"
    "ds_read_b64_tr_b16 a[44:45], v219 offset:8192\n\t"
    "v_mfma_f32_16x16x32_bf16 v[160:163], a[72:75], a[124:127], v[160:163]\n\t"
    "s_add_u32 m0, %[m0b], 65536\n\t"
    "ds_read_b64_tr_b16 a[46:47], v219 offset:8704\n\t"
    "v_mfma_f32_16x16x32_bf16 v[164:167], a[76:79], a[124:127], v[164:167]\n\t"
    "buffer_load_dwordx4 v210, %[srd0], %[sq] offen offset:0 lds\n\t"
    "v_mfma_f32_16x16x32_bf16 v[132:135], a[76:79], a[120:123], v[132:135]\n\t"
    "v_mfma_f32_16x16x32_bf16 v[136:139], a[80:83], a[120:123], v[136:139]\n\t"
    "ds_read_b64_tr_b16 a[48:49], v218 offset:16384\n\t"
    "v_mfma_f32_16x16x32_bf16 v[168:171], a[80:83], a[124:127], v[168:171]\n\t"
    "ds_read_b64_tr_b16 a[50:51], v218 offset:16896\n\t"
    "v_mfma_f32_16x16x32_bf16 v[172:175], a[84:87], a[124:127], v[172:175]\n\t"
    "buffer_load_dwordx4 v211, %[srd0], %[sq] offen offset:1024 lds\n\t"
    "v_mfma_f32_16x16x32_bf16 v[140:143], a[84:87], a[120:123], v[140:143]\n\t"
    "v_mfma_f32_16x16x32_bf16 v[144:147], a[88:91], a[120:123], v[144:147]\n\t"
    "s_add_u32 m0, %[m0b], 73728\n\t"
    "ds_read_b64_tr_b16 a[52:53], v219 offset:16384\n\t"
    "v_mfma_f32_16x16x32_bf16 v[176:179], a[88:91], a[124:127], v[176:179]\n\t"
    "s_add_u32 %[tmp], %[sq], 2048\n\t"
    "v_mfma_f32_16x16x32_bf16 v[180:183], a[92:95], a[124:127], v[180:183]\n\t"
    "buffer_load_dwordx4 v210, %[srd0], %[tmp] offen offset:0 lds\n\t"
    "v_mfma_f32_16x16x32_bf16 v[148:151], a[92:95], a[120:123], v[148:151]\n\t"
    "ds_read_b64_tr_b16 a[54:55], v219 offset:16896\n\t"
    "v_mfma_f32_16x16x32_bf16 v[152:155], a[96:99], a[120:123], v[152:155]\n\t"
    "ds_read_b64_tr_b16 a[56:57], v218 offset:24576\n\t"
    "v_mfma_f32_16x16x32_bf16 v[184:187], a[96:99], a[124:127], v[184:187]\n\t"
    "v_mfma_f32_16x16x32_bf16 v[188:191], a[100:103], a[124:127], v[188:191]\n\t"
    "buffer_load_dwordx4 v211, %[srd0], %[tmp] offen offset:1024 lds\n\t"
    "v_mfma_f32_16x16x32_bf16 v[156:159], a[100:103], a[120:123], v[156:159]\n\t"
    "ds_read_b64_tr_b16 a[58:59], v218 offset:25088\n\t"
    "v_mfma_f32_16x16x32_bf16 a[156:159], a[128:131], a[100:103], a[156:159]\n\t"
    "s_add_u32 m0, %[m0b], 81920\n\t"
    "v_mfma_f32_16x16x32_bf16 a[188:191], a[132:135], a[100:103], a[188:191]\n\t"
    "s_add_u32 %[tmp], %[sq], 4096\n\t"
    "ds_read_b64_tr_b16 a[60:61], v219 offset:24576\n\t"
    "v_mfma_f32_16x16x32_bf16 a[184:187], a[132:135], a[96:99], a[184:187]\n\t"
    "buffer_load_dwordx4 v210, %[srd0], %[tmp] offen offset:0 lds\n\t"
    "v_mfma_f32_16x16x32_bf16 a[152:155], a[128:131], a[96:99], a[152:155]\n\t"
    "ds_read_b64_tr_b16 a[62:63], v219 offset:25088\n\t"
    "v_mfma_f32_16x16x32_bf16 a[148:151], a[128:131], a[92:95], a[148:151]\n\t"
    "v_mfma_f32_16x16x32_bf16 a[180:183], a[132:135], a[92:95], a[180:183]\n\t"
    "ds_read_b64_tr_b16 a[64:65], v218 offset:32768\n\t"
    "v_mfma_f32_16x16x32_bf16 a[176:179], a[132:135], a[88:91], a[176:179]\n\t"
    "buffer_load_dwordx4 v211, %[srd0], %[tmp] offen offset:1024 lds\n\t"
    "v_mfma_f32_16x16x32_bf16 a[144:147], a[128:131], a[88:91], a[144:147]\n\t"
    "v_mfma_f32_16x16x32_bf16 v[204:207], a[128:131], a[84:87], v[204:207]\n\t"
    "ds_read_b64_tr_b16 a[66:67], v218 offset:33280\n\t"
    "v_mfma_f32_16x16x32_bf16 a[172:175], a[132:135], a[84:87], a[172:175]\n\t"
    "s_add_u32 m0, %[m0b], 90112\n\t"
    "ds_read_b64_tr_b16 a[68:69], v219 offset:32768\n\t"
    "v_mfma_f32_16x16x32_bf16 a[168:171], a[132:135], a[80:83], a[168:171]\n\t"
    "buffer_load_dwordx4 v212, %[srd1], %[sg] offen offset:0 lds\n\t"
    "v_mfma_f32_16x16x32_bf16 v[200:203], a[128:131], a[80:83], v[200:203]\n\t"
    "v_mfma_f32_16x16x32_bf16 v[196:199], a[128:131], a[76:79], v[196:199]\n\t"
    "ds_read_b64_tr_b16 a[70:71], v219 offset:33280\n\t"
    "v_mfma_f32_16x16x32_bf16 a[164:167], a[132:135], a[76:79], a[164:167]\n\t"
    "v_mfma_f32_16x16x32_bf16 a[160:163], a[132:135], a[72:75], a[160:163]\n\t"
    "buffer_load_dwordx4 v213, %[srd1], %[sg] offen offset:1024 lds\n\t"
    "v_mfma_f32_16x16x32_bf16 v[192:195], a[128:131], a[72:75], v[192:195]\n\t"
    "v_mfma_f32_16x16x32_bf16 a[192:195], a[136:139], a[72:75], a[192:195]\n\t"
    "v_mfma_f32_16x16x32_bf16 a[224:227], a[140:143], a[72:75], a[224:227]\n\t"
    "s_add_u32 m0, %[m0b], 98304\n\t"
    "s_cbranch_vccnz 110f\n\t"
    "21:\n\t"
    "v_mfma_f32_16x16x32_bf16 a[228:231], a[140:143], a[76:79], a[228:231]\n\t"
    "buffer_load_dwordx4 v212, %[srd2], %[sg] offen offset:0 lds\n\t"
    "v_mfma_f32_16x16x32_bf16 a[196:199], a[136:139], a[76:79], a[196:199]\n\t"
    "v_mfma_f32_16x16x32_bf16 a[200:203], a[136:139], a[80:83], a[200:203]\n\t"
    "v_mfma_f32_16x16x32_bf16 a[232:235], a[140:143], a[80:83], a[232:235]\n\t"
    "v_mfma_f32_16x16x32_bf16 a[236:239], a[140:143], a[84:87], a[236:239]\n\t"
    "buffer_load_dwordx4 v213, %[srd2], %[sg] offen offset:1024 lds\n\t"
    "v_mfma_f32_16x16x32_bf16 a[204:207], a[136:139], a[84:87], a[204:207]\n\t"
    "v_mfma_f32_16x16x32_bf16 a[208:211], a[136:139], a[88:91], a[208:211]\n\t"
    "v_mfma_f32_16x16x32_bf16 a[240:243], a[140:143], a[88:91], a[240:243]\n\t"
    "s_add_u32 %[sa], %[sa], 128\n\t"
    "v_mfma_f32_16x16x32_bf16 a[244:247], a[140:143], a[92:95], a[244:247]\n\t"
    "buffer_load_dword v223, v221, %[srdp], %[sp] offen \n\t"
    "s_add_u32 %[sq], %[sq], 393216\n\t"
    "v_mfma_f32_16x16x32_bf16 a[212:215], a[136:139], a[92:95], a[212:215]\n\t"
    "s_add_u32 %[sp], %[sp], %[pstep]\n\t"
    "s_add_u32 %[sg], %[sg], 458752\n\t"
    "v_mfma_f32_16x16x32_bf16 a[216:219], a[136:139], a[96:99], a[216:219]\n\t"
    "v_mfma_f32_16x16x32_bf16 a[248:251], a[140:143], a[96:99], a[248:251]\n\t"
    "v_mfma_f32_16x16x32_bf16 a[252:255], a[140:143], a[100:103], a[252:255]\n\t"
    "buffer_load_dword v223, v222, %[srdq], %[sp2] offen\n\t"
    "v_mfma_f32_16x16x32_bf16 a[220:223], a[136:139], a[100:103], a[220:223]\n\t"
    "s_add_u32 %[sp2], %[sp2], %[p2step]\n\t"
    "s_waitcnt lgkmcnt(0)\n\t"
    "s_bitcmp1_b32 %[spec], %[cnt]\n\t"
    "s_cbranch_scc1 33f\n\t"
    "34:\n\t"
    "v_mfma_f32_16x16x32_bf16 v[0:3], a[32:35], a[0:3], v[0:3]\n\t"
    "ds_read_b64_tr_b16 a[104:105], v218 offset:4096\n\t"
    "v_mfma_f32_16x16x32_bf16 v[32:35], a[36:39], a[0:3], v[32:35]\n\t"
    "v_mfma_f32_16x16x32_bf16 v[36:39], a[36:39], a[4:7], v[36:39]\n\t"
    "ds_read_b64_tr_b16 a[106:107], v218 offset:4608\n\t"
    "v_mfma_f32_16x16x32_bf16 v[4:7], a[32:35], a[4:7], v[4:7]\n\t"
    "v_mfma_f32_16x16x32_bf16 v[8:11], a[32:35], a[8:11], v[8:11]\n\t"
    "ds_read_b64_tr_b16 a[108:109], v219 offset:4096\n\t"
    "v_mfma_f32_16x16x32_bf16 v[40:43], a[36:39], a[8:11], v[40:43]\n\t"
    "v_mfma_f32_16x16x32_bf16 v[44:47], a[36:39], a[12:15], v[44:47]\n\t"
    "ds_read_b64_tr_b16 a[110:111], v219 offset:4608\n\t"
    "v_mfma_f32_16x16x32_bf16 v[12:15], a[32:35], a[12:15], v[12:15]\n\t"
    "v_mfma_f32_16x16x32_bf16 v[16:19], a[32:35], a[16:19], v[16:19]\n\t"
    "ds_read_b128 a[72:75], v215 offset:32768\n\t"
    "v_mfma_f32_16x16x32_bf16 v[48:51], a[36:39], a[16:19], v[48:51]\n\t"
    "v_mfma_f32_16x16x32_bf16 v[52:55], a[36:39], a[20:23], v[52:55]\n\t"
    "v_mfma_f32_16x16x32_bf16 v[20:23], a[32:35], a[20:23], v[20:23]\n\t"
    "ds_read_b128 a[76:79], v215 offset:34816\n\t"
    "v_mfma_f32_16x16x32_bf16 v[24:27], a[32:35], a[24:27], v[24:27]\n\t"
    "v_mfma_f32_16x16x32_bf16 v[56:59], a[36:39], a[24:27], v[56:59]\n\t"
    "ds_read_b128 a[80:83], v215 offset:36864\n\t"
    "v_mfma_f32_16x16x32_bf16 v[60:63], a[36:39], a[28:31], v[60:63]\n\t"
    "v_mfma_f32_16x16x32_bf16 v[28:31], a[32:35], a[28:31], v[28:31]\n\t"
    "ds_read_b128 a[84:87], v215 offset:38912\n\t"
    "v_mfma_f32_16x16x32_bf16 v[92:95], a[28:31], a[40:43], v[92:95]\n\t"
    "v_mfma_f32_16x16x32_bf16 v[124:127], a[28:31], a[44:47], v[124:127]\n\t"
    "ds_read_b128 a[88:91], v215 offset:40960\n\t"
    "v_mfma_f32_16x16x32_bf16 v[120:123], a[24:27], a[44:47], v[120:123]\n\t"
    "v_mfma_f32_16x16x32_bf16 v[88:91], a[24:27], a[40:43], v[88:91]\n\t"
    "ds_read_b128 a[92:95], v215 offset:43008\n\t"
    "v_mfma_f32_16x16x32_bf16 v[84:87], a[20:23], a[40:43], v[84:87]\n\t"
    "v_mfma_f32_16x16x32_bf16 v[116:119], a[20:23], a[44:47], v[116:119]\n\t"
    "v_mfma_f32_16x16x32_bf16 v[112:115], a[16:19], a[44:47], v[112:115]\n\t"
    "ds_read_b128 a[96:99], v215 offset:45056\n\t"
    "v_mfma_f32_16x16x32_bf16 v[80:83], a[16:19], a[40:43], v[80:83]\n\t"
    "v_mfma_f32_16x16x32_bf16 v[76:79], a[12:15], a[40:43], v[76:79]\n\t"
    "ds_read_b128 a[100:103], v215 offset:47104\n\t"
    "v_mfma_f32_16x16x32_bf16 v[108:111], a[12:15], a[44:47], v[108:111]\n\t"
    "v_mfma_f32_16x16x32_bf16 v[104:107], a[8:11], a[44:47], v[104:107]\n\t"
    "ds_read_b64_tr_b16 a[112:113], v218 offset:12288\n\t"
    "v_mfma_f32_16x16x32_bf16 v[72:75], a[8:11], a[40:43], v[72:75]\n\t"
    "v_mfma_f32_16x16x32_bf16 v[68:71], a[4:7], a[40:43], v[68:71]\n\t"
    "ds_read_b64_tr_b16 a[114:115], v218 offset:12800\n\t"
    "v_mfma_f32_16x16x32_bf16 v[100:103], a[4:7], a[44:47], v[100:103]\n\t"
    "v_mfma_f32_16x16x32_bf16 v[96:99], a[0:3], a[44:47], v[96:99]\n\t"
    "v_mfma_f32_16x16x32_bf16 v[64:67], a[0:3], a[40:43], v[64:67]\n\t"
    "ds_read_b64_tr_b16 a[116:117], v219 offset:12288\n\t"
    "v_mfma_f32_16x16x32_bf16 v[128:131], a[0:3], a[48:51], v[128:131]\n\t"
    "v_mfma_f32_16x16x32_bf16 v[160:163], a[0:3], a[52:55], v[160:163]\n\t"
    "ds_read_b64_tr_b16 a[118:119], v219 offset:12800\n\t"
    "v_mfma_f32_16x16x32_bf16 v[164:167], a[4:7], a[52:55], v[164:167]\n\t"
    "v_mfma_f32_16x16x32_bf16 v[132:135], a[4:7], a[48:51], v[132:135]\n\t"
    "ds_read_b64_tr_b16 a[120:121], v218 offset:20480\n\t"
    "v_mfma_f32_16x16x32_bf16 v[136:139], a[8:11], a[48:51], v[136:139]\n\t"
    "v_mfma_f32_16x16x32_bf16 v[168:171], a[8:11], a[52:55], v[168:171]\n\t"
    "ds_read_b64_tr_b16 a[122:123], v218 offset:20992\n\t"
    "v_mfma_f32_16x16x32_bf16 v[172:175], a[12:15], a[52:55], v[172:175]\n\t"
    "v_mfma_f32_16x16x32_bf16 v[140:143], a[12:15], a[48:51], v[140:143]\n\t"
    "ds_read_b64_tr_b16 a[124:125], v219 offset:20480\n\t"
    "v_mfma_f32_16x16x32_bf16 v[144:147], a[16:19], a[48:51], v[144:147]\n\t"
    "v_mfma_f32_16x16x32_bf16 v[176:179], a[16:19], a[52:55], v[176:179]\n\t"
    "v_mfma_f32_16x16x32_bf16 v[180:183], a[20:23], a[52:55], v[180:183]\n\t"
    "ds_read_b64_tr_b16 a[126:127], v219 offset:20992\n\t"
    "v_mfma_f32_16x16x32_bf16 v[148:151], a[20:23], a[48:51], v[148:151]\n\t"
    "v_mfma_f32_16x16x32_bf16 v[152:155], a[24:27], a[48:51], v[152:155]\n\t"
    "ds_read_b64_tr_b16 a[128:129], v218 offset:28672\n\t"
    "v_mfma_f32_16x16x32_bf16 v[184:187], a[24:27], a[52:55], v[184:187]\n\t"
    "v_mfma_f32_16x16x32_bf16 v[188:191], a[28:31], a[52:55], v[188:191]\n\t"
    "ds_read_b64_tr_b16 a[130:131], v218 offset:29184\n\t"
    "v_mfma_f32_16x16x32_bf16 v[156:159], a[28:31], a[48:51], v[156:159]\n\t"
    "v_mfma_f32_16x16x32_bf16 a[156:159], a[56:59], a[28:31], a[156:159]\n\t"
    "ds_read_b64_tr_b16 a[132:133], v219 offset:28672\n\t"
    "v_mfma_f32_16x16x32_bf16 a[188:191], a[60:63], a[28:31], a[188:191]\n\t"
    "v_mfma_f32_16x16x32_bf16 a[184:187], a[60:63], a[24:27], a[184:187]\n\t"
    "ds_read_b64_tr_b16 a[134:135], v219 offset:29184\n\t"
    "v_mfma_f32_16x16x32_bf16 a[152:155], a[56:59], a[24:27], a[152:155]\n\t"
    "v_mfma_f32_16x16x32_bf16 a[148:151], a[56:59], a[20:23], a[148:151]\n\t"
    "v_mfma_f32_16x16x32_bf16 a[180:183], a[60:63], a[20:23], a[180:183]\n\t"
    "ds_read_b64_tr_b16 a[136:137], v218 offset:36864\n\t"
    "v_mfma_f32_16x16x32_bf16 a[176:179], a[60:63], a[16:19], a[176:179]\n\t"
    "v_mfma_f32_16x16x32_bf16 a[144:147], a[56:59], a[16:19], a[144:147]\n\t"
    "ds_read_b64_tr_b16 a[138:139], v218 offset:37376\n\t"
    "v_mfma_f32_16x16x32_bf16 v[204:207], a[56:59], a[12:15], v[204:207]\n\t"
    "v_mfma_f32_16x16x32_bf16 a[172:175], a[60:63], a[12:15], a[172:175]\n\t"
    "ds_read_b64_tr_b16 a[140:141], v219 offset:36864\n\t"
    "v_mfma_f32_16x16x32_bf16 a[168:171], a[60:63], a[8:11], a[168:171]\n\t"
    "v_mfma_f32_16x16x32_bf16 v[200:203], a[56:59], a[8:11], v[200:203]\n\t"
    "ds_read_b64_tr_b16 a[142:143], v219 offset:37376\n\t"
    "v_mfma_f32_16x16x32_bf16 v[196:199], a[56:59], a[4:7], v[196:199]\n\t"
    "v_mfma_f32_16x16x32_bf16 a[164:167], a[60:63], a[4:7], a[164:167]\n\t"
    "v_mfma_f32_16x16x32_bf16 a[160:163], a[60:63], a[0:3], a[160:163]\n\t"
    "v_mfma_f32_16x16x32_bf16 v[192:195], a[56:59], a[0:3], v[192:195]\n\t"
    "v_mfma_f32_16x16x32_bf16 a[192:195], a[64:67], a[0:3], a[192:195]\n\t"
    "v_mfma_f32_16x16x32_bf16 a[224:227], a[68:71], a[0:3], a[224:227]\n\t"
    "v_mfma_f32_16x16x32_bf16 a[228:231], a[68:71], a[4:7], a[228:231]\n\t"
    "s_cbranch_vccnz 120f\n\t"
    "22:\n\t"
    "v_mfma_f32_16x16x32_bf16 a[196:199], a[64:67], a[4:7], a[196:199]\n\t"
    "v_mfma_f32_16x16x32_bf16 a[200:203], a[64:67], a[8:11], a[200:203]\n\t"
    "v_mfma_f32_16x16x32_bf16 a[232:235], a[68:71], a[8:11], a[232:235]\n\t"
    "v_mfma_f32_16x16x32_bf16 a[236:239], a[68:71], a[12:15], a[236:239]\n\t"
    "v_mfma_f32_16x16x32_bf16 a[204:207], a[64:67], a[12:15], a[204:207]\n\t"
    "v_mfma_f32_16x16x32_bf16 a[208:211], a[64:67], a[16:19], a[208:211]\n\t"
    "v_mfma_f32_16x16x32_bf16 a[240:243], a[68:71], a[16:19], a[240:243]\n\t"
    "v_mfma_f32_16x16x32_bf16 a[244:247], a[68:71], a[20:23], a[244:247]\n\t"
    "v_mfma_f32_16x16x32_bf16 a[212:215], a[64:67], a[20:23], a[212:215]\n\t"
    "v_mfma_f32_16x16x32_bf16 a[216:219], a[64:67], a[24:27], a[216:219]\n\t"
    "v_mfma_f32_16x16x32_bf16 a[248:251], a[68:71], a[24:27], a[248:251]\n\t"
    "v_mfma_f32_16x16x32_bf16 a[252:255], a[68:71], a[28:31], a[252:255]\n\t"
    "v_mfma_f32_16x16x32_bf16 a[220:223], a[64:67], a[28:31], a[220:223]\n\t"
    "s_waitcnt vmcnt(2) lgkmcnt(0)\n\t"
    "s_barrier\n\t"
    "v_mfma_f32_16x16x32_bf16 v[0:3], a[104:107], a[72:75], v[0:3]\n\t"
    "v_mfma_f32_16x16x32_bf16 v[32:35], a[108:111], a[72:75], v[32:35]\n\t"
    "s_add_u32 m0, %[m0a], 32768\n\t"
    "ds_read_b64_tr_b16 a[32:33], v216 offset:0\n\t"
    "v_mfma_f32_16x16x32_bf16 v[36:39], a[108:111], a[76:79], v[36:39]\n\t"
    "buffer_load_dwordx4 v208, %[srda], %[sa] offen offset:0 lds\n\t"
    "v_mfma_f32_16x16x32_bf16 v[4:7], a[104:107], a[76:79], v[4:7]\n\t"
    "ds_read_b64_tr_b16 a[34:35], v216 offset:512\n\t"
    "v_mfma_f32_16x16x32_bf16 v[8:11], a[104:107], a[80:83], v[8:11]\n\t"
    "v_mfma_f32_16x16x32_bf16 v[40:43], a[108:111], a[80:83], v[40:43]\n\t"
    "ds_read_b64_tr_b16 a[36:37], v217 offset:0\n\t"
    "v_mfma_f32_16x16x32_bf16 v[44:47], a[108:111], a[84:87], v[44:47]\n\t"
    "buffer_load_dwordx4 v209, %[srda], %[sa] offen offset:1024 lds\n\t"
    "v_mfma_f32_16x16x32_bf16 v[12:15], a[104:107], a[84:87], v[12:15]\n\t"
    "ds_read_b64_tr_b16 a[38:39], v217 offset:512\n\t"
    "v_mfma_f32_16x16x32_bf16 v[16:19], a[104:107], a[88:91], v[16:19]\n\t"
    "v_mfma_f32_16x16x32_bf16 v[48:51], a[108:111], a[88:91], v[48:51]\n\t"
    "s_add_u32 %[tmp], %[sa], 30720\n\t"
    "ds_read_b128 a[0:3], v214 offset:0\n\t"
    "v_mfma_f32_16x16x32_bf16 v[52:55], a[108:111], a[92:95], v[52:55]\n\t"
    "buffer_load_dwordx4 v208, %[srda], %[tmp] offen offset:2048 lds\n\t"
    "v_mfma_f32_16x16x32_bf16 v[20:23], a[104:107], a[92:95], v[20:23]\n\t"
    "v_mfma_f32_16x16x32_bf16 v[24:27], a[104:107], a[96:99], v[24:27]\n\t"
    "ds_read_b128 a[4:7], v214 offset:2048\n\t"
    "v_mfma_f32_16x16x32_bf16 v[56:59], a[108:111], a[96:99], v[56:59]\n\t"
    "ds_read_b128 a[8:11], v214 offset:4096\n\t"
    "v_mfma_f32_16x16x32_bf16 v[60:63], a[108:111], a[100:103], v[60:63]\n\t"
    "buffer_load_dwordx4 v209, %[srda], %[tmp] offen offset:3072 lds\n\t"
    "v_mfma_f32_16x16x32_bf16 v[28:31], a[104:107], a[100:103], v[28:31]\n\t"
    "v_mfma_f32_16x16x32_bf16 v[92:95], a[100:103], a[112:115], v[92:95]\n\t"
    "s_add_u32 m0, %[m0a], 36864\n\t"
    "ds_read_b128 a[12:15], v214 offset:6144\n\t"
    "v_mfma_f32_16x16x32_bf16 v[124:127], a[100:103], a[116:119], v[124:127]\n\t"
    "s_add_u32 %[tmp], %[sa], 65536\n\t"
    "v_mfma_f32_16x16x32_bf16 v[120:123], a[96:99], a[116:119], v[120:123]\n\t"
    "buffer_load_dwordx4 v208, %[srda], %[tmp] offen offset:0 lds\n\t"
    "v_mfma_f32_16x16x32_bf16 v[88:91], a[96:99], a[112:115], v[88:91]\n\t"
    "ds_read_b128 a[16:19], v214 offset:8192\n\t"
    "v_mfma_f32_16x16x32_bf16 v[84:87], a[92:95], a[112:115], v[84:87]\n\t"
    "ds_read_b128 a[20:23], v214 offset:10240\n\t"
    "v_mfma_f32_16x16x32_bf16 v[116:119], a[92:95], a[116:119], v[116:119]\n\t"
    "v_mfma_f32_16x16x32_bf16 v[112:115], a[88:91], a[116:119], v[112:115]\n\t"
    "buffer_load_dwordx4 v209, %[srda], %[tmp] offen offset:1024 lds\n\t"
    "v_mfma_f32_16x16x32_bf16 v[80:83], a[88:91], a[112:115], v[80:83]\n\t"
    "ds_read_b128 a[24:27], v214 offset:12288\n\t"
    "v_mfma_f32_16x16x32_bf16 v[76:79], a[84:87], a[112:115], v[76:79]\n\t"
    "v_mfma_f32_16x16x32_bf16 v[108:111], a[84:87], a[116:119], v[108:111]\n\t"
    "s_add_u32 %[tmp], %[sa], 96256\n\t"
    "ds_read_b128 a[28:31], v214 offset:14336\n\t"
    "v_mfma_f32_16x16x32_bf16 v[104:107], a[80:83], a[116:119], v[104:107]\n\t"
    "buffer_load_dwordx4 v208, %[srda], %[tmp] offen offset:2048 lds\n\t"
    "v_mfma_f32_16x16x32_bf16 v[72:75], a[80:83], a[112:115], v[72:75]\n\t"
    "ds_read_b64_tr_b16 a[40:41], v216 offset:8192\n\t"
    "v_mfma_f32_16x16x32_bf16 v[68:71], a[76:79], a[112:115], v[68:71]\n\t"
    "v_mfma_f32_16x16x32_bf16 v[100:103], a[76:79], a[116:119], v[100:103]\n\t"
    "ds_read_b64_tr_b16 a[42:43], v216 offset:8704\n\t"
    "v_mfma_f32_16x16x32_bf16 v[96:99], a[72:75], a[116:119], v[96:99]\n\t"
    "buffer_load_dwordx4 v209, %[srda], %[tmp] offen offset:3072 lds\n\t"
    "v_mfma_f32_16x16x32_bf16 v[64:67], a[72:75], a[112:115], v[64:67]\n\t"
    "v_mfma_f32_16x16x32_bf16 v[128:131], a[72:75], a[120:123], v[128:131]\n\t"
    "ds_read_b64_tr_b16 a[44:45], v217 offset:8192\n\t"
    "v_mfma_f32_16x16x32_bf16 v[160:163], a[72:75], a[124:127], v[160:163]\n\t"
    "s_add_u32 m0, %[m0b], 106496\n\t"
    "ds_read_b64_tr_b16 a[46:47], v217 offset:8704\n\t"
    "v_mfma_f32_16x16x32_bf16 v[164:167], a[76:79], a[124:127], v[164:167]\n\t"
    "buffer_load_dwordx4 v210, %[srd0], %[sq] offen offset:0 lds\n\t"
    "v_mfma_f32_16x16x32_bf16 v[132:135], a[76:79], a[120:123], v[132:135]\n\t"
    "v_mfma_f32_16x16x32_bf16 v[136:139], a[80:83], a[120:123], v[136:139]\n\t"
    "ds_read_b64_tr_b16 a[48:49], v216 offset:16384\n\t"
    "v_mfma_f32_16x16x32_bf16 v[168:171], a[80:83], a[124:127], v[168:171]\n\t"
    "ds_read_b64_tr_b16 a[50:51], v216 offset:16896\n\t"
    "v_mfma_f32_16x16x32_bf16 v[172:175], a[84:87], a[124:127], v[172:175]\n\t"
    "buffer_load_dwordx4 v211, %[srd0], %[sq] offen offset:1024 lds\n\t"
    "v_mfma_f32_16x16x32_bf16 v[140:143], a[84:87], a[120:123], v[140:143]\n\t"
    "v_mfma_f32_16x16x32_bf16 v[144:147], a[88:91], a[120:123], v[144:147]\n\t"
    "s_add_u32 m0, %[m0b], 114688\n\t"
    "ds_read_b64_tr_b16 a[52:53], v217 offset:16384\n\t"
    "v_mfma_f32_16x16x32_bf16 v[176:179], a[88:91], a[124:127], v[176:179]\n\t"
    "s_add_u32 %[tmp], %[sq], 2048\n\t"
    "v_mfma_f32_16x16x32_bf16 v[180:183], a[92:95], a[124:127], v[180:183]\n\t"
    "buffer_load_dwordx4 v210, %[srd0], %[tmp] offen offset:0 lds\n\t"
    "v_mfma_f32_16x16x32_bf16 v[148:151], a[92:95], a[120:123], v[148:151]\n\t"
    "ds_read_b64_tr_b16 a[54:55], v217 offset:16896\n\t"
    "v_mfma_f32_16x16x32_bf16 v[152:155], a[96:99], a[120:123], v[152:155]\n\t"
    "ds_read_b64_tr_b16 a[56:57], v216 offset:24576\n\t"
    "v_mfma_f32_16x16x32_bf16 v[184:187], a[96:99], a[124:127], v[184:187]\n\t"
    "v_mfma_f32_16x16x32_bf16 v[188:191], a[100:103], a[124:127], v[188:191]\n\t"
    "buffer_load_dwordx4 v211, %[srd0], %[tmp] offen offset:1024 lds\n\t"
    "v_mfma_f32_16x16x32_bf16 v[156:159], a[100:103], a[120:123], v[156:159]\n\t"
    "ds_read_b64_tr_b16 a[58:59], v216 offset:25088\n\t"
    "v_mfma_f32_16x16x32_bf16 a[156:159], a[128:131], a[100:103], a[156:159]\n\t"
    "s_add_u32 m0, %[m0b], 122880\n\t"
    "v_mfma_f32_16x16x32_bf16 a[188:191], a[132:135], a[100:103], a[188:191]\n\t"
    "s_add_u32 %[tmp], %[sq], 4096\n\t"
    "ds_read_b64_tr_b16 a[60:61], v217 offset:24576\n\t"
    "v_mfma_f32_16x16x32_bf16 a[184:187], a[132:135], a[96:99], a[184:187]\n\t"
    "buffer_load_dwordx4 v210, %[srd0], %[tmp] offen offset:0 lds\n\t"
    "v_mfma_f32_16x16x32_bf16 a[152:155], a[128:131], a[96:99], a[152:155]\n\t"
    "ds_read_b64_tr_b16 a[62:63], v217 offset:25088\n\t"
    "v_mfma_f32_16x16x32_bf16 a[148:151], a[128:131], a[92:95], a[148:151]\n\t"
    "v_mfma_f32_16x16x32_bf16 a[180:183], a[132:135], a[92:95], a[180:183]\n\t"
    "ds_read_b64_tr_b16 a[64:65], v216 offset:32768\n\t"
    "v_mfma_f32_16x16x32_bf16 a[176:179], a[132:135], a[88:91], a[176:179]\n\t"
    "buffer_load_dwordx4 v211, %[srd0], %[tmp] offen offset:1024 lds\n\t"
    "v_mfma_f32_16x16x32_bf16 a[144:147], a[128:131], a[88:91], a[144:147]\n\t"
    "v_mfma_f32_16x16x32_bf16 v[204:207], a[128:131], a[84:87], v[204:207]\n\t"
    "ds_read_b64_tr_b16 a[66:67], v216 offset:33280\n\t"
    "v_mfma_f32_16x16x32_bf16 a[172:175], a[132:135], a[84:87], a[172:175]\n\t"
    "s_add_u32 m0, %[m0b], 131072\n\t"
    "ds_read_b64_tr_b16 a[68:69], v217 offset:32768\n\t"
    "v_mfma_f32_16x16x32_bf16 a[168:171], a[132:135], a[80:83], a[168:171]\n\t"
    "buffer_load_dwordx4 v212, %[srd1], %[sg] offen offset:0 lds\n\t"
    "v_mfma_f32_16x16x32_bf16 v[200:203], a[128:131], a[80:83], v[200:203]\n\t"
    "v_mfma_f32_16x16x32_bf16 v[196:199], a[128:131], a[76:79], v[196:199]\n\t"
    "ds_read_b64_tr_b16 a[70:71], v217 offset:33280\n\t"
    "v_mfma_f32_16x16x32_bf16 a[164:167], a[132:135], a[76:79], a[164:167]\n\t"
    "v_mfma_f32_16x16x32_bf16 a[160:163], a[132:135], a[72:75], a[160:163]\n\t"
    "buffer_load_dwordx4 v213, %[srd1], %[sg] offen offset:1024 lds\n\t"
    "v_mfma_f32_16x16x32_bf16 v[192:195], a[128:131], a[72:75], v[192:195]\n\t"
    "v_mfma_f32_16x16x32_bf16 a[192:195], a[136:139], a[72:75], a[192:195]\n\t"
    "v_mfma_f32_16x16x32_bf16 a[224:227], a[140:143], a[72:75], a[224:227]\n\t"
    "s_add_u32 m0, %[m0b], 139264\n\t"
    "s_cbranch_vccnz 130f\n\t"
    "23:\n\t"
    "v_mfma_f32_16x16x32_bf16 a[228:231], a[140:143], a[76:79], a[228:231]\n\t"
    "buffer_load_dwordx4 v212, %[srd2], %[sg] offen offset:0 lds\n\t"
    "v_mfma_f32_16x16x32_bf16 a[196:199], a[136:139], a[76:79], a[196:199]\n\t"
    "v_mfma_f32_16x16x32_bf16 a[200:203], a[136:139], a[80:83], a[200:203]\n\t"
    "v_mfma_f32_16x16x32_bf16 a[232:235], a[140:143], a[80:83], a[232:235]\n\t"
    "v_mfma_f32_16x16x32_bf16 a[236:239], a[140:143], a[84:87], a[236:239]\n\t"
    "buffer_load_dwordx4 v213, %[srd2], %[sg] offen offset:1024 lds\n\t"
    "v_mfma_f32_16x16x32_bf16 a[204:207], a[136:139], a[84:87], a[204:207]\n\t"
    "v_mfma_f32_16x16x32_bf16 a[208:211], a[136:139], a[88:91], a[208:211]\n\t"
    "v_mfma_f32_16x16x32_bf16 a[240:243], a[140:143], a[88:91], a[240:243]\n\t"
    "s_and_b32 %[tmp], %[ord], 15\n\t"
    "v_mfma_f32_16x16x32_bf16 a[244:247], a[140:143], a[92:95], a[244:247]\n\t"
    "buffer_load_dword v223, v221, %[srdp], %[sp] offen \n\t"
    "s_lshr_b32 %[ord], %[ord], 4\n\t"
    "v_mfma_f32_16x16x32_bf16 a[212:215], a[136:139], a[92:95], a[212:215]\n\t"
    "s_add_u32 %[sp], %[sp], %[pstep]\n\t"
    "s_lshl_b32 %[sa], %[tmp], 8\n\t"
    "v_mfma_f32_16x16x32_bf16 a[216:219], a[136:139], a[96:99], a[216:219]\n\t"
    "s_mul_i32 %[sq], %[tmp], 786432\n\t"
    "v_mfma_f32_16x16x32_bf16 a[248:251], a[140:143], a[96:99], a[248:251]\n\t"
    "s_mul_i32 %[sg], %[tmp], 917504\n\t"
    "v_mfma_f32_16x16x32_bf16 a[252:255], a[140:143], a[100:103], a[252:255]\n\t"
    "buffer_load_dword v223, v222, %[srdq], %[sp2] offen\n\t"
    "v_mfma_f32_16x16x32_bf16 a[220:223], a[136:139], a[100:103], a[220:223]\n\t"
    "s_add_u32 %[sp2], %[sp2], %[p2step]\n\t"
    "s_waitcnt lgkmcnt(0)\n\t"
    "s_sub_u32 %[cnt], %[cnt], 1\n\t"
    "s_cmp_lg_u32 %[cnt], 0\n\t"
    "s_cbranch_scc1 1b\n\t"
    "s_waitcnt vmcnt(20)\n\t"
    "v_cmp_ne_u64 vcc, s[90:91], v[250:251]\n\t"
    "s_cbranch_vccnz 36f\n\t"
    "38:\n\t"
    "v_lshrrev_b32 v249, 1, v248\n\t"
    "v_add_u32 v252, %[stld], v249\n\t"
    "v_add_u32 v250, %[csld], v249\n\t"
    "v_mov_b32 v251, 0x40000000\n\t"
    "v_cmp_gt_u32 vcc, %[csn], v249\n\t"
    "s_nop 1\n\t"
    "v_cndmask_b32 v250, v251, v250, vcc\n\t"
    "v_add_u32 v251, 5120, v250\n\t"
    "v_mfma_f32_16x16x32_bf16 v[64:67], a[0:3], a[40:43], v[64:67]\n\t"
    "ds_read_b64_tr_b16 a[112:113], v216 offset:12288\n\t"
    "v_mfma_f32_16x16x32_bf16 v[96:99], a[0:3], a[44:47], v[96:99]\n\t"
    "ds_read_b64_tr_b16 a[114:115], v216 offset:12800\n\t"
    "buffer_load_dwordx2 v[224:225], v252, %[srdw], 0 offen sc1\n\t"
    "v_mfma_f32_16x16x32_bf16 v[100:103], a[4:7], a[44:47], v[100:103]\n\t"
    "ds_read_b64_tr_b16 a[116:117], v217 offset:12288\n\t"
    "v_mfma_f32_16x16x32_bf16 v[68:71], a[4:7], a[40:43], v[68:71]\n\t"
    "ds_read_b64_tr_b16 a[118:119], v217 offset:12800\n\t"
    "v_mfma_f32_16x16x32_bf16 v[72:75], a[8:11], a[40:43], v[72:75]\n\t"
    "buffer_load_dwordx2 v[226:227], v250, %[srdw], 0 offen offset:0 sc1\n\t"
    "v_mfma_f32_16x16x32_bf16 v[104:107], a[8:11], a[44:47], v[104:107]\n\t"
    "ds_read_b128 a[72:75], v215 offset:0\n\t"
    "v_mfma_f32_16x16x32_bf16 v[108:111], a[12:15], a[44:47], v[108:111]\n\t"
    "ds_read_b128 a[76:79], v215 offset:2048\n\t"
    "v_mfma_f32_16x16x32_bf16 v[76:79], a[12:15], a[40:43], v[76:79]\n\t"
    "ds_read_b128 a[80:83], v215 offset:4096\n\t"
    "buffer_load_dwordx2 v[228:229], v250, %[srdw], 0 offen offset:1280 sc1\n\t"
    "v_mfma_f32_16x16x32_bf16 v[80:83], a[16:19], a[40:43], v[80:83]\n\t"
    "v_mfma_f32_16x16x32_bf16 v[112:115], a[16:19], a[44:47], v[112:115]\n\t"
    "ds_read_b128 a[84:87], v215 offset:6144\n\t"
    "v_mfma_f32_16x16x32_bf16 v[116:119], a[20:23], a[44:47], v[116:119]\n\t"
    "ds_read_b128 a[88:91], v215 offset:8192\n\t"
    "buffer_load_dwordx2 v[230:231], v250, %[srdw], 0 offen offset:2560 sc1\n\t"
    "v_mfma_f32_16x16x32_bf16 v[84:87], a[20:23], a[40:43], v[84:87]\n\t"
    "ds_read_b128 a[92:95], v215 offset:10240\n\t"
    "v_mfma_f32_16x16x32_bf16 v[88:91], a[24:27], a[40:43], v[88:91]\n\t"
    "v_mfma_f32_16x16x32_bf16 v[120:123], a[24:27], a[44:47], v[120:123]\n\t"
    "ds_read_b128 a[96:99], v215 offset:12288\n\t"
    "buffer_load_dwordx2 v[232:233], v250, %[srdw], 0 offen offset:3840 sc1\n\t"
    "v_mfma_f32_16x16x32_bf16 v[124:127], a[28:31], a[44:47], v[124:127]\n\t"
    "ds_read_b128 a[100:103], v215 offset:14336\n\t"
    "v_mfma_f32_16x16x32_bf16 v[92:95], a[28:31], a[40:43], v[92:95]\n\t"
    "ds_read_b64_tr_b16 a[120:121], v216 offset:20480\n\t"
    "v_mfma_f32_16x16x32_bf16 v[156:159], a[28:31], a[48:51], v[156:159]\n\t"
    "buffer_load_dwordx2 v[234:235], v251, %[srdw], 0 offen offset:0 sc1\n\t"
    "v_mfma_f32_16x16x32_bf16 v[188:191], a[28:31], a[52:55], v[188:191]\n\t"
    "ds_read_b64_tr_b16 a[122:123], v216 offset:20992\n\t"
    "v_mfma_f32_16x16x32_bf16 v[184:187], a[24:27], a[52:55], v[184:187]\n\t"
    "ds_read_b64_tr_b16 a[124:125], v217 offset:20480\n\t"
    "v_mfma_f32_16x16x32_bf16 v[152:155], a[24:27], a[48:51], v[152:155]\n\t"
    "ds_read_b64_tr_b16 a[126:127], v217 offset:20992\n\t"
    "buffer_load_dwordx2 v[236:237], v251, %[srdw], 0 offen offset:1280 sc1\n\t"
    "v_mfma_f32_16x16x32_bf16 v[148:151], a[20:23], a[48:51], v[148:151]\n\t"
    "v_mfma_f32_16x16x32_bf16 v[180:183], a[20:23], a[52:55], v[180:183]\n\t"
    "v_mfma_f32_16x16x32_bf16 v[176:179], a[16:19], a[52:55], v[176:179]\n\t"
    "buffer_load_dwordx2 v[238:239], v251, %[srdw], 0 offen offset:2560 sc1\n\t"
    "v_mfma_f32_16x16x32_bf16 v[144:147], a[16:19], a[48:51], v[144:147]\n\t"
    "v_mfma_f32_16x16x32_bf16 v[140:143], a[12:15], a[48:51], v[140:143]\n\t"
    "v_mfma_f32_16x16x32_bf16 v[172:175], a[12:15], a[52:55], v[172:175]\n\t"
    "buffer_load_dwordx2 v[240:241], v251, %[srdw], 0 offen offset:3840 sc1\n\t"
    "v_mfma_f32_16x16x32_bf16 v[168:171], a[8:11], a[52:55], v[168:171]\n\t"
    "v_mfma_f32_16x16x32_bf16 v[136:139], a[8:11], a[48:51], v[136:139]\n\t"
    "v_mfma_f32_16x16x32_bf16 v[132:135], a[4:7], a[48:51], v[132:135]\n\t"
    "v_mfma_f32_16x16x32_bf16 v[164:167], a[4:7], a[52:55], v[164:167]\n\t"
    "v_mfma_f32_16x16x32_bf16 v[160:163], a[0:3], a[52:55], v[160:163]\n\t"
    "v_mfma_f32_16x16x32_bf16 v[128:131], a[0:3], a[48:51], v[128:131]\n\t"
    "s_waitcnt vmcnt(11) lgkmcnt(0)\n\t"
    "s_barrier\n\t"
    "v_mfma_f32_16x16x32_bf16 v[64:67], a[72:75], a[112:115], v[64:67]\n\t"
    "ds_read_b64_tr_b16 a[40:41], v218 offset:8192\n\t"
    "v_mfma_f32_16x16x32_bf16 v[96:99], a[72:75], a[116:119], v[96:99]\n\t"
    "ds_read_b64_tr_b16 a[42:43], v218 offset:8704\n\t"
    "v_lshrrev_b32 v211, 4, v248\n\t"
    "v_mfma_f32_16x16x32_bf16 v[100:103], a[76:79], a[116:119], v[100:103]\n\t"
    "ds_read_b64_tr_b16 a[44:45], v219 offset:8192\n\t"
    "v_mfma_f32_16x16x32_bf16 v[68:71], a[76:79], a[112:115], v[68:71]\n\t"
    "ds_read_b64_tr_b16 a[46:47], v219 offset:8704\n\t"
    "v_mfma_f32_16x16x32_bf16 v[72:75], a[80:83], a[112:115], v[72:75]\n\t"
    "v_and_b32 v211, 0x30, v211\n\t"
    "v_mfma_f32_16x16x32_bf16 v[104:107], a[80:83], a[116:119], v[104:107]\n\t"
    "ds_read_b128 a[0:3], v214 offset:32768\n\t"
    "v_mfma_f32_16x16x32_bf16 v[108:111], a[84:87], a[116:119], v[108:111]\n\t"
    "ds_read_b128 a[4:7], v214 offset:34816\n\t"
    "v_mfma_f32_16x16x32_bf16 v[76:79], a[84:87], a[112:115], v[76:79]\n\t"
    "ds_read_b128 a[8:11], v214 offset:36864\n\t"
    "v_add_u32 v208, %[lnmr], v211\n\t"
    "v_mfma_f32_16x16x32_bf16 v[80:83], a[88:91], a[112:115], v[80:83]\n\t"
    "v_mfma_f32_16x16x32_bf16 v[112:115], a[88:91], a[116:119], v[112:115]\n\t"
    "ds_read_b128 a[12:15], v214 offset:38912\n\t"
    "v_mfma_f32_16x16x32_bf16 v[116:119], a[92:95], a[116:119], v[116:119]\n\t"
    "ds_read_b128 a[16:19], v214 offset:40960\n\t"
    "v_mfma_f32_16x16x32_bf16 v[84:87], a[92:95], a[112:115], v[84:87]\n\t"
    "ds_read_b128 a[20:23], v214 offset:43008\n\t"
    "v_and_b32 v209, 0xf0, v248\n\t"
    "v_mfma_f32_16x16x32_bf16 v[88:91], a[96:99], a[112:115], v[88:91]\n\t"
    "v_mfma_f32_16x16x32_bf16 v[120:123], a[96:99], a[116:119], v[120:123]\n\t"
    "ds_read_b128 a[24:27], v214 offset:45056\n\t"
    "v_mfma_f32_16x16x32_bf16 v[124:127], a[100:103], a[116:119], v[124:127]\n\t"
    "ds_read_b128 a[28:31], v214 offset:47104\n\t"
    "v_lshrrev_b32 v209, 2, v209\n\t"
    "v_mfma_f32_16x16x32_bf16 v[92:95], a[100:103], a[112:115], v[92:95]\n\t"
    "ds_read_b64_tr_b16 a[48:49], v218 offset:16384\n\t"
    "v_mfma_f32_16x16x32_bf16 v[156:159], a[100:103], a[120:123], v[156:159]\n\t"
    "v_mfma_f32_16x16x32_bf16 v[188:191], a[100:103], a[124:127], v[188:191]\n\t"
    "ds_read_b64_tr_b16 a[50:51], v218 offset:16896\n\t"
    "v_mfma_f32_16x16x32_bf16 v[184:187], a[96:99], a[124:127], v[184:187]\n\t"
    "ds_read_b64_tr_b16 a[52:53], v219 offset:16384\n\t"
    "v_add_u32 v209, %[lcsr], v209\n\t"
    "v_mfma_f32_16x16x32_bf16 v[152:155], a[96:99], a[120:123], v[152:155]\n\t"
    "ds_read_b64_tr_b16 a[54:55], v219 offset:16896\n\t"
    "v_mfma_f32_16x16x32_bf16 v[148:151], a[92:95], a[120:123], v[148:151]\n\t"
    "v_mfma_f32_16x16x32_bf16 v[180:183], a[92:95], a[124:127], v[180:183]\n\t"
    "v_add_u32 v210, %[xt], v248\n\t"
    "v_mfma_f32_16x16x32_bf16 v[176:179], a[88:91], a[124:127], v[176:179]\n\t"
    "v_mfma_f32_16x16x32_bf16 v[144:147], a[88:91], a[120:123], v[144:147]\n\t"
    "v_mfma_f32_16x16x32_bf16 v[140:143], a[84:87], a[120:123], v[140:143]\n\t"
    "v_mfma_f32_16x16x32_bf16 v[172:175], a[84:87], a[124:127], v[172:175]\n\t"
    "v_mfma_f32_16x16x32_bf16 v[168:171], a[80:83], a[124:127], v[168:171]\n\t"
    "v_mfma_f32_16x16x32_bf16 v[136:139], a[80:83], a[120:123], v[136:139]\n\t"
    "v_mfma_f32_16x16x32_bf16 v[132:135], a[76:79], a[120:123], v[132:135]\n\t"
    "v_mfma_f32_16x16x32_bf16 v[164:167], a[76:79], a[124:127], v[164:167]\n\t"
    "v_mfma_f32_16x16x32_bf16 v[160:163], a[72:75], a[124:127], v[160:163]\n\t"
    "v_mfma_f32_16x16x32_bf16 v[128:131], a[72:75], a[120:123], v[128:131]\n\t"
    "s_waitcnt vmcnt(0) lgkmcnt(0)\n\t"
    "v_mfma_f32_16x16x32_bf16 v[64:67], a[0:3], a[40:43], v[64:67]\n\t"
    "ds_read_b64_tr_b16 a[112:113], v218 offset:12288\n\t"
    "v_cmp_ne_u32 vcc, s90, v225\n\t"
    "s_cbranch_vccnz 41f\n\t"
    "42:\n\t"
    "v_mfma_f32_16x16x32_bf16 v[96:99], a[0:3], a[44:47], v[96:99]\n\t"
    "ds_read_b64_tr_b16 a[114:115], v218 offset:12800\n\t"
    "v_lshlrev_b32 v252, 16, v224\n\t"
    "v_mfma_f32_16x16x32_bf16 v[100:103], a[4:7], a[44:47], v[100:103]\n\t"
    "ds_read_b64_tr_b16 a[116:117], v219 offset:12288\n\t"
    "v_and_b32 v254, 0xffff0000, v224\n\t"
    "v_mfma_f32_16x16x32_bf16 v[68:71], a[4:7], a[40:43], v[68:71]\n\t"
    "ds_read_b64_tr_b16 a[118:119], v219 offset:12800\n\t"
    "v_rcp_f32 v253, v254\n\t"
    "v_mfma_f32_16x16x32_bf16 v[72:75], a[8:11], a[40:43], v[72:75]\n\t"
    "v_xor_b32 v252, 0x80000000, v252\n\t"
    "v_mfma_f32_16x16x32_bf16 v[104:107], a[8:11], a[44:47], v[104:107]\n\t"
    "ds_read_b128 a[72:75], v215 offset:32768\n\t"
    "v_mul_f32 v254, v254, v254\n\t"
    "v_mfma_f32_16x16x32_bf16 v[108:111], a[12:15], a[44:47], v[108:111]\n\t"
    "ds_read_b128 a[76:79], v215 offset:34816\n\t"
    "v_mul_f32 v255, 0xbfb8aa3b, v253\n\t"
    "v_mfma_f32_16x16x32_bf16 v[76:79], a[12:15], a[40:43], v[76:79]\n\t"
    "ds_read_b128 a[80:83], v215 offset:36864\n\t"
    "v_add_u32 v249, %[lrow], v248\n\t"
    "v_mfma_f32_16x16x32_bf16 v[80:83], a[16:19], a[40:43], v[80:83]\n\t"
    "v_lshrrev_b32 v250, 2, v248\n\t"
    "v_mfma_f32_16x16x32_bf16 v[112:115], a[16:19], a[44:47], v[112:115]\n\t"
    "ds_read_b128 a[84:87], v215 offset:38912\n\t"
    "v_add_u32 v250, %[lnm], v250\n\t"
    "v_mfma_f32_16x16x32_bf16 v[116:119], a[20:23], a[44:47], v[116:119]\n\t"
    "ds_read_b128 a[88:91], v215 offset:40960\n\t"
    "ds_write_b128 v249, v[252:255]\n\t"
    "v_mfma_f32_16x16x32_bf16 v[84:87], a[20:23], a[40:43], v[84:87]\n\t"
    "ds_read_b128 a[92:95], v215 offset:43008\n\t"
    "ds_write_b32 v250, v252\n\t"
    "v_mfma_f32_16x16x32_bf16 v[88:91], a[24:27], a[40:43], v[88:91]\n\t"
    "ds_write_b32 v250, v253 offset:1024\n\t"
    "v_mfma_f32_16x16x32_bf16 v[120:123], a[24:27], a[44:47], v[120:123]\n\t"
    "ds_read_b128 a[96:99], v215 offset:45056\n\t"
    "v_add_f32 v226, v226, v228\n\t"
    "v_mfma_f32_16x16x32_bf16 v[124:127], a[28:31], a[44:47], v[124:127]\n\t"
    "ds_read_b128 a[100:103], v215 offset:47104\n\t"
    "v_add_f32 v227, v227, v229\n\t"
    "v_mfma_f32_16x16x32_bf16 v[92:95], a[28:31], a[40:43], v[92:95]\n\t"
    "ds_read_b64_tr_b16 a[120:121], v218 offset:20480\n\t"
    "v_add_f32 v230, v230, v232\n\t"
    "v_mfma_f32_16x16x32_bf16 v[156:159], a[28:31], a[48:51], v[156:159]\n\t"
    "v_add_f32 v231, v231, v233\n\t"
    "v_mfma_f32_16x16x32_bf16 v[188:191], a[28:31], a[52:55], v[188:191]\n\t"
    "ds_read_b64_tr_b16 a[122:123], v218 offset:20992\n\t"
    "v_add_f32 v234, v234, v236\n\t"
    "v_mfma_f32_16x16x32_bf16 v[184:187], a[24:27], a[52:55], v[184:187]\n\t"
    "ds_read_b64_tr_b16 a[124:125], v219 offset:20480\n\t"
    "v_add_f32 v235, v235, v237\n\t"
    "v_mfma_f32_16x16x32_bf16 v[152:155], a[24:27], a[48:51], v[152:155]\n\t"
    "ds_read_b64_tr_b16 a[126:127], v219 offset:20992\n\t"
    "v_add_f32 v238, v238, v240\n\t"
    "v_mfma_f32_16x16x32_bf16 v[148:151], a[20:23], a[48:51], v[148:151]\n\t"
    "v_add_f32 v239, v239, v241\n\t"
    "v_mfma_f32_16x16x32_bf16 v[180:183], a[20:23], a[52:55], v[180:183]\n\t"
    "v_add_f32 v226, v226, v230\n\t"
    "v_mfma_f32_16x16x32_bf16 v[176:179], a[16:19], a[52:55], v[176:179]\n\t"
    "v_add_f32 v227, v227, v231\n\t"
    "v_mfma_f32_16x16x32_bf16 v[144:147], a[16:19], a[48:51], v[144:147]\n\t"
    "v_add_f32 v234, v234, v238\n\t"
    "v_mfma_f32_16x16x32_bf16 v[140:143], a[12:15], a[48:51], v[140:143]\n\t"
    "v_add_f32 v235, v235, v239\n\t"
    "v_mfma_f32_16x16x32_bf16 v[172:175], a[12:15], a[52:55], v[172:175]\n\t"
    "v_add_f32 v226, v226, v234\n\t"
    "v_mfma_f32_16x16x32_bf16 v[168:171], a[8:11], a[52:55], v[168:171]\n\t"
    "v_add_f32 v227, v227, v235\n\t"
    "v_mfma_f32_16x16x32_bf16 v[136:139], a[8:11], a[48:51], v[136:139]\n\t"
    "v_lshrrev_b32 v250, 1, v248\n\t"
    "v_mfma_f32_16x16x32_bf16 v[132:135], a[4:7], a[48:51], v[132:135]\n\t"
    "v_add_u32 v250, %[lcs], v250\n\t"
    "v_mfma_f32_16x16x32_bf16 v[164:167], a[4:7], a[52:55], v[164:167]\n\t"
    "ds_write_b64 v250, v[226:227]\n\t"
    "v_mfma_f32_16x16x32_bf16 v[160:163], a[0:3], a[52:55], v[160:163]\n\t"
    "v_mfma_f32_16x16x32_bf16 v[128:131], a[0:3], a[48:51], v[128:131]\n\t"
    "s_waitcnt lgkmcnt(0)\n\t"
    "s_barrier\n\t"
    "v_mfma_f32_16x16x32_bf16 v[64:67], a[72:75], a[112:115], v[64:67]\n\t"
    "ds_read_b128 a[0:3], v214 offset:0\n\t"
    "v_mfma_f32_16x16x32_bf16 v[96:99], a[72:75], a[116:119], v[96:99]\n\t"
    "v_mfma_f32_16x16x32_bf16 v[100:103], a[76:79], a[116:119], v[100:103]\n\t"
    "ds_read_b128 a[4:7], v214 offset:2048\n\t"
    "ds_read_b32 v220, v209 offset:128\n\t"
    "v_mfma_f32_16x16x32_bf16 v[68:71], a[76:79], a[112:115], v[68:71]\n\t"
    "v_mfma_f32_16x16x32_bf16 v[72:75], a[80:83], a[112:115], v[72:75]\n\t"
    "ds_read_b32 v222, v209 offset:192\n\t"
    "v_mfma_f32_16x16x32_bf16 v[104:107], a[80:83], a[116:119], v[104:107]\n\t"
    "ds_read_b128 a[8:11], v214 offset:4096\n\t"
    "v_mfma_f32_16x16x32_bf16 v[108:111], a[84:87], a[116:119], v[108:111]\n\t"
    "ds_read_b32 v212, v209 offset:256\n\t"
    "v_mfma_f32_16x16x32_bf16 v[76:79], a[84:87], a[112:115], v[76:79]\n\t"
    "ds_read_b128 a[12:15], v214 offset:6144\n\t"
    "v_mfma_f32_16x16x32_bf16 v[80:83], a[88:91], a[112:115], v[80:83]\n\t"
    "v_mfma_f32_16x16x32_bf16 v[112:115], a[88:91], a[116:119], v[112:115]\n\t"
    "ds_read_b32 v250, v209 offset:320\n\t"
    "v_mfma_f32_16x16x32_bf16 v[116:119], a[92:95], a[116:119], v[116:119]\n\t"
    "ds_read_b128 a[16:19], v214 offset:8192\n\t"
    "v_mfma_f32_16x16x32_bf16 v[84:87], a[92:95], a[112:115], v[84:87]\n\t"
    "ds_read_b128 v[224:227], v208 offset:0\n\t"
    "v_mfma_f32_16x16x32_bf16 v[88:91], a[96:99], a[112:115], v[88:91]\n\t"
    "v_mfma_f32_16x16x32_bf16 v[120:123], a[96:99], a[116:119], v[120:123]\n\t"
    "ds_read_b128 a[20:23], v214 offset:10240\n\t"
    "ds_read_b128 v[228:231], v208 offset:64\n\t"
    "v_mfma_f32_16x16x32_bf16 v[124:127], a[100:103], a[116:119], v[124:127]\n\t"
    "v_mfma_f32_16x16x32_bf16 v[92:95], a[100:103], a[112:115], v[92:95]\n\t"
    "ds_read_b128 a[24:27], v214 offset:12288\n\t"
    "v_mfma_f32_16x16x32_bf16 v[156:159], a[100:103], a[120:123], v[156:159]\n\t"
    "ds_read_b128 v[232:235], v208 offset:1024\n\t"
    "v_mfma_f32_16x16x32_bf16 v[188:191], a[100:103], a[124:127], v[188:191]\n\t"
    "v_mfma_f32_16x16x32_bf16 v[184:187], a[96:99], a[124:127], v[184:187]\n\t"
    "ds_read_b128 a[28:31], v214 offset:14336\n\t"
    "ds_read_b128 v[236:239], v208 offset:1088\n\t"
    "v_mfma_f32_16x16x32_bf16 v[152:155], a[96:99], a[120:123], v[152:155]\n\t"
    "v_mfma_f32_16x16x32_bf16 v[148:151], a[92:95], a[120:123], v[148:151]\n\t"
    "v_mfma_f32_16x16x32_bf16 v[180:183], a[92:95], a[124:127], v[180:183]\n\t"
    "v_mfma_f32_16x16x32_bf16 v[176:179], a[88:91], a[124:127], v[176:179]\n\t"
    "v_mfma_f32_16x16x32_bf16 v[144:147], a[88:91], a[120:123], v[144:147]\n\t"
    "v_mfma_f32_16x16x32_bf16 v[140:143], a[84:87], a[120:123], v[140:143]\n\t"
    "v_mfma_f32_16x16x32_bf16 v[172:175], a[84:87], a[124:127], v[172:175]\n\t"
    "v_mfma_f32_16x16x32_bf16 v[168:171], a[80:83], a[124:127], v[168:171]\n\t"
    "v_mfma_f32_16x16x32_bf16 v[136:139], a[80:83], a[120:123], v[136:139]\n\t"
    "v_mfma_f32_16x16x32_bf16 v[132:135], a[76:79], a[120:123], v[132:135]\n\t"
    "v_mfma_f32_16x16x32_bf16 v[164:167], a[76:79], a[124:127], v[164:167]\n\t"
    "v_mfma_f32_16x16x32_bf16 v[160:163], a[72:75], a[124:127], v[160:163]\n\t"
    "v_mfma_f32_16x16x32_bf16 v[128:131], a[72:75], a[120:123], v[128:131]\n\t"
    "s_waitcnt lgkmcnt(0)\n\t"
    "v_mfma_f32_16x16x32_bf16 v[0:3], a[32:35], a[0:3], v[0:3]\n\t"
    "ds_read_b64_tr_b16 a[104:105], v216 offset:4096\n\t"
    "v_mfma_f32_16x16x32_bf16 v[32:35], a[36:39], a[0:3], v[32:35]\n\t"
    "ds_read_b64_tr_b16 a[106:107], v216 offset:4608\n\t"
    "v_mfma_f32_16x16x32_bf16 v[36:39], a[36:39], a[4:7], v[36:39]\n\t"
    "v_fmac_f32 v64, v224, v220\n\t"
    "v_fmac_f32 v65, v225, v220\n\t"
    "v_fmac_f32 v66, v226, v220\n\t"
    "v_fmac_f32 v67, v227, v220\n\t"
    "v_fma_f32 v64, v64, v232, 1.0\n\t"
    "v_mfma_f32_16x16x32_bf16 v[4:7], a[32:35], a[4:7], v[4:7]\n\t"
    "ds_read_b64_tr_b16 a[108:109], v217 offset:4096\n\t"
    "v_fma_f32 v65, v65, v233, 1.0\n\t"
    "v_fma_f32 v66, v66, v234, 1.0\n\t"
    "v_fma_f32 v67, v67, v235, 1.0\n\t"
    "v_fma_f32 v240, v64, s92, -s92\n\t"
    "v_mfma_f32_16x16x32_bf16 v[8:11], a[32:35], a[8:11], v[8:11]\n\t"
    "ds_read_b64_tr_b16 a[110:111], v217 offset:4608\n\t"
    "v_fma_f32 v241, v65, s92, -s92\n\t"
    "v_fma_f32 v242, v66, s92, -s92\n\t"
    "v_fma_f32 v243, v67, s92, -s92\n\t"
    "v_mfma_f32_16x16x32_bf16 v[40:43], a[36:39], a[8:11], v[40:43]\n\t"
    "v_exp_f32 v240, v240\n\t"
    "v_exp_f32 v241, v241\n\t"
    "v_mfma_f32_16x16x32_bf16 v[44:47], a[36:39], a[12:15], v[44:47]\n\t"
    "ds_read_b128 a[72:75], v215 offset:0\n\t"
    "v_exp_f32 v242, v242\n\t"
    "v_exp_f32 v243, v243\n\t"
    "v_med3_f32 v64, v64, v240, 1.0\n\t"
    "v_mfma_f32_16x16x32_bf16 v[12:15], a[32:35], a[12:15], v[12:15]\n\t"
    "ds_read_b128 a[76:79], v215 offset:2048\n\t"
    "v_med3_f32 v65, v65, v241, 1.0\n\t"
    "v_med3_f32 v66, v66, v242, 1.0\n\t"
    "v_med3_f32 v67, v67, v243, 1.0\n\t"
    "v_add_f32 v64, s94, v64\n\t"
    "v_mfma_f32_16x16x32_bf16 v[16:19], a[32:35], a[16:19], v[16:19]\n\t"
    "v_add_f32 v65, s94, v65\n\t"
    "v_add_f32 v66, s94, v66\n\t"
    "v_add_f32 v67, s94, v67\n\t"
    "v_fmac_f32 v68, v228, v220\n\t"
    "v_fmac_f32 v69, v229, v220\n\t"
    "v_mfma_f32_16x16x32_bf16 v[48:51], a[36:39], a[16:19], v[48:51]\n\t"
    "ds_read_b128 a[80:83], v215 offset:4096\n\t"
    "v_fmac_f32 v70, v230, v220\n\t"
    "v_fmac_f32 v71, v231, v220\n\t"
    "v_fma_f32 v68, v68, v236, 1.0\n\t"
    "v_mfma_f32_16x16x32_bf16 v[52:55], a[36:39], a[20:23], v[52:55]\n\t"
    "ds_read_b128 a[84:87], v215 offset:6144\n\t"
    "v_fma_f32 v69, v69, v237, 1.0\n\t"
    "v_fma_f32 v70, v70, v238, 1.0\n\t"
    "v_fma_f32 v71, v71, v239, 1.0\n\t"
    "v_fma_f32 v252, v68, s92, -s92\n\t"
    "v_mfma_f32_16x16x32_bf16 v[20:23], a[32:35], a[20:23], v[20:23]\n\t"
    "v_fma_f32 v253, v69, s92, -s92\n\t"
    "v_fma_f32 v254, v70, s92, -s92\n\t"
    "v_fma_f32 v255, v71, s92, -s92\n\t"
    "v_exp_f32 v252, v252\n\t"
    "v_mfma_f32_16x16x32_bf16 v[24:27], a[32:35], a[24:27], v[24:27]\n\t"
    "ds_read_b128 a[88:91], v215 offset:8192\n\t"
    "v_exp_f32 v253, v253\n\t"
    "v_exp_f32 v254, v254\n\t"
    "v_mfma_f32_16x16x32_bf16 v[56:59], a[36:39], a[24:27], v[56:59]\n\t"
    "ds_read_b128 a[92:95], v215 offset:10240\n\t"
    "v_exp_f32 v255, v255\n\t"
    "v_med3_f32 v68, v68, v252, 1.0\n\t"
    "v_med3_f32 v69, v69, v253, 1.0\n\t"
    "v_mfma_f32_16x16x32_bf16 v[60:63], a[36:39], a[28:31], v[60:63]\n\t"
    "v_med3_f32 v70, v70, v254, 1.0\n\t"
    "v_med3_f32 v71, v71, v255, 1.0\n\t"
    "v_add_f32 v68, s94, v68\n\t"
    "v_add_f32 v69, s94, v69\n\t"
    "v_mfma_f32_16x16x32_bf16 v[28:31], a[32:35], a[28:31], v[28:31]\n\t"
    "ds_read_b128 a[96:99], v215 offset:12288\n\t"
    "v_add_f32 v70, s94, v70\n\t"
    "v_add_f32 v71, s94, v71\n\t"
    "v_cvt_pk_bf16_f32 v64, v64, v65\n\t"
    "v_cvt_pk_bf16_f32 v65, v66, v67\n\t"
    "v_mfma_f32_16x16x32_bf16 a[156:159], a[56:59], a[28:31], a[156:159]\n\t"
    "v_cvt_pk_bf16_f32 v66, v68, v69\n\t"
    "v_cvt_pk_bf16_f32 v67, v70, v71\n\t"
    "v_fmac_f32 v96, v224, v222\n\t"
    "v_fmac_f32 v97, v225, v222\n\t"
    "v_fmac_f32 v98, v226, v222\n\t"
    "v_mfma_f32_16x16x32_bf16 a[188:191], a[60:63], a[28:31], a[188:191]\n\t"
    "ds_read_b128 a[100:103], v215 offset:14336\n\t"
    "v_fmac_f32 v99, v227, v222\n\t"
    "v_fma_f32 v96, v96, v232, 1.0\n\t"
    "v_fma_f32 v97, v97, v233, 1.0\n\t"
    "v_fma_f32 v98, v98, v234, 1.0\n\t"
    "v_mfma_f32_16x16x32_bf16 a[184:187], a[60:63], a[24:27], a[184:187]\n\t"
    "ds_read_b64_tr_b16 a[128:129], v216 offset:28672\n\t"
    "v_fma_f32 v99, v99, v235, 1.0\n\t"
    "v_fma_f32 v240, v96, s92, -s92\n\t"
    "v_fma_f32 v241, v97, s92, -s92\n\t"
    "v_fma_f32 v242, v98, s92, -s92\n\t"
    "v_mfma_f32_16x16x32_bf16 a[152:155], a[56:59], a[24:27], a[152:155]\n\t"
    "v_fma_f32 v243, v99, s92, -s92\n\t"
    "v_exp_f32 v240, v240\n\t"
    "v_mfma_f32_16x16x32_bf16 a[148:151], a[56:59], a[20:23], a[148:151]\n\t"
    "ds_read_b64_tr_b16 a[130:131], v216 offset:29184\n\t"
    "v_exp_f32 v241, v241\n\t"
    "v_exp_f32 v242, v242\n\t"
    "v_mfma_f32_16x16x32_bf16 a[180:183], a[60:63], a[20:23], a[180:183]\n\t"
    "ds_read_b64_tr_b16 a[132:133], v217 offset:28672\n\t"
    "v_exp_f32 v243, v243\n\t"
    "v_med3_f32 v96, v96, v240, 1.0\n\t"
    "v_med3_f32 v97, v97, v241, 1.0\n\t"
    "v_med3_f32 v98, v98, v242, 1.0\n\t"
    "v_mfma_f32_16x16x32_bf16 a[176:179], a[60:63], a[16:19], a[176:179]\n\t"
    "v_med3_f32 v99, v99, v243, 1.0\n\t"
    "v_add_f32 v96, s94, v96\n\t"
    "v_add_f32 v97, s94, v97\n\t"
    "v_add_f32 v98, s94, v98\n\t"
    "v_add_f32 v99, s94, v99\n\t"
    "v_mfma_f32_16x16x32_bf16 a[144:147], a[56:59], a[16:19], a[144:147]\n\t"
    "ds_read_b64_tr_b16 a[134:135], v217 offset:29184\n\t"
    "v_fmac_f32 v100, v228, v222\n\t"
    "v_fmac_f32 v101, v229, v222\n\t"
    "v_fmac_f32 v102, v230, v222\n\t"
    "v_fmac_f32 v103, v231, v222\n\t"
    "v_mfma_f32_16x16x32_bf16 v[204:207], a[56:59], a[12:15], v[204:207]\n\t"
    "ds_read_b64_tr_b16 a[136:137], v216 offset:36864\n\t"
    "v_fma_f32 v100, v100, v236, 1.0\n\t"
    "v_fma_f32 v101, v101, v237, 1.0\n\t"
    "v_fma_f32 v102, v102, v238, 1.0\n\t"
    "v_mfma_f32_16x16x32_bf16 a[172:175], a[60:63], a[12:15], a[172:175]\n\t"
    "v_fma_f32 v103, v103, v239, 1.0\n\t"
    "v_fma_f32 v252, v100, s92, -s92\n\t"
    "v_fma_f32 v253, v101, s92, -s92\n\t"
    "v_fma_f32 v254, v102, s92, -s92\n\t"
    "v_fma_f32 v255, v103, s92, -s92\n\t"
    "v_mfma_f32_16x16x32_bf16 a[168:171], a[60:63], a[8:11], a[168:171]\n\t"
    "ds_read_b64_tr_b16 a[138:139], v216 offset:37376\n\t"
    "v_exp_f32 v252, v252\n\t"
    "v_exp_f32 v253, v253\n\t"
    "v_mfma_f32_16x16x32_bf16 v[200:203], a[56:59], a[8:11], v[200:203]\n\t"
    "ds_read_b64_tr_b16 a[140:141], v217 offset:36864\n\t"
    "v_exp_f32 v254, v254\n\t"
    "v_exp_f32 v255, v255\n\t"
    "v_mfma_f32_16x16x32_bf16 v[196:199], a[56:59], a[4:7], v[196:199]\n\t"
    "v_med3_f32 v100, v100, v252, 1.0\n\t"
    "v_med3_f32 v101, v101, v253, 1.0\n\t"
    "v_med3_f32 v102, v102, v254, 1.0\n\t"
    "v_med3_f32 v103, v103, v255, 1.0\n\t"
    "v_add_f32 v100, s94, v100\n\t"
    "v_mfma_f32_16x16x32_bf16 a[164:167], a[60:63], a[4:7], a[164:167]\n\t"
    "ds_read_b64_tr_b16 a[142:143], v217 offset:37376\n\t"
    "v_add_f32 v101, s94, v101\n\t"
    "v_add_f32 v102, s94, v102\n\t"
    "v_add_f32 v103, s94, v103\n\t"
    "v_mfma_f32_16x16x32_bf16 a[160:163], a[60:63], a[0:3], a[160:163]\n\t"
    "v_cvt_pk_bf16_f32 v96, v96, v97\n\t"
    "v_cvt_pk_bf16_f32 v97, v98, v99\n\t"
    "v_cvt_pk_bf16_f32 v98, v100, v101\n\t"
    "v_cvt_pk_bf16_f32 v99, v102, v103\n\t"
    "v_fmac_f32 v128, v224, v212\n\t"
    "v_mfma_f32_16x16x32_bf16 v[192:195], a[56:59], a[0:3], v[192:195]\n\t"
    "v_fmac_f32 v129, v225, v212\n\t"
    "v_fmac_f32 v130, v226, v212\n\t"
    "v_fmac_f32 v131, v227, v212\n\t"
    "v_fmac_f32 v132, v228, v212\n\t"
    "v_fmac_f32 v133, v229, v212\n\t"
    "v_mfma_f32_16x16x32_bf16 a[192:195], a[64:67], a[0:3], a[192:195]\n\t"
    "v_fmac_f32 v134, v230, v212\n\t"
    "v_fmac_f32 v135, v231, v212\n\t"
    "v_fmac_f32 v160, v224, v250\n\t"
    "v_fmac_f32 v161, v225, v250\n\t"
    "v_fmac_f32 v162, v226, v250\n\t"
    "v_mfma_f32_16x16x32_bf16 a[224:227], a[68:71], a[0:3], a[224:227]\n\t"
    "v_fmac_f32 v163, v227, v250\n\t"
    "v_fmac_f32 v164, v228, v250\n\t"
    "v_fmac_f32 v165, v229, v250\n\t"
    "v_fmac_f32 v166, v230, v250\n\t"
    "v_fmac_f32 v167, v231, v250\n\t"
    "v_mfma_f32_16x16x32_bf16 a[228:231], a[68:71], a[4:7], a[228:231]\n\t"
    "v_mul_f32 v128, v128, v232\n\t"
    "v_mul_f32 v129, v129, v233\n\t"
    "v_mul_f32 v130, v130, v234\n\t"
    "v_mul_f32 v131, v131, v235\n\t"
    "v_mfma_f32_16x16x32_bf16 a[196:199], a[64:67], a[4:7], a[196:199]\n\t"
    "v_mul_f32 v132, v132, v236\n\t"
    "v_mul_f32 v133, v133, v237\n\t"
    "v_mul_f32 v134, v134, v238\n\t"
    "v_mul_f32 v135, v135, v239\n\t"
    "v_mul_f32 v160, v160, v232\n\t"
    "v_mfma_f32_16x16x32_bf16 a[200:203], a[64:67], a[8:11], a[200:203]\n\t"
    "v_mul_f32 v161, v161, v233\n\t"
    "v_mul_f32 v162, v162, v234\n\t"
    "v_mul_f32 v163, v163, v235\n\t"
    "v_mul_f32 v164, v164, v236\n\t"
    "v_mul_f32 v165, v165, v237\n\t"
    "v_mfma_f32_16x16x32_bf16 a[232:235], a[68:71], a[8:11], a[232:235]\n\t"
    "v_mul_f32 v166, v166, v238\n\t"
    "v_mul_f32 v167, v167, v239\n\t"
    "ds_read_b128 v[224:227], v208 offset:128\n\t"
    "ds_read_b128 v[228:231], v208 offset:192\n\t"
    "ds_read_b128 v[232:235], v208 offset:1152\n\t"
    "v_mfma_f32_16x16x32_bf16 a[236:239], a[68:71], a[12:15], a[236:239]\n\t"
    "ds_read_b128 v[236:239], v208 offset:1216\n\t"
    "v_cvt_pk_bf16_f32 v128, v128, v129\n\t"
    "v_cvt_pk_bf16_f32 v129, v130, v131\n\t"
    "v_cvt_pk_bf16_f32 v130, v132, v133\n\t"
    "v_mfma_f32_16x16x32_bf16 a[204:207], a[64:67], a[12:15], a[204:207]\n\t"
    "v_cvt_pk_bf16_f32 v131, v134, v135\n\t"
    "v_cvt_pk_bf16_f32 v160, v160, v161\n\t"
    "v_cvt_pk_bf16_f32 v161, v162, v163\n\t"
    "v_cvt_pk_bf16_f32 v162, v164, v165\n\t"
    "v_cvt_pk_bf16_f32 v163, v166, v167\n\t"
    "v_mfma_f32_16x16x32_bf16 a[208:211], a[64:67], a[16:19], a[208:211]\n\t"
    "v_mfma_f32_16x16x32_bf16 v[68:71], v[64:67], v[128:131], 0\n\t"
    "v_mfma_f32_16x16x32_bf16 v[100:103], v[64:67], v[160:163], 0\n\t"
    "v_mfma_f32_16x16x32_bf16 v[240:243], v[64:67], v[244:247], 0\n\t"
    "v_mfma_f32_16x16x32_bf16 v[132:135], v[96:99], v[128:131], 0\n\t"
    "v_mfma_f32_16x16x32_bf16 v[164:167], v[96:99], v[160:163], 0\n\t"
    "v_mfma_f32_16x16x32_bf16 v[252:255], v[96:99], v[244:247], 0\n\t"
    "s_waitcnt lgkmcnt(0)\n\t"
    "v_fmac_f32 v72, v224, v220\n\t"
    "v_fmac_f32 v73, v225, v220\n\t"
    "v_fmac_f32 v74, v226, v220\n\t"
    "v_fmac_f32 v75, v227, v220\n\t"
    "v_fma_f32 v72, v72, v232, 1.0\n\t"
    "v_fma_f32 v73, v73, v233, 1.0\n\t"
    "v_fma_f32 v74, v74, v234, 1.0\n\t"
    "v_fma_f32 v75, v75, v235, 1.0\n\t"
    "v_fma_f32 v64, v72, s92, -s92\n\t"
    "v_fma_f32 v65, v73, s92, -s92\n\t"
    "v_fma_f32 v66, v74, s92, -s92\n\t"
    "v_fma_f32 v67, v75, s92, -s92\n\t"
    "v_exp_f32 v64, v64\n\t"
    "v_exp_f32 v65, v65\n\t"
    "v_mfma_f32_16x16x32_bf16 a[240:243], a[68:71], a[16:19], a[240:243]\n\t"
    "v_exp_f32 v66, v66\n\t"
    "v_exp_f32 v67, v67\n\t"
    "v_med3_f32 v72, v72, v64, 1.0\n\t"
    "v_med3_f32 v73, v73, v65, 1.0\n\t"
    "v_mfma_f32_16x16x32_bf16 a[244:247], a[68:71], a[20:23], a[244:247]\n\t"
    "v_med3_f32 v74, v74, v66, 1.0\n\t"
    "v_med3_f32 v75, v75, v67, 1.0\n\t"
    "v_add_f32 v72, s94, v72\n\t"
    "v_add_f32 v73, s94, v73\n\t"
    "v_add_f32 v74, s94, v74\n\t"
    "v_mfma_f32_16x16x32_bf16 a[212:215], a[64:67], a[20:23], a[212:215]\n\t"
    "v_add_f32 v75, s94, v75\n\t"
    "v_fmac_f32 v76, v228, v220\n\t"
    "v_fmac_f32 v77, v229, v220\n\t"
    "v_fmac_f32 v78, v230, v220\n\t"
    "v_mfma_f32_16x16x32_bf16 a[216:219], a[64:67], a[24:27], a[216:219]\n\t"
    "v_fmac_f32 v79, v231, v220\n\t"
    "v_fma_f32 v76, v76, v236, 1.0\n\t"
    "v_fma_f32 v77, v77, v237, 1.0\n\t"
    "v_fma_f32 v78, v78, v238, 1.0\n\t"
    "v_fma_f32 v79, v79, v239, 1.0\n\t"
    "v_mfma_f32_16x16x32_bf16 a[248:251], a[68:71], a[24:27], a[248:251]\n\t"
    "v_fma_f32 v96, v76, s92, -s92\n\t"
    "v_fma_f32 v97, v77, s92, -s92\n\t"
    "v_fma_f32 v98, v78, s92, -s92\n\t"
    "v_fma_f32 v99, v79, s92, -s92\n\t"
    "v_mfma_f32_16x16x32_bf16 a[252:255], a[68:71], a[28:31], a[252:255]\n\t"
    "v_exp_f32 v96, v96\n\t"
    "v_exp_f32 v97, v97\n\t"
    "v_exp_f32 v98, v98\n\t"
    "v_mfma_f32_16x16x32_bf16 a[220:223], a[64:67], a[28:31], a[220:223]\n\t"
    "v_exp_f32 v99, v99\n\t"
    "v_med3_f32 v76, v76, v96, 1.0\n\t"
    "v_med3_f32 v77, v77, v97, 1.0\n\t"
    "v_med3_f32 v78, v78, v98, 1.0\n\t"
    "s_waitcnt lgkmcnt(0)\n\t"
    "v_mfma_f32_16x16x32_bf16 v[0:3], a[104:107], a[72:75], v[0:3]\n\t"
    "ds_read_b64_tr_b16 a[32:33], v218 offset:0\n\t"
    "v_med3_f32 v79, v79, v99, 1.0\n\t"
    "v_add_f32 v76, s94, v76\n\t"
    "v_add_f32 v77, s94, v77\n\t"
    "v_mfma_f32_16x16x32_bf16 v[32:35], a[108:111], a[72:75], v[32:35]\n\t"
    "ds_read_b64_tr_b16 a[34:35], v218 offset:512\n\t"
    "v_add_f32 v78, s94, v78\n\t"
    "v_add_f32 v79, s94, v79\n\t"
    "v_cvt_pk_bf16_f32 v72, v72, v73\n\t"
    "v_cvt_pk_bf16_f32 v73, v74, v75\n\t"
    "v_mfma_f32_16x16x32_bf16 v[36:39], a[108:111], a[76:79], v[36:39]\n\t"
    "v_cvt_pk_bf16_f32 v74, v76, v77\n\t"
    "v_cvt_pk_bf16_f32 v75, v78, v79\n\t"
    "v_fmac_f32 v104, v224, v222\n\t"
    "v_fmac_f32 v105, v225, v222\n\t"
    "v_fmac_f32 v106, v226, v222\n\t"
    "v_mfma_f32_16x16x32_bf16 v[4:7], a[104:107], a[76:79], v[4:7]\n\t"
    "ds_read_b64_tr_b16 a[36:37], v219 offset:0\n\t"
    "v_fmac_f32 v107, v227, v222\n\t"
    "v_fma_f32 v104, v104, v232, 1.0\n\t"
    "v_fma_f32 v105, v105, v233, 1.0\n\t"
    "v_fma_f32 v106, v106, v234, 1.0\n\t"
    "v_mfma_f32_16x16x32_bf16 v[8:11], a[104:107], a[80:83], v[8:11]\n\t"
    "ds_read_b64_tr_b16 a[38:39], v219 offset:512\n\t"
    "v_fma_f32 v107, v107, v235, 1.0\n\t"
    "v_fma_f32 v64, v104, s92, -s92\n\t"
    "v_fma_f32 v65, v105, s92, -s92\n\t"
    "v_fma_f32 v66, v106, s92, -s92\n\t"
    "v_mfma_f32_16x16x32_bf16 v[40:43], a[108:111], a[80:83], v[40:43]\n\t"
    "v_fma_f32 v67, v107, s92, -s92\n\t"
    "v_exp_f32 v64, v64\n\t"
    "v_mfma_f32_16x16x32_bf16 v[44:47], a[108:111], a[84:87], v[44:47]\n\t"
    "ds_read_b128 a[0:3], v214 offset:32768\n\t"
    "v_exp_f32 v65, v65\n\t"
    "v_exp_f32 v66, v66\n\t"
    "v_mfma_f32_16x16x32_bf16 v[12:15], a[104:107], a[84:87], v[12:15]\n\t"
    "ds_read_b128 a[4:7], v214 offset:34816\n\t"
    "v_exp_f32 v67, v67\n\t"
    "v_med3_f32 v104, v104, v64, 1.0\n\t"
    "v_med3_f32 v105, v105, v65, 1.0\n\t"
    "v_med3_f32 v106, v106, v66, 1.0\n\t"
    "v_mfma_f32_16x16x32_bf16 v[16:19], a[104:107], a[88:91], v[16:19]\n\t"
    "v_med3_f32 v107, v107, v67, 1.0\n\t"
    "v_add_f32 v104, s94, v104\n\t"
    "v_add_f32 v105, s94, v105\n\t"
    "v_add_f32 v106, s94, v106\n\t"
    "v_add_f32 v107, s94, v107\n\t"
    "v_mfma_f32_16x16x32_bf16 v[48:51], a[108:111], a[88:91], v[48:51]\n\t"
    "ds_read_b128 a[8:11], v214 offset:36864\n\t"
    "v_fmac_f32 v108, v228, v222\n\t"
    "v_fmac_f32 v109, v229, v222\n\t"
    "v_fmac_f32 v110, v230, v222\n\t"
    "v_fmac_f32 v111, v231, v222\n\t"
    "v_mfma_f32_16x16x32_bf16 v[52:55], a[108:111], a[92:95], v[52:55]\n\t"
    "ds_read_b128 a[12:15], v214 offset:38912\n\t"
    "v_fma_f32 v108, v108, v236, 1.0\n\t"
    "v_fma_f32 v109, v109, v237, 1.0\n\t"
    "v_fma_f32 v110, v110, v238, 1.0\n\t"
    "v_mfma_f32_16x16x32_bf16 v[20:23], a[104:107], a[92:95], v[20:23]\n\t"
    "v_fma_f32 v111, v111, v239, 1.0\n\t"
    "v_fma_f32 v96, v108, s92, -s92\n\t"
    "v_fma_f32 v97, v109, s92, -s92\n\t"
    "v_fma_f32 v98, v110, s92, -s92\n\t"
    "v_fma_f32 v99, v111, s92, -s92\n\t"
    "v_mfma_f32_16x16x32_bf16 v[24:27], a[104:107], a[96:99], v[24:27]\n\t"
    "ds_read_b128 a[16:19], v214 offset:40960\n\t"
    "v_exp_f32 v96, v96\n\t"
    "v_exp_f32 v97, v97\n\t"
    "v_mfma_f32_16x16x32_bf16 v[56:59], a[108:111], a[96:99], v[56:59]\n\t"
    "ds_read_b128 a[20:23], v214 offset:43008\n\t"
    "v_exp_f32 v98, v98\n\t"
    "v_exp_f32 v99, v99\n\t"
    "v_mfma_f32_16x16x32_bf16 v[60:63], a[108:111], a[100:103], v[60:63]\n\t"
    "v_med3_f32 v108, v108, v96, 1.0\n\t"
    "v_med3_f32 v109, v109, v97, 1.0\n\t"
    "v_med3_f32 v110, v110, v98, 1.0\n\t"
    "v_med3_f32 v111, v111, v99, 1.0\n\t"
    "v_add_f32 v108, s94, v108\n\t"
    "v_mfma_f32_16x16x32_bf16 v[28:31], a[104:107], a[100:103], v[28:31]\n\t"
    "ds_read_b128 a[24:27], v214 offset:45056\n\t"
    "v_add_f32 v109, s94, v109\n\t"
    "v_add_f32 v110, s94, v110\n\t"
    "v_add_f32 v111, s94, v111\n\t"
    "v_mfma_f32_16x16x32_bf16 a[156:159], a[128:131], a[100:103], a[156:159]\n\t"
    "v_cvt_pk_bf16_f32 v104, v104, v105\n\t"
    "v_cvt_pk_bf16_f32 v105, v106, v107\n\t"
    "v_cvt_pk_bf16_f32 v106, v108, v109\n\t"
    "v_cvt_pk_bf16_f32 v107, v110, v111\n\t"
    "v_fmac_f32 v136, v224, v212\n\t"
    "v_mfma_f32_16x16x32_bf16 a[188:191], a[132:135], a[100:103], a[188:191]\n\t"
    "ds_read_b128 a[28:31], v214 offset:47104\n\t"
    "v_fmac_f32 v137, v225, v212\n\t"
    "v_fmac_f32 v138, v226, v212\n\t"
    "v_fmac_f32 v139, v227, v212\n\t"
    "v_fmac_f32 v140, v228, v212\n\t"
    "v_mfma_f32_16x16x32_bf16 a[184:187], a[132:135], a[96:99], a[184:187]\n\t"
    "ds_read_b64_tr_b16 a[56:57], v218 offset:24576\n\t"
    "v_fmac_f32 v141, v229, v212\n\t"
    "v_fmac_f32 v142, v230, v212\n\t"
    "v_fmac_f32 v143, v231, v212\n\t"
    "v_fmac_f32 v168, v224, v250\n\t"
    "v_mfma_f32_16x16x32_bf16 a[152:155], a[128:131], a[96:99], a[152:155]\n\t"
    "v_fmac_f32 v169, v225, v250\n\t"
    "v_fmac_f32 v170, v226, v250\n\t"
    "v_fmac_f32 v171, v227, v250\n\t"
    "v_fmac_f32 v172, v228, v250\n\t"
    "v_fmac_f32 v173, v229, v250\n\t"
    "v_mfma_f32_16x16x32_bf16 a[148:151], a[128:131], a[92:95], a[148:151]\n\t"
    "ds_read_b64_tr_b16 a[58:59], v218 offset:25088\n\t"
    "v_fmac_f32 v174, v230, v250\n\t"
    "v_fmac_f32 v175, v231, v250\n\t"
    "v_mul_f32 v136, v136, v232\n\t"
    "v_mfma_f32_16x16x32_bf16 a[180:183], a[132:135], a[92:95], a[180:183]\n\t"
    "ds_read_b64_tr_b16 a[60:61], v219 offset:24576\n\t"
    "v_mul_f32 v137, v137, v233\n\t"
    "v_mul_f32 v138, v138, v234\n\t"
    "v_mul_f32 v139, v139, v235\n\t"
    "v_mul_f32 v140, v140, v236\n\t"
    "v_mfma_f32_16x16x32_bf16 a[176:179], a[132:135], a[88:91], a[176:179]\n\t"
    "v_mul_f32 v141, v141, v237\n\t"
    "v_mul_f32 v142, v142, v238\n\t"
    "v_mul_f32 v143, v143, v239\n\t"
    "v_mul_f32 v168, v168, v232\n\t"
    "v_mul_f32 v169, v169, v233\n\t"
    "v_mfma_f32_16x16x32_bf16 a[144:147], a[128:131], a[88:91], a[144:147]\n\t"
    "ds_read_b64_tr_b16 a[62:63], v219 offset:25088\n\t"
    "v_mul_f32 v170, v170, v234\n\t"
    "v_mul_f32 v171, v171, v235\n\t"
    "v_mul_f32 v172, v172, v236\n\t"
    "v_mul_f32 v173, v173, v237\n\t"
    "v_mfma_f32_16x16x32_bf16 v[204:207], a[128:131], a[84:87], v[204:207]\n\t"
    "ds_read_b64_tr_b16 a[64:65], v218 offset:32768\n\t"
    "v_mul_f32 v174, v174, v238\n\t"
    "v_mul_f32 v175, v175, v239\n\t"
    "ds_read_b128 v[224:227], v208 offset:256\n\t"
    "ds_read_b128 v[228:231], v208 offset:320\n\t"
    "v_mfma_f32_16x16x32_bf16 a[172:175], a[132:135], a[84:87], a[172:175]\n\t"
    "ds_read_b128 v[232:235], v208 offset:1280\n\t"
    "ds_read_b128 v[236:239], v208 offset:1344\n\t"
    "v_cvt_pk_bf16_f32 v136, v136, v137\n\t"
    "v_cvt_pk_bf16_f32 v137, v138, v139\n\t"
    "v_mfma_f32_16x16x32_bf16 a[168:171], a[132:135], a[80:83], a[168:171]\n\t"
    "ds_read_b64_tr_b16 a[66:67], v218 offset:33280\n\t"
    "v_cvt_pk_bf16_f32 v138, v140, v141\n\t"
    "v_cvt_pk_bf16_f32 v139, v142, v143\n\t"
    "v_cvt_pk_bf16_f32 v168, v168, v169\n\t"
    "v_cvt_pk_bf16_f32 v169, v170, v171\n\t"
    "v_mfma_f32_16x16x32_bf16 v[200:203], a[128:131], a[80:83], v[200:203]\n\t"
    "ds_read_b64_tr_b16 a[68:69], v219 offset:32768\n\t"
    "v_cvt_pk_bf16_f32 v170, v172, v173\n\t"
    "v_cvt_pk_bf16_f32 v171, v174, v175\n\t"
    "v_mfma_f32_16x16x32_bf16 v[68:71], v[72:75], v[136:139], v[68:71]\n\t"
    "s_nop 0\n\t"
    "v_mfma_f32_16x16x32_bf16 v[100:103], v[72:75], v[168:171], v[100:103]\n\t"
    "v_mfma_f32_16x16x32_bf16 v[240:243], v[72:75], v[244:247], v[240:243]\n\t"
    "v_mfma_f32_16x16x32_bf16 v[132:135], v[104:107], v[136:139], v[132:135]\n\t"
    "v_mfma_f32_16x16x32_bf16 v[164:167], v[104:107], v[168:171], v[164:167]\n\t"
    "v_mfma_f32_16x16x32_bf16 v[252:255], v[104:107], v[244:247], v[252:255]\n\t"
    "s_waitcnt lgkmcnt(0)\n\t"
    "v_fmac_f32 v80, v224, v220\n\t"
    "v_fmac_f32 v81, v225, v220\n\t"
    "v_fmac_f32 v82, v226, v220\n\t"
    "v_fmac_f32 v83, v227, v220\n\t"
    "v_fma_f32 v80, v80, v232, 1.0\n\t"
    "v_fma_f32 v81, v81, v233, 1.0\n\t"
    "v_fma_f32 v82, v82, v234, 1.0\n\t"
    "v_fma_f32 v83, v83, v235, 1.0\n\t"
    "v_fma_f32 v72, v80, s92, -s92\n\t"
    "v_fma_f32 v73, v81, s92, -s92\n\t"
    "v_fma_f32 v74, v82, s92, -s92\n\t"
    "v_fma_f32 v75, v83, s92, -s92\n\t"
    "v_exp_f32 v72, v72\n\t"
    "v_mfma_f32_16x16x32_bf16 v[196:199], a[128:131], a[76:79], v[196:199]\n\t"
    "v_exp_f32 v73, v73\n\t"
    "v_exp_f32 v74, v74\n\t"
    "v_mfma_f32_16x16x32_bf16 a[164:167], a[132:135], a[76:79], a[164:167]\n\t"
    "ds_read_b64_tr_b16 a[70:71], v219 offset:33280\n\t"
    "v_exp_f32 v75, v75\n\t"
    "v_med3_f32 v80, v80, v72, 1.0\n\t"
    "v_med3_f32 v81, v81, v73, 1.0\n\t"
    "v_med3_f32 v82, v82, v74, 1.0\n\t"
    "v_mfma_f32_16x16x32_bf16 a[160:163], a[132:135], a[72:75], a[160:163]\n\t"
    "v_med3_f32 v83, v83, v75, 1.0\n\t"
    "v_add_f32 v80, s94, v80\n\t"
    "v_add_f32 v81, s94, v81\n\t"
    "v_add_f32 v82, s94, v82\n\t"
    "v_mfma_f32_16x16x32_bf16 v[192:195], a[128:131], a[72:75], v[192:195]\n\t"
    "v_add_f32 v83, s94, v83\n\t"
    "v_fmac_f32 v84, v228, v220\n\t"
    "v_fmac_f32 v85, v229, v220\n\t"
    "v_fmac_f32 v86, v230, v220\n\t"
    "v_fmac_f32 v87, v231, v220\n\t"
    "v_mfma_f32_16x16x32_bf16 a[192:195], a[136:139], a[72:75], a[192:195]\n\t"
    "v_fma_f32 v84, v84, v236, 1.0\n\t"
    "v_fma_f32 v85, v85, v237, 1.0\n\t"
    "v_fma_f32 v86, v86, v238, 1.0\n\t"
    "v_fma_f32 v87, v87, v239, 1.0\n\t"
    "v_fma_f32 v104, v84, s92, -s92\n\t"
    "v_mfma_f32_16x16x32_bf16 a[224:227], a[140:143], a[72:75], a[224:227]\n\t"
    "v_fma_f32 v105, v85, s92, -s92\n\t"
    "v_fma_f32 v106, v86, s92, -s92\n\t"
    "v_fma_f32 v107, v87, s92, -s92\n\t"
    "v_exp_f32 v104, v104\n\t"
    "v_mfma_f32_16x16x32_bf16 a[228:231], a[140:143], a[76:79], a[228:231]\n\t"
    "v_exp_f32 v105, v105\n\t"
    "v_exp_f32 v106, v106\n\t"
    "v_mfma_f32_16x16x32_bf16 a[196:199], a[136:139], a[76:79], a[196:199]\n\t"
    "v_exp_f32 v107, v107\n\t"
    "v_med3_f32 v84, v84, v104, 1.0\n\t"
    "v_med3_f32 v85, v85, v105, 1.0\n\t"
    "v_med3_f32 v86, v86, v106, 1.0\n\t"
    "v_mfma_f32_16x16x32_bf16 a[200:203], a[136:139], a[80:83], a[200:203]\n\t"
    "v_med3_f32 v87, v87, v107, 1.0\n\t"
    "v_add_f32 v84, s94, v84\n\t"
    "v_add_f32 v85, s94, v85\n\t"
    "v_add_f32 v86, s94, v86\n\t"
    "v_add_f32 v87, s94, v87\n\t"
    "v_mfma_f32_16x16x32_bf16 a[232:235], a[140:143], a[80:83], a[232:235]\n\t"
    "v_cvt_pk_bf16_f32 v80, v80, v81\n\t"
    "v_cvt_pk_bf16_f32 v81, v82, v83\n\t"
    "v_cvt_pk_bf16_f32 v82, v84, v85\n\t"
    "v_cvt_pk_bf16_f32 v83, v86, v87\n\t"
    "v_fmac_f32 v112, v224, v222\n\t"
    "v_mfma_f32_16x16x32_bf16 a[236:239], a[140:143], a[84:87], a[236:239]\n\t"
    "v_fmac_f32 v113, v225, v222\n\t"
    "v_fmac_f32 v114, v226, v222\n\t"
    "v_fmac_f32 v115, v227, v222\n\t"
    "v_fma_f32 v112, v112, v232, 1.0\n\t"
    "v_fma_f32 v113, v113, v233, 1.0\n\t"
    "v_mfma_f32_16x16x32_bf16 a[204:207], a[136:139], a[84:87], a[204:207]\n\t"
    "v_fma_f32 v114, v114, v234, 1.0\n\t"
    "v_fma_f32 v115, v115, v235, 1.0\n\t"
    "v_fma_f32 v72, v112, s92, -s92\n\t"
    "v_fma_f32 v73, v113, s92, -s92\n\t"
    "v_fma_f32 v74, v114, s92, -s92\n\t"
    "v_mfma_f32_16x16x32_bf16 a[208:211], a[136:139], a[88:91], a[208:211]\n\t"
    "v_fma_f32 v75, v115, s92, -s92\n\t"
    "v_exp_f32 v72, v72\n\t"
    "v_mfma_f32_16x16x32_bf16 a[240:243], a[140:143], a[88:91], a[240:243]\n\t"
    "v_exp_f32 v73, v73\n\t"
    "v_exp_f32 v74, v74\n\t"
    "v_exp_f32 v75, v75\n\t"
    "v_mfma_f32_16x16x32_bf16 a[244:247], a[140:143], a[92:95], a[244:247]\n\t"
    "v_med3_f32 v112, v112, v72, 1.0\n\t"
    "v_med3_f32 v113, v113, v73, 1.0\n\t"
    "v_med3_f32 v114, v114, v74, 1.0\n\t"
    "v_med3_f32 v115, v115, v75, 1.0\n\t"
    "v_add_f32 v112, s94, v112\n\t"
    "v_mfma_f32_16x16x32_bf16 a[212:215], a[136:139], a[92:95], a[212:215]\n\t"
    "v_add_f32 v113, s94, v113\n\t"
    "v_add_f32 v114, s94, v114\n\t"
    "v_add_f32 v115, s94, v115\n\t"
    "v_fmac_f32 v116, v228, v222\n\t"
    "v_fmac_f32 v117, v229, v222\n\t"
    "v_mfma_f32_16x16x32_bf16 a[216:219], a[136:139], a[96:99], a[216:219]\n\t"
    "v_fmac_f32 v118, v230, v222\n\t"
    "v_fmac_f32 v119, v231, v222\n\t"
    "v_fma_f32 v116, v116, v236, 1.0\n\t"
    "v_fma_f32 v117, v117, v237, 1.0\n\t"
    "v_fma_f32 v118, v118, v238, 1.0\n\t"
    "v_mfma_f32_16x16x32_bf16 a[248:251], a[140:143], a[96:99], a[248:251]\n\t"
    "v_fma_f32 v119, v119, v239, 1.0\n\t"
    "v_fma_f32 v104, v116, s92, -s92\n\t"
    "v_fma_f32 v105, v117, s92, -s92\n\t"
    "v_fma_f32 v106, v118, s92, -s92\n\t"
    "v_mfma_f32_16x16x32_bf16 a[252:255], a[140:143], a[100:103], a[252:255]\n\t"
    "v_fma_f32 v107, v119, s92, -s92\n\t"
    "v_exp_f32 v104, v104\n\t"
    "v_exp_f32 v105, v105\n\t"
    "v_mfma_f32_16x16x32_bf16 a[220:223], a[136:139], a[100:103], a[220:223]\n\t"
    "v_exp_f32 v106, v106\n\t"
    "v_exp_f32 v107, v107\n\t"
    "v_med3_f32 v116, v116, v104, 1.0\n\t"
    "s_waitcnt lgkmcnt(0)\n\t"
    "v_mfma_f32_16x16x32_bf16 v[0:3], a[32:35], a[0:3], v[0:3]\n\t"
    "ds_read_b64_tr_b16 a[104:105], v218 offset:4096\n\t"
    "v_med3_f32 v117, v117, v105, 1.0\n\t"
    "v_med3_f32 v118, v118, v106, 1.0\n\t"
    "v_med3_f32 v119, v119, v107, 1.0\n\t"
    "v_add_f32 v116, s94, v116\n\t"
    "v_mfma_f32_16x16x32_bf16 v[32:35], a[36:39], a[0:3], v[32:35]\n\t"
    "ds_read_b64_tr_b16 a[106:107], v218 offset:4608\n\t"
    "v_add_f32 v117, s94, v117\n\t"
    "v_add_f32 v118, s94, v118\n\t"
    "v_add_f32 v119, s94, v119\n\t"
    "v_cvt_pk_bf16_f32 v112, v112, v113\n\t"
    "v_mfma_f32_16x16x32_bf16 v[36:39], a[36:39], a[4:7], v[36:39]\n\t"
    "v_cvt_pk_bf16_f32 v113, v114, v115\n\t"
    "v_cvt_pk_bf16_f32 v114, v116, v117\n\t"
    "v_cvt_pk_bf16_f32 v115, v118, v119\n\t"
    "v_fmac_f32 v144, v224, v212\n\t"
    "v_mfma_f32_16x16x32_bf16 v[4:7], a[32:35], a[4:7], v[4:7]\n\t"
    "ds_read_b64_tr_b16 a[108:109], v219 offset:4096\n\t"
    "v_fmac_f32 v145, v225, v212\n\t"
    "v_fmac_f32 v146, v226, v212\n\t"
    "v_fmac_f32 v147, v227, v212\n\t"
    "v_fmac_f32 v148, v228, v212\n\t"
    "v_mfma_f32_16x16x32_bf16 v[8:11], a[32:35], a[8:11], v[8:11]\n\t"
    "ds_read_b64_tr_b16 a[110:111], v219 offset:4608\n\t"
    "v_fmac_f32 v149, v229, v212\n\t"
    "v_fmac_f32 v150, v230, v212\n\t"
    "v_fmac_f32 v151, v231, v212\n\t"
    "v_fmac_f32 v176, v224, v250\n\t"
    "v_mfma_f32_16x16x32_bf16 v[40:43], a[36:39], a[8:11], v[40:43]\n\t"
    "v_fmac_f32 v177, v225, v250\n\t"
    "v_fmac_f32 v178, v226, v250\n\t"
    "v_fmac_f32 v179, v227, v250\n\t"
    "v_fmac_f32 v180, v228, v250\n\t"
    "v_fmac_f32 v181, v229, v250\n\t"
    "v_mfma_f32_16x16x32_bf16 v[44:47], a[36:39], a[12:15], v[44:47]\n\t"
    "ds_read_b128 a[72:75], v215 offset:32768\n\t"
    "v_fmac_f32 v182, v230, v250\n\t"
    "v_fmac_f32 v183, v231, v250\n\t"
    "v_mul_f32 v144, v144, v232\n\t"
    "v_mul_f32 v145, v145, v233\n\t"
    "v_mfma_f32_16x16x32_bf16 v[12:15], a[32:35], a[12:15], v[12:15]\n\t"
    "ds_read_b128 a[76:79], v215 offset:34816\n\t"
    "v_mul_f32 v146, v146, v234\n\t"
    "v_mul_f32 v147, v147, v235\n\t"
    "v_mul_f32 v148, v148, v236\n\t"
    "v_mfma_f32_16x16x32_bf16 v[16:19], a[32:35], a[16:19], v[16:19]\n\t"
    "v_mul_f32 v149, v149, v237\n\t"
    "v_mul_f32 v150, v150, v238\n\t"
    "v_mul_f32 v151, v151, v239\n\t"
    "v_mul_f32 v176, v176, v232\n\t"
    "v_mul_f32 v177, v177, v233\n\t"
    "v_mfma_f32_16x16x32_bf16 v[48:51], a[36:39], a[16:19], v[48:51]\n\t"
    "ds_read_b128 a[80:83], v215 offset:36864\n\t"
    "v_mul_f32 v178, v178, v234\n\t"
    "v_mul_f32 v179, v179, v235\n\t"
    "v_mul_f32 v180, v180, v236\n\t"
    "v_mul_f32 v181, v181, v237\n\t"
    "v_mfma_f32_16x16x32_bf16 v[52:55], a[36:39], a[20:23], v[52:55]\n\t"
    "ds_read_b128 a[84:87], v215 offset:38912\n\t"
    "v_mul_f32 v182, v182, v238\n\t"
    "v_mul_f32 v183, v183, v239\n\t"
    "ds_read_b128 v[224:227], v208 offset:384\n\t"
    "ds_read_b128 v[228:231], v208 offset:448\n\t"
    "v_mfma_f32_16x16x32_bf16 v[20:23], a[32:35], a[20:23], v[20:23]\n\t"
    "ds_read_b128 v[232:235], v208 offset:1408\n\t"
    "ds_read_b128 v[236:239], v208 offset:1472\n\t"
    "v_cvt_pk_bf16_f32 v144, v144, v145\n\t"
    "v_cvt_pk_bf16_f32 v145, v146, v147\n\t"
    "v_mfma_f32_16x16x32_bf16 v[24:27], a[32:35], a[24:27], v[24:27]\n\t"
    "ds_read_b128 a[88:91], v215 offset:40960\n\t"
    "v_cvt_pk_bf16_f32 v146, v148, v149\n\t"
    "v_cvt_pk_bf16_f32 v147, v150, v151\n\t"
    "v_cvt_pk_bf16_f32 v176, v176, v177\n\t"
    "v_cvt_pk_bf16_f32 v177, v178, v179\n\t"
    "v_mfma_f32_16x16x32_bf16 v[56:59], a[36:39], a[24:27], v[56:59]\n\t"
    "ds_read_b128 a[92:95], v215 offset:43008\n\t"
    "v_cvt_pk_bf16_f32 v178, v180, v181\n\t"
    "v_cvt_pk_bf16_f32 v179, v182, v183\n\t"
    "v_mfma_f32_16x16x32_bf16 v[68:71], v[80:83], v[144:147], v[68:71]\n\t"
    "s_nop 0\n\t"
    "v_mfma_f32_16x16x32_bf16 v[100:103], v[80:83], v[176:179], v[100:103]\n\t"
    "v_mfma_f32_16x16x32_bf16 v[240:243], v[80:83], v[244:247], v[240:243]\n\t"
    "v_mfma_f32_16x16x32_bf16 v[132:135], v[112:115], v[144:147], v[132:135]\n\t"
    "v_mfma_f32_16x16x32_bf16 v[164:167], v[112:115], v[176:179], v[164:167]\n\t"
    "v_mfma_f32_16x16x32_bf16 v[252:255], v[112:115], v[244:247], v[252:255]\n\t"
    "s_waitcnt lgkmcnt(0)\n\t"
    "v_fmac_f32 v88, v224, v220\n\t"
    "v_fmac_f32 v89, v225, v220\n\t"
    "v_fmac_f32 v90, v226, v220\n\t"
    "v_fmac_f32 v91, v227, v220\n\t"
    "v_fma_f32 v88, v88, v232, 1.0\n\t"
    "v_fma_f32 v89, v89, v233, 1.0\n\t"
    "v_fma_f32 v90, v90, v234, 1.0\n\t"
    "v_fma_f32 v91, v91, v235, 1.0\n\t"
    "v_fma_f32 v80, v88, s92, -s92\n\t"
    "v_fma_f32 v81, v89, s92, -s92\n\t"
    "v_fma_f32 v82, v90, s92, -s92\n\t"
    "v_fma_f32 v83, v91, s92, -s92\n\t"
    "v_exp_f32 v80, v80\n\t"
    "v_mfma_f32_16x16x32_bf16 v[60:63], a[36:39], a[28:31], v[60:63]\n\t"
    "v_exp_f32 v81, v81\n\t"
    "v_exp_f32 v82, v82\n\t"
    "v_mfma_f32_16x16x32_bf16 v[28:31], a[32:35], a[28:31], v[28:31]\n\t"
    "ds_read_b128 a[96:99], v215 offset:45056\n\t"
    "v_exp_f32 v83, v83\n\t"
    "v_med3_f32 v88, v88, v80, 1.0\n\t"
    "v_med3_f32 v89, v89, v81, 1.0\n\t"
    "v_med3_f32 v90, v90, v82, 1.0\n\t"
    "v_mfma_f32_16x16x32_bf16 a[156:159], a[56:59], a[28:31], a[156:159]\n\t"
    "v_med3_f32 v91, v91, v83, 1.0\n\t"
    "v_add_f32 v88, s94, v88\n\t"
    "v_add_f32 v89, s94, v89\n\t"
    "v_add_f32 v90, s94, v90\n\t"
    "v_mfma_f32_16x16x32_bf16 a[188:191], a[60:63], a[28:31], a[188:191]\n\t"
    "ds_read_b128 a[100:103], v215 offset:47104\n\t"
    "v_add_f32 v91, s94, v91\n\t"
    "v_fmac_f32 v92, v228, v220\n\t"
    "v_fmac_f32 v93, v229, v220\n\t"
    "v_fmac_f32 v94, v230, v220\n\t"
    "v_mfma_f32_16x16x32_bf16 a[184:187], a[60:63], a[24:27], a[184:187]\n\t"
    "ds_read_b64_tr_b16 a[128:129], v218 offset:28672\n\t"
    "v_fmac_f32 v95, v231, v220\n\t"
    "v_fma_f32 v92, v92, v236, 1.0\n\t"
    "v_fma_f32 v93, v93, v237, 1.0\n\t"
    "v_fma_f32 v94, v94, v238, 1.0\n\t"
    "v_mfma_f32_16x16x32_bf16 a[152:155], a[56:59], a[24:27], a[152:155]\n\t"
    "v_fma_f32 v95, v95, v239, 1.0\n\t"
    "v_fma_f32 v112, v92, s92, -s92\n\t"
    "v_fma_f32 v113, v93, s92, -s92\n\t"
    "v_fma_f32 v114, v94, s92, -s92\n\t"
    "v_fma_f32 v115, v95, s92, -s92\n\t"
    "v_mfma_f32_16x16x32_bf16 a[148:151], a[56:59], a[20:23], a[148:151]\n\t"
    "ds_read_b64_tr_b16 a[130:131], v218 offset:29184\n\t"
    "v_exp_f32 v112, v112\n\t"
    "v_exp_f32 v113, v113\n\t"
    "v_mfma_f32_16x16x32_bf16 a[180:183], a[60:63], a[20:23], a[180:183]\n\t"
    "ds_read_b64_tr_b16 a[132:133], v219 offset:28672\n\t"
    "v_exp_f32 v114, v114\n\t"
    "v_mfma_f32_16x16x32_bf16 a[176:179], a[60:63], a[16:19], a[176:179]\n\t"
    "v_exp_f32 v115, v115\n\t"
    "v_med3_f32 v92, v92, v112, 1.0\n\t"
    "v_med3_f32 v93, v93, v113, 1.0\n\t"
    "v_med3_f32 v94, v94, v114, 1.0\n\t"
    "v_med3_f32 v95, v95, v115, 1.0\n\t"
    "v_mfma_f32_16x16x32_bf16 a[144:147], a[56:59], a[16:19], a[144:147]\n\t"
    "ds_read_b64_tr_b16 a[134:135], v219 offset:29184\n\t"
    "v_add_f32 v92, s94, v92\n\t"
    "v_add_f32 v93, s94, v93\n\t"
    "v_add_f32 v94, s94, v94\n\t"
    "v_add_f32 v95, s94, v95\n\t"
    "v_mfma_f32_16x16x32_bf16 v[204:207], a[56:59], a[12:15], v[204:207]\n\t"
    "ds_read_b64_tr_b16 a[136:137], v218 offset:36864\n\t"
    "v_cvt_pk_bf16_f32 v88, v88, v89\n\t"
    "v_cvt_pk_bf16_f32 v89, v90, v91\n\t"
    "v_cvt_pk_bf16_f32 v90, v92, v93\n\t"
    "v_cvt_pk_bf16_f32 v91, v94, v95\n\t"
    "v_mfma_f32_16x16x32_bf16 a[172:175], a[60:63], a[12:15], a[172:175]\n\t"
    "v_fmac_f32 v120, v224, v222\n\t"
    "v_fmac_f32 v121, v225, v222\n\t"
    "v_fmac_f32 v122, v226, v222\n\t"
    "v_fmac_f32 v123, v227, v222\n\t"
    "v_fma_f32 v120, v120, v232, 1.0\n\t"
    "v_mfma_f32_16x16x32_bf16 a[168:171], a[60:63], a[8:11], a[168:171]\n\t"
    "ds_read_b64_tr_b16 a[138:139], v218 offset:37376\n\t"
    "v_fma_f32 v121, v121, v233, 1.0\n\t"
    "v_fma_f32 v122, v122, v234, 1.0\n\t"
    "v_fma_f32 v123, v123, v235, 1.0\n\t"
    "v_mfma_f32_16x16x32_bf16 v[200:203], a[56:59], a[8:11], v[200:203]\n\t"
    "ds_read_b64_tr_b16 a[140:141], v219 offset:36864\n\t"
    "v_fma_f32 v80, v120, s92, -s92\n\t"
    "v_fma_f32 v81, v121, s92, -s92\n\t"
    "v_fma_f32 v82, v122, s92, -s92\n\t"
    "v_fma_f32 v83, v123, s92, -s92\n\t"
    "v_mfma_f32_16x16x32_bf16 v[196:199], a[56:59], a[4:7], v[196:199]\n\t"
    "v_exp_f32 v80, v80\n\t"
    "v_exp_f32 v81, v81\n\t"
    "v_mfma_f32_16x16x32_bf16 a[164:167], a[60:63], a[4:7], a[164:167]\n\t"
    "ds_read_b64_tr_b16 a[142:143], v219 offset:37376\n\t"
    "v_exp_f32 v82, v82\n\t"
    "v_exp_f32 v83, v83\n\t"
    "v_med3_f32 v120, v120, v80, 1.0\n\t"
    "v_mfma_f32_16x16x32_bf16 a[160:163], a[60:63], a[0:3], a[160:163]\n\t"
    "v_med3_f32 v121, v121, v81, 1.0\n\t"
    "v_med3_f32 v122, v122, v82, 1.0\n\t"
    "v_med3_f32 v123, v123, v83, 1.0\n\t"
    "v_add_f32 v120, s94, v120\n\t"
    "v_add_f32 v121, s94, v121\n\t"
    "v_mfma_f32_16x16x32_bf16 v[192:195], a[56:59], a[0:3], v[192:195]\n\t"
    "v_add_f32 v122, s94, v122\n\t"
    "v_add_f32 v123, s94, v123\n\t"
    "v_fmac_f32 v124, v228, v222\n\t"
    "v_fmac_f32 v125, v229, v222\n\t"
    "v_mfma_f32_16x16x32_bf16 a[192:195], a[64:67], a[0:3], a[192:195]\n\t"
    "v_fmac_f32 v126, v230, v222\n\t"
    "v_fmac_f32 v127, v231, v222\n\t"
    "v_fma_f32 v124, v124, v236, 1.0\n\t"
    "v_fma_f32 v125, v125, v237, 1.0\n\t"
    "v_fma_f32 v126, v126, v238, 1.0\n\t"
    "v_mfma_f32_16x16x32_bf16 a[224:227], a[68:71], a[0:3], a[224:227]\n\t"
    "v_fma_f32 v127, v127, v239, 1.0\n\t"
    "v_fma_f32 v112, v124, s92, -s92\n\t"
    "v_fma_f32 v113, v125, s92, -s92\n\t"
    "v_fma_f32 v114, v126, s92, -s92\n\t"
    "v_fma_f32 v115, v127, s92, -s92\n\t"
    "v_mfma_f32_16x16x32_bf16 a[228:231], a[68:71], a[4:7], a[228:231]\n\t"
    "v_exp_f32 v112, v112\n\t"
    "v_exp_f32 v113, v113\n\t"
    "v_mfma_f32_16x16x32_bf16 a[196:199], a[64:67], a[4:7], a[196:199]\n\t"
    "v_exp_f32 v114, v114\n\t"
    "v_exp_f32 v115, v115\n\t"
    "v_med3_f32 v124, v124, v112, 1.0\n\t"
    "v_med3_f32 v125, v125, v113, 1.0\n\t"
    "v_mfma_f32_16x16x32_bf16 a[200:203], a[64:67], a[8:11], a[200:203]\n\t"
    "v_med3_f32 v126, v126, v114, 1.0\n\t"
    "v_med3_f32 v127, v127, v115, 1.0\n\t"
    "v_add_f32 v124, s94, v124\n\t"
    "v_add_f32 v125, s94, v125\n\t"
    "v_mfma_f32_16x16x32_bf16 a[232:235], a[68:71], a[8:11], a[232:235]\n\t"
    "v_add_f32 v126, s94, v126\n\t"
    "v_add_f32 v127, s94, v127\n\t"
    "v_cvt_pk_bf16_f32 v120, v120, v121\n\t"
    "v_cvt_pk_bf16_f32 v121, v122, v123\n\t"
    "v_cvt_pk_bf16_f32 v122, v124, v125\n\t"
    "v_mfma_f32_16x16x32_bf16 a[236:239], a[68:71], a[12:15], a[236:239]\n\t"
    "v_cvt_pk_bf16_f32 v123, v126, v127\n\t"
    "v_fmac_f32 v152, v224, v212\n\t"
    "v_fmac_f32 v153, v225, v212\n\t"
    "v_fmac_f32 v154, v226, v212\n\t"
    "v_fmac_f32 v155, v227, v212\n\t"
    "v_mfma_f32_16x16x32_bf16 a[204:207], a[64:67], a[12:15], a[204:207]\n\t"
    "v_fmac_f32 v156, v228, v212\n\t"
    "v_fmac_f32 v157, v229, v212\n\t"
    "v_fmac_f32 v158, v230, v212\n\t"
    "v_fmac_f32 v159, v231, v212\n\t"
    "v_fmac_f32 v184, v224, v250\n\t"
    "v_mfma_f32_16x16x32_bf16 a[208:211], a[64:67], a[16:19], a[208:211]\n\t"
    "v_fmac_f32 v185, v225, v250\n\t"
    "v_fmac_f32 v186, v226, v250\n\t"
    "v_fmac_f32 v187, v227, v250\n\t"
    "v_fmac_f32 v188, v228, v250\n\t"
    "v_fmac_f32 v189, v229, v250\n\t"
    "v_mfma_f32_16x16x32_bf16 a[240:243], a[68:71], a[16:19], a[240:243]\n\t"
    "v_fmac_f32 v190, v230, v250\n\t"
    "v_fmac_f32 v191, v231, v250\n\t"
    "v_mul_f32 v152, v152, v232\n\t"
    "v_mul_f32 v153, v153, v233\n\t"
    "v_mfma_f32_16x16x32_bf16 a[244:247], a[68:71], a[20:23], a[244:247]\n\t"
    "v_mul_f32 v154, v154, v234\n\t"
    "v_mul_f32 v155, v155, v235\n\t"
    "v_mul_f32 v156, v156, v236\n\t"
    "v_mul_f32 v157, v157, v237\n\t"
    "v_mul_f32 v158, v158, v238\n\t"
    "v_mfma_f32_16x16x32_bf16 a[212:215], a[64:67], a[20:23], a[212:215]\n\t"
    "v_mul_f32 v159, v159, v239\n\t"
    "v_mul_f32 v184, v184, v232\n\t"
    "v_mul_f32 v185, v185, v233\n\t"
    "v_mul_f32 v186, v186, v234\n\t"
    "v_mul_f32 v187, v187, v235\n\t"
    "v_mfma_f32_16x16x32_bf16 a[216:219], a[64:67], a[24:27], a[216:219]\n\t"
    "v_mul_f32 v188, v188, v236\n\t"
    "v_mul_f32 v189, v189, v237\n\t"
    "v_mul_f32 v190, v190, v238\n\t"
    "v_mul_f32 v191, v191, v239\n\t"
    "v_cvt_pk_bf16_f32 v152, v152, v153\n\t"
    "v_mfma_f32_16x16x32_bf16 a[248:251], a[68:71], a[24:27], a[248:251]\n\t"
    "v_cvt_pk_bf16_f32 v153, v154, v155\n\t"
    "v_cvt_pk_bf16_f32 v154, v156, v157\n\t"
    "v_cvt_pk_bf16_f32 v155, v158, v159\n\t"
    "v_cvt_pk_bf16_f32 v184, v184, v185\n\t"
    "v_cvt_pk_bf16_f32 v185, v186, v187\n\t"
    "v_mfma_f32_16x16x32_bf16 a[252:255], a[68:71], a[28:31], a[252:255]\n\t"
    "v_cvt_pk_bf16_f32 v186, v188, v189\n\t"
    "v_cvt_pk_bf16_f32 v187, v190, v191\n\t"
    "v_mfma_f32_16x16x32_bf16 v[68:71], v[88:91], v[152:155], v[68:71]\n\t"
    "s_nop 0\n\t"
    "v_mfma_f32_16x16x32_bf16 v[100:103], v[88:91], v[184:187], v[100:103]\n\t"
    "v_mfma_f32_16x16x32_bf16 v[240:243], v[88:91], v[244:247], v[240:243]\n\t"
    "v_mfma_f32_16x16x32_bf16 v[132:135], v[120:123], v[152:155], v[132:135]\n\t"
    "v_mfma_f32_16x16x32_bf16 v[164:167], v[120:123], v[184:187], v[164:167]\n\t"
    "v_mfma_f32_16x16x32_bf16 v[252:255], v[120:123], v[244:247], v[252:255]\n\t"
    "v_mfma_f32_16x16x32_bf16 a[220:223], a[64:67], a[28:31], a[220:223]\n\t"
    "s_waitcnt lgkmcnt(0)\n\t"
    "v_mfma_f32_16x16x32_bf16 v[0:3], a[104:107], a[72:75], v[0:3]\n\t"
    "s_cmp_eq_u32 %[wr], 0\n\t"
    "s_cbranch_scc1 51f\n\t"
    "v_swap_b32 v68, v100\n\t"
    "v_swap_b32 v69, v101\n\t"
    "v_swap_b32 v70, v102\n\t"
    "v_swap_b32 v71, v103\n\t"
    "v_swap_b32 v132, v164\n\t"
    "v_swap_b32 v133, v165\n\t"
    "v_swap_b32 v134, v166\n\t"
    "v_swap_b32 v135, v167\n\t"
    "v_swap_b32 v240, v252\n\t"
    "v_swap_b32 v241, v253\n\t"
    "v_swap_b32 v242, v254\n\t"
    "v_swap_b32 v243, v255\n\t"
    "51:\n\t"
    "v_mfma_f32_16x16x32_bf16 v[32:35], a[108:111], a[72:75], v[32:35]\n\t"
    "v_add_u32 v72, %[xp], v248\n\t"
    "v_add_u32 v73, %[xp], v211\n\t"
    "v_mfma_f32_16x16x32_bf16 v[36:39], a[108:111], a[76:79], v[36:39]\n\t"
    "v_add_u32 v211, %[xt], v211\n\t"
    "v_mfma_f32_16x16x32_bf16 v[4:7], a[104:107], a[76:79], v[4:7]\n\t"
    "v_mfma_f32_16x16x32_bf16 v[8:11], a[104:107], a[80:83], v[8:11]\n\t"
    "v_mfma_f32_16x16x32_bf16 v[40:43], a[108:111], a[80:83], v[40:43]\n\t"
    "ds_write_b128 v72, v[100:103]\n\t"
    "v_mfma_f32_16x16x32_bf16 v[44:47], a[108:111], a[84:87], v[44:47]\n\t"
    "v_mfma_f32_16x16x32_bf16 v[12:15], a[104:107], a[84:87], v[12:15]\n\t"
    "v_mfma_f32_16x16x32_bf16 v[16:19], a[104:107], a[88:91], v[16:19]\n\t"
    "ds_write_b128 v72, v[164:167] offset:1024\n\t"
    "v_mfma_f32_16x16x32_bf16 v[48:51], a[108:111], a[88:91], v[48:51]\n\t"
    "v_mfma_f32_16x16x32_bf16 v[52:55], a[108:111], a[92:95], v[52:55]\n\t"
    "v_mfma_f32_16x16x32_bf16 v[20:23], a[104:107], a[92:95], v[20:23]\n\t"
    "ds_write_b128 v73, v[252:255] offset:2048\n\t"
    "v_mfma_f32_16x16x32_bf16 v[24:27], a[104:107], a[96:99], v[24:27]\n\t"
    "v_mfma_f32_16x16x32_bf16 v[56:59], a[108:111], a[96:99], v[56:59]\n\t"
    "v_mfma_f32_16x16x32_bf16 v[60:63], a[108:111], a[100:103], v[60:63]\n\t"
    "v_mfma_f32_16x16x32_bf16 v[28:31], a[104:107], a[100:103], v[28:31]\n\t"
    "v_mfma_f32_16x16x32_bf16 a[156:159], a[128:131], a[100:103], a[156:159]\n\t"
    "v_mfma_f32_16x16x32_bf16 a[188:191], a[132:135], a[100:103], a[188:191]\n\t"
    "s_waitcnt lgkmcnt(0)\n\t"
    "s_barrier\n\t"
    "ds_read_b128 v[100:103], v210\n\t"
    "ds_read_b128 v[164:167], v210 offset:1024\n\t"
    "ds_read_b128 v[252:255], v211 offset:2048\n\t"
    "v_add_u32 v72, %[kvst], v248\n\t"
    "v_mfma_f32_16x16x32_bf16 a[184:187], a[132:135], a[96:99], a[184:187]\n\t"
    "v_and_b32 v74, 0xf0, v248\n\t"
    "v_lshrrev_b32 v73, 4, v248\n\t"
    "v_mfma_f32_16x16x32_bf16 a[152:155], a[128:131], a[96:99], a[152:155]\n\t"
    "v_add_u32 v73, %[kvz], v73\n\t"
    "v_mov_b32 v75, 0x40000000\n\t"
    "v_mfma_f32_16x16x32_bf16 a[148:151], a[128:131], a[92:95], a[148:151]\n\t"
    "v_cmp_eq_u32 vcc, 0, v74\n\t"
    "s_nop 1\n\t"
    "v_cndmask_b32 v73, v75, v73, vcc\n\t"
    "v_mfma_f32_16x16x32_bf16 a[180:183], a[132:135], a[92:95], a[180:183]\n\t"
    "v_mfma_f32_16x16x32_bf16 a[176:179], a[132:135], a[88:91], a[176:179]\n\t"
    "v_mfma_f32_16x16x32_bf16 a[144:147], a[128:131], a[88:91], a[144:147]\n\t"
    "v_mfma_f32_16x16x32_bf16 v[204:207], a[128:131], a[84:87], v[204:207]\n\t"
    "v_mfma_f32_16x16x32_bf16 a[172:175], a[132:135], a[84:87], a[172:175]\n\t"
    "v_mfma_f32_16x16x32_bf16 a[168:171], a[132:135], a[80:83], a[168:171]\n\t"
    "s_waitcnt lgkmcnt(0)\n\t"
    "v_mfma_f32_16x16x32_bf16 v[200:203], a[128:131], a[80:83], v[200:203]\n\t"
    "v_add_f32 v68, v68, v100\n\t"
    "v_add_f32 v69, v69, v101\n\t"
    "v_mfma_f32_16x16x32_bf16 v[196:199], a[128:131], a[76:79], v[196:199]\n\t"
    "v_add_f32 v70, v70, v102\n\t"
    "v_add_f32 v71, v71, v103\n\t"
    "v_mfma_f32_16x16x32_bf16 a[164:167], a[132:135], a[76:79], a[164:167]\n\t"
    "v_add_f32 v132, v132, v164\n\t"
    "v_add_f32 v133, v133, v165\n\t"
    "v_mfma_f32_16x16x32_bf16 a[160:163], a[132:135], a[72:75], a[160:163]\n\t"
    "v_add_f32 v134, v134, v166\n\t"
    "v_add_f32 v135, v135, v167\n\t"
    "v_mfma_f32_16x16x32_bf16 v[192:195], a[128:131], a[72:75], v[192:195]\n\t"
    "v_add_f32 v240, v240, v252\n\t"
    "v_add_f32 v241, v241, v253\n\t"
    "v_mfma_f32_16x16x32_bf16 a[192:195], a[136:139], a[72:75], a[192:195]\n\t"
    "v_add_f32 v242, v242, v254\n\t"
    "v_add_f32 v243, v243, v255\n\t"
    "v_mfma_f32_16x16x32_bf16 a[224:227], a[140:143], a[72:75], a[224:227]\n\t"
    "v_mfma_f32_16x16x32_bf16 a[228:231], a[140:143], a[76:79], a[228:231]\n\t"
    "v_mfma_f32_16x16x32_bf16 a[196:199], a[136:139], a[76:79], a[196:199]\n\t"
    "buffer_store_dwordx4 v[68:71], v72, %[srdw], 0 offen sc1\n\t"
    "v_mfma_f32_16x16x32_bf16 a[200:203], a[136:139], a[80:83], a[200:203]\n\t"
    "v_mfma_f32_16x16x32_bf16 a[232:235], a[140:143], a[80:83], a[232:235]\n\t"
    "v_mfma_f32_16x16x32_bf16 a[236:239], a[140:143], a[84:87], a[236:239]\n\t"
    "buffer_store_dwordx4 v[132:135], v72, %[srdw], 0 offen offset:1024 sc1\n\t"
    "v_mfma_f32_16x16x32_bf16 a[204:207], a[136:139], a[84:87], a[204:207]\n\t"
    "v_mfma_f32_16x16x32_bf16 a[208:211], a[136:139], a[88:91], a[208:211]\n\t"
    "v_mfma_f32_16x16x32_bf16 a[240:243], a[140:143], a[88:91], a[240:243]\n\t"
    "buffer_store_dwordx4 v[240:243], v73, %[srdw], 0 offen sc1\n\t"
    "v_mfma_f32_16x16x32_bf16 a[244:247], a[140:143], a[92:95], a[244:247]\n\t"
    "v_mfma_f32_16x16x32_bf16 a[212:215], a[136:139], a[92:95], a[212:215]\n\t"
    "v_mfma_f32_16x16x32_bf16 a[216:219], a[136:139], a[96:99], a[216:219]\n\t"
    "v_mfma_f32_16x16x32_bf16 a[248:251], a[140:143], a[96:99], a[248:251]\n\t"
    "v_mfma_f32_16x16x32_bf16 a[252:255], a[140:143], a[100:103], a[252:255]\n\t"
    "v_mfma_f32_16x16x32_bf16 a[220:223], a[136:139], a[100:103], a[220:223]\n\t"
    "s_waitcnt lgkmcnt(0)\n\t"
    "v_and_b32 v224, 0xf0, v248\n\t"
    "v_add_u32 v224, %[lrowr], v224\n\t"
    "v_lshrrev_b32 v225, 4, v248\n\t"
    "v_and_b32 v225, 0x30, v225\n\t"
    "v_add_u32 v226, %[lcsr], v225\n\t"
    "ds_read_b128 v[160:163], v226 offset:0\n\t"
    "ds_read_b128 v[164:167], v226 offset:64\n\t"
    "ds_read_b128 v[128:131], v224 offset:0\n\t"
    "ds_read_b128 v[132:135], v224 offset:256\n\t"
    "ds_read_b128 v[136:139], v224 offset:512\n\t"
    "ds_read_b128 v[140:143], v224 offset:768\n\t"
    "ds_read_b128 v[144:147], v224 offset:1024\n\t"
    "ds_read_b128 v[148:151], v224 offset:1280\n\t"
    "ds_read_b128 v[152:155], v224 offset:1536\n\t"
    "ds_read_b128 v[156:159], v224 offset:1792\n\t"
    "ds_read_b128 v[168:171], v226 offset:384\n\t"
    "ds_read_b128 v[172:175], v226 offset:448\n\t"
    "ds_read_b128 v[176:179], v226 offset:512\n\t"
    "ds_read_b128 v[180:183], v226 offset:576\n\t"
    "s_waitcnt vmcnt(0)\n\t"
    "s_barrier\n\t"
    "s_cmp_lg_u32 %[w], 0\n\t"
    "s_cbranch_scc1 52f\n\t"
    "v_mov_b32 v250, s90\n\t"
    "v_mov_b32 v251, s91\n\t"
    "v_mov_b32 v249, %[fkvp]\n\t"
    "s_mov_b64 exec, 1\n\t"
    "buffer_store_dwordx2 v[250:251], v249, %[srdw], 0 offen sc1\n\t"
    "s_mov_b64 exec, -1\n\t"
    "52:\n\t"
    "s_branch 9f\n\t"
    "140:\n\t"
    "s_cmp_lg_u32 %[wr], 0\n\t"
    "s_cbranch_scc1 141f\n\t"
    "v_mfma_f32_16x16x32_bf16 v[224:227], a[32:35], v[244:247], v[224:227]\n\t"
    "v_mfma_f32_16x16x32_bf16 v[228:231], a[36:39], v[244:247], v[228:231]\n\t"
    "v_mfma_f32_16x16x32_bf16 v[232:235], a[40:43], v[244:247], v[232:235]\n\t"
    "v_mfma_f32_16x16x32_bf16 v[236:239], a[44:47], v[244:247], v[236:239]\n\t"
    "v_mfma_f32_16x16x32_bf16 v[240:243], a[48:51], v[244:247], v[240:243]\n\t"
    "s_branch 24b\n\t"
    "141:\n\t"
    "v_mfma_f32_16x16x32_bf16 v[224:227], a[52:55], v[244:247], v[224:227]\n\t"
    "v_mfma_f32_16x16x32_bf16 v[228:231], a[56:59], v[244:247], v[228:231]\n\t"
    "v_mfma_f32_16x16x32_bf16 v[232:235], a[60:63], v[244:247], v[232:235]\n\t"
    "v_mfma_f32_16x16x32_bf16 v[236:239], a[64:67], v[244:247], v[236:239]\n\t"
    "v_mfma_f32_16x16x32_bf16 v[240:243], a[68:71], v[244:247], v[240:243]\n\t"
    "s_branch 24b\n\t"
    "100:\n\t"
    "s_cmp_lg_u32 %[wr], 0\n\t"
    "s_cbranch_scc1 101f\n\t"
    "v_mfma_f32_16x16x32_bf16 v[224:227], a[32:35], v[244:247], v[224:227]\n\t"
    "v_mfma_f32_16x16x32_bf16 v[228:231], a[36:39], v[244:247], v[228:231]\n\t"
    "v_mfma_f32_16x16x32_bf16 v[232:235], a[40:43], v[244:247], v[232:235]\n\t"
    "v_mfma_f32_16x16x32_bf16 v[236:239], a[44:47], v[244:247], v[236:239]\n\t"
    "v_mfma_f32_16x16x32_bf16 v[240:243], a[48:51], v[244:247], v[240:243]\n\t"
    "s_branch 20b\n\t"
    "101:\n\t"
    "v_mfma_f32_16x16x32_bf16 v[224:227], a[52:55], v[244:247], v[224:227]\n\t"
    "v_mfma_f32_16x16x32_bf16 v[228:231], a[56:59], v[244:247], v[228:231]\n\t"
    "v_mfma_f32_16x16x32_bf16 v[232:235], a[60:63], v[244:247], v[232:235]\n\t"
    "v_mfma_f32_16x16x32_bf16 v[236:239], a[64:67], v[244:247], v[236:239]\n\t"
    "v_mfma_f32_16x16x32_bf16 v[240:243], a[68:71], v[244:247], v[240:243]\n\t"
    "s_branch 20b\n\t"
    "110:\n\t"
    "s_cmp_lg_u32 %[wr], 0\n\t"
    "s_cbranch_scc1 111f\n\t"
    "v_mfma_f32_16x16x32_bf16 v[224:227], a[104:107], v[244:247], v[224:227]\n\t"
    "v_mfma_f32_16x16x32_bf16 v[228:231], a[108:111], v[244:247], v[228:231]\n\t"
    "v_mfma_f32_16x16x32_bf16 v[232:235], a[112:115], v[244:247], v[232:235]\n\t"
    "v_mfma_f32_16x16x32_bf16 v[236:239], a[116:119], v[244:247], v[236:239]\n\t"
    "v_mfma_f32_16x16x32_bf16 v[240:243], a[120:123], v[244:247], v[240:243]\n\t"
    "s_branch 21b\n\t"
    "111:\n\t"
    "v_mfma_f32_16x16x32_bf16 v[224:227], a[124:127], v[244:247], v[224:227]\n\t"
    "v_mfma_f32_16x16x32_bf16 v[228:231], a[128:131], v[244:247], v[228:231]\n\t"
    "v_mfma_f32_16x16x32_bf16 v[232:235], a[132:135], v[244:247], v[232:235]\n\t"
    "v_mfma_f32_16x16x32_bf16 v[236:239], a[136:139], v[244:247], v[236:239]\n\t"
    "v_mfma_f32_16x16x32_bf16 v[240:243], a[140:143], v[244:247], v[240:243]\n\t"
    "s_branch 21b\n\t"
    "120:\n\t"
    "s_cmp_lg_u32 %[wr], 0\n\t"
    "s_cbranch_scc1 121f\n\t"
    "v_mfma_f32_16x16x32_bf16 v[224:227], a[32:35], v[244:247], v[224:227]\n\t"
    "v_mfma_f32_16x16x32_bf16 v[228:231], a[36:39], v[244:247], v[228:231]\n\t"
    "v_mfma_f32_16x16x32_bf16 v[232:235], a[40:43], v[244:247], v[232:235]\n\t"
    "v_mfma_f32_16x16x32_bf16 v[236:239], a[44:47], v[244:247], v[236:239]\n\t"
    "v_mfma_f32_16x16x32_bf16 v[240:243], a[48:51], v[244:247], v[240:243]\n\t"
    "s_branch 22b\n\t"
    "121:\n\t"
    "v_mfma_f32_16x16x32_bf16 v[224:227], a[52:55], v[244:247], v[224:227]\n\t"
    "v_mfma_f32_16x16x32_bf16 v[228:231], a[56:59], v[244:247], v[228:231]\n\t"
    "v_mfma_f32_16x16x32_bf16 v[232:235], a[60:63], v[244:247], v[232:235]\n\t"
    "v_mfma_f32_16x16x32_bf16 v[236:239], a[64:67], v[244:247], v[236:239]\n\t"
    "v_mfma_f32_16x16x32_bf16 v[240:243], a[68:71], v[244:247], v[240:243]\n\t"
    "s_branch 22b\n\t"
    "130:\n\t"
    "s_cmp_lg_u32 %[wr], 0\n\t"
    "s_cbranch_scc1 131f\n\t"
    "v_mfma_f32_16x16x32_bf16 v[224:227], a[104:107], v[244:247], v[224:227]\n\t"
    "v_mfma_f32_16x16x32_bf16 v[228:231], a[108:111], v[244:247], v[228:231]\n\t"
    "v_mfma_f32_16x16x32_bf16 v[232:235], a[112:115], v[244:247], v[232:235]\n\t"
    "v_mfma_f32_16x16x32_bf16 v[236:239], a[116:119], v[244:247], v[236:239]\n\t"
    "v_mfma_f32_16x16x32_bf16 v[240:243], a[120:123], v[244:247], v[240:243]\n\t"
    "s_branch 23b\n\t"
    "131:\n\t"
    "v_mfma_f32_16x16x32_bf16 v[224:227], a[124:127], v[244:247], v[224:227]\n\t"
    "v_mfma_f32_16x16x32_bf16 v[228:231], a[128:131], v[244:247], v[228:231]\n\t"
    "v_mfma_f32_16x16x32_bf16 v[232:235], a[132:135], v[244:247], v[232:235]\n\t"
    "v_mfma_f32_16x16x32_bf16 v[236:239], a[136:139], v[244:247], v[236:239]\n\t"
    "v_mfma_f32_16x16x32_bf16 v[240:243], a[140:143], v[244:247], v[240:243]\n\t"
    "s_branch 23b\n\t"
    "31:\n\t"
    "v_and_b32 v249, 0xf0, v248\n\t"
    "v_cmp_eq_u32 vcc, 16, v249\n\t"
    "s_nop 1\n\t"
    "v_cndmask_b32 v224, v224, v228, vcc\n\t"
    "v_cndmask_b32 v225, v225, v229, vcc\n\t"
    "v_cndmask_b32 v226, v226, v230, vcc\n\t"
    "v_cndmask_b32 v227, v227, v231, vcc\n\t"
    "v_cmp_eq_u32 vcc, 32, v249\n\t"
    "s_nop 1\n\t"
    "v_cndmask_b32 v224, v224, v232, vcc\n\t"
    "v_cndmask_b32 v225, v225, v233, vcc\n\t"
    "v_cndmask_b32 v226, v226, v234, vcc\n\t"
    "v_cndmask_b32 v227, v227, v235, vcc\n\t"
    "v_cmp_eq_u32 vcc, 48, v249\n\t"
    "s_nop 1\n\t"
    "v_cndmask_b32 v224, v224, v236, vcc\n\t"
    "v_cndmask_b32 v225, v225, v237, vcc\n\t"
    "v_cndmask_b32 v226, v226, v238, vcc\n\t"
    "v_cndmask_b32 v227, v227, v239, vcc\n\t"
    "v_cmp_eq_u32 vcc, 64, v249\n\t"
    "s_nop 1\n\t"
    "v_cndmask_b32 v224, v224, v240, vcc\n\t"
    "v_cndmask_b32 v225, v225, v241, vcc\n\t"
    "v_cndmask_b32 v226, v226, v242, vcc\n\t"
    "v_cndmask_b32 v227, v227, v243, vcc\n\t"
    "v_lshrrev_b32 v250, 8, v248\n\t"
    "v_lshlrev_b32 v250, 4, v250\n\t"
    "v_lshl_add_u32 v250, v249, 2, v250\n\t"
    "v_add_u32 v250, %[csst], v250\n\t"
    "v_mov_b32 v251, 0x40000000\n\t"
    "v_cmp_gt_u32 vcc, 80, v249\n\t"
    "s_nop 1\n\t"
    "v_cndmask_b32 v249, v251, v250, vcc\n\t"
    "buffer_store_dwordx4 v[224:227], v249, %[srdw], 0 offen sc1\n\t"
    "s_mov_b64 vcc, 0\n\t"
    "s_branch 34b\n\t"
    "33:\n\t"
    "s_cmp_eq_u32 %[cnt], 1\n\t"
    "s_cbranch_scc1 35f\n\t"
    "s_add_u32 %[tmp], %[cnt], 1\n\t"
    "s_cmp_eq_u32 %[tmp], %[mine]\n\t"
    "s_cbranch_scc1 31b\n\t"
    "s_cmp_lg_u32 %[w], 0\n\t"
    "s_cbranch_scc1 34b\n\t"
    "v_mov_b32 v250, s90\n\t"
    "v_mov_b32 v251, s91\n\t"
    "v_mov_b32 v249, %[fcsp]\n\t"
    "s_mov_b64 exec, 1\n\t"
    "buffer_store_dwordx2 v[250:251], v249, %[srdw], 0 offen sc1\n\t"
    "s_mov_b64 exec, -1\n\t"
    "s_branch 34b\n\t"
    "35:\n\t"
    "v_and_b32 v249, 0x70, v248\n\t"
    "v_lshrrev_b32 v249, 1, v249\n\t"
    "v_add_u32 v249, %[fcs], v249\n\t"
    "buffer_load_dwordx2 v[250:251], v249, %[srdw], 0 offen sc1\n\t"
    "s_branch 34b\n\t"
    "36:\n\t"
    "s_mov_b32 %[tmp], 0x10000\n\t"
    "37:\n\t"
    "s_sleep 2\n\t"
    "buffer_load_dwordx2 v[250:251], v249, %[srdw], 0 offen sc1\n\t"
    "s_sub_u32 %[tmp], %[tmp], 1\n\t"
    "s_waitcnt vmcnt(0)\n\t"
    "s_cbranch_scc1 38b\n\t"
    "v_cmp_ne_u64 vcc, s[90:91], v[250:251]\n\t"
    "s_cbranch_vccnz 37b\n\t"
    "s_branch 38b\n\t"
    "41:\n\t"
    "s_mov_b32 %[tmp], 0x10000\n\t"
    "v_lshrrev_b32 v249, 1, v248\n\t"
    "v_add_u32 v252, %[stld], v249\n\t"
    "43:\n\t"
    "s_sleep 2\n\t"
    "buffer_load_dwordx2 v[224:225], v252, %[srdw], 0 offen sc1\n\t"
    "s_sub_u32 %[tmp], %[tmp], 1\n\t"
    "s_waitcnt vmcnt(0)\n\t"
    "s_cbranch_scc1 42b\n\t"
    "v_cmp_ne_u32 vcc, s90, v225\n\t"
    "s_cbranch_vccnz 43b\n\t"
    "s_branch 42b\n\t"
    "9:\n\t"
        : "={v[0:15]}"(c[0]), "={v[16:31]}"(c[1]), "={v[32:47]}"(c[2]), "={v[48:63]}"(c[3]), "={v[64:79]}"(c[4]), "={v[80:95]}"(c[5]), "={v[96:111]}"(c[6]), "={v[112:127]}"(c[7]), "={v[128:143]}"(c[8]), "={v[144:159]}"(c[9]), "={v[160:175]}"(c[10]), "={v[176:191]}"(c[11]), "={v[192:207]}"(c[12]), "+{v[208:223]}"(a0), G1_IO : G1_IN0, [lnmr] "s"(lx + T_NM + wr * 512), [xt] "s"(lx + T_X + (wc * 2 + wr) * 2176), [xp] "s"(lx + T_X + (wc * 2 + (wr ^ 1)) * 2176),
          [kvst] "s"((unsigned)WS_KV + (2 * col + wc) * 65536 + p * 8192 + wr * 2048), [kvz] "s"((unsigned)WS_KV + (2 * col + wc) * 65536 + p * 8192 + 4096 + wr * 64), [fkvp] "s"((unsigned)(WS_FLAG + FL_KV) + col * 1024 + p * 128) : G1_CLOB);
    else asm volatile(
    "v_mov_b32 v244, 0x3f803f80\n\t"
    "v_mov_b32 v245, 0x3f803f80\n\t"
    "v_mov_b32 v246, 0x3f803f80\n\t"
    "v_mov_b32 v247, 0x3f803f80\n\t"
    "v_mov_b64 v[224:225], 0\n\t"
    "v_mov_b64 v[226:227], 0\n\t"
    "v_mov_b64 v[228:229], 0\n\t"
    "v_mov_b64 v[230:231], 0\n\t"
    "v_mov_b64 v[232:233], 0\n\t"
    "v_mov_b64 v[234:235], 0\n\t"
    "v_mov_b64 v[236:237], 0\n\t"
    "v_mov_b64 v[238:239], 0\n\t"
    "v_mov_b64 v[240:241], 0\n\t"
    "v_mov_b64 v[242:243], 0\n\t"
    "s_mov_b32 %[cnt], 7\n\t"
    "s_cmp_eq_u32 %[mine], 7\n\t"
    "s_cselect_b64 vcc, -1, 0\n\t"
    "s_waitcnt vmcnt(2)\n\t"
    "s_barrier\n\t"
    "ds_read_b64_tr_b16 a[32:33], v216 offset:0\n\t"
    "ds_read_b64_tr_b16 a[34:35], v216 offset:512\n\t"
    "ds_read_b64_tr_b16 a[36:37], v217 offset:0\n\t"
    "ds_read_b64_tr_b16 a[38:39], v217 offset:512\n\t"
    "ds_read_b128 a[0:3], v214 offset:0\n\t"
    "ds_read_b128 a[4:7], v214 offset:2048\n\t"
    "ds_read_b128 a[8:11], v214 offset:4096\n\t"
    "ds_read_b128 a[12:15], v214 offset:6144\n\t"
    "ds_read_b128 a[16:19], v214 offset:8192\n\t"
    "ds_read_b128 a[20:23], v214 offset:10240\n\t"
    "ds_read_b128 a[24:27], v214 offset:12288\n\t"
    "ds_read_b128 a[28:31], v214 offset:14336\n\t"
    "ds_read_b64_tr_b16 a[40:41], v216 offset:8192\n\t"
    "ds_read_b64_tr_b16 a[42:43], v216 offset:8704\n\t"
    "ds_read_b64_tr_b16 a[44:45], v217 offset:8192\n\t"
    "ds_read_b64_tr_b16 a[46:47], v217 offset:8704\n\t"
    "ds_read_b64_tr_b16 a[48:49], v216 offset:16384\n\t"
    "ds_read_b64_tr_b16 a[50:51], v216 offset:16896\n\t"
    "ds_read_b64_tr_b16 a[52:53], v217 offset:16384\n\t"
    "ds_read_b64_tr_b16 a[54:55], v217 offset:16896\n\t"
    "ds_read_b64_tr_b16 a[56:57], v216 offset:24576\n\t"
    "ds_read_b64_tr_b16 a[58:59], v216 offset:25088\n\t"
    "ds_read_b64_tr_b16 a[60:61], v217 offset:24576\n\t"
    "ds_read_b64_tr_b16 a[62:63], v217 offset:25088\n\t"
    "ds_read_b64_tr_b16 a[64:65], v220 offset:0\n\t"
    "ds_read_b64_tr_b16 a[66:67], v220 offset:256\n\t"
    "ds_read_b64_tr_b16 a[68:69], v220 offset:4096\n\t"
    "ds_read_b64_tr_b16 a[70:71], v220 offset:4352\n\t"
    "s_waitcnt lgkmcnt(0)\n\t"
    "v_mfma_f32_16x16x32_bf16 v[0:3], a[32:35], a[0:3], 0\n\t"
    "ds_read_b64_tr_b16 a[104:105], v216 offset:4096\n\t"
    "v_mfma_f32_16x16x32_bf16 v[32:35], a[36:39], a[0:3], 0\n\t"
    "s_add_u32 m0, %[m0a], 32768\n\t"
    "ds_read_b64_tr_b16 a[106:107], v216 offset:4608\n\t"
    "v_mfma_f32_16x16x32_bf16 v[36:39], a[36:39], a[4:7], 0\n\t"
    "buffer_load_dwordx4 v208, %[srda], %[sa] offen offset:0 lds\n\t"
    "v_mfma_f32_16x16x32_bf16 v[4:7], a[32:35], a[4:7], 0\n\t"
    "v_mfma_f32_16x16x32_bf16 v[8:11], a[32:35], a[8:11], 0\n\t"
    "ds_read_b64_tr_b16 a[108:109], v217 offset:4096\n\t"
    "v_mfma_f32_16x16x32_bf16 v[40:43], a[36:39], a[8:11], 0\n\t"
    "buffer_load_dwordx4 v209, %[srda], %[sa] offen offset:1024 lds\n\t"
    "v_mfma_f32_16x16x32_bf16 v[44:47], a[36:39], a[12:15], 0\n\t"
    "ds_read_b64_tr_b16 a[110:111], v217 offset:4608\n\t"
    "v_mfma_f32_16x16x32_bf16 v[12:15], a[32:35], a[12:15], 0\n\t"
    "s_add_u32 %[tmp], %[sa], 30720\n\t"
    "v_mfma_f32_16x16x32_bf16 v[16:19], a[32:35], a[16:19], 0\n\t"
    "buffer_load_dwordx4 v208, %[srda], %[tmp] offen offset:2048 lds\n\t"
    "v_mfma_f32_16x16x32_bf16 v[48:51], a[36:39], a[16:19], 0\n\t"
    "ds_read_b128 a[72:75], v215 offset:0\n\t"
    "v_mfma_f32_16x16x32_bf16 v[52:55], a[36:39], a[20:23], 0\n\t"
    "ds_read_b128 a[76:79], v215 offset:2048\n\t"
    "v_mfma_f32_16x16x32_bf16 v[20:23], a[32:35], a[20:23], 0\n\t"
    "buffer_load_dwordx4 v209, %[srda], %[tmp] offen offset:3072 lds\n\t"
    "v_mfma_f32_16x16x32_bf16 v[24:27], a[32:35], a[24:27], 0\n\t"
    "s_add_u32 m0, %[m0a], 36864\n\t"
    "v_mfma_f32_16x16x32_bf16 v[56:59], a[36:39], a[24:27], 0\n\t"
    "s_add_u32 %[tmp], %[sa], 65536\n\t"
    "ds_read_b128 a[80:83], v215 offset:4096\n\t"
    "v_mfma_f32_16x16x32_bf16 v[60:63], a[36:39], a[28:31], 0\n\t"
    "buffer_load_dwordx4 v208, %[srda], %[tmp] offen offset:0 lds\n\t"
    "v_mfma_f32_16x16x32_bf16 v[28:31], a[32:35], a[28:31], 0\n\t"
    "ds_read_b128 a[84:87], v215 offset:6144\n\t"
    "v_mfma_f32_16x16x32_bf16 v[92:95], a[40:43], a[28:31], 0\n\t"
    "v_mfma_f32_16x16x32_bf16 v[124:127], a[44:47], a[28:31], 0\n\t"
    "buffer_load_dwordx4 v209, %[srda], %[tmp] offen offset:1024 lds\n\t"
    "v_mfma_f32_16x16x32_bf16 v[120:123], a[44:47], a[24:27], 0\n\t"
    "ds_read_b128 a[88:91], v215 offset:8192\n\t"
    "v_mfma_f32_16x16x32_bf16 v[88:91], a[40:43], a[24:27], 0\n\t"
    "s_add_u32 %[tmp], %[sa], 96256\n\t"
    "ds_read_b128 a[92:95], v215 offset:10240\n\t"
    "v_mfma_f32_16x16x32_bf16 v[84:87], a[40:43], a[20:23], 0\n\t"
    "buffer_load_dwordx4 v208, %[srda], %[tmp] offen offset:2048 lds\n\t"
    "v_mfma_f32_16x16x32_bf16 v[116:119], a[44:47], a[20:23], 0\n\t"
    "v_mfma_f32_16x16x32_bf16 v[112:115], a[44:47], a[16:19], 0\n\t"
    "ds_read_b128 a[96:99], v215 offset:12288\n\t"
    "v_mfma_f32_16x16x32_bf16 v[80:83], a[40:43], a[16:19], 0\n\t"
    "buffer_load_dwordx4 v209, %[srda], %[tmp] offen offset:3072 lds\n\t"
    "v_mfma_f32_16x16x32_bf16 v[76:79], a[40:43], a[12:15], 0\n\t"
    "ds_read_b128 a[100:103], v215 offset:14336\n\t"
    "v_mfma_f32_16x16x32_bf16 v[108:111], a[44:47], a[12:15], 0\n\t"
    "s_add_u32 m0, %[m0b], 106496\n\t"
    "v_mfma_f32_16x16x32_bf16 v[104:107], a[44:47], a[8:11], 0\n\t"
    "buffer_load_dwordx4 v210, %[srd1], %[sg] offen offset:0 lds\n\t"
    "v_mfma_f32_16x16x32_bf16 v[72:75], a[40:43], a[8:11], 0\n\t"
    "ds_read_b64_tr_b16 a[112:113], v216 offset:12288\n\t"
    "v_mfma_f32_16x16x32_bf16 v[68:71], a[40:43], a[4:7], 0\n\t"
    "ds_read_b64_tr_b16 a[114:115], v216 offset:12800\n\t"
    "v_mfma_f32_16x16x32_bf16 v[100:103], a[44:47], a[4:7], 0\n\t"
    "buffer_load_dwordx4 v211, %[srd1], %[sg] offen offset:1024 lds\n\t"
    "v_mfma_f32_16x16x32_bf16 v[96:99], a[44:47], a[0:3], 0\n\t"
    "s_add_u32 m0, %[m0b], 114688\n\t"
    "v_mfma_f32_16x16x32_bf16 v[64:67], a[40:43], a[0:3], 0\n\t"
    "s_add_u32 %[tmp], %[sg], 128\n\t"
    "ds_read_b64_tr_b16 a[116:117], v217 offset:12288\n\t"
    "v_mfma_f32_16x16x32_bf16 v[128:131], a[48:51], a[0:3], 0\n\t"
    "buffer_load_dwordx4 v210, %[srd1], %[tmp] offen offset:0 lds\n\t"
    "v_mfma_f32_16x16x32_bf16 v[160:163], a[52:55], a[0:3], 0\n\t"
    "ds_read_b64_tr_b16 a[118:119], v217 offset:12800\n\t"
    "v_mfma_f32_16x16x32_bf16 v[164:167], a[52:55], a[4:7], 0\n\t"
    "v_mfma_f32_16x16x32_bf16 v[132:135], a[48:51], a[4:7], 0\n\t"
    "buffer_load_dwordx4 v211, %[srd1], %[tmp] offen offset:1024 lds\n\t"
    "v_mfma_f32_16x16x32_bf16 v[136:139], a[48:51], a[8:11], 0\n\t"
    "ds_read_b64_tr_b16 a[120:121], v216 offset:20480\n\t"
    "v_mfma_f32_16x16x32_bf16 v[168:171], a[52:55], a[8:11], 0\n\t"
    "s_add_u32 m0, %[m0b], 122880\n\t"
    "ds_read_b64_tr_b16 a[122:123], v216 offset:20992\n\t"
    "v_mfma_f32_16x16x32_bf16 v[172:175], a[52:55], a[12:15], 0\n\t"
    "buffer_load_dwordx4 v210, %[srd2], %[sg] offen offset:0 lds\n\t"
    "v_mfma_f32_16x16x32_bf16 v[140:143], a[48:51], a[12:15], 0\n\t"
    "v_mfma_f32_16x16x32_bf16 v[144:147], a[48:51], a[16:19], 0\n\t"
    "ds_read_b64_tr_b16 a[124:125], v217 offset:20480\n\t"
    "v_mfma_f32_16x16x32_bf16 v[176:179], a[52:55], a[16:19], 0\n\t"
    "buffer_load_dwordx4 v211, %[srd2], %[sg] offen offset:1024 lds\n\t"
    "v_mfma_f32_16x16x32_bf16 v[180:183], a[52:55], a[20:23], 0\n\t"
    "ds_read_b64_tr_b16 a[126:127], v217 offset:20992\n\t"
    "v_mfma_f32_16x16x32_bf16 v[148:151], a[48:51], a[20:23], 0\n\t"
    "s_add_u32 m0, %[m0b], 131072\n\t"
    "v_mfma_f32_16x16x32_bf16 v[152:155], a[48:51], a[24:27], 0\n\t"
    "buffer_load_dwordx4 v210, %[srd2], %[tmp] offen offset:0 lds\n\t"
    "v_mfma_f32_16x16x32_bf16 v[184:187], a[52:55], a[24:27], 0\n\t"
    "ds_read_b64_tr_b16 a[128:129], v216 offset:28672\n\t"
    "v_mfma_f32_16x16x32_bf16 v[188:191], a[52:55], a[28:31], 0\n\t"
    "ds_read_b64_tr_b16 a[130:131], v216 offset:29184\n\t"
    "v_mfma_f32_16x16x32_bf16 v[156:159], a[48:51], a[28:31], 0\n\t"
    "buffer_load_dwordx4 v211, %[srd2], %[tmp] offen offset:1024 lds\n\t"
    "v_mfma_f32_16x16x32_bf16 a[156:159], a[56:59], a[28:31], 0\n\t"
    "v_mfma_f32_16x16x32_bf16 a[188:191], a[60:63], a[28:31], 0\n\t"
    "s_add_u32 m0, %[m0c], 139264\n\t"
    "ds_read_b64_tr_b16 a[132:133], v217 offset:28672\n\t"
    "v_mfma_f32_16x16x32_bf16 a[184:187], a[60:63], a[24:27], 0\n\t"
    "buffer_load_dwordx4 v212, %[srd1], %[sg] offen lds\n\t"
    "v_mfma_f32_16x16x32_bf16 a[152:155], a[56:59], a[24:27], 0\n\t"
    "ds_read_b64_tr_b16 a[134:135], v217 offset:29184\n\t"
    "v_mfma_f32_16x16x32_bf16 a[148:151], a[56:59], a[20:23], 0\n\t"
    "s_add_u32 m0, %[m0c], 143360\n\t"
    "v_mfma_f32_16x16x32_bf16 a[180:183], a[60:63], a[20:23], 0\n\t"
    "buffer_load_dwordx4 v212, %[srd2], %[sg] offen lds\n\t"
    "v_mfma_f32_16x16x32_bf16 a[176:179], a[60:63], a[16:19], 0\n\t"
    "ds_read_b64_tr_b16 a[136:137], v220 offset:2048\n\t"
    "v_mfma_f32_16x16x32_bf16 a[144:147], a[56:59], a[16:19], 0\n\t"
    "s_and_b32 %[tmp], %[ord], 15\n\t"
    "ds_read_b64_tr_b16 a[138:139], v220 offset:2304\n\t"
    "v_mfma_f32_16x16x32_bf16 v[204:207], a[56:59], a[12:15], 0\n\t"
    "buffer_load_dword v223, v221, %[srdp], %[sp] offen \n\t"
    "s_lshr_b32 %[ord], %[ord], 4\n\t"
    "v_mfma_f32_16x16x32_bf16 a[172:175], a[60:63], a[12:15], 0\n\t"
    "s_add_u32 %[sp], %[sp], %[pstep]\n\t"
    "s_lshl_b32 %[sa], %[tmp], 8\n\t"
    "v_mfma_f32_16x16x32_bf16 a[168:171], a[60:63], a[8:11], 0\n\t"
    "s_mul_i32 %[sg], %[tmp], 917504\n\t"
    "ds_read_b64_tr_b16 a[140:141], v220 offset:6144\n\t"
    "v_mfma_f32_16x16x32_bf16 v[200:203], a[56:59], a[8:11], 0\n\t"
    "ds_read_b64_tr_b16 a[142:143], v220 offset:6400\n\t"
    "v_mfma_f32_16x16x32_bf16 v[196:199], a[56:59], a[4:7], 0\n\t"
    "buffer_load_dword v223, v221, %[srdp], %[sp] offen \n\t"
    "v_mfma_f32_16x16x32_bf16 a[164:167], a[60:63], a[4:7], 0\n\t"
    "s_add_u32 %[sp], %[sp], %[pstep]\n\t"
    "v_mfma_f32_16x16x32_bf16 a[160:163], a[60:63], a[0:3], 0\n\t"
    "v_mfma_f32_16x16x32_bf16 v[192:195], a[56:59], a[0:3], 0\n\t"
    "v_mfma_f32_16x16x32_bf16 a[192:195], a[64:67], a[0:3], 0\n\t"
    "v_mfma_f32_16x16x32_bf16 a[224:227], a[68:71], a[0:3], 0\n\t"
    "v_mfma_f32_16x16x32_bf16 a[228:231], a[68:71], a[4:7], 0\n\t"
    "s_cbranch_vccnz 140f\n\t"
    "24:\n\t"
    "v_mfma_f32_16x16x32_bf16 a[196:199], a[64:67], a[4:7], 0\n\t"
    "v_mfma_f32_16x16x32_bf16 a[200:203], a[64:67], a[8:11], 0\n\t"
    "v_mfma_f32_16x16x32_bf16 a[232:235], a[68:71], a[8:11], 0\n\t"
    "v_mfma_f32_16x16x32_bf16 a[236:239], a[68:71], a[12:15], 0\n\t"
    "v_mfma_f32_16x16x32_bf16 a[204:207], a[64:67], a[12:15], 0\n\t"
    "v_mfma_f32_16x16x32_bf16 a[208:211], a[64:67], a[16:19], 0\n\t"
    "v_mfma_f32_16x16x32_bf16 a[240:243], a[68:71], a[16:19], 0\n\t"
    "v_mfma_f32_16x16x32_bf16 a[244:247], a[68:71], a[20:23], 0\n\t"
    "v_mfma_f32_16x16x32_bf16 a[212:215], a[64:67], a[20:23], 0\n\t"
    "v_mfma_f32_16x16x32_bf16 a[216:219], a[64:67], a[24:27], 0\n\t"
    "v_mfma_f32_16x16x32_bf16 a[248:251], a[68:71], a[24:27], 0\n\t"
    "v_mfma_f32_16x16x32_bf16 a[252:255], a[68:71], a[28:31], 0\n\t"
    "v_mfma_f32_16x16x32_bf16 a[220:223], a[64:67], a[28:31], 0\n\t"
    "s_branch 8f\n\t"
    "1:\n\t"
    "s_cmp_eq_u32 %[cnt], %[mine]\n\t"
    "s_cselect_b64 vcc, -1, 0\n\t"
    "v_mfma_f32_16x16x32_bf16 v[0:3], a[32:35], a[0:3], v[0:3]\n\t"
    "ds_read_b64_tr_b16 a[104:105], v216 offset:4096\n\t"
    "v_mfma_f32_16x16x32_bf16 v[32:35], a[36:39], a[0:3], v[32:35]\n\t"
    "v_mfma_f32_16x16x32_bf16 v[36:39], a[36:39], a[4:7], v[36:39]\n\t"
    "ds_read_b64_tr_b16 a[106:107], v216 offset:4608\n\t"
    "v_mfma_f32_16x16x32_bf16 v[4:7], a[32:35], a[4:7], v[4:7]\n\t"
    "v_mfma_f32_16x16x32_bf16 v[8:11], a[32:35], a[8:11], v[8:11]\n\t"
    "ds_read_b64_tr_b16 a[108:109], v217 offset:4096\n\t"
    "v_mfma_f32_16x16x32_bf16 v[40:43], a[36:39], a[8:11], v[40:43]\n\t"
    "v_mfma_f32_16x16x32_bf16 v[44:47], a[36:39], a[12:15], v[44:47]\n\t"
    "ds_read_b64_tr_b16 a[110:111], v217 offset:4608\n\t"
    "v_mfma_f32_16x16x32_bf16 v[12:15], a[32:35], a[12:15], v[12:15]\n\t"
    "v_mfma_f32_16x16x32_bf16 v[16:19], a[32:35], a[16:19], v[16:19]\n\t"
    "ds_read_b128 a[72:75], v215 offset:0\n\t"
    "v_mfma_f32_16x16x32_bf16 v[48:51], a[36:39], a[16:19], v[48:51]\n\t"
    "v_mfma_f32_16x16x32_bf16 v[52:55], a[36:39], a[20:23], v[52:55]\n\t"
    "v_mfma_f32_16x16x32_bf16 v[20:23], a[32:35], a[20:23], v[20:23]\n\t"
    "ds_read_b128 a[76:79], v215 offset:2048\n\t"
    "v_mfma_f32_16x16x32_bf16 v[24:27], a[32:35], a[24:27], v[24:27]\n\t"
    "v_mfma_f32_16x16x32_bf16 v[56:59], a[36:39], a[24:27], v[56:59]\n\t"
    "ds_read_b128 a[80:83], v215 offset:4096\n\t"
    "v_mfma_f32_16x16x32_bf16 v[60:63], a[36:39], a[28:31], v[60:63]\n\t"
    "v_mfma_f32_16x16x32_bf16 v[28:31], a[32:35], a[28:31], v[28:31]\n\t"
    "ds_read_b128 a[84:87], v215 offset:6144\n\t"
    "v_mfma_f32_16x16x32_bf16 v[92:95], a[40:43], a[28:31], v[92:95]\n\t"
    "v_mfma_f32_16x16x32_bf16 v[124:127], a[44:47], a[28:31], v[124:127]\n\t"
    "ds_read_b128 a[88:91], v215 offset:8192\n\t"
    "v_mfma_f32_16x16x32_bf16 v[120:123], a[44:47], a[24:27], v[120:123]\n\t"
    "v_mfma_f32_16x16x32_bf16 v[88:91], a[40:43], a[24:27], v[88:91]\n\t"
    "ds_read_b128 a[92:95], v215 offset:10240\n\t"
    "v_mfma_f32_16x16x32_bf16 v[84:87], a[40:43], a[20:23], v[84:87]\n\t"
    "v_mfma_f32_16x16x32_bf16 v[116:119], a[44:47], a[20:23], v[116:119]\n\t"
    "v_mfma_f32_16x16x32_bf16 v[112:115], a[44:47], a[16:19], v[112:115]\n\t"
    "ds_read_b128 a[96:99], v215 offset:12288\n\t"
    "v_mfma_f32_16x16x32_bf16 v[80:83], a[40:43], a[16:19], v[80:83]\n\t"
    "v_mfma_f32_16x16x32_bf16 v[76:79], a[40:43], a[12:15], v[76:79]\n\t"
    "ds_read_b128 a[100:103], v215 offset:14336\n\t"
    "v_mfma_f32_16x16x32_bf16 v[108:111], a[44:47], a[12:15], v[108:111]\n\t"
    "v_mfma_f32_16x16x32_bf16 v[104:107], a[44:47], a[8:11], v[104:107]\n\t"
    "ds_read_b64_tr_b16 a[112:113], v216 offset:12288\n\t"
    "v_mfma_f32_16x16x32_bf16 v[72:75], a[40:43], a[8:11], v[72:75]\n\t"
    "v_mfma_f32_16x16x32_bf16 v[68:71], a[40:43], a[4:7], v[68:71]\n\t"
    "ds_read_b64_tr_b16 a[114:115], v216 offset:12800\n\t"
    "v_mfma_f32_16x16x32_bf16 v[100:103], a[44:47], a[4:7], v[100:103]\n\t"
    "v_mfma_f32_16x16x32_bf16 v[96:99], a[44:47], a[0:3], v[96:99]\n\t"
    "v_mfma_f32_16x16x32_bf16 v[64:67], a[40:43], a[0:3], v[64:67]\n\t"
    "ds_read_b64_tr_b16 a[116:117], v217 offset:12288\n\t"
    "v_mfma_f32_16x16x32_bf16 v[128:131], a[48:51], a[0:3], v[128:131]\n\t"
    "v_mfma_f32_16x16x32_bf16 v[160:163], a[52:55], a[0:3], v[160:163]\n\t"
    "ds_read_b64_tr_b16 a[118:119], v217 offset:12800\n\t"
    "v_mfma_f32_16x16x32_bf16 v[164:167], a[52:55], a[4:7], v[164:167]\n\t"
    "v_mfma_f32_16x16x32_bf16 v[132:135], a[48:51], a[4:7], v[132:135]\n\t"
    "ds_read_b64_tr_b16 a[120:121], v216 offset:20480\n\t"
    "v_mfma_f32_16x16x32_bf16 v[136:139], a[48:51], a[8:11], v[136:139]\n\t"
    "v_mfma_f32_16x16x32_bf16 v[168:171], a[52:55], a[8:11], v[168:171]\n\t"
    "ds_read_b64_tr_b16 a[122:123], v216 offset:20992\n\t"
    "v_mfma_f32_16x16x32_bf16 v[172:175], a[52:55], a[12:15], v[172:175]\n\t"
    "v_mfma_f32_16x16x32_bf16 v[140:143], a[48:51], a[12:15], v[140:143]\n\t"
    "ds_read_b64_tr_b16 a[124:125], v217 offset:20480\n\t"
    "v_mfma_f32_16x16x32_bf16 v[144:147], a[48:51], a[16:19], v[144:147]\n\t"
    "v_mfma_f32_16x16x32_bf16 v[176:179], a[52:55], a[16:19], v[176:179]\n\t"
    "v_mfma_f32_16x16x32_bf16 v[180:183], a[52:55], a[20:23], v[180:183]\n\t"
    "ds_read_b64_tr_b16 a[126:127], v217 offset:20992\n\t"
    "v_mfma_f32_16x16x32_bf16 v[148:151], a[48:51], a[20:23], v[148:151]\n\t"
    "v_mfma_f32_16x16x32_bf16 v[152:155], a[48:51], a[24:27], v[152:155]\n\t"
    "ds_read_b64_tr_b16 a[128:129], v216 offset:28672\n\t"
    "v_mfma_f32_16x16x32_bf16 v[184:187], a[52:55], a[24:27], v[184:187]\n\t"
    "v_mfma_f32_16x16x32_bf16 v[188:191], a[52:55], a[28:31], v[188:191]\n\t"
    "ds_read_b64_tr_b16 a[130:131], v216 offset:29184\n\t"
    "v_mfma_f32_16x16x32_bf16 v[156:159], a[48:51], a[28:31], v[156:159]\n\t"
    "v_mfma_f32_16x16x32_bf16 a[156:159], a[56:59], a[28:31], a[156:159]\n\t"
    "ds_read_b64_tr_b16 a[132:133], v217 offset:28672\n\t"
    "v_mfma_f32_16x16x32_bf16 a[188:191], a[60:63], a[28:31], a[188:191]\n\t"
    "v_mfma_f32_16x16x32_bf16 a[184:187], a[60:63], a[24:27], a[184:187]\n\t"
    "ds_read_b64_tr_b16 a[134:135], v217 offset:29184\n\t"
    "v_mfma_f32_16x16x32_bf16 a[152:155], a[56:59], a[24:27], a[152:155]\n\t"
    "v_mfma_f32_16x16x32_bf16 a[148:151], a[56:59], a[20:23], a[148:151]\n\t"
    "v_mfma_f32_16x16x32_bf16 a[180:183], a[60:63], a[20:23], a[180:183]\n\t"
    "ds_read_b64_tr_b16 a[136:137], v220 offset:2048\n\t"
    "v_mfma_f32_16x16x32_bf16 a[176:179], a[60:63], a[16:19], a[176:179]\n\t"
    "v_mfma_f32_16x16x32_bf16 a[144:147], a[56:59], a[16:19], a[144:147]\n\t"
    "ds_read_b64_tr_b16 a[138:139], v220 offset:2304\n\t"
    "v_mfma_f32_16x16x32_bf16 v[204:207], a[56:59], a[12:15], v[204:207]\n\t"
    "v_mfma_f32_16x16x32_bf16 a[172:175], a[60:63], a[12:15], a[172:175]\n\t"
    "ds_read_b64_tr_b16 a[140:141], v220 offset:6144\n\t"
    "v_mfma_f32_16x16x32_bf16 a[168:171], a[60:63], a[8:11], a[168:171]\n\t"
    "v_mfma_f32_16x16x32_bf16 v[200:203], a[56:59], a[8:11], v[200:203]\n\t"
    "ds_read_b64_tr_b16 a[142:143], v220 offset:6400\n\t"
    "v_mfma_f32_16x16x32_bf16 v[196:199], a[56:59], a[4:7], v[196:199]\n\t"
    "v_mfma_f32_16x16x32_bf16 a[164:167], a[60:63], a[4:7], a[164:167]\n\t"
    "v_mfma_f32_16x16x32_bf16 a[160:163], a[60:63], a[0:3], a[160:163]\n\t"
    "v_mfma_f32_16x16x32_bf16 v[192:195], a[56:59], a[0:3], v[192:195]\n\t"
    "v_mfma_f32_16x16x32_bf16 a[192:195], a[64:67], a[0:3], a[192:195]\n\t"
    "v_mfma_f32_16x16x32_bf16 a[224:227], a[68:71], a[0:3], a[224:227]\n\t"
    "v_mfma_f32_16x16x32_bf16 a[228:231], a[68:71], a[4:7], a[228:231]\n\t"
    "s_cbranch_vccnz 100f\n\t"
    "20:\n\t"
    "v_mfma_f32_16x16x32_bf16 a[196:199], a[64:67], a[4:7], a[196:199]\n\t"
    "v_mfma_f32_16x16x32_bf16 a[200:203], a[64:67], a[8:11], a[200:203]\n\t"
    "v_mfma_f32_16x16x32_bf16 a[232:235], a[68:71], a[8:11], a[232:235]\n\t"
    "v_mfma_f32_16x16x32_bf16 a[236:239], a[68:71], a[12:15], a[236:239]\n\t"
    "v_mfma_f32_16x16x32_bf16 a[204:207], a[64:67], a[12:15], a[204:207]\n\t"
    "v_mfma_f32_16x16x32_bf16 a[208:211], a[64:67], a[16:19], a[208:211]\n\t"
    "v_mfma_f32_16x16x32_bf16 a[240:243], a[68:71], a[16:19], a[240:243]\n\t"
    "v_mfma_f32_16x16x32_bf16 a[244:247], a[68:71], a[20:23], a[244:247]\n\t"
    "v_mfma_f32_16x16x32_bf16 a[212:215], a[64:67], a[20:23], a[212:215]\n\t"
    "v_mfma_f32_16x16x32_bf16 a[216:219], a[64:67], a[24:27], a[216:219]\n\t"
    "v_mfma_f32_16x16x32_bf16 a[248:251], a[68:71], a[24:27], a[248:251]\n\t"
    "v_mfma_f32_16x16x32_bf16 a[252:255], a[68:71], a[28:31], a[252:255]\n\t"
    "v_mfma_f32_16x16x32_bf16 a[220:223], a[64:67], a[28:31], a[220:223]\n\t"
    "8:\n\t"
    "s_waitcnt vmcnt(2) lgkmcnt(0)\n\t"
    "s_barrier\n\t"
    "v_mfma_f32_16x16x32_bf16 v[0:3], a[104:107], a[72:75], v[0:3]\n\t"
    "v_mfma_f32_16x16x32_bf16 v[32:35], a[108:111], a[72:75], v[32:35]\n\t"
    "s_add_u32 m0, %[m0a], 0\n\t"
    "ds_read_b64_tr_b16 a[32:33], v218 offset:0\n\t"
    "v_mfma_f32_16x16x32_bf16 v[36:39], a[108:111], a[76:79], v[36:39]\n\t"
    "buffer_load_dwordx4 v208, %[srda], %[sa] offen offset:0 lds\n\t"
    "v_mfma_f32_16x16x32_bf16 v[4:7], a[104:107], a[76:79], v[4:7]\n\t"
    "ds_read_b64_tr_b16 a[34:35], v218 offset:512\n\t"
    "v_mfma_f32_16x16x32_bf16 v[8:11], a[104:107], a[80:83], v[8:11]\n\t"
    "v_mfma_f32_16x16x32_bf16 v[40:43], a[108:111], a[80:83], v[40:43]\n\t"
    "ds_read_b64_tr_b16 a[36:37], v219 offset:0\n\t"
    "v_mfma_f32_16x16x32_bf16 v[44:47], a[108:111], a[84:87], v[44:47]\n\t"
    "buffer_load_dwordx4 v209, %[srda], %[sa] offen offset:1024 lds\n\t"
    "v_mfma_f32_16x16x32_bf16 v[12:15], a[104:107], a[84:87], v[12:15]\n\t"
    "ds_read_b64_tr_b16 a[38:39], v219 offset:512\n\t"
    "v_mfma_f32_16x16x32_bf16 v[16:19], a[104:107], a[88:91], v[16:19]\n\t"
    "v_mfma_f32_16x16x32_bf16 v[48:51], a[108:111], a[88:91], v[48:51]\n\t"
    "s_add_u32 %[tmp], %[sa], 30720\n\t"
    "ds_read_b128 a[0:3], v214 offset:32768\n\t"
    "v_mfma_f32_16x16x32_bf16 v[52:55], a[108:111], a[92:95], v[52:55]\n\t"
    "buffer_load_dwordx4 v208, %[srda], %[tmp] offen offset:2048 lds\n\t"
    "v_mfma_f32_16x16x32_bf16 v[20:23], a[104:107], a[92:95], v[20:23]\n\t"
    "v_mfma_f32_16x16x32_bf16 v[24:27], a[104:107], a[96:99], v[24:27]\n\t"
    "ds_read_b128 a[4:7], v214 offset:34816\n\t"
    "v_mfma_f32_16x16x32_bf16 v[56:59], a[108:111], a[96:99], v[56:59]\n\t"
    "ds_read_b128 a[8:11], v214 offset:36864\n\t"
    "v_mfma_f32_16x16x32_bf16 v[60:63], a[108:111], a[100:103], v[60:63]\n\t"
    "buffer_load_dwordx4 v209, %[srda], %[tmp] offen offset:3072 lds\n\t"
    "v_mfma_f32_16x16x32_bf16 v[28:31], a[104:107], a[100:103], v[28:31]\n\t"
    "v_mfma_f32_16x16x32_bf16 v[92:95], a[112:115], a[100:103], v[92:95]\n\t"
    "s_add_u32 m0, %[m0a], 4096\n\t"
    "ds_read_b128 a[12:15], v214 offset:38912\n\t"
    "v_mfma_f32_16x16x32_bf16 v[124:127], a[116:119], a[100:103], v[124:127]\n\t"
    "s_add_u32 %[tmp], %[sa], 65536\n\t"
    "v_mfma_f32_16x16x32_bf16 v[120:123], a[116:119], a[96:99], v[120:123]\n\t"
    "buffer_load_dwordx4 v208, %[srda], %[tmp] offen offset:0 lds\n\t"
    "v_mfma_f32_16x16x32_bf16 v[88:91], a[112:115], a[96:99], v[88:91]\n\t"
    "ds_read_b128 a[16:19], v214 offset:40960\n\t"
    "v_mfma_f32_16x16x32_bf16 v[84:87], a[112:115], a[92:95], v[84:87]\n\t"
    "ds_read_b128 a[20:23], v214 offset:43008\n\t"
    "v_mfma_f32_16x16x32_bf16 v[116:119], a[116:119], a[92:95], v[116:119]\n\t"
    "v_mfma_f32_16x16x32_bf16 v[112:115], a[116:119], a[88:91], v[112:115]\n\t"
    "buffer_load_dwordx4 v209, %[srda], %[tmp] offen offset:1024 lds\n\t"
    "v_mfma_f32_16x16x32_bf16 v[80:83], a[112:115], a[88:91], v[80:83]\n\t"
    "ds_read_b128 a[24:27], v214 offset:45056\n\t"
    "v_mfma_f32_16x16x32_bf16 v[76:79], a[112:115], a[84:87], v[76:79]\n\t"
    "v_mfma_f32_16x16x32_bf16 v[108:111], a[116:119], a[84:87], v[108:111]\n\t"
    "s_add_u32 %[tmp], %[sa], 96256\n\t"
    "ds_read_b128 a[28:31], v214 offset:47104\n\t"
    "v_mfma_f32_16x16x32_bf16 v[104:107], a[116:119], a[80:83], v[104:107]\n\t"
    "buffer_load_dwordx4 v208, %[srda], %[tmp] offen offset:2048 lds\n\t"
    "v_mfma_f32_16x16x32_bf16 v[72:75], a[112:115], a[80:83], v[72:75]\n\t"
    "ds_read_b64_tr_b16 a[40:41], v218 offset:8192\n\t"
    "v_mfma_f32_16x16x32_bf16 v[68:71], a[112:115], a[76:79], v[68:71]\n\t"
    "v_mfma_f32_16x16x32_bf16 v[100:103], a[116:119], a[76:79], v[100:103]\n\t"
    "ds_read_b64_tr_b16 a[42:43], v218 offset:8704\n\t"
    "v_mfma_f32_16x16x32_bf16 v[96:99], a[116:119], a[72:75], v[96:99]\n\t"
    "buffer_load_dwordx4 v209, %[srda], %[tmp] offen offset:3072 lds\n\t"
    "v_mfma_f32_16x16x32_bf16 v[64:67], a[112:115], a[72:75], v[64:67]\n\t"
    "v_mfma_f32_16x16x32_bf16 v[128:131], a[120:123], a[72:75], v[128:131]\n\t"
    "ds_read_b64_tr_b16 a[44:45], v219 offset:8192\n\t"
    "v_mfma_f32_16x16x32_bf16 v[160:163], a[124:127], a[72:75], v[160:163]\n\t"
    "s_add_u32 m0, %[m0b], 65536\n\t"
    "ds_read_b64_tr_b16 a[46:47], v219 offset:8704\n\t"
    "v_mfma_f32_16x16x32_bf16 v[164:167], a[124:127], a[76:79], v[164:167]\n\t"
    "buffer_load_dwordx4 v210, %[srd1], %[sg] offen offset:0 lds\n\t"
    "v_mfma_f32_16x16x32_bf16 v[132:135], a[120:123], a[76:79], v[132:135]\n\t"
    "v_mfma_f32_16x16x32_bf16 v[136:139], a[120:123], a[80:83], v[136:139]\n\t"
    "ds_read_b64_tr_b16 a[48:49], v218 offset:16384\n\t"
    "v_mfma_f32_16x16x32_bf16 v[168:171], a[124:127], a[80:83], v[168:171]\n\t"
    "ds_read_b64_tr_b16 a[50:51], v218 offset:16896\n\t"
    "v_mfma_f32_16x16x32_bf16 v[172:175], a[124:127], a[84:87], v[172:175]\n\t"
    "buffer_load_dwordx4 v211, %[srd1], %[sg] offen offset:1024 lds\n\t"
    "v_mfma_f32_16x16x32_bf16 v[140:143], a[120:123], a[84:87], v[140:143]\n\t"
    "v_mfma_f32_16x16x32_bf16 v[144:147], a[120:123], a[88:91], v[144:147]\n\t"
    "s_add_u32 m0, %[m0b], 73728\n\t"
    "ds_read_b64_tr_b16 a[52:53], v219 offset:16384\n\t"
    "v_mfma_f32_16x16x32_bf16 v[176:179], a[124:127], a[88:91], v[176:179]\n\t"
    "s_add_u32 %[tmp], %[sg], 128\n\t"
    "v_mfma_f32_16x16x32_bf16 v[180:183], a[124:127], a[92:95], v[180:183]\n\t"
    "buffer_load_dwordx4 v210, %[srd1], %[tmp] offen offset:0 lds\n\t"
    "v_mfma_f32_16x16x32_bf16 v[148:151], a[120:123], a[92:95], v[148:151]\n\t"
    "ds_read_b64_tr_b16 a[54:55], v219 offset:16896\n\t"
    "v_mfma_f32_16x16x32_bf16 v[152:155], a[120:123], a[96:99], v[152:155]\n\t"
    "ds_read_b64_tr_b16 a[56:57], v218 offset:24576\n\t"
    "v_mfma_f32_16x16x32_bf16 v[184:187], a[124:127], a[96:99], v[184:187]\n\t"
    "v_mfma_f32_16x16x32_bf16 v[188:191], a[124:127], a[100:103], v[188:191]\n\t"
    "buffer_load_dwordx4 v211, %[srd1], %[tmp] offen offset:1024 lds\n\t"
    "v_mfma_f32_16x16x32_bf16 v[156:159], a[120:123], a[100:103], v[156:159]\n\t"
    "ds_read_b64_tr_b16 a[58:59], v218 offset:25088\n\t"
    "v_mfma_f32_16x16x32_bf16 a[156:159], a[128:131], a[100:103], a[156:159]\n\t"
    "v_mfma_f32_16x16x32_bf16 a[188:191], a[132:135], a[100:103], a[188:191]\n\t"
    "s_add_u32 m0, %[m0b], 81920\n\t"
    "ds_read_b64_tr_b16 a[60:61], v219 offset:24576\n\t"
    "v_mfma_f32_16x16x32_bf16 a[184:187], a[132:135], a[96:99], a[184:187]\n\t"
    "buffer_load_dwordx4 v210, %[srd2], %[sg] offen offset:0 lds\n\t"
    "v_mfma_f32_16x16x32_bf16 a[152:155], a[128:131], a[96:99], a[152:155]\n\t"
    "ds_read_b64_tr_b16 a[62:63], v219 offset:25088\n\t"
    "v_mfma_f32_16x16x32_bf16 a[148:151], a[128:131], a[92:95], a[148:151]\n\t"
    "v_mfma_f32_16x16x32_bf16 a[180:183], a[132:135], a[92:95], a[180:183]\n\t"
    "ds_read_b64_tr_b16 a[64:65], v220 offset:40960\n\t"
    "v_mfma_f32_16x16x32_bf16 a[176:179], a[132:135], a[88:91], a[176:179]\n\t"
    "buffer_load_dwordx4 v211, %[srd2], %[sg] offen offset:1024 lds\n\t"
    "v_mfma_f32_16x16x32_bf16 a[144:147], a[128:131], a[88:91], a[144:147]\n\t"
    "v_mfma_f32_16x16x32_bf16 v[204:207], a[128:131], a[84:87], v[204:207]\n\t"
    "ds_read_b64_tr_b16 a[66:67], v220 offset:41216\n\t"
    "v_mfma_f32_16x16x32_bf16 a[172:175], a[132:135], a[84:87], a[172:175]\n\t"
    "s_add_u32 m0, %[m0b], 90112\n\t"
    "ds_read_b64_tr_b16 a[68:69], v220 offset:45056\n\t"
    "v_mfma_f32_16x16x32_bf16 a[168:171], a[132:135], a[80:83], a[168:171]\n\t"
    "buffer_load_dwordx4 v210, %[srd2], %[tmp] offen offset:0 lds\n\t"
    "v_mfma_f32_16x16x32_bf16 v[200:203], a[128:131], a[80:83], v[200:203]\n\t"
    "v_mfma_f32_16x16x32_bf16 v[196:199], a[128:131], a[76:79], v[196:199]\n\t"
    "ds_read_b64_tr_b16 a[70:71], v220 offset:45312\n\t"
    "v_mfma_f32_16x16x32_bf16 a[164:167], a[132:135], a[76:79], a[164:167]\n\t"
    "v_mfma_f32_16x16x32_bf16 a[160:163], a[132:135], a[72:75], a[160:163]\n\t"
    "buffer_load_dwordx4 v211, %[srd2], %[tmp] offen offset:1024 lds\n\t"
    "v_mfma_f32_16x16x32_bf16 v[192:195], a[128:131], a[72:75], v[192:195]\n\t"
    "v_mfma_f32_16x16x32_bf16 a[192:195], a[136:139], a[72:75], a[192:195]\n\t"
    "v_mfma_f32_16x16x32_bf16 a[224:227], a[140:143], a[72:75], a[224:227]\n\t"
    "s_add_u32 m0, %[m0c], 98304\n\t"
    "s_cbranch_vccnz 110f\n\t"
    "21:\n\t"
    "v_mfma_f32_16x16x32_bf16 a[228:231], a[140:143], a[76:79], a[228:231]\n\t"
    "buffer_load_dwordx4 v212, %[srd1], %[sg] offen lds\n\t"
    "v_mfma_f32_16x16x32_bf16 a[196:199], a[136:139], a[76:79], a[196:199]\n\t"
    "v_mfma_f32_16x16x32_bf16 a[200:203], a[136:139], a[80:83], a[200:203]\n\t"
    "v_mfma_f32_16x16x32_bf16 a[232:235], a[140:143], a[80:83], a[232:235]\n\t"
    "s_add_u32 m0, %[m0c], 102400\n\t"
    "v_mfma_f32_16x16x32_bf16 a[236:239], a[140:143], a[84:87], a[236:239]\n\t"
    "buffer_load_dwordx4 v212, %[srd2], %[sg] offen lds\n\t"
    "v_mfma_f32_16x16x32_bf16 a[204:207], a[136:139], a[84:87], a[204:207]\n\t"
    "v_mfma_f32_16x16x32_bf16 a[208:211], a[136:139], a[88:91], a[208:211]\n\t"
    "v_mfma_f32_16x16x32_bf16 a[240:243], a[140:143], a[88:91], a[240:243]\n\t"
    "s_add_u32 %[sa], %[sa], 128\n\t"
    "v_mfma_f32_16x16x32_bf16 a[244:247], a[140:143], a[92:95], a[244:247]\n\t"
    "buffer_load_dword v223, v221, %[srdp], %[sp] offen \n\t"
    "s_add_u32 %[sg], %[sg], 458752\n\t"
    "v_mfma_f32_16x16x32_bf16 a[212:215], a[136:139], a[92:95], a[212:215]\n\t"
    "s_add_u32 %[sp], %[sp], %[pstep]\n\t"
    "v_mfma_f32_16x16x32_bf16 a[216:219], a[136:139], a[96:99], a[216:219]\n\t"
    "v_mfma_f32_16x16x32_bf16 a[248:251], a[140:143], a[96:99], a[248:251]\n\t"
    "v_mfma_f32_16x16x32_bf16 a[252:255], a[140:143], a[100:103], a[252:255]\n\t"
    "buffer_load_dword v223, v222, %[srdq], %[sp2] offen\n\t"
    "v_mfma_f32_16x16x32_bf16 a[220:223], a[136:139], a[100:103], a[220:223]\n\t"
    "s_add_u32 %[sp2], %[sp2], %[p2step]\n\t"
    "s_waitcnt lgkmcnt(0)\n\t"
    "s_bitcmp1_b32 %[spec], %[cnt]\n\t"
    "s_cbranch_scc1 33f\n\t"
    "34:\n\t"
    "v_mfma_f32_16x16x32_bf16 v[0:3], a[32:35], a[0:3], v[0:3]\n\t"
    "ds_read_b64_tr_b16 a[104:105], v218 offset:4096\n\t"
    "v_mfma_f32_16x16x32_bf16 v[32:35], a[36:39], a[0:3], v[32:35]\n\t"
    "v_mfma_f32_16x16x32_bf16 v[36:39], a[36:39], a[4:7], v[36:39]\n\t"
    "ds_read_b64_tr_b16 a[106:107], v218 offset:4608\n\t"
    "v_mfma_f32_16x16x32_bf16 v[4:7], a[32:35], a[4:7], v[4:7]\n\t"
    "v_mfma_f32_16x16x32_bf16 v[8:11], a[32:35], a[8:11], v[8:11]\n\t"
    "ds_read_b64_tr_b16 a[108:109], v219 offset:4096\n\t"
    "v_mfma_f32_16x16x32_bf16 v[40:43], a[36:39], a[8:11], v[40:43]\n\t"
    "v_mfma_f32_16x16x32_bf16 v[44:47], a[36:39], a[12:15], v[44:47]\n\t"
    "ds_read_b64_tr_b16 a[110:111], v219 offset:4608\n\t"
    "v_mfma_f32_16x16x32_bf16 v[12:15], a[32:35], a[12:15], v[12:15]\n\t"
    "v_mfma_f32_16x16x32_bf16 v[16:19], a[32:35], a[16:19], v[16:19]\n\t"
    "ds_read_b128 a[72:75], v215 offset:32768\n\t"
    "v_mfma_f32_16x16x32_bf16 v[48:51], a[36:39], a[16:19], v[48:51]\n\t"
    "v_mfma_f32_16x16x32_bf16 v[52:55], a[36:39], a[20:23], v[52:55]\n\t"
    "v_mfma_f32_16x16x32_bf16 v[20:23], a[32:35], a[20:23], v[20:23]\n\t"
    "ds_read_b128 a[76:79], v215 offset:34816\n\t"
    "v_mfma_f32_16x16x32_bf16 v[24:27], a[32:35], a[24:27], v[24:27]\n\t"
    "v_mfma_f32_16x16x32_bf16 v[56:59], a[36:39], a[24:27], v[56:59]\n\t"
    "ds_read_b128 a[80:83], v215 offset:36864\n\t"
    "v_mfma_f32_16x16x32_bf16 v[60:63], a[36:39], a[28:31], v[60:63]\n\t"
    "v_mfma_f32_16x16x32_bf16 v[28:31], a[32:35], a[28:31], v[28:31]\n\t"
    "ds_read_b128 a[84:87], v215 offset:38912\n\t"
    "v_mfma_f32_16x16x32_bf16 v[92:95], a[40:43], a[28:31], v[92:95]\n\t"
    "v_mfma_f32_16x16x32_bf16 v[124:127], a[44:47], a[28:31], v[124:127]\n\t"
    "ds_read_b128 a[88:91], v215 offset:40960\n\t"
    "v_mfma_f32_16x16x32_bf16 v[120:123], a[44:47], a[24:27], v[120:123]\n\t"
    "v_mfma_f32_16x16x32_bf16 v[88:91], a[40:43], a[24:27], v[88:91]\n\t"
    "ds_read_b128 a[92:95], v215 offset:43008\n\t"
    "v_mfma_f32_16x16x32_bf16 v[84:87], a[40:43], a[20:23], v[84:87]\n\t"
    "v_mfma_f32_16x16x32_bf16 v[116:119], a[44:47], a[20:23], v[116:119]\n\t"
    "v_mfma_f32_16x16x32_bf16 v[112:115], a[44:47], a[16:19], v[112:115]\n\t"
    "ds_read_b128 a[96:99], v215 offset:45056\n\t"
    "v_mfma_f32_16x16x32_bf16 v[80:83], a[40:43], a[16:19], v[80:83]\n\t"
    "v_mfma_f32_16x16x32_bf16 v[76:79], a[40:43], a[12:15], v[76:79]\n\t"
    "ds_read_b128 a[100:103], v215 offset:47104\n\t"
    "v_mfma_f32_16x16x32_bf16 v[108:111], a[44:47], a[12:15], v[108:111]\n\t"
    "v_mfma_f32_16x16x32_bf16 v[104:107], a[44:47], a[8:11], v[104:107]\n\t"
    "ds_read_b64_tr_b16 a[112:113], v218 offset:12288\n\t"
    "v_mfma_f32_16x16x32_bf16 v[72:75], a[40:43], a[8:11], v[72:75]\n\t"
    "v_mfma_f32_16x16x32_bf16 v[68:71], a[40:43], a[4:7], v[68:71]\n\t"
    "ds_read_b64_tr_b16 a[114:115], v218 offset:12800\n\t"
    "v_mfma_f32_16x16x32_bf16 v[100:103], a[44:47], a[4:7], v[100:103]\n\t"
    "v_mfma_f32_16x16x32_bf16 v[96:99], a[44:47], a[0:3], v[96:99]\n\t"
    "v_mfma_f32_16x16x32_bf16 v[64:67], a[40:43], a[0:3], v[64:67]\n\t"
    "ds_read_b64_tr_b16 a[116:117], v219 offset:12288\n\t"
    "v_mfma_f32_16x16x32_bf16 v[128:131], a[48:51], a[0:3], v[128:131]\n\t"
    "v_mfma_f32_16x16x32_bf16 v[160:163], a[52:55], a[0:3], v[160:163]\n\t"
    "ds_read_b64_tr_b16 a[118:119], v219 offset:12800\n\t"
    "v_mfma_f32_16x16x32_bf16 v[164:167], a[52:55], a[4:7], v[164:167]\n\t"
    "v_mfma_f32_16x16x32_bf16 v[132:135], a[48:51], a[4:7], v[132:135]\n\t"
    "ds_read_b64_tr_b16 a[120:121], v218 offset:20480\n\t"
    "v_mfma_f32_16x16x32_bf16 v[136:139], a[48:51], a[8:11], v[136:139]\n\t"
    "v_mfma_f32_16x16x32_bf16 v[168:171], a[52:55], a[8:11], v[168:171]\n\t"
    "ds_read_b64_tr_b16 a[122:123], v218 offset:20992\n\t"
    "v_mfma_f32_16x16x32_bf16 v[172:175], a[52:55], a[12:15], v[172:175]\n\t"
    "v_mfma_f32_16x16x32_bf16 v[140:143], a[48:51], a[12:15], v[140:143]\n\t"
    "ds_read_b64_tr_b16 a[124:125], v219 offset:20480\n\t"
    "v_mfma_f32_16x16x32_bf16 v[144:147], a[48:51], a[16:19], v[144:147]\n\t"
    "v_mfma_f32_16x16x32_bf16 v[176:179], a[52:55], a[16:19], v[176:179]\n\t"
    "v_mfma_f32_16x16x32_bf16 v[180:183], a[52:55], a[20:23], v[180:183]\n\t"
    "ds_read_b64_tr_b16 a[126:127], v219 offset:20992\n\t"
    "v_mfma_f32_16x16x32_bf16 v[148:151], a[48:51], a[20:23], v[148:151]\n\t"
    "v_mfma_f32_16x16x32_bf16 v[152:155], a[48:51], a[24:27], v[152:155]\n\t"
    "ds_read_b64_tr_b16 a[128:129], v218 offset:28672\n\t"
    "v_mfma_f32_16x16x32_bf16 v[184:187], a[52:55], a[24:27], v[184:187]\n\t"
    "v_mfma_f32_16x16x32_bf16 v[188:191], a[52:55], a[28:31], v[188:191]\n\t"
    "ds_read_b64_tr_b16 a[130:131], v218 offset:29184\n\t"
    "v_mfma_f32_16x16x32_bf16 v[156:159], a[48:51], a[28:31], v[156:159]\n\t"
    "v_mfma_f32_16x16x32_bf16 a[156:159], a[56:59], a[28:31], a[156:159]\n\t"
    "ds_read_b64_tr_b16 a[132:133], v219 offset:28672\n\t"
    "v_mfma_f32_16x16x32_bf16 a[188:191], a[60:63], a[28:31], a[188:191]\n\t"
    "v_mfma_f32_16x16x32_bf16 a[184:187], a[60:63], a[24:27], a[184:187]\n\t"
    "ds_read_b64_tr_b16 a[134:135], v219 offset:29184\n\t"
    "v_mfma_f32_16x16x32_bf16 a[152:155], a[56:59], a[24:27], a[152:155]\n\t"
    "v_mfma_f32_16x16x32_bf16 a[148:151], a[56:59], a[20:23], a[148:151]\n\t"
    "v_mfma_f32_16x16x32_bf16 a[180:183], a[60:63], a[20:23], a[180:183]\n\t"
    "ds_read_b64_tr_b16 a[136:137], v220 offset:43008\n\t"
    "v_mfma_f32_16x16x32_bf16 a[176:179], a[60:63], a[16:19], a[176:179]\n\t"
    "v_mfma_f32_16x16x32_bf16 a[144:147], a[56:59], a[16:19], a[144:147]\n\t"
    "ds_read_b64_tr_b16 a[138:139], v220 offset:43264\n\t"
    "v_mfma_f32_16x16x32_bf16 v[204:207], a[56:59], a[12:15], v[204:207]\n\t"
    "v_mfma_f32_16x16x32_bf16 a[172:175], a[60:63], a[12:15], a[172:175]\n\t"
    "ds_read_b64_tr_b16 a[140:141], v220 offset:47104\n\t"
    "v_mfma_f32_16x16x32_bf16 a[168:171], a[60:63], a[8:11], a[168:171]\n\t"
    "v_mfma_f32_16x16x32_bf16 v[200:203], a[56:59], a[8:11], v[200:203]\n\t"
    "ds_read_b64_tr_b16 a[142:143], v220 offset:47360\n\t"
    "v_mfma_f32_16x16x32_bf16 v[196:199], a[56:59], a[4:7], v[196:199]\n\t"
    "v_mfma_f32_16x16x32_bf16 a[164:167], a[60:63], a[4:7], a[164:167]\n\t"
    "v_mfma_f32_16x16x32_bf16 a[160:163], a[60:63], a[0:3], a[160:163]\n\t"
    "v_mfma_f32_16x16x32_bf16 v[192:195], a[56:59], a[0:3], v[192:195]\n\t"
    "v_mfma_f32_16x16x32_bf16 a[192:195], a[64:67], a[0:3], a[192:195]\n\t"
    "v_mfma_f32_16x16x32_bf16 a[224:227], a[68:71], a[0:3], a[224:227]\n\t"
    "v_mfma_f32_16x16x32_bf16 a[228:231], a[68:71], a[4:7], a[228:231]\n\t"
    "s_cbranch_vccnz 120f\n\t"
    "22:\n\t"
    "v_mfma_f32_16x16x32_bf16 a[196:199], a[64:67], a[4:7], a[196:199]\n\t"
    "v_mfma_f32_16x16x32_bf16 a[200:203], a[64:67], a[8:11], a[200:203]\n\t"
    "v_mfma_f32_16x16x32_bf16 a[232:235], a[68:71], a[8:11], a[232:235]\n\t"
    "v_mfma_f32_16x16x32_bf16 a[236:239], a[68:71], a[12:15], a[236:239]\n\t"
    "v_mfma_f32_16x16x32_bf16 a[204:207], a[64:67], a[12:15], a[204:207]\n\t"
    "v_mfma_f32_16x16x32_bf16 a[208:211], a[64:67], a[16:19], a[208:211]\n\t"
    "v_mfma_f32_16x16x32_bf16 a[240:243], a[68:71], a[16:19], a[240:243]\n\t"
    "v_mfma_f32_16x16x32_bf16 a[244:247], a[68:71], a[20:23], a[244:247]\n\t"
    "v_mfma_f32_16x16x32_bf16 a[212:215], a[64:67], a[20:23], a[212:215]\n\t"
    "v_mfma_f32_16x16x32_bf16 a[216:219], a[64:67], a[24:27], a[216:219]\n\t"
    "v_mfma_f32_16x16x32_bf16 a[248:251], a[68:71], a[24:27], a[248:251]\n\t"
    "v_mfma_f32_16x16x32_bf16 a[252:255], a[68:71], a[28:31], a[252:255]\n\t"
    "v_mfma_f32_16x16x32_bf16 a[220:223], a[64:67], a[28:31], a[220:223]\n\t"
    "s_waitcnt vmcnt(2) lgkmcnt(0)\n\t"
    "s_barrier\n\t"
    "v_mfma_f32_16x16x32_bf16 v[0:3], a[104:107], a[72:75], v[0:3]\n\t"
    "v_mfma_f32_16x16x32_bf16 v[32:35], a[108:111], a[72:75], v[32:35]\n\t"
    "s_add_u32 m0, %[m0a], 32768\n\t"
    "ds_read_b64_tr_b16 a[32:33], v216 offset:0\n\t"
    "v_mfma_f32_16x16x32_bf16 v[36:39], a[108:111], a[76:79], v[36:39]\n\t"
    "buffer_load_dwordx4 v208, %[srda], %[sa] offen offset:0 lds\n\t"
    "v_mfma_f32_16x16x32_bf16 v[4:7], a[104:107], a[76:79], v[4:7]\n\t"
    "ds_read_b64_tr_b16 a[34:35], v216 offset:512\n\t"
    "v_mfma_f32_16x16x32_bf16 v[8:11], a[104:107], a[80:83], v[8:11]\n\t"
    "v_mfma_f32_16x16x32_bf16 v[40:43], a[108:111], a[80:83], v[40:43]\n\t"
    "ds_read_b64_tr_b16 a[36:37], v217 offset:0\n\t"
    "v_mfma_f32_16x16x32_bf16 v[44:47], a[108:111], a[84:87], v[44:47]\n\t"
    "buffer_load_dwordx4 v209, %[srda], %[sa] offen offset:1024 lds\n\t"
    "v_mfma_f32_16x16x32_bf16 v[12:15], a[104:107], a[84:87], v[12:15]\n\t"
    "ds_read_b64_tr_b16 a[38:39], v217 offset:512\n\t"
    "v_mfma_f32_16x16x32_bf16 v[16:19], a[104:107], a[88:91], v[16:19]\n\t"
    "v_mfma_f32_16x16x32_bf16 v[48:51], a[108:111], a[88:91], v[48:51]\n\t"
    "s_add_u32 %[tmp], %[sa], 30720\n\t"
    "ds_read_b128 a[0:3], v214 offset:0\n\t"
    "v_mfma_f32_16x16x32_bf16 v[52:55], a[108:111], a[92:95], v[52:55]\n\t"
    "buffer_load_dwordx4 v208, %[srda], %[tmp] offen offset:2048 lds\n\t"
    "v_mfma_f32_16x16x32_bf16 v[20:23], a[104:107], a[92:95], v[20:23]\n\t"
    "v_mfma_f32_16x16x32_bf16 v[24:27], a[104:107], a[96:99], v[24:27]\n\t"
    "ds_read_b128 a[4:7], v214 offset:2048\n\t"
    "v_mfma_f32_16x16x32_bf16 v[56:59], a[108:111], a[96:99], v[56:59]\n\t"
    "ds_read_b128 a[8:11], v214 offset:4096\n\t"
    "v_mfma_f32_16x16x32_bf16 v[60:63], a[108:111], a[100:103], v[60:63]\n\t"
    "buffer_load_dwordx4 v209, %[srda], %[tmp] offen offset:3072 lds\n\t"
    "v_mfma_f32_16x16x32_bf16 v[28:31], a[104:107], a[100:103], v[28:31]\n\t"
    "v_mfma_f32_16x16x32_bf16 v[92:95], a[112:115], a[100:103], v[92:95]\n\t"
    "s_add_u32 m0, %[m0a], 36864\n\t"
    "ds_read_b128 a[12:15], v214 offset:6144\n\t"
    "v_mfma_f32_16x16x32_bf16 v[124:127], a[116:119], a[100:103], v[124:127]\n\t"
    "s_add_u32 %[tmp], %[sa], 65536\n\t"
    "v_mfma_f32_16x16x32_bf16 v[120:123], a[116:119], a[96:99], v[120:123]\n\t"
    "buffer_load_dwordx4 v208, %[srda], %[tmp] offen offset:0 lds\n\t"
    "v_mfma_f32_16x16x32_bf16 v[88:91], a[112:115], a[96:99], v[88:91]\n\t"
    "ds_read_b128 a[16:19], v214 offset:8192\n\t"
    "v_mfma_f32_16x16x32_bf16 v[84:87], a[112:115], a[92:95], v[84:87]\n\t"
    "ds_read_b128 a[20:23], v214 offset:10240\n\t"
    "v_mfma_f32_16x16x32_bf16 v[116:119], a[116:119], a[92:95], v[116:119]\n\t"
    "v_mfma_f32_16x16x32_bf16 v[112:115], a[116:119], a[88:91], v[112:115]\n\t"
    "buffer_load_dwordx4 v209, %[srda], %[tmp] offen offset:1024 lds\n\t"
    "v_mfma_f32_16x16x32_bf16 v[80:83], a[112:115], a[88:91], v[80:83]\n\t"
    "ds_read_b128 a[24:27], v214 offset:12288\n\t"
    "v_mfma_f32_16x16x32_bf16 v[76:79], a[112:115], a[84:87], v[76:79]\n\t"
    "v_mfma_f32_16x16x32_bf16 v[108:111], a[116:119], a[84:87], v[108:111]\n\t"
    "s_add_u32 %[tmp], %[sa], 96256\n\t"
    "ds_read_b128 a[28:31], v214 offset:14336\n\t"
    "v_mfma_f32_16x16x32_bf16 v[104:107], a[116:119], a[80:83], v[104:107]\n\t"
    "buffer_load_dwordx4 v208, %[srda], %[tmp] offen offset:2048 lds\n\t"
    "v_mfma_f32_16x16x32_bf16 v[72:75], a[112:115], a[80:83], v[72:75]\n\t"
    "ds_read_b64_tr_b16 a[40:41], v216 offset:8192\n\t"
    "v_mfma_f32_16x16x32_bf16 v[68:71], a[112:115], a[76:79], v[68:71]\n\t"
    "v_mfma_f32_16x16x32_bf16 v[100:103], a[116:119], a[76:79], v[100:103]\n\t"
    "ds_read_b64_tr_b16 a[42:43], v216 offset:8704\n\t"
    "v_mfma_f32_16x16x32_bf16 v[96:99], a[116:119], a[72:75], v[96:99]\n\t"
    "buffer_load_dwordx4 v209, %[srda], %[tmp] offen offset:3072 lds\n\t"
    "v_mfma_f32_16x16x32_bf16 v[64:67], a[112:115], a[72:75], v[64:67]\n\t"
    "v_mfma_f32_16x16x32_bf16 v[128:131], a[120:123], a[72:75], v[128:131]\n\t"
    "ds_read_b64_tr_b16 a[44:45], v217 offset:8192\n\t"
    "v_mfma_f32_16x16x32_bf16 v[160:163], a[124:127], a[72:75], v[160:163]\n\t"
    "s_add_u32 m0, %[m0b], 106496\n\t"
    "ds_read_b64_tr_b16 a[46:47], v217 offset:8704\n\t"
    "v_mfma_f32_16x16x32_bf16 v[164:167], a[124:127], a[76:79], v[164:167]\n\t"
    "buffer_load_dwordx4 v210, %[srd1], %[sg] offen offset:0 lds\n\t"
    "v_mfma_f32_16x16x32_bf16 v[132:135], a[120:123], a[76:79], v[132:135]\n\t"
    "v_mfma_f32_16x16x32_bf16 v[136:139], a[120:123], a[80:83], v[136:139]\n\t"
    "ds_read_b64_tr_b16 a[48:49], v216 offset:16384\n\t"
    "v_mfma_f32_16x16x32_bf16 v[168:171], a[124:127], a[80:83], v[168:171]\n\t"
    "ds_read_b64_tr_b16 a[50:51], v216 offset:16896\n\t"
    "v_mfma_f32_16x16x32_bf16 v[172:175], a[124:127], a[84:87], v[172:175]\n\t"
    "buffer_load_dwordx4 v211, %[srd1], %[sg] offen offset:1024 lds\n\t"
    "v_mfma_f32_16x16x32_bf16 v[140:143], a[120:123], a[84:87], v[140:143]\n\t"
    "v_mfma_f32_16x16x32_bf16 v[144:147], a[120:123], a[88:91], v[144:147]\n\t"
    "s_add_u32 m0, %[m0b], 114688\n\t"
    "ds_read_b64_tr_b16 a[52:53], v217 offset:16384\n\t"
    "v_mfma_f32_16x16x32_bf16 v[176:179], a[124:127], a[88:91], v[176:179]\n\t"
    "s_add_u32 %[tmp], %[sg], 128\n\t"
    "v_mfma_f32_16x16x32_bf16 v[180:183], a[124:127], a[92:95], v[180:183]\n\t"
    "buffer_load_dwordx4 v210, %[srd1], %[tmp] offen offset:0 lds\n\t"
    "v_mfma_f32_16x16x32_bf16 v[148:151], a[120:123], a[92:95], v[148:151]\n\t"
    "ds_read_b64_tr_b16 a[54:55], v217 offset:16896\n\t"
    "v_mfma_f32_16x16x32_bf16 v[152:155], a[120:123], a[96:99], v[152:155]\n\t"
    "ds_read_b64_tr_b16 a[56:57], v216 offset:24576\n\t"
    "v_mfma_f32_16x16x32_bf16 v[184:187], a[124:127], a[96:99], v[184:187]\n\t"
    "v_mfma_f32_16x16x32_bf16 v[188:191], a[124:127], a[100:103], v[188:191]\n\t"
    "buffer_load_dwordx4 v211, %[srd1], %[tmp] offen offset:1024 lds\n\t"
    "v_mfma_f32_16x16x32_bf16 v[156:159], a[120:123], a[100:103], v[156:159]\n\t"
    "ds_read_b64_tr_b16 a[58:59], v216 offset:25088\n\t"
    "v_mfma_f32_16x16x32_bf16 a[156:159], a[128:131], a[100:103], a[156:159]\n\t"
    "v_mfma_f32_16x16x32_bf16 a[188:191], a[132:135], a[100:103], a[188:191]\n\t"
    "s_add_u32 m0, %[m0b], 122880\n\t"
    "ds_read_b64_tr_b16 a[60:61], v217 offset:24576\n\t"
    "v_mfma_f32_16x16x32_bf16 a[184:187], a[132:135], a[96:99], a[184:187]\n\t"
    "buffer_load_dwordx4 v210, %[srd2], %[sg] offen offset:0 lds\n\t"
    "v_mfma_f32_16x16x32_bf16 a[152:155], a[128:131], a[96:99], a[152:155]\n\t"
    "ds_read_b64_tr_b16 a[62:63], v217 offset:25088\n\t"
    "v_mfma_f32_16x16x32_bf16 a[148:151], a[128:131], a[92:95], a[148:151]\n\t"
    "v_mfma_f32_16x16x32_bf16 a[180:183], a[132:135], a[92:95], a[180:183]\n\t"
    "ds_read_b64_tr_b16 a[64:65], v220 offset:0\n\t"
    "v_mfma_f32_16x16x32_bf16 a[176:179], a[132:135], a[88:91], a[176:179]\n\t"
    "buffer_load_dwordx4 v211, %[srd2], %[sg] offen offset:1024 lds\n\t"
    "v_mfma_f32_16x16x32_bf16 a[144:147], a[128:131], a[88:91], a[144:147]\n\t"
    "v_mfma_f32_16x16x32_bf16 v[204:207], a[128:131], a[84:87], v[204:207]\n\t"
    "ds_read_b64_tr_b16 a[66:67], v220 offset:256\n\t"
    "v_mfma_f32_16x16x32_bf16 a[172:175], a[132:135], a[84:87], a[172:175]\n\t"
    "s_add_u32 m0, %[m0b], 131072\n\t"
    "ds_read_b64_tr_b16 a[68:69], v220 offset:4096\n\t"
    "v_mfma_f32_16x16x32_bf16 a[168:171], a[132:135], a[80:83], a[168:171]\n\t"
    "buffer_load_dwordx4 v210, %[srd2], %[tmp] offen offset:0 lds\n\t"
    "v_mfma_f32_16x16x32_bf16 v[200:203], a[128:131], a[80:83], v[200:203]\n\t"
    "v_mfma_f32_16x16x32_bf16 v[196:199], a[128:131], a[76:79], v[196:199]\n\t"
    "ds_read_b64_tr_b16 a[70:71], v220 offset:4352\n\t"
    "v_mfma_f32_16x16x32_bf16 a[164:167], a[132:135], a[76:79], a[164:167]\n\t"
    "v_mfma_f32_16x16x32_bf16 a[160:163], a[132:135], a[72:75], a[160:163]\n\t"
    "buffer_load_dwordx4 v211, %[srd2], %[tmp] offen offset:1024 lds\n\t"
    "v_mfma_f32_16x16x32_bf16 v[192:195], a[128:131], a[72:75], v[192:195]\n\t"
    "v_mfma_f32_16x16x32_bf16 a[192:195], a[136:139], a[72:75], a[192:195]\n\t"
    "v_mfma_f32_16x16x32_bf16 a[224:227], a[140:143], a[72:75], a[224:227]\n\t"
    "s_add_u32 m0, %[m0c], 139264\n\t"
    "s_cbranch_vccnz 130f\n\t"
    "23:\n\t"
    "v_mfma_f32_16x16x32_bf16 a[228:231], a[140:143], a[76:79], a[228:231]\n\t"
    "buffer_load_dwordx4 v212, %[srd1], %[sg] offen lds\n\t"
    "v_mfma_f32_16x16x32_bf16 a[196:199], a[136:139], a[76:79], a[196:199]\n\t"
    "v_mfma_f32_16x16x32_bf16 a[200:203], a[136:139], a[80:83], a[200:203]\n\t"
    "v_mfma_f32_16x16x32_bf16 a[232:235], a[140:143], a[80:83], a[232:235]\n\t"
    "s_add_u32 m0, %[m0c], 143360\n\t"
    "v_mfma_f32_16x16x32_bf16 a[236:239], a[140:143], a[84:87], a[236:239]\n\t"
    "buffer_load_dwordx4 v212, %[srd2], %[sg] offen lds\n\t"
    "v_mfma_f32_16x16x32_bf16 a[204:207], a[136:139], a[84:87], a[204:207]\n\t"
    "v_mfma_f32_16x16x32_bf16 a[208:211], a[136:139], a[88:91], a[208:211]\n\t"
    "v_mfma_f32_16x16x32_bf16 a[240:243], a[140:143], a[88:91], a[240:243]\n\t"
    "s_and_b32 %[tmp], %[ord], 15\n\t"
    "v_mfma_f32_16x16x32_bf16 a[244:247], a[140:143], a[92:95], a[244:247]\n\t"
    "buffer_load_dword v223, v221, %[srdp], %[sp] offen \n\t"
    "s_lshr_b32 %[ord], %[ord], 4\n\t"
    "v_mfma_f32_16x16x32_bf16 a[212:215], a[136:139], a[92:95], a[212:215]\n\t"
    "s_add_u32 %[sp], %[sp], %[pstep]\n\t"
    "s_lshl_b32 %[sa], %[tmp], 8\n\t"
    "v_mfma_f32_16x16x32_bf16 a[216:219], a[136:139], a[96:99], a[216:219]\n\t"
    "s_mul_i32 %[sg], %[tmp], 917504\n\t"
    "v_mfma_f32_16x16x32_bf16 a[248:251], a[140:143], a[96:99], a[248:251]\n\t"
    "v_mfma_f32_16x16x32_bf16 a[252:255], a[140:143], a[100:103], a[252:255]\n\t"
    "buffer_load_dword v223, v222, %[srdq], %[sp2] offen\n\t"
    "v_mfma_f32_16x16x32_bf16 a[220:223], a[136:139], a[100:103], a[220:223]\n\t"
    "s_add_u32 %[sp2], %[sp2], %[p2step]\n\t"
    "s_waitcnt lgkmcnt(0)\n\t"
    "s_sub_u32 %[cnt], %[cnt], 1\n\t"
    "s_cmp_lg_u32 %[cnt], 0\n\t"
    "s_cbranch_scc1 1b\n\t"
    "s_waitcnt vmcnt(20)\n\t"
    "v_cmp_ne_u64 vcc, s[90:91], v[250:251]\n\t"
    "s_cbranch_vccnz 36f\n\t"
    "38:\n\t"
    "v_lshrrev_b32 v249, 1, v248\n\t"
    "v_add_u32 v252, %[stld], v249\n\t"
    "v_add_u32 v250, %[csld], v249\n\t"
    "v_mov_b32 v251, 0x40000000\n\t"
    "v_cmp_gt_u32 vcc, %[csn], v249\n\t"
    "s_nop 1\n\t"
    "v_cndmask_b32 v250, v251, v250, vcc\n\t"
    "v_add_u32 v251, 5120, v250\n\t"
    "v_mfma_f32_16x16x32_bf16 v[0:3], a[32:35], a[0:3], v[0:3]\n\t"
    "ds_read_b64_tr_b16 a[104:105], v216 offset:4096\n\t"
    "v_mfma_f32_16x16x32_bf16 v[32:35], a[36:39], a[0:3], v[32:35]\n\t"
    "ds_read_b64_tr_b16 a[106:107], v216 offset:4608\n\t"
    "buffer_load_dwordx2 v[224:225], v252, %[srdw], 0 offen sc1\n\t"
    "v_mfma_f32_16x16x32_bf16 v[36:39], a[36:39], a[4:7], v[36:39]\n\t"
    "ds_read_b64_tr_b16 a[108:109], v217 offset:4096\n\t"
    "v_mfma_f32_16x16x32_bf16 v[4:7], a[32:35], a[4:7], v[4:7]\n\t"
    "ds_read_b64_tr_b16 a[110:111], v217 offset:4608\n\t"
    "v_mfma_f32_16x16x32_bf16 v[8:11], a[32:35], a[8:11], v[8:11]\n\t"
    "buffer_load_dwordx2 v[226:227], v250, %[srdw], 0 offen offset:0 sc1\n\t"
    "v_mfma_f32_16x16x32_bf16 v[40:43], a[36:39], a[8:11], v[40:43]\n\t"
    "ds_read_b128 a[72:75], v215 offset:0\n\t"
    "v_mfma_f32_16x16x32_bf16 v[44:47], a[36:39], a[12:15], v[44:47]\n\t"
    "ds_read_b128 a[76:79], v215 offset:2048\n\t"
    "v_mfma_f32_16x16x32_bf16 v[12:15], a[32:35], a[12:15], v[12:15]\n\t"
    "ds_read_b128 a[80:83], v215 offset:4096\n\t"
    "buffer_load_dwordx2 v[228:229], v250, %[srdw], 0 offen offset:1280 sc1\n\t"
    "v_mfma_f32_16x16x32_bf16 v[16:19], a[32:35], a[16:19], v[16:19]\n\t"
    "v_mfma_f32_16x16x32_bf16 v[48:51], a[36:39], a[16:19], v[48:51]\n\t"
    "ds_read_b128 a[84:87], v215 offset:6144\n\t"
    "v_mfma_f32_16x16x32_bf16 v[52:55], a[36:39], a[20:23], v[52:55]\n\t"
    "ds_read_b128 a[88:91], v215 offset:8192\n\t"
    "buffer_load_dwordx2 v[230:231], v250, %[srdw], 0 offen offset:2560 sc1\n\t"
    "v_mfma_f32_16x16x32_bf16 v[20:23], a[32:35], a[20:23], v[20:23]\n\t"
    "ds_read_b128 a[92:95], v215 offset:10240\n\t"
    "v_mfma_f32_16x16x32_bf16 v[24:27], a[32:35], a[24:27], v[24:27]\n\t"
    "v_mfma_f32_16x16x32_bf16 v[56:59], a[36:39], a[24:27], v[56:59]\n\t"
    "ds_read_b128 a[96:99], v215 offset:12288\n\t"
    "buffer_load_dwordx2 v[232:233], v250, %[srdw], 0 offen offset:3840 sc1\n\t"
    "v_mfma_f32_16x16x32_bf16 v[60:63], a[36:39], a[28:31], v[60:63]\n\t"
    "ds_read_b128 a[100:103], v215 offset:14336\n\t"
    "v_mfma_f32_16x16x32_bf16 v[28:31], a[32:35], a[28:31], v[28:31]\n\t"
    "ds_read_b64_tr_b16 a[120:121], v216 offset:20480\n\t"
    "v_mfma_f32_16x16x32_bf16 v[156:159], a[48:51], a[28:31], v[156:159]\n\t"
    "buffer_load_dwordx2 v[234:235], v251, %[srdw], 0 offen offset:0 sc1\n\t"
    "v_mfma_f32_16x16x32_bf16 v[188:191], a[52:55], a[28:31], v[188:191]\n\t"
    "ds_read_b64_tr_b16 a[122:123], v216 offset:20992\n\t"
    "v_mfma_f32_16x16x32_bf16 v[184:187], a[52:55], a[24:27], v[184:187]\n\t"
    "ds_read_b64_tr_b16 a[124:125], v217 offset:20480\n\t"
    "v_mfma_f32_16x16x32_bf16 v[152:155], a[48:51], a[24:27], v[152:155]\n\t"
    "ds_read_b64_tr_b16 a[126:127], v217 offset:20992\n\t"
    "buffer_load_dwordx2 v[236:237], v251, %[srdw], 0 offen offset:1280 sc1\n\t"
    "v_mfma_f32_16x16x32_bf16 v[148:151], a[48:51], a[20:23], v[148:151]\n\t"
    "v_mfma_f32_16x16x32_bf16 v[180:183], a[52:55], a[20:23], v[180:183]\n\t"
    "v_mfma_f32_16x16x32_bf16 v[176:179], a[52:55], a[16:19], v[176:179]\n\t"
    "buffer_load_dwordx2 v[238:239], v251, %[srdw], 0 offen offset:2560 sc1\n\t"
    "v_mfma_f32_16x16x32_bf16 v[144:147], a[48:51], a[16:19], v[144:147]\n\t"
    "v_mfma_f32_16x16x32_bf16 v[140:143], a[48:51], a[12:15], v[140:143]\n\t"
    "v_mfma_f32_16x16x32_bf16 v[172:175], a[52:55], a[12:15], v[172:175]\n\t"
    "buffer_load_dwordx2 v[240:241], v251, %[srdw], 0 offen offset:3840 sc1\n\t"
    "v_mfma_f32_16x16x32_bf16 v[168:171], a[52:55], a[8:11], v[168:171]\n\t"
    "v_mfma_f32_16x16x32_bf16 v[136:139], a[48:51], a[8:11], v[136:139]\n\t"
    "v_mfma_f32_16x16x32_bf16 v[132:135], a[48:51], a[4:7], v[132:135]\n\t"
    "v_mfma_f32_16x16x32_bf16 v[164:167], a[52:55], a[4:7], v[164:167]\n\t"
    "v_mfma_f32_16x16x32_bf16 v[160:163], a[52:55], a[0:3], v[160:163]\n\t"
    "v_mfma_f32_16x16x32_bf16 v[128:131], a[48:51], a[0:3], v[128:131]\n\t"
    "s_waitcnt vmcnt(11) lgkmcnt(0)\n\t"
    "s_barrier\n\t"
    "v_mfma_f32_16x16x32_bf16 v[0:3], a[104:107], a[72:75], v[0:3]\n\t"
    "ds_read_b64_tr_b16 a[32:33], v218 offset:0\n\t"
    "v_mfma_f32_16x16x32_bf16 v[32:35], a[108:111], a[72:75], v[32:35]\n\t"
    "ds_read_b64_tr_b16 a[34:35], v218 offset:512\n\t"
    "v_and_b32 v208, 0xf0, v248\n\t"
    "v_mfma_f32_16x16x32_bf16 v[36:39], a[108:111], a[76:79], v[36:39]\n\t"
    "ds_read_b64_tr_b16 a[36:37], v219 offset:0\n\t"
    "v_mfma_f32_16x16x32_bf16 v[4:7], a[104:107], a[76:79], v[4:7]\n\t"
    "ds_read_b64_tr_b16 a[38:39], v219 offset:512\n\t"
    "v_mfma_f32_16x16x32_bf16 v[8:11], a[104:107], a[80:83], v[8:11]\n\t"
    "v_mfma_f32_16x16x32_bf16 v[40:43], a[108:111], a[80:83], v[40:43]\n\t"
    "ds_read_b128 a[0:3], v214 offset:32768\n\t"
    "v_add_u32 v208, %[lrowr], v208\n\t"
    "v_mfma_f32_16x16x32_bf16 v[44:47], a[108:111], a[84:87], v[44:47]\n\t"
    "ds_read_b128 a[4:7], v214 offset:34816\n\t"
    "v_mfma_f32_16x16x32_bf16 v[12:15], a[104:107], a[84:87], v[12:15]\n\t"
    "ds_read_b128 a[8:11], v214 offset:36864\n\t"
    "v_mfma_f32_16x16x32_bf16 v[16:19], a[104:107], a[88:91], v[16:19]\n\t"
    "v_mfma_f32_16x16x32_bf16 v[48:51], a[108:111], a[88:91], v[48:51]\n\t"
    "ds_read_b128 a[12:15], v214 offset:38912\n\t"
    "v_lshrrev_b32 v209, 4, v248\n\t"
    "v_mfma_f32_16x16x32_bf16 v[52:55], a[108:111], a[92:95], v[52:55]\n\t"
    "ds_read_b128 a[16:19], v214 offset:40960\n\t"
    "v_mfma_f32_16x16x32_bf16 v[20:23], a[104:107], a[92:95], v[20:23]\n\t"
    "ds_read_b128 a[20:23], v214 offset:43008\n\t"
    "v_mfma_f32_16x16x32_bf16 v[24:27], a[104:107], a[96:99], v[24:27]\n\t"
    "v_mfma_f32_16x16x32_bf16 v[56:59], a[108:111], a[96:99], v[56:59]\n\t"
    "ds_read_b128 a[24:27], v214 offset:45056\n\t"
    "v_and_b32 v209, 0x30, v209\n\t"
    "v_mfma_f32_16x16x32_bf16 v[60:63], a[108:111], a[100:103], v[60:63]\n\t"
    "ds_read_b128 a[28:31], v214 offset:47104\n\t"
    "v_mfma_f32_16x16x32_bf16 v[28:31], a[104:107], a[100:103], v[28:31]\n\t"
    "ds_read_b64_tr_b16 a[48:49], v218 offset:16384\n\t"
    "v_mfma_f32_16x16x32_bf16 v[156:159], a[120:123], a[100:103], v[156:159]\n\t"
    "v_mfma_f32_16x16x32_bf16 v[188:191], a[124:127], a[100:103], v[188:191]\n\t"
    "ds_read_b64_tr_b16 a[50:51], v218 offset:16896\n\t"
    "v_add_u32 v209, %[lcsr], v209\n\t"
    "v_mfma_f32_16x16x32_bf16 v[184:187], a[124:127], a[96:99], v[184:187]\n\t"
    "ds_read_b64_tr_b16 a[52:53], v219 offset:16384\n\t"
    "v_mfma_f32_16x16x32_bf16 v[152:155], a[120:123], a[96:99], v[152:155]\n\t"
    "ds_read_b64_tr_b16 a[54:55], v219 offset:16896\n\t"
    "v_mfma_f32_16x16x32_bf16 v[148:151], a[120:123], a[92:95], v[148:151]\n\t"
    "v_mfma_f32_16x16x32_bf16 v[180:183], a[124:127], a[92:95], v[180:183]\n\t"
    "v_add_u32 v210, %[aost], v248\n\t"
    "v_mfma_f32_16x16x32_bf16 v[176:179], a[124:127], a[88:91], v[176:179]\n\t"
    "v_mfma_f32_16x16x32_bf16 v[144:147], a[120:123], a[88:91], v[144:147]\n\t"
    "v_mfma_f32_16x16x32_bf16 v[140:143], a[120:123], a[84:87], v[140:143]\n\t"
    "v_mfma_f32_16x16x32_bf16 v[172:175], a[124:127], a[84:87], v[172:175]\n\t"
    "v_mfma_f32_16x16x32_bf16 v[168:171], a[124:127], a[80:83], v[168:171]\n\t"
    "v_mfma_f32_16x16x32_bf16 v[136:139], a[120:123], a[80:83], v[136:139]\n\t"
    "v_mfma_f32_16x16x32_bf16 v[132:135], a[120:123], a[76:79], v[132:135]\n\t"
    "v_mfma_f32_16x16x32_bf16 v[164:167], a[124:127], a[76:79], v[164:167]\n\t"
    "v_mfma_f32_16x16x32_bf16 v[160:163], a[124:127], a[72:75], v[160:163]\n\t"
    "v_mfma_f32_16x16x32_bf16 v[128:131], a[120:123], a[72:75], v[128:131]\n\t"
    "s_waitcnt vmcnt(0) lgkmcnt(0)\n\t"
    "v_mfma_f32_16x16x32_bf16 v[0:3], a[32:35], a[0:3], v[0:3]\n\t"
    "ds_read_b64_tr_b16 a[104:105], v218 offset:4096\n\t"
    "v_cmp_ne_u32 vcc, s90, v225\n\t"
    "s_cbranch_vccnz 41f\n\t"
    "42:\n\t"
    "v_mfma_f32_16x16x32_bf16 v[32:35], a[36:39], a[0:3], v[32:35]\n\t"
    "ds_read_b64_tr_b16 a[106:107], v218 offset:4608\n\t"
    "v_lshlrev_b32 v252, 16, v224\n\t"
    "v_mfma_f32_16x16x32_bf16 v[36:39], a[36:39], a[4:7], v[36:39]\n\t"
    "ds_read_b64_tr_b16 a[108:109], v219 offset:4096\n\t"
    "v_and_b32 v254, 0xffff0000, v224\n\t"
    "v_mfma_f32_16x16x32_bf16 v[4:7], a[32:35], a[4:7], v[4:7]\n\t"
    "ds_read_b64_tr_b16 a[110:111], v219 offset:4608\n\t"
    "v_rcp_f32 v253, v254\n\t"
    "v_mfma_f32_16x16x32_bf16 v[8:11], a[32:35], a[8:11], v[8:11]\n\t"
    "v_xor_b32 v252, 0x80000000, v252\n\t"
    "v_mfma_f32_16x16x32_bf16 v[40:43], a[36:39], a[8:11], v[40:43]\n\t"
    "ds_read_b128 a[72:75], v215 offset:32768\n\t"
    "v_mul_f32 v254, v254, v254\n\t"
    "v_mfma_f32_16x16x32_bf16 v[44:47], a[36:39], a[12:15], v[44:47]\n\t"
    "ds_read_b128 a[76:79], v215 offset:34816\n\t"
    "v_mul_f32 v255, 0xbfb8aa3b, v253\n\t"
    "v_mfma_f32_16x16x32_bf16 v[12:15], a[32:35], a[12:15], v[12:15]\n\t"
    "ds_read_b128 a[80:83], v215 offset:36864\n\t"
    "v_add_u32 v249, %[lrow], v248\n\t"
    "v_mfma_f32_16x16x32_bf16 v[16:19], a[32:35], a[16:19], v[16:19]\n\t"
    "v_lshrrev_b32 v250, 2, v248\n\t"
    "v_mfma_f32_16x16x32_bf16 v[48:51], a[36:39], a[16:19], v[48:51]\n\t"
    "ds_read_b128 a[84:87], v215 offset:38912\n\t"
    "v_add_u32 v250, %[lnm], v250\n\t"
    "v_mfma_f32_16x16x32_bf16 v[52:55], a[36:39], a[20:23], v[52:55]\n\t"
    "ds_read_b128 a[88:91], v215 offset:40960\n\t"
    "ds_write_b128 v249, v[252:255]\n\t"
    "v_mfma_f32_16x16x32_bf16 v[20:23], a[32:35], a[20:23], v[20:23]\n\t"
    "ds_read_b128 a[92:95], v215 offset:43008\n\t"
    "ds_write_b32 v250, v252\n\t"
    "v_mfma_f32_16x16x32_bf16 v[24:27], a[32:35], a[24:27], v[24:27]\n\t"
    "ds_write_b32 v250, v253 offset:1024\n\t"
    "v_mfma_f32_16x16x32_bf16 v[56:59], a[36:39], a[24:27], v[56:59]\n\t"
    "ds_read_b128 a[96:99], v215 offset:45056\n\t"
    "v_add_f32 v226, v226, v228\n\t"
    "v_mfma_f32_16x16x32_bf16 v[60:63], a[36:39], a[28:31], v[60:63]\n\t"
    "ds_read_b128 a[100:103], v215 offset:47104\n\t"
    "v_add_f32 v227, v227, v229\n\t"
    "v_mfma_f32_16x16x32_bf16 v[28:31], a[32:35], a[28:31], v[28:31]\n\t"
    "ds_read_b64_tr_b16 a[120:121], v218 offset:20480\n\t"
    "v_add_f32 v230, v230, v232\n\t"
    "v_mfma_f32_16x16x32_bf16 v[156:159], a[48:51], a[28:31], v[156:159]\n\t"
    "v_add_f32 v231, v231, v233\n\t"
    "v_mfma_f32_16x16x32_bf16 v[188:191], a[52:55], a[28:31], v[188:191]\n\t"
    "ds_read_b64_tr_b16 a[122:123], v218 offset:20992\n\t"
    "v_add_f32 v234, v234, v236\n\t"
    "v_mfma_f32_16x16x32_bf16 v[184:187], a[52:55], a[24:27], v[184:187]\n\t"
    "ds_read_b64_tr_b16 a[124:125], v219 offset:20480\n\t"
    "v_add_f32 v235, v235, v237\n\t"
    "v_mfma_f32_16x16x32_bf16 v[152:155], a[48:51], a[24:27], v[152:155]\n\t"
    "ds_read_b64_tr_b16 a[126:127], v219 offset:20992\n\t"
    "v_add_f32 v238, v238, v240\n\t"
    "v_mfma_f32_16x16x32_bf16 v[148:151], a[48:51], a[20:23], v[148:151]\n\t"
    "v_add_f32 v239, v239, v241\n\t"
    "v_mfma_f32_16x16x32_bf16 v[180:183], a[52:55], a[20:23], v[180:183]\n\t"
    "v_add_f32 v226, v226, v230\n\t"
    "v_mfma_f32_16x16x32_bf16 v[176:179], a[52:55], a[16:19], v[176:179]\n\t"
    "v_add_f32 v227, v227, v231\n\t"
    "v_mfma_f32_16x16x32_bf16 v[144:147], a[48:51], a[16:19], v[144:147]\n\t"
    "v_add_f32 v234, v234, v238\n\t"
    "v_mfma_f32_16x16x32_bf16 v[140:143], a[48:51], a[12:15], v[140:143]\n\t"
    "v_add_f32 v235, v235, v239\n\t"
    "v_mfma_f32_16x16x32_bf16 v[172:175], a[52:55], a[12:15], v[172:175]\n\t"
    "v_add_f32 v226, v226, v234\n\t"
    "v_mfma_f32_16x16x32_bf16 v[168:171], a[52:55], a[8:11], v[168:171]\n\t"
    "v_add_f32 v227, v227, v235\n\t"
    "v_mfma_f32_16x16x32_bf16 v[136:139], a[48:51], a[8:11], v[136:139]\n\t"
    "v_lshrrev_b32 v250, 1, v248\n\t"
    "v_mfma_f32_16x16x32_bf16 v[132:135], a[48:51], a[4:7], v[132:135]\n\t"
    "v_add_u32 v250, %[lcs], v250\n\t"
    "v_mfma_f32_16x16x32_bf16 v[164:167], a[52:55], a[4:7], v[164:167]\n\t"
    "ds_write_b64 v250, v[226:227]\n\t"
    "v_mfma_f32_16x16x32_bf16 v[160:163], a[52:55], a[0:3], v[160:163]\n\t"
    "v_mfma_f32_16x16x32_bf16 v[128:131], a[48:51], a[0:3], v[128:131]\n\t"
    "s_waitcnt lgkmcnt(0)\n\t"
    "s_barrier\n\t"
    "v_mfma_f32_16x16x32_bf16 v[0:3], a[104:107], a[72:75], v[0:3]\n\t"
    "ds_read_b128 a[0:3], v214 offset:0\n\t"
    "v_mfma_f32_16x16x32_bf16 v[32:35], a[108:111], a[72:75], v[32:35]\n\t"
    "v_mfma_f32_16x16x32_bf16 v[36:39], a[108:111], a[76:79], v[36:39]\n\t"
    "ds_read_b128 a[4:7], v214 offset:2048\n\t"
    "ds_read_b128 v[224:227], v209 offset:0\n\t"
    "v_mfma_f32_16x16x32_bf16 v[4:7], a[104:107], a[76:79], v[4:7]\n\t"
    "v_mfma_f32_16x16x32_bf16 v[8:11], a[104:107], a[80:83], v[8:11]\n\t"
    "v_mfma_f32_16x16x32_bf16 v[40:43], a[108:111], a[80:83], v[40:43]\n\t"
    "ds_read_b128 a[8:11], v214 offset:4096\n\t"
    "ds_read_b128 v[228:231], v209 offset:64\n\t"
    "v_mfma_f32_16x16x32_bf16 v[44:47], a[108:111], a[84:87], v[44:47]\n\t"
    "v_mfma_f32_16x16x32_bf16 v[12:15], a[104:107], a[84:87], v[12:15]\n\t"
    "ds_read_b128 a[12:15], v214 offset:6144\n\t"
    "v_mfma_f32_16x16x32_bf16 v[16:19], a[104:107], a[88:91], v[16:19]\n\t"
    "v_mfma_f32_16x16x32_bf16 v[48:51], a[108:111], a[88:91], v[48:51]\n\t"
    "ds_read_b128 v[232:235], v209 offset:256\n\t"
    "v_mfma_f32_16x16x32_bf16 v[52:55], a[108:111], a[92:95], v[52:55]\n\t"
    "ds_read_b128 a[16:19], v214 offset:8192\n\t"
    "v_mfma_f32_16x16x32_bf16 v[20:23], a[104:107], a[92:95], v[20:23]\n\t"
    "v_mfma_f32_16x16x32_bf16 v[24:27], a[104:107], a[96:99], v[24:27]\n\t"
    "v_mfma_f32_16x16x32_bf16 v[56:59], a[108:111], a[96:99], v[56:59]\n\t"
    "ds_read_b128 a[20:23], v214 offset:10240\n\t"
    "ds_read_b128 v[236:239], v209 offset:320\n\t"
    "v_mfma_f32_16x16x32_bf16 v[60:63], a[108:111], a[100:103], v[60:63]\n\t"
    "v_mfma_f32_16x16x32_bf16 v[28:31], a[104:107], a[100:103], v[28:31]\n\t"
    "ds_read_b128 a[24:27], v214 offset:12288\n\t"
    "v_mfma_f32_16x16x32_bf16 v[156:159], a[120:123], a[100:103], v[156:159]\n\t"
    "v_mfma_f32_16x16x32_bf16 v[188:191], a[124:127], a[100:103], v[188:191]\n\t"
    "ds_read_b128 v[240:243], v208 offset:0\n\t"
    "v_mfma_f32_16x16x32_bf16 v[184:187], a[124:127], a[96:99], v[184:187]\n\t"
    "ds_read_b128 a[28:31], v214 offset:14336\n\t"
    "v_mfma_f32_16x16x32_bf16 v[152:155], a[120:123], a[96:99], v[152:155]\n\t"
    "v_mfma_f32_16x16x32_bf16 v[148:151], a[120:123], a[92:95], v[148:151]\n\t"
    "v_mfma_f32_16x16x32_bf16 v[180:183], a[124:127], a[92:95], v[180:183]\n\t"
    "v_mfma_f32_16x16x32_bf16 v[176:179], a[124:127], a[88:91], v[176:179]\n\t"
    "v_mfma_f32_16x16x32_bf16 v[144:147], a[120:123], a[88:91], v[144:147]\n\t"
    "v_mfma_f32_16x16x32_bf16 v[140:143], a[120:123], a[84:87], v[140:143]\n\t"
    "v_mfma_f32_16x16x32_bf16 v[172:175], a[124:127], a[84:87], v[172:175]\n\t"
    "v_mfma_f32_16x16x32_bf16 v[168:171], a[124:127], a[80:83], v[168:171]\n\t"
    "v_mfma_f32_16x16x32_bf16 v[136:139], a[120:123], a[80:83], v[136:139]\n\t"
    "v_mfma_f32_16x16x32_bf16 v[132:135], a[120:123], a[76:79], v[132:135]\n\t"
    "v_mfma_f32_16x16x32_bf16 v[164:167], a[124:127], a[76:79], v[164:167]\n\t"
    "v_mfma_f32_16x16x32_bf16 v[160:163], a[124:127], a[72:75], v[160:163]\n\t"
    "v_mfma_f32_16x16x32_bf16 v[128:131], a[120:123], a[72:75], v[128:131]\n\t"
    "s_waitcnt lgkmcnt(0)\n\t"
    "v_mfma_f32_16x16x32_bf16 v[64:67], a[40:43], a[0:3], v[64:67]\n\t"
    "ds_read_b64_tr_b16 a[112:113], v216 offset:12288\n\t"
    "v_mfma_f32_16x16x32_bf16 v[96:99], a[44:47], a[0:3], v[96:99]\n\t"
    "ds_read_b64_tr_b16 a[114:115], v216 offset:12800\n\t"
    "v_mfma_f32_16x16x32_bf16 v[100:103], a[44:47], a[4:7], v[100:103]\n\t"
    "ds_read_b128 v[244:247], v208 offset:256\n\t"
    "v_fmac_f32 v0, v240, v224\n\t"
    "v_fmac_f32 v1, v240, v225\n\t"
    "v_mfma_f32_16x16x32_bf16 v[68:71], a[40:43], a[4:7], v[68:71]\n\t"
    "ds_read_b64_tr_b16 a[116:117], v217 offset:12288\n\t"
    "v_fmac_f32 v2, v240, v226\n\t"
    "v_mfma_f32_16x16x32_bf16 v[72:75], a[40:43], a[8:11], v[72:75]\n\t"
    "ds_read_b64_tr_b16 a[118:119], v217 offset:12800\n\t"
    "v_fmac_f32 v3, v240, v227\n\t"
    "v_fmac_f32 v128, v240, v232\n\t"
    "v_mfma_f32_16x16x32_bf16 v[104:107], a[44:47], a[8:11], v[104:107]\n\t"
    "v_fmac_f32 v129, v240, v233\n\t"
    "v_fmac_f32 v130, v240, v234\n\t"
    "v_mfma_f32_16x16x32_bf16 v[108:111], a[44:47], a[12:15], v[108:111]\n\t"
    "ds_read_b128 a[72:75], v215 offset:0\n\t"
    "v_fmac_f32 v131, v240, v235\n\t"
    "v_mul_f32 v128, v0, v128\n\t"
    "v_mfma_f32_16x16x32_bf16 v[76:79], a[40:43], a[12:15], v[76:79]\n\t"
    "ds_read_b128 a[76:79], v215 offset:2048\n\t"
    "v_mul_f32 v129, v1, v129\n\t"
    "v_mul_f32 v130, v2, v130\n\t"
    "v_mfma_f32_16x16x32_bf16 v[80:83], a[40:43], a[16:19], v[80:83]\n\t"
    "v_mul_f32 v131, v3, v131\n\t"
    "v_mul_f32 v0, v0, v243\n\t"
    "v_mfma_f32_16x16x32_bf16 v[112:115], a[44:47], a[16:19], v[112:115]\n\t"
    "ds_read_b128 a[80:83], v215 offset:4096\n\t"
    "v_mul_f32 v1, v1, v243\n\t"
    "v_mul_f32 v2, v2, v243\n\t"
    "v_mfma_f32_16x16x32_bf16 v[116:119], a[44:47], a[20:23], v[116:119]\n\t"
    "ds_read_b128 a[84:87], v215 offset:6144\n\t"
    "v_mul_f32 v3, v3, v243\n\t"
    "v_mfma_f32_16x16x32_bf16 v[84:87], a[40:43], a[20:23], v[84:87]\n\t"
    "v_exp_f32 v0, v0\n\t"
    "v_mfma_f32_16x16x32_bf16 v[88:91], a[40:43], a[24:27], v[88:91]\n\t"
    "ds_read_b128 a[88:91], v215 offset:8192\n\t"
    "v_exp_f32 v1, v1\n\t"
    "v_mfma_f32_16x16x32_bf16 v[120:123], a[44:47], a[24:27], v[120:123]\n\t"
    "ds_read_b128 a[92:95], v215 offset:10240\n\t"
    "v_exp_f32 v2, v2\n\t"
    "v_mfma_f32_16x16x32_bf16 v[124:127], a[44:47], a[28:31], v[124:127]\n\t"
    "v_exp_f32 v3, v3\n\t"
    "v_fma_f32 v0, v0, v242, v242\n\t"
    "v_mfma_f32_16x16x32_bf16 v[92:95], a[40:43], a[28:31], v[92:95]\n\t"
    "ds_read_b128 a[96:99], v215 offset:12288\n\t"
    "v_fma_f32 v1, v1, v242, v242\n\t"
    "v_mfma_f32_16x16x32_bf16 a[156:159], a[56:59], a[28:31], a[156:159]\n\t"
    "v_fma_f32 v2, v2, v242, v242\n\t"
    "v_fma_f32 v3, v3, v242, v242\n\t"
    "v_mfma_f32_16x16x32_bf16 a[188:191], a[60:63], a[28:31], a[188:191]\n\t"
    "ds_read_b128 a[100:103], v215 offset:14336\n\t"
    "v_rcp_f32 v0, v0\n\t"
    "v_mfma_f32_16x16x32_bf16 a[184:187], a[60:63], a[24:27], a[184:187]\n\t"
    "ds_read_b64_tr_b16 a[128:129], v216 offset:28672\n\t"
    "v_rcp_f32 v1, v1\n\t"
    "v_mfma_f32_16x16x32_bf16 a[152:155], a[56:59], a[24:27], a[152:155]\n\t"
    "v_rcp_f32 v2, v2\n\t"
    "v_mfma_f32_16x16x32_bf16 a[148:151], a[56:59], a[20:23], a[148:151]\n\t"
    "ds_read_b64_tr_b16 a[130:131], v216 offset:29184\n\t"
    "v_rcp_f32 v3, v3\n\t"
    "v_mfma_f32_16x16x32_bf16 a[180:183], a[60:63], a[20:23], a[180:183]\n\t"
    "ds_read_b64_tr_b16 a[132:133], v217 offset:28672\n\t"
    "v_mul_f32 v128, v128, v0\n\t"
    "v_mul_f32 v129, v129, v1\n\t"
    "v_mfma_f32_16x16x32_bf16 a[176:179], a[60:63], a[16:19], a[176:179]\n\t"
    "v_mul_f32 v130, v130, v2\n\t"
    "v_mul_f32 v131, v131, v3\n\t"
    "v_mfma_f32_16x16x32_bf16 a[144:147], a[56:59], a[16:19], a[144:147]\n\t"
    "ds_read_b64_tr_b16 a[134:135], v217 offset:29184\n\t"
    "v_fmac_f32 v32, v240, v228\n\t"
    "v_fmac_f32 v33, v240, v229\n\t"
    "v_mfma_f32_16x16x32_bf16 v[204:207], a[56:59], a[12:15], v[204:207]\n\t"
    "ds_read_b64_tr_b16 a[136:137], v220 offset:2048\n\t"
    "v_fmac_f32 v34, v240, v230\n\t"
    "v_fmac_f32 v35, v240, v231\n\t"
    "v_mfma_f32_16x16x32_bf16 a[172:175], a[60:63], a[12:15], a[172:175]\n\t"
    "v_fmac_f32 v160, v240, v236\n\t"
    "v_fmac_f32 v161, v240, v237\n\t"
    "v_mfma_f32_16x16x32_bf16 a[168:171], a[60:63], a[8:11], a[168:171]\n\t"
    "ds_read_b64_tr_b16 a[138:139], v220 offset:2304\n\t"
    "v_fmac_f32 v162, v240, v238\n\t"
    "v_fmac_f32 v163, v240, v239\n\t"
    "v_mfma_f32_16x16x32_bf16 v[200:203], a[56:59], a[8:11], v[200:203]\n\t"
    "ds_read_b64_tr_b16 a[140:141], v220 offset:6144\n\t"
    "v_mul_f32 v160, v32, v160\n\t"
    "v_mfma_f32_16x16x32_bf16 v[196:199], a[56:59], a[4:7], v[196:199]\n\t"
    "v_mul_f32 v161, v33, v161\n\t"
    "v_mul_f32 v162, v34, v162\n\t"
    "v_mul_f32 v163, v35, v163\n\t"
    "v_mfma_f32_16x16x32_bf16 a[164:167], a[60:63], a[4:7], a[164:167]\n\t"
    "ds_read_b64_tr_b16 a[142:143], v220 offset:6400\n\t"
    "v_mul_f32 v32, v32, v243\n\t"
    "v_mul_f32 v33, v33, v243\n\t"
    "v_mfma_f32_16x16x32_bf16 a[160:163], a[60:63], a[0:3], a[160:163]\n\t"
    "v_mul_f32 v34, v34, v243\n\t"
    "v_mul_f32 v35, v35, v243\n\t"
    "v_mfma_f32_16x16x32_bf16 v[192:195], a[56:59], a[0:3], v[192:195]\n\t"
    "v_exp_f32 v32, v32\n\t"
    "v_mfma_f32_16x16x32_bf16 a[192:195], a[64:67], a[0:3], a[192:195]\n\t"
    "v_exp_f32 v33, v33\n\t"
    "v_mfma_f32_16x16x32_bf16 a[224:227], a[68:71], a[0:3], a[224:227]\n\t"
    "v_exp_f32 v34, v34\n\t"
    "v_exp_f32 v35, v35\n\t"
    "v_mfma_f32_16x16x32_bf16 a[228:231], a[68:71], a[4:7], a[228:231]\n\t"
    "v_fma_f32 v32, v32, v242, v242\n\t"
    "v_fma_f32 v33, v33, v242, v242\n\t"
    "v_fma_f32 v34, v34, v242, v242\n\t"
    "v_mfma_f32_16x16x32_bf16 a[196:199], a[64:67], a[4:7], a[196:199]\n\t"
    "v_fma_f32 v35, v35, v242, v242\n\t"
    "v_mfma_f32_16x16x32_bf16 a[200:203], a[64:67], a[8:11], a[200:203]\n\t"
    "v_rcp_f32 v32, v32\n\t"
    "v_rcp_f32 v33, v33\n\t"
    "v_mfma_f32_16x16x32_bf16 a[232:235], a[68:71], a[8:11], a[232:235]\n\t"
    "v_rcp_f32 v34, v34\n\t"
    "v_mfma_f32_16x16x32_bf16 a[236:239], a[68:71], a[12:15], a[236:239]\n\t"
    "v_rcp_f32 v35, v35\n\t"
    "v_mul_f32 v160, v160, v32\n\t"
    "v_mfma_f32_16x16x32_bf16 a[204:207], a[64:67], a[12:15], a[204:207]\n\t"
    "v_mul_f32 v161, v161, v33\n\t"
    "v_mul_f32 v162, v162, v34\n\t"
    "v_mul_f32 v163, v163, v35\n\t"
    "v_mfma_f32_16x16x32_bf16 a[208:211], a[64:67], a[16:19], a[208:211]\n\t"
    "v_cvt_pk_bf16_f32 v0, v128, v129\n\t"
    "v_cvt_pk_bf16_f32 v1, v130, v131\n\t"
    "v_mfma_f32_16x16x32_bf16 a[240:243], a[68:71], a[16:19], a[240:243]\n\t"
    "v_cvt_pk_bf16_f32 v2, v160, v161\n\t"
    "v_cvt_pk_bf16_f32 v3, v162, v163\n\t"
    "s_mov_b32 %[tmp], 0\n\t"
    "v_mfma_f32_16x16x32_bf16 a[244:247], a[68:71], a[20:23], a[244:247]\n\t"
    "buffer_store_dwordx4 v[0:3], v210, %[srdw], %[tmp] offen sc1\n\t"
    "s_waitcnt lgkmcnt(0)\n\t"
    "ds_read_b128 v[240:243], v208 offset:512\n\t"
    "v_mfma_f32_16x16x32_bf16 a[212:215], a[64:67], a[20:23], a[212:215]\n\t"
    "v_fmac_f32 v4, v244, v224\n\t"
    "v_fmac_f32 v5, v244, v225\n\t"
    "v_fmac_f32 v6, v244, v226\n\t"
    "v_mfma_f32_16x16x32_bf16 a[216:219], a[64:67], a[24:27], a[216:219]\n\t"
    "v_fmac_f32 v7, v244, v227\n\t"
    "v_fmac_f32 v132, v244, v232\n\t"
    "v_fmac_f32 v133, v244, v233\n\t"
    "v_mfma_f32_16x16x32_bf16 a[248:251], a[68:71], a[24:27], a[248:251]\n\t"
    "v_fmac_f32 v134, v244, v234\n\t"
    "v_fmac_f32 v135, v244, v235\n\t"
    "v_mfma_f32_16x16x32_bf16 a[252:255], a[68:71], a[28:31], a[252:255]\n\t"
    "v_mul_f32 v132, v4, v132\n\t"
    "v_mul_f32 v133, v5, v133\n\t"
    "v_mul_f32 v134, v6, v134\n\t"
    "v_mfma_f32_16x16x32_bf16 a[220:223], a[64:67], a[28:31], a[220:223]\n\t"
    "v_mul_f32 v135, v7, v135\n\t"
    "v_mul_f32 v4, v4, v247\n\t"
    "s_waitcnt lgkmcnt(0)\n\t"
    "v_mfma_f32_16x16x32_bf16 v[64:67], a[112:115], a[72:75], v[64:67]\n\t"
    "ds_read_b64_tr_b16 a[40:41], v218 offset:8192\n\t"
    "v_mul_f32 v5, v5, v247\n\t"
    "v_mul_f32 v6, v6, v247\n\t"
    "v_mfma_f32_16x16x32_bf16 v[96:99], a[116:119], a[72:75], v[96:99]\n\t"
    "ds_read_b64_tr_b16 a[42:43], v218 offset:8704\n\t"
    "v_mul_f32 v7, v7, v247\n\t"
    "v_mfma_f32_16x16x32_bf16 v[100:103], a[116:119], a[76:79], v[100:103]\n\t"
    "v_exp_f32 v4, v4\n\t"
    "v_mfma_f32_16x16x32_bf16 v[68:71], a[112:115], a[76:79], v[68:71]\n\t"
    "ds_read_b64_tr_b16 a[44:45], v219 offset:8192\n\t"
    "v_exp_f32 v5, v5\n\t"
    "v_mfma_f32_16x16x32_bf16 v[72:75], a[112:115], a[80:83], v[72:75]\n\t"
    "ds_read_b64_tr_b16 a[46:47], v219 offset:8704\n\t"
    "v_exp_f32 v6, v6\n\t"
    "v_mfma_f32_16x16x32_bf16 v[104:107], a[116:119], a[80:83], v[104:107]\n\t"
    "v_exp_f32 v7, v7\n\t"
    "v_fma_f32 v4, v4, v246, v246\n\t"
    "v_mfma_f32_16x16x32_bf16 v[108:111], a[116:119], a[84:87], v[108:111]\n\t"
    "ds_read_b128 a[0:3], v214 offset:32768\n\t"
    "v_fma_f32 v5, v5, v246, v246\n\t"
    "v_fma_f32 v6, v6, v246, v246\n\t"
    "v_mfma_f32_16x16x32_bf16 v[76:79], a[112:115], a[84:87], v[76:79]\n\t"
    "ds_read_b128 a[4:7], v214 offset:34816\n\t"
    "v_fma_f32 v7, v7, v246, v246\n\t"
    "v_mfma_f32_16x16x32_bf16 v[80:83], a[112:115], a[88:91], v[80:83]\n\t"
    "v_rcp_f32 v4, v4\n\t"
    "v_mfma_f32_16x16x32_bf16 v[112:115], a[116:119], a[88:91], v[112:115]\n\t"
    "ds_read_b128 a[8:11], v214 offset:36864\n\t"
    "v_rcp_f32 v5, v5\n\t"
    "v_mfma_f32_16x16x32_bf16 v[116:119], a[116:119], a[92:95], v[116:119]\n\t"
    "ds_read_b128 a[12:15], v214 offset:38912\n\t"
    "v_rcp_f32 v6, v6\n\t"
    "v_mfma_f32_16x16x32_bf16 v[84:87], a[112:115], a[92:95], v[84:87]\n\t"
    "v_rcp_f32 v7, v7\n\t"
    "v_mfma_f32_16x16x32_bf16 v[88:91], a[112:115], a[96:99], v[88:91]\n\t"
    "ds_read_b128 a[16:19], v214 offset:40960\n\t"
    "v_mul_f32 v132, v132, v4\n\t"
    "v_mul_f32 v133, v133, v5\n\t"
    "v_mfma_f32_16x16x32_bf16 v[120:123], a[116:119], a[96:99], v[120:123]\n\t"
    "ds_read_b128 a[20:23], v214 offset:43008\n\t"
    "v_mul_f32 v134, v134, v6\n\t"
    "v_mul_f32 v135, v135, v7\n\t"
    "v_mfma_f32_16x16x32_bf16 v[124:127], a[116:119], a[100:103], v[124:127]\n\t"
    "v_fmac_f32 v36, v244, v228\n\t"
    "v_fmac_f32 v37, v244, v229\n\t"
    "v_mfma_f32_16x16x32_bf16 v[92:95], a[112:115], a[100:103], v[92:95]\n\t"
    "ds_read_b128 a[24:27], v214 offset:45056\n\t"
    "v_fmac_f32 v38, v244, v230\n\t"
    "v_fmac_f32 v39, v244, v231\n\t"
    "v_mfma_f32_16x16x32_bf16 a[156:159], a[128:131], a[100:103], a[156:159]\n\t"
    "v_fmac_f32 v164, v244, v236\n\t"
    "v_fmac_f32 v165, v244, v237\n\t"
    "v_mfma_f32_16x16x32_bf16 a[188:191], a[132:135], a[100:103], a[188:191]\n\t"
    "ds_read_b128 a[28:31], v214 offset:47104\n\t"
    "v_fmac_f32 v166, v244, v238\n\t"
    "v_fmac_f32 v167, v244, v239\n\t"
    "v_mfma_f32_16x16x32_bf16 a[184:187], a[132:135], a[96:99], a[184:187]\n\t"
    "ds_read_b64_tr_b16 a[56:57], v218 offset:24576\n\t"
    "v_mul_f32 v164, v36, v164\n\t"
    "v_mul_f32 v165, v37, v165\n\t"
    "v_mfma_f32_16x16x32_bf16 a[152:155], a[128:131], a[96:99], a[152:155]\n\t"
    "v_mul_f32 v166, v38, v166\n\t"
    "v_mul_f32 v167, v39, v167\n\t"
    "v_mfma_f32_16x16x32_bf16 a[148:151], a[128:131], a[92:95], a[148:151]\n\t"
    "ds_read_b64_tr_b16 a[58:59], v218 offset:25088\n\t"
    "v_mul_f32 v36, v36, v247\n\t"
    "v_mul_f32 v37, v37, v247\n\t"
    "v_mfma_f32_16x16x32_bf16 a[180:183], a[132:135], a[92:95], a[180:183]\n\t"
    "ds_read_b64_tr_b16 a[60:61], v219 offset:24576\n\t"
    "v_mul_f32 v38, v38, v247\n\t"
    "v_mfma_f32_16x16x32_bf16 a[176:179], a[132:135], a[88:91], a[176:179]\n\t"
    "v_mul_f32 v39, v39, v247\n\t"
    "v_exp_f32 v36, v36\n\t"
    "v_mfma_f32_16x16x32_bf16 a[144:147], a[128:131], a[88:91], a[144:147]\n\t"
    "ds_read_b64_tr_b16 a[62:63], v219 offset:25088\n\t"
    "v_exp_f32 v37, v37\n\t"
    "v_mfma_f32_16x16x32_bf16 v[204:207], a[128:131], a[84:87], v[204:207]\n\t"
    "ds_read_b64_tr_b16 a[64:65], v220 offset:40960\n\t"
    "v_mfma_f32_16x16x32_bf16 a[172:175], a[132:135], a[84:87], a[172:175]\n\t"
    "v_exp_f32 v38, v38\n\t"
    "v_exp_f32 v39, v39\n\t"
    "v_mfma_f32_16x16x32_bf16 a[168:171], a[132:135], a[80:83], a[168:171]\n\t"
    "ds_read_b64_tr_b16 a[66:67], v220 offset:41216\n\t"
    "v_fma_f32 v36, v36, v246, v246\n\t"
    "v_mfma_f32_16x16x32_bf16 v[200:203], a[128:131], a[80:83], v[200:203]\n\t"
    "ds_read_b64_tr_b16 a[68:69], v220 offset:45056\n\t"
    "v_fma_f32 v37, v37, v246, v246\n\t"
    "v_fma_f32 v38, v38, v246, v246\n\t"
    "v_mfma_f32_16x16x32_bf16 v[196:199], a[128:131], a[76:79], v[196:199]\n\t"
    "v_fma_f32 v39, v39, v246, v246\n\t"
    "v_rcp_f32 v36, v36\n\t"
    "v_mfma_f32_16x16x32_bf16 a[164:167], a[132:135], a[76:79], a[164:167]\n\t"
    "ds_read_b64_tr_b16 a[70:71], v220 offset:45312\n\t"
    "v_mfma_f32_16x16x32_bf16 a[160:163], a[132:135], a[72:75], a[160:163]\n\t"
    "v_rcp_f32 v37, v37\n\t"
    "v_rcp_f32 v38, v38\n\t"
    "v_mfma_f32_16x16x32_bf16 v[192:195], a[128:131], a[72:75], v[192:195]\n\t"
    "v_rcp_f32 v39, v39\n\t"
    "v_mfma_f32_16x16x32_bf16 a[192:195], a[136:139], a[72:75], a[192:195]\n\t"
    "v_mul_f32 v164, v164, v36\n\t"
    "v_mul_f32 v165, v165, v37\n\t"
    "v_mul_f32 v166, v166, v38\n\t"
    "v_mfma_f32_16x16x32_bf16 a[224:227], a[140:143], a[72:75], a[224:227]\n\t"
    "v_mul_f32 v167, v167, v39\n\t"
    "v_cvt_pk_bf16_f32 v4, v132, v133\n\t"
    "v_cvt_pk_bf16_f32 v5, v134, v135\n\t"
    "v_mfma_f32_16x16x32_bf16 a[228:231], a[140:143], a[76:79], a[228:231]\n\t"
    "v_cvt_pk_bf16_f32 v6, v164, v165\n\t"
    "v_cvt_pk_bf16_f32 v7, v166, v167\n\t"
    "v_mfma_f32_16x16x32_bf16 a[196:199], a[136:139], a[76:79], a[196:199]\n\t"
    "s_mov_b32 %[tmp], 147456\n\t"
    "buffer_store_dwordx4 v[4:7], v210, %[srdw], %[tmp] offen sc1\n\t"
    "s_waitcnt lgkmcnt(0)\n\t"
    "ds_read_b128 v[244:247], v208 offset:768\n\t"
    "v_mfma_f32_16x16x32_bf16 a[200:203], a[136:139], a[80:83], a[200:203]\n\t"
    "v_fmac_f32 v8, v240, v224\n\t"
    "v_fmac_f32 v9, v240, v225\n\t"
    "v_mfma_f32_16x16x32_bf16 a[232:235], a[140:143], a[80:83], a[232:235]\n\t"
    "v_fmac_f32 v10, v240, v226\n\t"
    "v_fmac_f32 v11, v240, v227\n\t"
    "v_fmac_f32 v136, v240, v232\n\t"
    "v_mfma_f32_16x16x32_bf16 a[236:239], a[140:143], a[84:87], a[236:239]\n\t"
    "v_fmac_f32 v137, v240, v233\n\t"
    "v_fmac_f32 v138, v240, v234\n\t"
    "v_fmac_f32 v139, v240, v235\n\t"
    "v_mfma_f32_16x16x32_bf16 a[204:207], a[136:139], a[84:87], a[204:207]\n\t"
    "v_mul_f32 v136, v8, v136\n\t"
    "v_mul_f32 v137, v9, v137\n\t"
    "v_mfma_f32_16x16x32_bf16 a[208:211], a[136:139], a[88:91], a[208:211]\n\t"
    "v_mul_f32 v138, v10, v138\n\t"
    "v_mul_f32 v139, v11, v139\n\t"
    "v_mul_f32 v8, v8, v243\n\t"
    "v_mfma_f32_16x16x32_bf16 a[240:243], a[140:143], a[88:91], a[240:243]\n\t"
    "v_mul_f32 v9, v9, v243\n\t"
    "v_mul_f32 v10, v10, v243\n\t"
    "v_mfma_f32_16x16x32_bf16 a[244:247], a[140:143], a[92:95], a[244:247]\n\t"
    "v_mul_f32 v11, v11, v243\n\t"
    "v_exp_f32 v8, v8\n\t"
    "v_mfma_f32_16x16x32_bf16 a[212:215], a[136:139], a[92:95], a[212:215]\n\t"
    "v_exp_f32 v9, v9\n\t"
    "v_mfma_f32_16x16x32_bf16 a[216:219], a[136:139], a[96:99], a[216:219]\n\t"
    "v_exp_f32 v10, v10\n\t"
    "v_mfma_f32_16x16x32_bf16 a[248:251], a[140:143], a[96:99], a[248:251]\n\t"
    "v_exp_f32 v11, v11\n\t"
    "v_fma_f32 v8, v8, v242, v242\n\t"
    "v_fma_f32 v9, v9, v242, v242\n\t"
    "v_mfma_f32_16x16x32_bf16 a[252:255], a[140:143], a[100:103], a[252:255]\n\t"
    "v_fma_f32 v10, v10, v242, v242\n\t"
    "v_fma_f32 v11, v11, v242, v242\n\t"
    "v_mfma_f32_16x16x32_bf16 a[220:223], a[136:139], a[100:103], a[220:223]\n\t"
    "v_rcp_f32 v8, v8\n\t"
    "s_waitcnt lgkmcnt(0)\n\t"
    "v_mfma_f32_16x16x32_bf16 v[64:67], a[40:43], a[0:3], v[64:67]\n\t"
    "ds_read_b64_tr_b16 a[112:113], v218 offset:12288\n\t"
    "v_rcp_f32 v9, v9\n\t"
    "v_mfma_f32_16x16x32_bf16 v[96:99], a[44:47], a[0:3], v[96:99]\n\t"
    "ds_read_b64_tr_b16 a[114:115], v218 offset:12800\n\t"
    "v_rcp_f32 v10, v10\n\t"
    "v_mfma_f32_16x16x32_bf16 v[100:103], a[44:47], a[4:7], v[100:103]\n\t"
    "v_rcp_f32 v11, v11\n\t"
    "v_mul_f32 v136, v136, v8\n\t"
    "v_mfma_f32_16x16x32_bf16 v[68:71], a[40:43], a[4:7], v[68:71]\n\t"
    "ds_read_b64_tr_b16 a[116:117], v219 offset:12288\n\t"
    "v_mul_f32 v137, v137, v9\n\t"
    "v_mfma_f32_16x16x32_bf16 v[72:75], a[40:43], a[8:11], v[72:75]\n\t"
    "ds_read_b64_tr_b16 a[118:119], v219 offset:12800\n\t"
    "v_mul_f32 v138, v138, v10\n\t"
    "v_mul_f32 v139, v139, v11\n\t"
    "v_mfma_f32_16x16x32_bf16 v[104:107], a[44:47], a[8:11], v[104:107]\n\t"
    "v_fmac_f32 v40, v240, v228\n\t"
    "v_fmac_f32 v41, v240, v229\n\t"
    "v_mfma_f32_16x16x32_bf16 v[108:111], a[44:47], a[12:15], v[108:111]\n\t"
    "ds_read_b128 a[72:75], v215 offset:32768\n\t"
    "v_fmac_f32 v42, v240, v230\n\t"
    "v_fmac_f32 v43, v240, v231\n\t"
    "v_mfma_f32_16x16x32_bf16 v[76:79], a[40:43], a[12:15], v[76:79]\n\t"
    "ds_read_b128 a[76:79], v215 offset:34816\n\t"
    "v_fmac_f32 v168, v240, v236\n\t"
    "v_fmac_f32 v169, v240, v237\n\t"
    "v_mfma_f32_16x16x32_bf16 v[80:83], a[40:43], a[16:19], v[80:83]\n\t"
    "v_fmac_f32 v170, v240, v238\n\t"
    "v_fmac_f32 v171, v240, v239\n\t"
    "v_mfma_f32_16x16x32_bf16 v[112:115], a[44:47], a[16:19], v[112:115]\n\t"
    "ds_read_b128 a[80:83], v215 offset:36864\n\t"
    "v_mul_f32 v168, v40, v168\n\t"
    "v_mul_f32 v169, v41, v169\n\t"
    "v_mfma_f32_16x16x32_bf16 v[116:119], a[44:47], a[20:23], v[116:119]\n\t"
    "ds_read_b128 a[84:87], v215 offset:38912\n\t"
    "v_mul_f32 v170, v42, v170\n\t"
    "v_mfma_f32_16x16x32_bf16 v[84:87], a[40:43], a[20:23], v[84:87]\n\t"
    "v_mul_f32 v171, v43, v171\n\t"
    "v_mul_f32 v40, v40, v243\n\t"
    "v_mul_f32 v41, v41, v243\n\t"
    "v_mfma_f32_16x16x32_bf16 v[88:91], a[40:43], a[24:27], v[88:91]\n\t"
    "ds_read_b128 a[88:91], v215 offset:40960\n\t"
    "v_mul_f32 v42, v42, v243\n\t"
    "v_mul_f32 v43, v43, v243\n\t"
    "v_mfma_f32_16x16x32_bf16 v[120:123], a[44:47], a[24:27], v[120:123]\n\t"
    "ds_read_b128 a[92:95], v215 offset:43008\n\t"
    "v_mfma_f32_16x16x32_bf16 v[124:127], a[44:47], a[28:31], v[124:127]\n\t"
    "v_exp_f32 v40, v40\n\t"
    "v_exp_f32 v41, v41\n\t"
    "v_mfma_f32_16x16x32_bf16 v[92:95], a[40:43], a[28:31], v[92:95]\n\t"
    "ds_read_b128 a[96:99], v215 offset:45056\n\t"
    "v_mfma_f32_16x16x32_bf16 a[156:159], a[56:59], a[28:31], a[156:159]\n\t"
    "v_exp_f32 v42, v42\n\t"
    "v_exp_f32 v43, v43\n\t"
    "v_mfma_f32_16x16x32_bf16 a[188:191], a[60:63], a[28:31], a[188:191]\n\t"
    "ds_read_b128 a[100:103], v215 offset:47104\n\t"
    "v_fma_f32 v40, v40, v242, v242\n\t"
    "v_fma_f32 v41, v41, v242, v242\n\t"
    "v_mfma_f32_16x16x32_bf16 a[184:187], a[60:63], a[24:27], a[184:187]\n\t"
    "ds_read_b64_tr_b16 a[128:129], v218 offset:28672\n\t"
    "v_fma_f32 v42, v42, v242, v242\n\t"
    "v_mfma_f32_16x16x32_bf16 a[152:155], a[56:59], a[24:27], a[152:155]\n\t"
    "v_fma_f32 v43, v43, v242, v242\n\t"
    "v_rcp_f32 v40, v40\n\t"
    "v_mfma_f32_16x16x32_bf16 a[148:151], a[56:59], a[20:23], a[148:151]\n\t"
    "ds_read_b64_tr_b16 a[130:131], v218 offset:29184\n\t"
    "v_mfma_f32_16x16x32_bf16 a[180:183], a[60:63], a[20:23], a[180:183]\n\t"
    "ds_read_b64_tr_b16 a[132:133], v219 offset:28672\n\t"
    "v_rcp_f32 v41, v41\n\t"
    "v_mfma_f32_16x16x32_bf16 a[176:179], a[60:63], a[16:19], a[176:179]\n\t"
    "v_rcp_f32 v42, v42\n\t"
    "v_rcp_f32 v43, v43\n\t"
    "v_mfma_f32_16x16x32_bf16 a[144:147], a[56:59], a[16:19], a[144:147]\n\t"
    "ds_read_b64_tr_b16 a[134:135], v219 offset:29184\n\t"
    "v_mul_f32 v168, v168, v40\n\t"
    "v_mfma_f32_16x16x32_bf16 v[204:207], a[56:59], a[12:15], v[204:207]\n\t"
    "ds_read_b64_tr_b16 a[136:137], v220 offset:43008\n\t"
    "v_mul_f32 v169, v169, v41\n\t"
    "v_mul_f32 v170, v170, v42\n\t"
    "v_mfma_f32_16x16x32_bf16 a[172:175], a[60:63], a[12:15], a[172:175]\n\t"
    "v_mul_f32 v171, v171, v43\n\t"
    "v_cvt_pk_bf16_f32 v8, v136, v137\n\t"
    "v_mfma_f32_16x16x32_bf16 a[168:171], a[60:63], a[8:11], a[168:171]\n\t"
    "ds_read_b64_tr_b16 a[138:139], v220 offset:43264\n\t"
    "v_cvt_pk_bf16_f32 v9, v138, v139\n\t"
    "v_cvt_pk_bf16_f32 v10, v168, v169\n\t"
    "v_mfma_f32_16x16x32_bf16 v[200:203], a[56:59], a[8:11], v[200:203]\n\t"
    "ds_read_b64_tr_b16 a[140:141], v220 offset:47104\n\t"
    "v_cvt_pk_bf16_f32 v11, v170, v171\n\t"
    "s_mov_b32 %[tmp], 294912\n\t"
    "v_mfma_f32_16x16x32_bf16 v[196:199], a[56:59], a[4:7], v[196:199]\n\t"
    "buffer_store_dwordx4 v[8:11], v210, %[srdw], %[tmp] offen sc1\n\t"
    "s_waitcnt lgkmcnt(0)\n\t"
    "ds_read_b128 v[240:243], v208 offset:1024\n\t"
    "v_mfma_f32_16x16x32_bf16 a[164:167], a[60:63], a[4:7], a[164:167]\n\t"
    "ds_read_b64_tr_b16 a[142:143], v220 offset:47360\n\t"
    "v_fmac_f32 v12, v244, v224\n\t"
    "v_fmac_f32 v13, v244, v225\n\t"
    "v_mfma_f32_16x16x32_bf16 a[160:163], a[60:63], a[0:3], a[160:163]\n\t"
    "v_fmac_f32 v14, v244, v226\n\t"
    "v_fmac_f32 v15, v244, v227\n\t"
    "v_mfma_f32_16x16x32_bf16 v[192:195], a[56:59], a[0:3], v[192:195]\n\t"
    "v_fmac_f32 v140, v244, v232\n\t"
    "v_fmac_f32 v141, v244, v233\n\t"
    "v_fmac_f32 v142, v244, v234\n\t"
    "v_mfma_f32_16x16x32_bf16 a[192:195], a[64:67], a[0:3], a[192:195]\n\t"
    "v_fmac_f32 v143, v244, v235\n\t"
    "v_mul_f32 v140, v12, v140\n\t"
    "v_mul_f32 v141, v13, v141\n\t"
    "v_mfma_f32_16x16x32_bf16 a[224:227], a[68:71], a[0:3], a[224:227]\n\t"
    "v_mul_f32 v142, v14, v142\n\t"
    "v_mul_f32 v143, v15, v143\n\t"
    "v_mfma_f32_16x16x32_bf16 a[228:231], a[68:71], a[4:7], a[228:231]\n\t"
    "v_mul_f32 v12, v12, v247\n\t"
    "v_mul_f32 v13, v13, v247\n\t"
    "v_mul_f32 v14, v14, v247\n\t"
    "v_mfma_f32_16x16x32_bf16 a[196:199], a[64:67], a[4:7], a[196:199]\n\t"
    "v_mul_f32 v15, v15, v247\n\t"
    "v_mfma_f32_16x16x32_bf16 a[200:203], a[64:67], a[8:11], a[200:203]\n\t"
    "v_exp_f32 v12, v12\n\t"
    "v_exp_f32 v13, v13\n\t"
    "v_mfma_f32_16x16x32_bf16 a[232:235], a[68:71], a[8:11], a[232:235]\n\t"
    "v_exp_f32 v14, v14\n\t"
    "v_mfma_f32_16x16x32_bf16 a[236:239], a[68:71], a[12:15], a[236:239]\n\t"
    "v_exp_f32 v15, v15\n\t"
    "v_fma_f32 v12, v12, v246, v246\n\t"
    "v_mfma_f32_16x16x32_bf16 a[204:207], a[64:67], a[12:15], a[204:207]\n\t"
    "v_fma_f32 v13, v13, v246, v246\n\t"
    "v_fma_f32 v14, v14, v246, v246\n\t"
    "v_fma_f32 v15, v15, v246, v246\n\t"
    "v_mfma_f32_16x16x32_bf16 a[208:211], a[64:67], a[16:19], a[208:211]\n\t"
    "v_rcp_f32 v12, v12\n\t"
    "v_mfma_f32_16x16x32_bf16 a[240:243], a[68:71], a[16:19], a[240:243]\n\t"
    "v_rcp_f32 v13, v13\n\t"
    "v_mfma_f32_16x16x32_bf16 a[244:247], a[68:71], a[20:23], a[244:247]\n\t"
    "v_rcp_f32 v14, v14\n\t"
    "v_mfma_f32_16x16x32_bf16 a[212:215], a[64:67], a[20:23], a[212:215]\n\t"
    "v_rcp_f32 v15, v15\n\t"
    "v_mul_f32 v140, v140, v12\n\t"
    "v_mul_f32 v141, v141, v13\n\t"
    "v_mfma_f32_16x16x32_bf16 a[216:219], a[64:67], a[24:27], a[216:219]\n\t"
    "v_mul_f32 v142, v142, v14\n\t"
    "v_mul_f32 v143, v143, v15\n\t"
    "v_fmac_f32 v44, v244, v228\n\t"
    "v_mfma_f32_16x16x32_bf16 a[248:251], a[68:71], a[24:27], a[248:251]\n\t"
    "v_fmac_f32 v45, v244, v229\n\t"
    "v_fmac_f32 v46, v244, v230\n\t"
    "v_mfma_f32_16x16x32_bf16 a[252:255], a[68:71], a[28:31], a[252:255]\n\t"
    "v_fmac_f32 v47, v244, v231\n\t"
    "v_fmac_f32 v172, v244, v236\n\t"
    "v_fmac_f32 v173, v244, v237\n\t"
    "v_mfma_f32_16x16x32_bf16 a[220:223], a[64:67], a[28:31], a[220:223]\n\t"
    "v_fmac_f32 v174, v244, v238\n\t"
    "v_fmac_f32 v175, v244, v239\n\t"
    "s_waitcnt lgkmcnt(0)\n\t"
    "v_mfma_f32_16x16x32_bf16 v[64:67], a[112:115], a[72:75], v[64:67]\n\t"
    "v_mul_f32 v172, v44, v172\n\t"
    "v_mul_f32 v173, v45, v173\n\t"
    "v_mul_f32 v174, v46, v174\n\t"
    "v_mfma_f32_16x16x32_bf16 v[96:99], a[116:119], a[72:75], v[96:99]\n\t"
    "v_mul_f32 v175, v47, v175\n\t"
    "v_mul_f32 v44, v44, v247\n\t"
    "v_mul_f32 v45, v45, v247\n\t"
    "v_mfma_f32_16x16x32_bf16 v[100:103], a[116:119], a[76:79], v[100:103]\n\t"
    "v_mul_f32 v46, v46, v247\n\t"
    "v_mul_f32 v47, v47, v247\n\t"
    "v_mfma_f32_16x16x32_bf16 v[68:71], a[112:115], a[76:79], v[68:71]\n\t"
    "v_exp_f32 v44, v44\n\t"
    "v_mfma_f32_16x16x32_bf16 v[72:75], a[112:115], a[80:83], v[72:75]\n\t"
    "v_exp_f32 v45, v45\n\t"
    "v_mfma_f32_16x16x32_bf16 v[104:107], a[116:119], a[80:83], v[104:107]\n\t"
    "v_exp_f32 v46, v46\n\t"
    "v_exp_f32 v47, v47\n\t"
    "v_mfma_f32_16x16x32_bf16 v[108:111], a[116:119], a[84:87], v[108:111]\n\t"
    "v_fma_f32 v44, v44, v246, v246\n\t"
    "v_fma_f32 v45, v45, v246, v246\n\t"
    "v_fma_f32 v46, v46, v246, v246\n\t"
    "v_mfma_f32_16x16x32_bf16 v[76:79], a[112:115], a[84:87], v[76:79]\n\t"
    "v_fma_f32 v47, v47, v246, v246\n\t"
    "v_mfma_f32_16x16x32_bf16 v[80:83], a[112:115], a[88:91], v[80:83]\n\t"
    "v_rcp_f32 v44, v44\n\t"
    "v_rcp_f32 v45, v45\n\t"
    "v_mfma_f32_16x16x32_bf16 v[112:115], a[116:119], a[88:91], v[112:115]\n\t"
    "v_rcp_f32 v46, v46\n\t"
    "v_mfma_f32_16x16x32_bf16 v[116:119], a[116:119], a[92:95], v[116:119]\n\t"
    "v_rcp_f32 v47, v47\n\t"
    "v_mul_f32 v172, v172, v44\n\t"
    "v_mfma_f32_16x16x32_bf16 v[84:87], a[112:115], a[92:95], v[84:87]\n\t"
    "v_mul_f32 v173, v173, v45\n\t"
    "v_mul_f32 v174, v174, v46\n\t"
    "v_mul_f32 v175, v175, v47\n\t"
    "v_mfma_f32_16x16x32_bf16 v[88:91], a[112:115], a[96:99], v[88:91]\n\t"
    "v_cvt_pk_bf16_f32 v12, v140, v141\n\t"
    "v_cvt_pk_bf16_f32 v13, v142, v143\n\t"
    "v_mfma_f32_16x16x32_bf16 v[120:123], a[116:119], a[96:99], v[120:123]\n\t"
    "v_cvt_pk_bf16_f32 v14, v172, v173\n\t"
    "v_cvt_pk_bf16_f32 v15, v174, v175\n\t"
    "s_mov_b32 %[tmp], 442368\n\t"
    "v_mfma_f32_16x16x32_bf16 v[124:127], a[116:119], a[100:103], v[124:127]\n\t"
    "buffer_store_dwordx4 v[12:15], v210, %[srdw], %[tmp] offen sc1\n\t"
    "s_waitcnt lgkmcnt(0)\n\t"
    "ds_read_b128 v[244:247], v208 offset:1280\n\t"
    "v_mfma_f32_16x16x32_bf16 v[92:95], a[112:115], a[100:103], v[92:95]\n\t"
    "v_fmac_f32 v16, v240, v224\n\t"
    "v_fmac_f32 v17, v240, v225\n\t"
    "v_fmac_f32 v18, v240, v226\n\t"
    "v_mfma_f32_16x16x32_bf16 a[156:159], a[128:131], a[100:103], a[156:159]\n\t"
    "v_fmac_f32 v19, v240, v227\n\t"
    "v_fmac_f32 v144, v240, v232\n\t"
    "v_fmac_f32 v145, v240, v233\n\t"
    "v_mfma_f32_16x16x32_bf16 a[188:191], a[132:135], a[100:103], a[188:191]\n\t"
    "v_fmac_f32 v146, v240, v234\n\t"
    "v_fmac_f32 v147, v240, v235\n\t"
    "v_mfma_f32_16x16x32_bf16 a[184:187], a[132:135], a[96:99], a[184:187]\n\t"
    "v_mul_f32 v144, v16, v144\n\t"
    "v_mul_f32 v145, v17, v145\n\t"
    "v_mul_f32 v146, v18, v146\n\t"
    "v_mfma_f32_16x16x32_bf16 a[152:155], a[128:131], a[96:99], a[152:155]\n\t"
    "v_mul_f32 v147, v19, v147\n\t"
    "v_mul_f32 v16, v16, v243\n\t"
    "v_mfma_f32_16x16x32_bf16 a[148:151], a[128:131], a[92:95], a[148:151]\n\t"
    "v_mul_f32 v17, v17, v243\n\t"
    "v_mul_f32 v18, v18, v243\n\t"
    "v_mul_f32 v19, v19, v243\n\t"
    "v_mfma_f32_16x16x32_bf16 a[180:183], a[132:135], a[92:95], a[180:183]\n\t"
    "v_exp_f32 v16, v16\n\t"
    "v_mfma_f32_16x16x32_bf16 a[176:179], a[132:135], a[88:91], a[176:179]\n\t"
    "v_exp_f32 v17, v17\n\t"
    "v_mfma_f32_16x16x32_bf16 a[144:147], a[128:131], a[88:91], a[144:147]\n\t"
    "v_exp_f32 v18, v18\n\t"
    "v_exp_f32 v19, v19\n\t"
    "v_mfma_f32_16x16x32_bf16 v[204:207], a[128:131], a[84:87], v[204:207]\n\t"
    "v_fma_f32 v16, v16, v242, v242\n\t"
    "v_fma_f32 v17, v17, v242, v242\n\t"
    "v_mfma_f32_16x16x32_bf16 a[172:175], a[132:135], a[84:87], a[172:175]\n\t"
    "v_fma_f32 v18, v18, v242, v242\n\t"
    "v_fma_f32 v19, v19, v242, v242\n\t"
    "v_mfma_f32_16x16x32_bf16 a[168:171], a[132:135], a[80:83], a[168:171]\n\t"
    "v_rcp_f32 v16, v16\n\t"
    "v_rcp_f32 v17, v17\n\t"
    "v_mfma_f32_16x16x32_bf16 v[200:203], a[128:131], a[80:83], v[200:203]\n\t"
    "v_rcp_f32 v18, v18\n\t"
    "v_mfma_f32_16x16x32_bf16 v[196:199], a[128:131], a[76:79], v[196:199]\n\t"
    "v_rcp_f32 v19, v19\n\t"
    "v_mul_f32 v144, v144, v16\n\t"
    "v_mfma_f32_16x16x32_bf16 a[164:167], a[132:135], a[76:79], a[164:167]\n\t"
    "v_mul_f32 v145, v145, v17\n\t"
    "v_mul_f32 v146, v146, v18\n\t"
    "v_mfma_f32_16x16x32_bf16 a[160:163], a[132:135], a[72:75], a[160:163]\n\t"
    "v_mul_f32 v147, v147, v19\n\t"
    "v_fmac_f32 v48, v240, v228\n\t"
    "v_fmac_f32 v49, v240, v229\n\t"
    "v_mfma_f32_16x16x32_bf16 v[192:195], a[128:131], a[72:75], v[192:195]\n\t"
    "v_fmac_f32 v50, v240, v230\n\t"
    "v_fmac_f32 v51, v240, v231\n\t"
    "v_mfma_f32_16x16x32_bf16 a[192:195], a[136:139], a[72:75], a[192:195]\n\t"
    "v_fmac_f32 v176, v240, v236\n\t"
    "v_fmac_f32 v177, v240, v237\n\t"
    "v_fmac_f32 v178, v240, v238\n\t"
    "v_mfma_f32_16x16x32_bf16 a[224:227], a[140:143], a[72:75], a[224:227]\n\t"
    "v_fmac_f32 v179, v240, v239\n\t"
    "v_mul_f32 v176, v48, v176\n\t"
    "v_mul_f32 v177, v49, v177\n\t"
    "v_mfma_f32_16x16x32_bf16 a[228:231], a[140:143], a[76:79], a[228:231]\n\t"
    "v_mul_f32 v178, v50, v178\n\t"
    "v_mul_f32 v179, v51, v179\n\t"
    "v_mfma_f32_16x16x32_bf16 a[196:199], a[136:139], a[76:79], a[196:199]\n\t"
    "v_mul_f32 v48, v48, v243\n\t"
    "v_mul_f32 v49, v49, v243\n\t"
    "v_mul_f32 v50, v50, v243\n\t"
    "v_mfma_f32_16x16x32_bf16 a[200:203], a[136:139], a[80:83], a[200:203]\n\t"
    "v_mul_f32 v51, v51, v243\n\t"
    "v_mfma_f32_16x16x32_bf16 a[232:235], a[140:143], a[80:83], a[232:235]\n\t"
    "v_exp_f32 v48, v48\n\t"
    "v_exp_f32 v49, v49\n\t"
    "v_mfma_f32_16x16x32_bf16 a[236:239], a[140:143], a[84:87], a[236:239]\n\t"
    "v_exp_f32 v50, v50\n\t"
    "v_mfma_f32_16x16x32_bf16 a[204:207], a[136:139], a[84:87], a[204:207]\n\t"
    "v_exp_f32 v51, v51\n\t"
    "v_fma_f32 v48, v48, v242, v242\n\t"
    "v_mfma_f32_16x16x32_bf16 a[208:211], a[136:139], a[88:91], a[208:211]\n\t"
    "v_fma_f32 v49, v49, v242, v242\n\t"
    "v_fma_f32 v50, v50, v242, v242\n\t"
    "v_fma_f32 v51, v51, v242, v242\n\t"
    "v_mfma_f32_16x16x32_bf16 a[240:243], a[140:143], a[88:91], a[240:243]\n\t"
    "v_rcp_f32 v48, v48\n\t"
    "v_mfma_f32_16x16x32_bf16 a[244:247], a[140:143], a[92:95], a[244:247]\n\t"
    "v_rcp_f32 v49, v49\n\t"
    "v_mfma_f32_16x16x32_bf16 a[212:215], a[136:139], a[92:95], a[212:215]\n\t"
    "v_rcp_f32 v50, v50\n\t"
    "v_rcp_f32 v51, v51\n\t"
    "v_mfma_f32_16x16x32_bf16 a[216:219], a[136:139], a[96:99], a[216:219]\n\t"
    "v_mul_f32 v176, v176, v48\n\t"
    "v_mul_f32 v177, v177, v49\n\t"
    "v_mfma_f32_16x16x32_bf16 a[248:251], a[140:143], a[96:99], a[248:251]\n\t"
    "v_mul_f32 v178, v178, v50\n\t"
    "v_mul_f32 v179, v179, v51\n\t"
    "v_cvt_pk_bf16_f32 v16, v144, v145\n\t"
    "v_mfma_f32_16x16x32_bf16 a[252:255], a[140:143], a[100:103], a[252:255]\n\t"
    "v_cvt_pk_bf16_f32 v17, v146, v147\n\t"
    "v_cvt_pk_bf16_f32 v18, v176, v177\n\t"
    "v_mfma_f32_16x16x32_bf16 a[220:223], a[136:139], a[100:103], a[220:223]\n\t"
    "v_cvt_pk_bf16_f32 v19, v178, v179\n\t"
    "s_mov_b32 %[tmp], 589824\n\t"
    "buffer_store_dwordx4 v[16:19], v210, %[srdw], %[tmp] offen sc1\n\t"
    "s_waitcnt lgkmcnt(0)\n\t"
    "s_waitcnt lgkmcnt(0)\n\t"
    "ds_read_b128 v[240:243], v208 offset:1536\n\t"
    "v_mov_b32 v250, v247\n\t"
    "v_pk_fma_f32 v[20:21], v[244:245], v[224:225], v[20:21] op_sel_hi:[0,1,1]\n\t"
    "v_pk_fma_f32 v[22:23], v[244:245], v[226:227], v[22:23] op_sel_hi:[0,1,1]\n\t"
    "v_pk_fma_f32 v[52:53], v[244:245], v[228:229], v[52:53] op_sel_hi:[0,1,1]\n\t"
    "v_pk_fma_f32 v[54:55], v[244:245], v[230:231], v[54:55] op_sel_hi:[0,1,1]\n\t"
    "v_pk_fma_f32 v[148:149], v[244:245], v[232:233], v[148:149] op_sel_hi:[0,1,1]\n\t"
    "v_pk_fma_f32 v[150:151], v[244:245], v[234:235], v[150:151] op_sel_hi:[0,1,1]\n\t"
    "v_pk_fma_f32 v[180:181], v[244:245], v[236:237], v[180:181] op_sel_hi:[0,1,1]\n\t"
    "v_pk_fma_f32 v[182:183], v[244:245], v[238:239], v[182:183] op_sel_hi:[0,1,1]\n\t"
    "v_pk_mul_f32 v[148:149], v[20:21], v[148:149]\n\t"
    "v_pk_mul_f32 v[150:151], v[22:23], v[150:151]\n\t"
    "v_pk_mul_f32 v[180:181], v[52:53], v[180:181]\n\t"
    "v_pk_mul_f32 v[182:183], v[54:55], v[182:183]\n\t"
    "v_pk_mul_f32 v[20:21], v[20:21], v[250:251] op_sel_hi:[1,0]\n\t"
    "v_pk_mul_f32 v[22:23], v[22:23], v[250:251] op_sel_hi:[1,0]\n\t"
    "v_pk_mul_f32 v[52:53], v[52:53], v[250:251] op_sel_hi:[1,0]\n\t"
    "v_pk_mul_f32 v[54:55], v[54:55], v[250:251] op_sel_hi:[1,0]\n\t"
    "v_exp_f32 v20, v20\n\t"
    "v_exp_f32 v22, v22\n\t"
    "v_exp_f32 v52, v52\n\t"
    "v_exp_f32 v54, v54\n\t"
    "v_exp_f32 v21, v21\n\t"
    "v_exp_f32 v23, v23\n\t"
    "v_exp_f32 v53, v53\n\t"
    "v_exp_f32 v55, v55\n\t"
    "v_pk_fma_f32 v[20:21], v[20:21], v[246:247], v[246:247] op_sel_hi:[1,0,0]\n\t"
    "v_pk_fma_f32 v[22:23], v[22:23], v[246:247], v[246:247] op_sel_hi:[1,0,0]\n\t"
    "v_pk_fma_f32 v[52:53], v[52:53], v[246:247], v[246:247] op_sel_hi:[1,0,0]\n\t"
    "v_pk_fma_f32 v[54:55], v[54:55], v[246:247], v[246:247] op_sel_hi:[1,0,0]\n\t"
    "v_rcp_f32 v20, v20\n\t"
    "v_rcp_f32 v22, v22\n\t"
    "v_rcp_f32 v52, v52\n\t"
    "v_rcp_f32 v54, v54\n\t"
    "v_rcp_f32 v21, v21\n\t"
    "v_rcp_f32 v23, v23\n\t"
    "v_rcp_f32 v53, v53\n\t"
    "v_rcp_f32 v55, v55\n\t"
    "v_pk_mul_f32 v[148:149], v[148:149], v[20:21]\n\t"
    "v_pk_mul_f32 v[150:151], v[150:151], v[22:23]\n\t"
    "v_pk_mul_f32 v[180:181], v[180:181], v[52:53]\n\t"
    "v_pk_mul_f32 v[182:183], v[182:183], v[54:55]\n\t"
    "v_cvt_pk_bf16_f32 v20, v148, v149\n\t"
    "v_cvt_pk_bf16_f32 v21, v150, v151\n\t"
    "v_cvt_pk_bf16_f32 v22, v180, v181\n\t"
    "v_cvt_pk_bf16_f32 v23, v182, v183\n\t"
    "s_mov_b32 %[tmp], 737280\n\t"
    "buffer_store_dwordx4 v[20:23], v210, %[srdw], %[tmp] offen sc1\n\t"
    "s_waitcnt lgkmcnt(0)\n\t"
    "ds_read_b128 v[244:247], v208 offset:1792\n\t"
    "v_mov_b32 v250, v243\n\t"
    "v_pk_fma_f32 v[24:25], v[240:241], v[224:225], v[24:25] op_sel_hi:[0,1,1]\n\t"
    "v_pk_fma_f32 v[26:27], v[240:241], v[226:227], v[26:27] op_sel_hi:[0,1,1]\n\t"
    "v_pk_fma_f32 v[56:57], v[240:241], v[228:229], v[56:57] op_sel_hi:[0,1,1]\n\t"
    "v_pk_fma_f32 v[58:59], v[240:241], v[230:231], v[58:59] op_sel_hi:[0,1,1]\n\t"
    "v_pk_fma_f32 v[152:153], v[240:241], v[232:233], v[152:153] op_sel_hi:[0,1,1]\n\t"
    "v_pk_fma_f32 v[154:155], v[240:241], v[234:235], v[154:155] op_sel_hi:[0,1,1]\n\t"
    "v_pk_fma_f32 v[184:185], v[240:241], v[236:237], v[184:185] op_sel_hi:[0,1,1]\n\t"
    "v_pk_fma_f32 v[186:187], v[240:241], v[238:239], v[186:187] op_sel_hi:[0,1,1]\n\t"
    "v_pk_mul_f32 v[152:153], v[24:25], v[152:153]\n\t"
    "v_pk_mul_f32 v[154:155], v[26:27], v[154:155]\n\t"
    "v_pk_mul_f32 v[184:185], v[56:57], v[184:185]\n\t"
    "v_pk_mul_f32 v[186:187], v[58:59], v[186:187]\n\t"
    "v_pk_mul_f32 v[24:25], v[24:25], v[250:251] op_sel_hi:[1,0]\n\t"
    "v_pk_mul_f32 v[26:27], v[26:27], v[250:251] op_sel_hi:[1,0]\n\t"
    "v_pk_mul_f32 v[56:57], v[56:57], v[250:251] op_sel_hi:[1,0]\n\t"
    "v_pk_mul_f32 v[58:59], v[58:59], v[250:251] op_sel_hi:[1,0]\n\t"
    "v_exp_f32 v24, v24\n\t"
    "v_exp_f32 v26, v26\n\t"
    "v_exp_f32 v56, v56\n\t"
    "v_exp_f32 v58, v58\n\t"
    "v_exp_f32 v25, v25\n\t"
    "v_exp_f32 v27, v27\n\t"
    "v_exp_f32 v57, v57\n\t"
    "v_exp_f32 v59, v59\n\t"
    "v_pk_fma_f32 v[24:25], v[24:25], v[242:243], v[242:243] op_sel_hi:[1,0,0]\n\t"
    "v_pk_fma_f32 v[26:27], v[26:27], v[242:243], v[242:243] op_sel_hi:[1,0,0]\n\t"
    "v_pk_fma_f32 v[56:57], v[56:57], v[242:243], v[242:243] op_sel_hi:[1,0,0]\n\t"
    "v_pk_fma_f32 v[58:59], v[58:59], v[242:243], v[242:243] op_sel_hi:[1,0,0]\n\t"
    "v_rcp_f32 v24, v24\n\t"
    "v_rcp_f32 v26, v26\n\t"
    "v_rcp_f32 v56, v56\n\t"
    "v_rcp_f32 v58, v58\n\t"
    "v_rcp_f32 v25, v25\n\t"
    "v_rcp_f32 v27, v27\n\t"
    "v_rcp_f32 v57, v57\n\t"
    "v_rcp_f32 v59, v59\n\t"
    "v_pk_mul_f32 v[152:153], v[152:153], v[24:25]\n\t"
    "v_pk_mul_f32 v[154:155], v[154:155], v[26:27]\n\t"
    "v_pk_mul_f32 v[184:185], v[184:185], v[56:57]\n\t"
    "v_pk_mul_f32 v[186:187], v[186:187], v[58:59]\n\t"
    "v_cvt_pk_bf16_f32 v24, v152, v153\n\t"
    "v_cvt_pk_bf16_f32 v25, v154, v155\n\t"
    "v_cvt_pk_bf16_f32 v26, v184, v185\n\t"
    "v_cvt_pk_bf16_f32 v27, v186, v187\n\t"
    "s_mov_b32 %[tmp], 884736\n\t"
    "buffer_store_dwordx4 v[24:27], v210, %[srdw], %[tmp] offen sc1\n\t"
    "s_waitcnt lgkmcnt(0)\n\t"
    "v_mov_b32 v250, v247\n\t"
    "v_pk_fma_f32 v[28:29], v[244:245], v[224:225], v[28:29] op_sel_hi:[0,1,1]\n\t"
    "v_pk_fma_f32 v[30:31], v[244:245], v[226:227], v[30:31] op_sel_hi:[0,1,1]\n\t"
    "v_pk_fma_f32 v[60:61], v[244:245], v[228:229], v[60:61] op_sel_hi:[0,1,1]\n\t"
    "v_pk_fma_f32 v[62:63], v[244:245], v[230:231], v[62:63] op_sel_hi:[0,1,1]\n\t"
    "v_pk_fma_f32 v[156:157], v[244:245], v[232:233], v[156:157] op_sel_hi:[0,1,1]\n\t"
    "v_pk_fma_f32 v[158:159], v[244:245], v[234:235], v[158:159] op_sel_hi:[0,1,1]\n\t"
    "v_pk_fma_f32 v[188:189], v[244:245], v[236:237], v[188:189] op_sel_hi:[0,1,1]\n\t"
    "v_pk_fma_f32 v[190:191], v[244:245], v[238:239], v[190:191] op_sel_hi:[0,1,1]\n\t"
    "v_pk_mul_f32 v[156:157], v[28:29], v[156:157]\n\t"
    "v_pk_mul_f32 v[158:159], v[30:31], v[158:159]\n\t"
    "v_pk_mul_f32 v[188:189], v[60:61], v[188:189]\n\t"
    "v_pk_mul_f32 v[190:191], v[62:63], v[190:191]\n\t"
    "v_pk_mul_f32 v[28:29], v[28:29], v[250:251] op_sel_hi:[1,0]\n\t"
    "v_pk_mul_f32 v[30:31], v[30:31], v[250:251] op_sel_hi:[1,0]\n\t"
    "v_pk_mul_f32 v[60:61], v[60:61], v[250:251] op_sel_hi:[1,0]\n\t"
    "v_pk_mul_f32 v[62:63], v[62:63], v[250:251] op_sel_hi:[1,0]\n\t"
    "v_exp_f32 v28, v28\n\t"
    "v_exp_f32 v30, v30\n\t"
    "v_exp_f32 v60, v60\n\t"
    "v_exp_f32 v62, v62\n\t"
    "v_exp_f32 v29, v29\n\t"
    "v_exp_f32 v31, v31\n\t"
    "v_exp_f32 v61, v61\n\t"
    "v_exp_f32 v63, v63\n\t"
    "v_pk_fma_f32 v[28:29], v[28:29], v[246:247], v[246:247] op_sel_hi:[1,0,0]\n\t"
    "v_pk_fma_f32 v[30:31], v[30:31], v[246:247], v[246:247] op_sel_hi:[1,0,0]\n\t"
    "v_pk_fma_f32 v[60:61], v[60:61], v[246:247], v[246:247] op_sel_hi:[1,0,0]\n\t"
    "v_pk_fma_f32 v[62:63], v[62:63], v[246:247], v[246:247] op_sel_hi:[1,0,0]\n\t"
    "v_rcp_f32 v28, v28\n\t"
    "v_rcp_f32 v30, v30\n\t"
    "v_rcp_f32 v60, v60\n\t"
    "v_rcp_f32 v62, v62\n\t"
    "v_rcp_f32 v29, v29\n\t"
    "v_rcp_f32 v31, v31\n\t"
    "v_rcp_f32 v61, v61\n\t"
    "v_rcp_f32 v63, v63\n\t"
    "v_pk_mul_f32 v[156:157], v[156:157], v[28:29]\n\t"
    "v_pk_mul_f32 v[158:159], v[158:159], v[30:31]\n\t"
    "v_pk_mul_f32 v[188:189], v[188:189], v[60:61]\n\t"
    "v_pk_mul_f32 v[190:191], v[190:191], v[62:63]\n\t"
    "v_cvt_pk_bf16_f32 v28, v156, v157\n\t"
    "v_cvt_pk_bf16_f32 v29, v158, v159\n\t"
    "v_cvt_pk_bf16_f32 v30, v188, v189\n\t"
    "v_cvt_pk_bf16_f32 v31, v190, v191\n\t"
    "s_mov_b32 %[tmp], 1032192\n\t"
    "buffer_store_dwordx4 v[28:31], v210, %[srdw], %[tmp] offen sc1\n\t"
    "v_and_b32 v224, 0xf0, v248\n\t"
    "v_add_u32 v224, %[lrowr], v224\n\t"
    "v_lshrrev_b32 v225, 4, v248\n\t"
    "v_and_b32 v225, 0x30, v225\n\t"
    "v_add_u32 v226, %[lcsr], v225\n\t"
    "ds_read_b128 v[160:163], v226 offset:128\n\t"
    "ds_read_b128 v[164:167], v226 offset:192\n\t"
    "ds_read_b128 v[168:171], v226 offset:384\n\t"
    "ds_read_b128 v[172:175], v226 offset:448\n\t"
    "ds_read_b128 v[128:131], v224 offset:0\n\t"
    "ds_read_b128 v[132:135], v224 offset:256\n\t"
    "ds_read_b128 v[136:139], v224 offset:512\n\t"
    "ds_read_b128 v[140:143], v224 offset:768\n\t"
    "ds_read_b128 v[144:147], v224 offset:1024\n\t"
    "ds_read_b128 v[148:151], v224 offset:1280\n\t"
    "ds_read_b128 v[152:155], v224 offset:1536\n\t"
    "ds_read_b128 v[156:159], v224 offset:1792\n\t"
    "ds_read_b128 v[176:179], v226 offset:512\n\t"
    "ds_read_b128 v[180:183], v226 offset:576\n\t"
    "s_barrier\n\t"
    "s_branch 9f\n\t"
    "140:\n\t"
    "s_cmp_lg_u32 %[wr], 0\n\t"
    "s_cbranch_scc1 141f\n\t"
    "v_mfma_f32_16x16x32_bf16 v[224:227], a[32:35], v[244:247], v[224:227]\n\t"
    "v_mfma_f32_16x16x32_bf16 v[228:231], a[36:39], v[244:247], v[228:231]\n\t"
    "v_mfma_f32_16x16x32_bf16 v[232:235], a[40:43], v[244:247], v[232:235]\n\t"
    "v_mfma_f32_16x16x32_bf16 v[236:239], a[44:47], v[244:247], v[236:239]\n\t"
    "v_mfma_f32_16x16x32_bf16 v[240:243], a[48:51], v[244:247], v[240:243]\n\t"
    "s_branch 24b\n\t"
    "141:\n\t"
    "v_mfma_f32_16x16x32_bf16 v[224:227], a[52:55], v[244:247], v[224:227]\n\t"
    "v_mfma_f32_16x16x32_bf16 v[228:231], a[56:59], v[244:247], v[228:231]\n\t"
    "v_mfma_f32_16x16x32_bf16 v[232:235], a[60:63], v[244:247], v[232:235]\n\t"
    "v_mfma_f32_16x16x32_bf16 v[236:239], a[64:67], v[244:247], v[236:239]\n\t"
    "v_mfma_f32_16x16x32_bf16 v[240:243], a[68:71], v[244:247], v[240:243]\n\t"
    "s_branch 24b\n\t"
    "100:\n\t"
    "s_cmp_lg_u32 %[wr], 0\n\t"
    "s_cbranch_scc1 101f\n\t"
    "v_mfma_f32_16x16x32_bf16 v[224:227], a[32:35], v[244:247], v[224:227]\n\t"
    "v_mfma_f32_16x16x32_bf16 v[228:231], a[36:39], v[244:247], v[228:231]\n\t"
    "v_mfma_f32_16x16x32_bf16 v[232:235], a[40:43], v[244:247], v[232:235]\n\t"
    "v_mfma_f32_16x16x32_bf16 v[236:239], a[44:47], v[244:247], v[236:239]\n\t"
    "v_mfma_f32_16x16x32_bf16 v[240:243], a[48:51], v[244:247], v[240:243]\n\t"
    "s_branch 20b\n\t"
    "101:\n\t"
    "v_mfma_f32_16x16x32_bf16 v[224:227], a[52:55], v[244:247], v[224:227]\n\t"
    "v_mfma_f32_16x16x32_bf16 v[228:231], a[56:59], v[244:247], v[228:231]\n\t"
    "v_mfma_f32_16x16x32_bf16 v[232:235], a[60:63], v[244:247], v[232:235]\n\t"
    "v_mfma_f32_16x16x32_bf16 v[236:239], a[64:67], v[244:247], v[236:239]\n\t"
    "v_mfma_f32_16x16x32_bf16 v[240:243], a[68:71], v[244:247], v[240:243]\n\t"
    "s_branch 20b\n\t"
    "110:\n\t"
    "s_cmp_lg_u32 %[wr], 0\n\t"
    "s_cbranch_scc1 111f\n\t"
    "v_mfma_f32_16x16x32_bf16 v[224:227], a[104:107], v[244:247], v[224:227]\n\t"
    "v_mfma_f32_16x16x32_bf16 v[228:231], a[108:111], v[244:247], v[228:231]\n\t"
    "v_mfma_f32_16x16x32_bf16 v[232:235], a[112:115], v[244:247], v[232:235]\n\t"
    "v_mfma_f32_16x16x32_bf16 v[236:239], a[116:119], v[244:247], v[236:239]\n\t"
    "v_mfma_f32_16x16x32_bf16 v[240:243], a[120:123], v[244:247], v[240:243]\n\t"
    "s_branch 21b\n\t"
    "111:\n\t"
    "v_mfma_f32_16x16x32_bf16 v[224:227], a[124:127], v[244:247], v[224:227]\n\t"
    "v_mfma_f32_16x16x32_bf16 v[228:231], a[128:131], v[244:247], v[228:231]\n\t"
    "v_mfma_f32_16x16x32_bf16 v[232:235], a[132:135], v[244:247], v[232:235]\n\t"
    "v_mfma_f32_16x16x32_bf16 v[236:239], a[136:139], v[244:247], v[236:239]\n\t"
    "v_mfma_f32_16x16x32_bf16 v[240:243], a[140:143], v[244:247], v[240:243]\n\t"
    "s_branch 21b\n\t"
    "120:\n\t"
    "s_cmp_lg_u32 %[wr], 0\n\t"
    "s_cbranch_scc1 121f\n\t"
    "v_mfma_f32_16x16x32_bf16 v[224:227], a[32:35], v[244:247], v[224:227]\n\t"
    "v_mfma_f32_16x16x32_bf16 v[228:231], a[36:39], v[244:247], v[228:231]\n\t"
    "v_mfma_f32_16x16x32_bf16 v[232:235], a[40:43], v[244:247], v[232:235]\n\t"
    "v_mfma_f32_16x16x32_bf16 v[236:239], a[44:47], v[244:247], v[236:239]\n\t"
    "v_mfma_f32_16x16x32_bf16 v[240:243], a[48:51], v[244:247], v[240:243]\n\t"
    "s_branch 22b\n\t"
    "121:\n\t"
    "v_mfma_f32_16x16x32_bf16 v[224:227], a[52:55], v[244:247], v[224:227]\n\t"
    "v_mfma_f32_16x16x32_bf16 v[228:231], a[56:59], v[244:247], v[228:231]\n\t"
    "v_mfma_f32_16x16x32_bf16 v[232:235], a[60:63], v[244:247], v[232:235]\n\t"
    "v_mfma_f32_16x16x32_bf16 v[236:239], a[64:67], v[244:247], v[236:239]\n\t"
    "v_mfma_f32_16x16x32_bf16 v[240:243], a[68:71], v[244:247], v[240:243]\n\t"
    "s_branch 22b\n\t"
    "130:\n\t"
    "s_cmp_lg_u32 %[wr], 0\n\t"
    "s_cbranch_scc1 131f\n\t"
    "v_mfma_f32_16x16x32_bf16 v[224:227], a[104:107], v[244:247], v[224:227]\n\t"
    "v_mfma_f32_16x16x32_bf16 v[228:231], a[108:111], v[244:247], v[228:231]\n\t"
    "v_mfma_f32_16x16x32_bf16 v[232:235], a[112:115], v[244:247], v[232:235]\n\t"
    "v_mfma_f32_16x16x32_bf16 v[236:239], a[116:119], v[244:247], v[236:239]\n\t"
    "v_mfma_f32_16x16x32_bf16 v[240:243], a[120:123], v[244:247], v[240:243]\n\t"
    "s_branch 23b\n\t"
    "131:\n\t"
    "v_mfma_f32_16x16x32_bf16 v[224:227], a[124:127], v[244:247], v[224:227]\n\t"
    "v_mfma_f32_16x16x32_bf16 v[228:231], a[128:131], v[244:247], v[228:231]\n\t"
    "v_mfma_f32_16x16x32_bf16 v[232:235], a[132:135], v[244:247], v[232:235]\n\t"
    "v_mfma_f32_16x16x32_bf16 v[236:239], a[136:139], v[244:247], v[236:239]\n\t"
    "v_mfma_f32_16x16x32_bf16 v[240:243], a[140:143], v[244:247], v[240:243]\n\t"
    "s_branch 23b\n\t"
    "31:\n\t"
    "v_and_b32 v249, 0xf0, v248\n\t"
    "v_cmp_eq_u32 vcc, 16, v249\n\t"
    "s_nop 1\n\t"
    "v_cndmask_b32 v224, v224, v228, vcc\n\t"
    "v_cndmask_b32 v225, v225, v229, vcc\n\t"
    "v_cndmask_b32 v226, v226, v230, vcc\n\t"
    "v_cndmask_b32 v227, v227, v231, vcc\n\t"
    "v_cmp_eq_u32 vcc, 32, v249\n\t"
    "s_nop 1\n\t"
    "v_cndmask_b32 v224, v224, v232, vcc\n\t"
    "v_cndmask_b32 v225, v225, v233, vcc\n\t"
    "v_cndmask_b32 v226, v226, v234, vcc\n\t"
    "v_cndmask_b32 v227, v227, v235, vcc\n\t"
    "v_cmp_eq_u32 vcc, 48, v249\n\t"
    "s_nop 1\n\t"
    "v_cndmask_b32 v224, v224, v236, vcc\n\t"
    "v_cndmask_b32 v225, v225, v237, vcc\n\t"
    "v_cndmask_b32 v226, v226, v238, vcc\n\t"
    "v_cndmask_b32 v227, v227, v239, vcc\n\t"
    "v_cmp_eq_u32 vcc, 64, v249\n\t"
    "s_nop 1\n\t"
    "v_cndmask_b32 v224, v224, v240, vcc\n\t"
    "v_cndmask_b32 v225, v225, v241, vcc\n\t"
    "v_cndmask_b32 v226, v226, v242, vcc\n\t"
    "v_cndmask_b32 v227, v227, v243, vcc\n\t"
    "v_lshrrev_b32 v250, 8, v248\n\t"
    "v_lshlrev_b32 v250, 4, v250\n\t"
    "v_lshl_add_u32 v250, v249, 2, v250\n\t"
    "v_add_u32 v250, %[csst], v250\n\t"
    "v_mov_b32 v251, 0x40000000\n\t"
    "v_cmp_gt_u32 vcc, 80, v249\n\t"
    "s_nop 1\n\t"
    "v_cndmask_b32 v249, v251, v250, vcc\n\t"
    "buffer_store_dwordx4 v[224:227], v249, %[srdw], 0 offen sc1\n\t"
    "s_mov_b64 vcc, 0\n\t"
    "s_branch 34b\n\t"
    "33:\n\t"
    "s_cmp_eq_u32 %[cnt], 1\n\t"
    "s_cbranch_scc1 35f\n\t"
    "s_add_u32 %[tmp], %[cnt], 1\n\t"
    "s_cmp_eq_u32 %[tmp], %[mine]\n\t"
    "s_cbranch_scc1 31b\n\t"
    "s_cmp_lg_u32 %[w], 0\n\t"
    "s_cbranch_scc1 34b\n\t"
    "v_mov_b32 v250, s90\n\t"
    "v_mov_b32 v251, s91\n\t"
    "v_mov_b32 v249, %[fcsp]\n\t"
    "s_mov_b64 exec, 1\n\t"
    "buffer_store_dwordx2 v[250:251], v249, %[srdw], 0 offen sc1\n\t"
    "s_mov_b64 exec, -1\n\t"
    "s_branch 34b\n\t"
    "35:\n\t"
    "v_and_b32 v249, 0x70, v248\n\t"
    "v_lshrrev_b32 v249, 1, v249\n\t"
    "v_add_u32 v249, %[fcs], v249\n\t"
    "buffer_load_dwordx2 v[250:251], v249, %[srdw], 0 offen sc1\n\t"
    "s_branch 34b\n\t"
    "36:\n\t"
    "s_mov_b32 %[tmp], 0x10000\n\t"
    "37:\n\t"
    "s_sleep 2\n\t"
    "buffer_load_dwordx2 v[250:251], v249, %[srdw], 0 offen sc1\n\t"
    "s_sub_u32 %[tmp], %[tmp], 1\n\t"
    "s_waitcnt vmcnt(0)\n\t"
    "s_cbranch_scc1 38b\n\t"
    "v_cmp_ne_u64 vcc, s[90:91], v[250:251]\n\t"
    "s_cbranch_vccnz 37b\n\t"
    "s_branch 38b\n\t"
    "41:\n\t"
    "s_mov_b32 %[tmp], 0x10000\n\t"
    "v_lshrrev_b32 v249, 1, v248\n\t"
    "v_add_u32 v252, %[stld], v249\n\t"
    "43:\n\t"
    "s_sleep 2\n\t"
    "buffer_load_dwordx2 v[224:225], v252, %[srdw], 0 offen sc1\n\t"
    "s_sub_u32 %[tmp], %[tmp], 1\n\t"
    "s_waitcnt vmcnt(0)\n\t"
    "s_cbranch_scc1 42b\n\t"
    "v_cmp_ne_u32 vcc, s90, v225\n\t"
    "s_cbranch_vccnz 43b\n\t"
    "s_branch 42b\n\t"
    "9:\n\t"
        : "={v[0:15]}"(c[0]), "={v[16:31]}"(c[1]), "={v[32:47]}"(c[2]), "={v[48:63]}"(c[3]), "={v[64:79]}"(c[4]), "={v[80:95]}"(c[5]), "={v[96:111]}"(c[6]), "={v[112:127]}"(c[7]), "={v[128:143]}"(c[8]), "={v[144:159]}"(c[9]), "={v[160:175]}"(c[10]), "={v[176:191]}"(c[11]), "={v[192:207]}"(c[12]), "+{v[208:223]}"(a0), G1_IO : G1_IN0, [aost] "s"((unsigned)WS_AO + (16 * p + 8 * wr) * 147456 + (5 * col + odd + wc) * 1024) : G1_CLOB);
  }
  asm volatile("" : "+s"(bid), "+s"(w));
  col = map_col(bid); p = (bid >> 3) & 7; wr = w >> 1; wc = w & 1; odd = col & 1;
  const int role = map_role(bid), pm2 = map_pm2(bid), pn2 = map_pn2(bid), tile = pm2 * 8 + pn2;
  u32x16 a0, h0; u32x8 h1; unsigned kb[RING + 1]; const unsigned fp1 = WS_FLAG + FL_P1 + 1024, fh = fp1 + p * 10240 + (col < 8 ? 8192 + (col ^ 4) * 128 : (col - 16) * 128); const f32x4 kc = {1.4426950408889634f, 0.f, 1e-6f, 0.f};
  { const unsigned lx = ldsb + LDS_X, aob = WS_AO + (16 * p + 8 * wr) * 147456; unsigned tmp, tmp2, tmpa;
#define E_IO "+{v[0:15]}"(c[0]), "+{v[16:31]}"(c[1]), "+{v[32:47]}"(c[2]), "+{v[48:63]}"(c[3]), "+{v[64:79]}"(c[4]), "+{v[80:95]}"(c[5]), "+{v[96:111]}"(c[6]), "+{v[112:127]}"(c[7]), "+{v[128:143]}"(c[8]), "+{v[144:159]}"(c[9]), "+{v[160:175]}"(c[10]), "+{v[176:191]}"(c[11]), "+{v[192:207]}"(c[12]), "={v[208:223]}"(a0), "={v[224:239]}"(h0), "={v[240:247]}"(h1), [kb0] "=&s"(kb[0]), [kb1] "=&s"(kb[1]), [kb2] "=&s"(kb[2]), [kb3] "=&s"(kb[3]), [kb4] "=&s"(kb[4]), [tmp] "=&s"(tmp), [tmp2] "=&s"(tmp2), [tmpa] "=&s"(tmpa)
#define E_IN "{v248}"(l16), "{s[90:91]}"(tag), "{s[92:95]}"(kc), [srdw] "s"(srdw), [srda] "s"(srda2), [srdb] "s"(srdb), [w] "s"(w), [m0b] "s"(ldsb + w * 2048), \
      [rot] "s"(ROT ? (pm2 + pn2) & 3 : 0), [av0] "s"((w * 4 + role) * 147456), [av1] "s"((w * 4 + ((role + 1) & 3)) * 147456), [av2] "s"((w * 4 + ((role + 2) & 3)) * 147456), [av3] "s"((w * 4 + ((role + 3) & 3)) * 147456), \
      [bvc] "s"(w * 32768), [ldsb] "s"(ldsb), [role] "s"(role)
#define E_CLOB "memory", "scc", "vcc", "s96", "s97", "v249", "v250", "v251", "v252", "v253", "v254", "v255"
    if constexpr (TA) { const unsigned kvo = WS_KV + (2 * col + wc) * 65536;
      asm volatile(
    "v_add_u32 v227, %[aost], v248\n\t"
    "v_lshrrev_b32 v249, 4, v248\n\t"
    "v_add_u32 v250, %[rot], v249\n\t"
    "v_bfi_b32 v250, 3, v250, v249\n\t"
    "v_lshrrev_b32 v251, 2, v250\n\t"
    "v_lshrrev_b32 v252, 3, v250\n\t"
    "v_xnor_b32 v251, v251, v252\n\t"
    "v_and_b32 v251, 1, v251\n\t"
    "v_mul_u32_u24 v251, 12, v251\n\t"
    "v_xor_b32 v250, v250, v251\n\t"
    "v_lshrrev_b32 v251, 1, v250\n\t"
    "v_and_b32 v252, 1, v250\n\t"
    "v_mad_u32_u24 v251, v251, 5, 1\n\t"
    "v_mad_u32_u24 v251, v252, 3, v251\n\t"
    "v_add_u32 v252, 56, v250\n\t"
    "s_cmp_eq_u32 %[role], 3\n\t"
    "s_cselect_b64 vcc, -1, 0\n\t"
    "s_nop 0\n\t"
    "v_cndmask_b32 v251, v251, v252, vcc\n\t"
    "v_lshlrev_b32 v222, 17, v251\n\t"
    "v_cmp_gt_u32 vcc, 16, v249\n\t"
    "v_mov_b32 v253, 0x40000000\n\t"
    "s_nop 0\n\t"
    "v_cndmask_b32 v222, v253, v222, vcc\n\t"
    "v_and_b32 v212, 0x70, v248\n\t"
    "v_lshrrev_b32 v250, 2, v248\n\t"
    "v_and_b32 v250, 0x40, v250\n\t"
    "v_and_b32 v251, 0x20, v249\n\t"
    "v_xor_b32 v212, v212, v250\n\t"
    "v_xor_b32 v212, v212, v251\n\t"
    "v_and_b32 v250, 0x380, v248\n\t"
    "v_lshl_add_u32 v212, v250, 4, v212\n\t"
    "v_add_u32 v212, %[bvc], v212\n\t"
    "v_add_u32 v213, 0x3c00, v212\n\t"
    "v_readlane_b32 %[kb0], v222, 0\n\t"
    "v_readlane_b32 %[kb1], v222, 1\n\t"
    "v_readlane_b32 %[kb2], v222, 2\n\t"
    "v_readlane_b32 %[kb3], v222, 3\n\t"
    "v_readlane_b32 %[kb4], v222, 4\n\t"
    "v_add_u32 v208, %[av0], v248\n\t"
    "v_add_u32 v209, %[av1], v248\n\t"
    "v_add_u32 v210, %[av2], v248\n\t"
    "v_add_u32 v211, %[av3], v248\n\t"
    "v_lshrrev_b32 v250, 1, v248\n\t"
    "v_lshrrev_b32 v251, 3, v248\n\t"
    "v_and_b32 v252, 0x50, v250\n\t"
    "v_and_b32 v251, 0x20, v251\n\t"
    "v_or_b32 v252, v252, v251\n\t"
    "v_and_b32 v250, 8, v250\n\t"
    "v_and_b32 v251, 0x3c0, v248\n\t"
    "v_lshl_add_u32 v250, v251, 1, v250\n\t"
    "v_add_u32 v250, %[ldsb], v250\n\t"
    "v_add_u32 v214, v252, v250\n\t"
    "v_xad_u32 v215, v252, 32, v250\n\t"
    "v_xad_u32 v216, v252, 64, v250\n\t"
    "v_xor_b32 v251, 0x60, v252\n\t"
    "v_add_u32 v217, v251, v250\n\t"
    "v_and_b32 v223, 0xf0, v248\n\t"
    "v_lshlrev_b32 v223, 3, v223\n\t"
    "v_and_b32 v229, 0x380, v223\n\t"
    "v_add_u32 v229, %[fkv], v229\n\t"
    "v_add_u32 v228, %[kvld], v248\n\t"
    "v_and_b32 v252, 0x1c0, v248\n\t"
    "v_lshlrev_b32 v252, 7, v252\n\t"
    "v_and_b32 v253, 0x30, v248\n\t"
    "v_add3_u32 v252, v252, v253, %[kvz]\n\t"
    "s_waitcnt lgkmcnt(11)\n\t"
    "v_mov_b32 v232, v129\n\t"
    "v_pk_fma_f32 v[0:1], v[128:129], v[160:161], v[0:1] op_sel_hi:[0,1,1]\n\t"
    "v_pk_fma_f32 v[2:3], v[128:129], v[162:163], v[2:3] op_sel_hi:[0,1,1]\n\t"
    "v_pk_fma_f32 v[32:33], v[128:129], v[164:165], v[32:33] op_sel_hi:[0,1,1]\n\t"
    "v_pk_fma_f32 v[34:35], v[128:129], v[166:167], v[34:35] op_sel_hi:[0,1,1]\n\t"
    "v_pk_fma_f32 v[0:1], v[0:1], v[232:233], 1.0 op_sel_hi:[1,0,0]\n\t"
    "v_pk_fma_f32 v[2:3], v[2:3], v[232:233], 1.0 op_sel_hi:[1,0,0]\n\t"
    "v_pk_fma_f32 v[32:33], v[32:33], v[232:233], 1.0 op_sel_hi:[1,0,0]\n\t"
    "v_pk_fma_f32 v[34:35], v[34:35], v[232:233], 1.0 op_sel_hi:[1,0,0]\n\t"
    "v_pk_fma_f32 v[64:65], v[0:1], s[92:93], s[92:93] op_sel_hi:[1,0,0] neg_lo:[0,0,1] neg_hi:[0,0,1]\n\t"
    "v_pk_fma_f32 v[66:67], v[2:3], s[92:93], s[92:93] op_sel_hi:[1,0,0] neg_lo:[0,0,1] neg_hi:[0,0,1]\n\t"
    "v_pk_fma_f32 v[68:69], v[32:33], s[92:93], s[92:93] op_sel_hi:[1,0,0] neg_lo:[0,0,1] neg_hi:[0,0,1]\n\t"
    "v_pk_fma_f32 v[70:71], v[34:35], s[92:93], s[92:93] op_sel_hi:[1,0,0] neg_lo:[0,0,1] neg_hi:[0,0,1]\n\t"
    "v_exp_f32 v64, v64\n\t"
    "v_exp_f32 v66, v66\n\t"
    "v_exp_f32 v68, v68\n\t"
    "v_exp_f32 v70, v70\n\t"
    "v_exp_f32 v65, v65\n\t"
    "v_exp_f32 v67, v67\n\t"
    "v_exp_f32 v69, v69\n\t"
    "v_exp_f32 v71, v71\n\t"
    "v_med3_f32 v0, v0, v64, 1.0\n\t"
    "v_med3_f32 v2, v2, v66, 1.0\n\t"
    "v_med3_f32 v32, v32, v68, 1.0\n\t"
    "v_med3_f32 v34, v34, v70, 1.0\n\t"
    "v_med3_f32 v1, v1, v65, 1.0\n\t"
    "v_med3_f32 v3, v3, v67, 1.0\n\t"
    "v_med3_f32 v33, v33, v69, 1.0\n\t"
    "v_med3_f32 v35, v35, v71, 1.0\n\t"
    "v_pk_add_f32 v[0:1], v[0:1], s[94:95] op_sel_hi:[1,0]\n\t"
    "v_pk_add_f32 v[2:3], v[2:3], s[94:95] op_sel_hi:[1,0]\n\t"
    "v_pk_add_f32 v[32:33], v[32:33], s[94:95] op_sel_hi:[1,0]\n\t"
    "v_pk_add_f32 v[34:35], v[34:35], s[94:95] op_sel_hi:[1,0]\n\t"
    "v_cvt_pk_bf16_f32 v0, v0, v1\n\t"
    "v_cvt_pk_bf16_f32 v1, v2, v3\n\t"
    "v_cvt_pk_bf16_f32 v2, v32, v33\n\t"
    "v_cvt_pk_bf16_f32 v3, v34, v35\n\t"
    "s_waitcnt lgkmcnt(0)\n\t"
    "v_mov_b32 v234, v133\n\t"
    "v_pk_fma_f32 v[4:5], v[132:133], v[160:161], v[4:5] op_sel_hi:[0,1,1]\n\t"
    "v_pk_fma_f32 v[6:7], v[132:133], v[162:163], v[6:7] op_sel_hi:[0,1,1]\n\t"
    "v_pk_fma_f32 v[36:37], v[132:133], v[164:165], v[36:37] op_sel_hi:[0,1,1]\n\t"
    "v_pk_fma_f32 v[38:39], v[132:133], v[166:167], v[38:39] op_sel_hi:[0,1,1]\n\t"
    "v_pk_fma_f32 v[4:5], v[4:5], v[234:235], 1.0 op_sel_hi:[1,0,0]\n\t"
    "v_pk_fma_f32 v[6:7], v[6:7], v[234:235], 1.0 op_sel_hi:[1,0,0]\n\t"
    "v_pk_fma_f32 v[36:37], v[36:37], v[234:235], 1.0 op_sel_hi:[1,0,0]\n\t"
    "v_pk_fma_f32 v[38:39], v[38:39], v[234:235], 1.0 op_sel_hi:[1,0,0]\n\t"
    "v_pk_fma_f32 v[72:73], v[4:5], s[92:93], s[92:93] op_sel_hi:[1,0,0] neg_lo:[0,0,1] neg_hi:[0,0,1]\n\t"
    "v_pk_fma_f32 v[74:75], v[6:7], s[92:93], s[92:93] op_sel_hi:[1,0,0] neg_lo:[0,0,1] neg_hi:[0,0,1]\n\t"
    "v_pk_fma_f32 v[76:77], v[36:37], s[92:93], s[92:93] op_sel_hi:[1,0,0] neg_lo:[0,0,1] neg_hi:[0,0,1]\n\t"
    "v_pk_fma_f32 v[78:79], v[38:39], s[92:93], s[92:93] op_sel_hi:[1,0,0] neg_lo:[0,0,1] neg_hi:[0,0,1]\n\t"
    "v_exp_f32 v72, v72\n\t"
    "v_exp_f32 v74, v74\n\t"
    "v_exp_f32 v76, v76\n\t"
    "v_exp_f32 v78, v78\n\t"
    "v_exp_f32 v73, v73\n\t"
    "v_exp_f32 v75, v75\n\t"
    "v_exp_f32 v77, v77\n\t"
    "v_exp_f32 v79, v79\n\t"
    "v_med3_f32 v4, v4, v72, 1.0\n\t"
    "v_med3_f32 v6, v6, v74, 1.0\n\t"
    "v_med3_f32 v36, v36, v76, 1.0\n\t"
    "v_med3_f32 v38, v38, v78, 1.0\n\t"
    "v_med3_f32 v5, v5, v73, 1.0\n\t"
    "v_med3_f32 v7, v7, v75, 1.0\n\t"
    "v_med3_f32 v37, v37, v77, 1.0\n\t"
    "v_med3_f32 v39, v39, v79, 1.0\n\t"
    "v_pk_add_f32 v[4:5], v[4:5], s[94:95] op_sel_hi:[1,0]\n\t"
    "v_pk_add_f32 v[6:7], v[6:7], s[94:95] op_sel_hi:[1,0]\n\t"
    "v_pk_add_f32 v[36:37], v[36:37], s[94:95] op_sel_hi:[1,0]\n\t"
    "v_pk_add_f32 v[38:39], v[38:39], s[94:95] op_sel_hi:[1,0]\n\t"
    "v_cvt_pk_bf16_f32 v4, v4, v5\n\t"
    "v_cvt_pk_bf16_f32 v5, v6, v7\n\t"
    "v_cvt_pk_bf16_f32 v6, v36, v37\n\t"
    "v_cvt_pk_bf16_f32 v7, v38, v39\n\t"
    "v_mov_b32 v232, v137\n\t"
    "v_pk_fma_f32 v[8:9], v[136:137], v[160:161], v[8:9] op_sel_hi:[0,1,1]\n\t"
    "s_add_u32 m0, %[m0b], 0\n\t"
    "s_add_u32 %[tmp], %[kb0], 0\n\t"
    "buffer_load_dwordx4 v212, %[srdb], %[tmp] offen lds\n\t"
    "v_pk_fma_f32 v[10:11], v[136:137], v[162:163], v[10:11] op_sel_hi:[0,1,1]\n\t"
    "v_pk_fma_f32 v[40:41], v[136:137], v[164:165], v[40:41] op_sel_hi:[0,1,1]\n\t"
    "v_pk_fma_f32 v[42:43], v[136:137], v[166:167], v[42:43] op_sel_hi:[0,1,1]\n\t"
    "v_pk_fma_f32 v[8:9], v[8:9], v[232:233], 1.0 op_sel_hi:[1,0,0]\n\t"
    "v_pk_fma_f32 v[10:11], v[10:11], v[232:233], 1.0 op_sel_hi:[1,0,0]\n\t"
    "v_pk_fma_f32 v[40:41], v[40:41], v[232:233], 1.0 op_sel_hi:[1,0,0]\n\t"
    "v_pk_fma_f32 v[42:43], v[42:43], v[232:233], 1.0 op_sel_hi:[1,0,0]\n\t"
    "v_pk_fma_f32 v[64:65], v[8:9], s[92:93], s[92:93] op_sel_hi:[1,0,0] neg_lo:[0,0,1] neg_hi:[0,0,1]\n\t"
    "v_pk_fma_f32 v[66:67], v[10:11], s[92:93], s[92:93] op_sel_hi:[1,0,0] neg_lo:[0,0,1] neg_hi:[0,0,1]\n\t"
    "v_pk_fma_f32 v[68:69], v[40:41], s[92:93], s[92:93] op_sel_hi:[1,0,0] neg_lo:[0,0,1] neg_hi:[0,0,1]\n\t"
    "v_pk_fma_f32 v[70:71], v[42:43], s[92:93], s[92:93] op_sel_hi:[1,0,0] neg_lo:[0,0,1] neg_hi:[0,0,1]\n\t"
    "v_exp_f32 v64, v64\n\t"
    "buffer_load_dwordx4 v213, %[srdb], %[tmp] offen offset:1024 lds\n\t"
    "v_exp_f32 v66, v66\n\t"
    "v_exp_f32 v68, v68\n\t"
    "v_exp_f32 v70, v70\n\t"
    "v_exp_f32 v65, v65\n\t"
    "v_exp_f32 v67, v67\n\t"
    "v_exp_f32 v69, v69\n\t"
    "v_exp_f32 v71, v71\n\t"
    "v_med3_f32 v8, v8, v64, 1.0\n\t"
    "v_med3_f32 v10, v10, v66, 1.0\n\t"
    "v_med3_f32 v40, v40, v68, 1.0\n\t"
    "v_med3_f32 v42, v42, v70, 1.0\n\t"
    "v_med3_f32 v9, v9, v65, 1.0\n\t"
    "s_add_u32 m0, %[m0b], 8192\n\t"
    "s_add_u32 %[tmp], %[kb0], 128\n\t"
    "buffer_load_dwordx4 v212, %[srdb], %[tmp] offen lds\n\t"
    "v_med3_f32 v11, v11, v67, 1.0\n\t"
    "v_med3_f32 v41, v41, v69, 1.0\n\t"
    "v_med3_f32 v43, v43, v71, 1.0\n\t"
    "v_pk_add_f32 v[8:9], v[8:9], s[94:95] op_sel_hi:[1,0]\n\t"
    "v_pk_add_f32 v[10:11], v[10:11], s[94:95] op_sel_hi:[1,0]\n\t"
    "v_pk_add_f32 v[40:41], v[40:41], s[94:95] op_sel_hi:[1,0]\n\t"
    "v_pk_add_f32 v[42:43], v[42:43], s[94:95] op_sel_hi:[1,0]\n\t"
    "v_cvt_pk_bf16_f32 v8, v8, v9\n\t"
    "v_cvt_pk_bf16_f32 v9, v10, v11\n\t"
    "v_cvt_pk_bf16_f32 v10, v40, v41\n\t"
    "v_cvt_pk_bf16_f32 v11, v42, v43\n\t"
    "v_mov_b32 v234, v141\n\t"
    "v_pk_fma_f32 v[12:13], v[140:141], v[160:161], v[12:13] op_sel_hi:[0,1,1]\n\t"
    "buffer_load_dwordx4 v213, %[srdb], %[tmp] offen offset:1024 lds\n\t"
    "v_pk_fma_f32 v[14:15], v[140:141], v[162:163], v[14:15] op_sel_hi:[0,1,1]\n\t"
    "v_pk_fma_f32 v[44:45], v[140:141], v[164:165], v[44:45] op_sel_hi:[0,1,1]\n\t"
    "v_pk_fma_f32 v[46:47], v[140:141], v[166:167], v[46:47] op_sel_hi:[0,1,1]\n\t"
    "v_pk_fma_f32 v[12:13], v[12:13], v[234:235], 1.0 op_sel_hi:[1,0,0]\n\t"
    "v_pk_fma_f32 v[14:15], v[14:15], v[234:235], 1.0 op_sel_hi:[1,0,0]\n\t"
    "v_pk_fma_f32 v[44:45], v[44:45], v[234:235], 1.0 op_sel_hi:[1,0,0]\n\t"
    "v_pk_fma_f32 v[46:47], v[46:47], v[234:235], 1.0 op_sel_hi:[1,0,0]\n\t"
    "v_pk_fma_f32 v[72:73], v[12:13], s[92:93], s[92:93] op_sel_hi:[1,0,0] neg_lo:[0,0,1] neg_hi:[0,0,1]\n\t"
    "v_pk_fma_f32 v[74:75], v[14:15], s[92:93], s[92:93] op_sel_hi:[1,0,0] neg_lo:[0,0,1] neg_hi:[0,0,1]\n\t"
    "v_pk_fma_f32 v[76:77], v[44:45], s[92:93], s[92:93] op_sel_hi:[1,0,0] neg_lo:[0,0,1] neg_hi:[0,0,1]\n\t"
    "v_pk_fma_f32 v[78:79], v[46:47], s[92:93], s[92:93] op_sel_hi:[1,0,0] neg_lo:[0,0,1] neg_hi:[0,0,1]\n\t"
    "v_exp_f32 v72, v72\n\t"
    "s_add_u32 m0, %[m0b], 16384\n\t"
    "s_add_u32 %[tmp], %[kb1], 0\n\t"
    "buffer_load_dwordx4 v212, %[srdb], %[tmp] offen lds\n\t"
    "v_exp_f32 v74, v74\n\t"
    "v_exp_f32 v76, v76\n\t"
    "v_exp_f32 v78, v78\n\t"
    "v_exp_f32 v73, v73\n\t"
    "v_exp_f32 v75, v75\n\t"
    "v_exp_f32 v77, v77\n\t"
    "v_exp_f32 v79, v79\n\t"
    "v_med3_f32 v12, v12, v72, 1.0\n\t"
    "v_med3_f32 v14, v14, v74, 1.0\n\t"
    "v_med3_f32 v44, v44, v76, 1.0\n\t"
    "v_med3_f32 v46, v46, v78, 1.0\n\t"
    "v_med3_f32 v13, v13, v73, 1.0\n\t"
    "buffer_load_dwordx4 v213, %[srdb], %[tmp] offen offset:1024 lds\n\t"
    "v_med3_f32 v15, v15, v75, 1.0\n\t"
    "v_med3_f32 v45, v45, v77, 1.0\n\t"
    "v_med3_f32 v47, v47, v79, 1.0\n\t"
    "v_pk_add_f32 v[12:13], v[12:13], s[94:95] op_sel_hi:[1,0]\n\t"
    "v_pk_add_f32 v[14:15], v[14:15], s[94:95] op_sel_hi:[1,0]\n\t"
    "v_pk_add_f32 v[44:45], v[44:45], s[94:95] op_sel_hi:[1,0]\n\t"
    "v_pk_add_f32 v[46:47], v[46:47], s[94:95] op_sel_hi:[1,0]\n\t"
    "v_cvt_pk_bf16_f32 v12, v12, v13\n\t"
    "v_cvt_pk_bf16_f32 v13, v14, v15\n\t"
    "v_cvt_pk_bf16_f32 v14, v44, v45\n\t"
    "v_cvt_pk_bf16_f32 v15, v46, v47\n\t"
    "v_mov_b32 v232, v145\n\t"
    "v_pk_fma_f32 v[16:17], v[144:145], v[160:161], v[16:17] op_sel_hi:[0,1,1]\n\t"
    "s_add_u32 m0, %[m0b], 24576\n\t"
    "s_add_u32 %[tmp], %[kb1], 128\n\t"
    "buffer_load_dwordx4 v212, %[srdb], %[tmp] offen lds\n\t"
    "v_pk_fma_f32 v[18:19], v[144:145], v[162:163], v[18:19] op_sel_hi:[0,1,1]\n\t"
    "v_pk_fma_f32 v[48:49], v[144:145], v[164:165], v[48:49] op_sel_hi:[0,1,1]\n\t"
    "v_pk_fma_f32 v[50:51], v[144:145], v[166:167], v[50:51] op_sel_hi:[0,1,1]\n\t"
    "v_pk_fma_f32 v[16:17], v[16:17], v[232:233], 1.0 op_sel_hi:[1,0,0]\n\t"
    "v_pk_fma_f32 v[18:19], v[18:19], v[232:233], 1.0 op_sel_hi:[1,0,0]\n\t"
    "v_pk_fma_f32 v[48:49], v[48:49], v[232:233], 1.0 op_sel_hi:[1,0,0]\n\t"
    "v_pk_fma_f32 v[50:51], v[50:51], v[232:233], 1.0 op_sel_hi:[1,0,0]\n\t"
    "v_pk_fma_f32 v[64:65], v[16:17], s[92:93], s[92:93] op_sel_hi:[1,0,0] neg_lo:[0,0,1] neg_hi:[0,0,1]\n\t"
    "v_pk_fma_f32 v[66:67], v[18:19], s[92:93], s[92:93] op_sel_hi:[1,0,0] neg_lo:[0,0,1] neg_hi:[0,0,1]\n\t"
    "v_pk_fma_f32 v[68:69], v[48:49], s[92:93], s[92:93] op_sel_hi:[1,0,0] neg_lo:[0,0,1] neg_hi:[0,0,1]\n\t"
    "v_pk_fma_f32 v[70:71], v[50:51], s[92:93], s[92:93] op_sel_hi:[1,0,0] neg_lo:[0,0,1] neg_hi:[0,0,1]\n\t"
    "v_exp_f32 v64, v64\n\t"
    "buffer_load_dwordx4 v213, %[srdb], %[tmp] offen offset:1024 lds\n\t"
    "v_exp_f32 v66, v66\n\t"
    "v_exp_f32 v68, v68\n\t"
    "v_exp_f32 v70, v70\n\t"
    "v_exp_f32 v65, v65\n\t"
    "v_exp_f32 v67, v67\n\t"
    "v_exp_f32 v69, v69\n\t"
    "v_exp_f32 v71, v71\n\t"
    "v_med3_f32 v16, v16, v64, 1.0\n\t"
    "v_med3_f32 v18, v18, v66, 1.0\n\t"
    "v_med3_f32 v48, v48, v68, 1.0\n\t"
    "v_med3_f32 v50, v50, v70, 1.0\n\t"
    "v_med3_f32 v17, v17, v65, 1.0\n\t"
    "s_add_u32 m0, %[m0b], 32768\n\t"
    "s_add_u32 %[tmp], %[kb2], 0\n\t"
    "buffer_load_dwordx4 v212, %[srdb], %[tmp] offen lds\n\t"
    "v_med3_f32 v19, v19, v67, 1.0\n\t"
    "v_med3_f32 v49, v49, v69, 1.0\n\t"
    "v_med3_f32 v51, v51, v71, 1.0\n\t"
    "v_pk_add_f32 v[16:17], v[16:17], s[94:95] op_sel_hi:[1,0]\n\t"
    "v_pk_add_f32 v[18:19], v[18:19], s[94:95] op_sel_hi:[1,0]\n\t"
    "v_pk_add_f32 v[48:49], v[48:49], s[94:95] op_sel_hi:[1,0]\n\t"
    "v_pk_add_f32 v[50:51], v[50:51], s[94:95] op_sel_hi:[1,0]\n\t"
    "v_cvt_pk_bf16_f32 v16, v16, v17\n\t"
    "v_cvt_pk_bf16_f32 v17, v18, v19\n\t"
    "v_cvt_pk_bf16_f32 v18, v48, v49\n\t"
    "v_cvt_pk_bf16_f32 v19, v50, v51\n\t"
    "v_mov_b32 v234, v149\n\t"
    "v_pk_fma_f32 v[20:21], v[148:149], v[160:161], v[20:21] op_sel_hi:[0,1,1]\n\t"
    "buffer_load_dwordx4 v213, %[srdb], %[tmp] offen offset:1024 lds\n\t"
    "v_pk_fma_f32 v[22:23], v[148:149], v[162:163], v[22:23] op_sel_hi:[0,1,1]\n\t"
    "v_pk_fma_f32 v[52:53], v[148:149], v[164:165], v[52:53] op_sel_hi:[0,1,1]\n\t"
    "v_pk_fma_f32 v[54:55], v[148:149], v[166:167], v[54:55] op_sel_hi:[0,1,1]\n\t"
    "v_pk_fma_f32 v[20:21], v[20:21], v[234:235], 1.0 op_sel_hi:[1,0,0]\n\t"
    "v_pk_fma_f32 v[22:23], v[22:23], v[234:235], 1.0 op_sel_hi:[1,0,0]\n\t"
    "v_pk_fma_f32 v[52:53], v[52:53], v[234:235], 1.0 op_sel_hi:[1,0,0]\n\t"
    "v_pk_fma_f32 v[54:55], v[54:55], v[234:235], 1.0 op_sel_hi:[1,0,0]\n\t"
    "v_pk_fma_f32 v[72:73], v[20:21], s[92:93], s[92:93] op_sel_hi:[1,0,0] neg_lo:[0,0,1] neg_hi:[0,0,1]\n\t"
    "v_pk_fma_f32 v[74:75], v[22:23], s[92:93], s[92:93] op_sel_hi:[1,0,0] neg_lo:[0,0,1] neg_hi:[0,0,1]\n\t"
    "v_pk_fma_f32 v[76:77], v[52:53], s[92:93], s[92:93] op_sel_hi:[1,0,0] neg_lo:[0,0,1] neg_hi:[0,0,1]\n\t"
    "v_pk_fma_f32 v[78:79], v[54:55], s[92:93], s[92:93] op_sel_hi:[1,0,0] neg_lo:[0,0,1] neg_hi:[0,0,1]\n\t"
    "v_exp_f32 v72, v72\n\t"
    "s_add_u32 m0, %[m0b], 40960\n\t"
    "s_add_u32 %[tmp], %[kb2], 128\n\t"
    "buffer_load_dwordx4 v212, %[srdb], %[tmp] offen lds\n\t"
    "v_exp_f32 v74, v74\n\t"
    "v_exp_f32 v76, v76\n\t"
    "v_exp_f32 v78, v78\n\t"
    "v_exp_f32 v73, v73\n\t"
    "v_exp_f32 v75, v75\n\t"
    "v_exp_f32 v77, v77\n\t"
    "v_exp_f32 v79, v79\n\t"
    "v_med3_f32 v20, v20, v72, 1.0\n\t"
    "v_med3_f32 v22, v22, v74, 1.0\n\t"
    "v_med3_f32 v52, v52, v76, 1.0\n\t"
    "v_med3_f32 v54, v54, v78, 1.0\n\t"
    "v_med3_f32 v21, v21, v73, 1.0\n\t"
    "buffer_load_dwordx4 v213, %[srdb], %[tmp] offen offset:1024 lds\n\t"
    "v_med3_f32 v23, v23, v75, 1.0\n\t"
    "v_med3_f32 v53, v53, v77, 1.0\n\t"
    "v_med3_f32 v55, v55, v79, 1.0\n\t"
    "v_pk_add_f32 v[20:21], v[20:21], s[94:95] op_sel_hi:[1,0]\n\t"
    "v_pk_add_f32 v[22:23], v[22:23], s[94:95] op_sel_hi:[1,0]\n\t"
    "v_pk_add_f32 v[52:53], v[52:53], s[94:95] op_sel_hi:[1,0]\n\t"
    "v_pk_add_f32 v[54:55], v[54:55], s[94:95] op_sel_hi:[1,0]\n\t"
    "v_cvt_pk_bf16_f32 v20, v20, v21\n\t"
    "v_cvt_pk_bf16_f32 v21, v22, v23\n\t"
    "v_cvt_pk_bf16_f32 v22, v52, v53\n\t"
    "v_cvt_pk_bf16_f32 v23, v54, v55\n\t"
    "v_mov_b32 v232, v153\n\t"
    "v_pk_fma_f32 v[24:25], v[152:153], v[160:161], v[24:25] op_sel_hi:[0,1,1]\n\t"
    "s_add_u32 m0, %[m0b], 49152\n\t"
    "s_add_u32 %[tmp], %[kb3], 0\n\t"
    "buffer_load_dwordx4 v212, %[srdb], %[tmp] offen lds\n\t"
    "v_pk_fma_f32 v[26:27], v[152:153], v[162:163], v[26:27] op_sel_hi:[0,1,1]\n\t"
    "v_pk_fma_f32 v[56:57], v[152:153], v[164:165], v[56:57] op_sel_hi:[0,1,1]\n\t"
    "v_pk_fma_f32 v[58:59], v[152:153], v[166:167], v[58:59] op_sel_hi:[0,1,1]\n\t"
    "v_pk_fma_f32 v[24:25], v[24:25], v[232:233], 1.0 op_sel_hi:[1,0,0]\n\t"
    "v_pk_fma_f32 v[26:27], v[26:27], v[232:233], 1.0 op_sel_hi:[1,0,0]\n\t"
    "v_pk_fma_f32 v[56:57], v[56:57], v[232:233], 1.0 op_sel_hi:[1,0,0]\n\t"
    "v_pk_fma_f32 v[58:59], v[58:59], v[232:233], 1.0 op_sel_hi:[1,0,0]\n\t"
    "v_pk_fma_f32 v[64:65], v[24:25], s[92:93], s[92:93] op_sel_hi:[1,0,0] neg_lo:[0,0,1] neg_hi:[0,0,1]\n\t"
    "v_pk_fma_f32 v[66:67], v[26:27], s[92:93], s[92:93] op_sel_hi:[1,0,0] neg_lo:[0,0,1] neg_hi:[0,0,1]\n\t"
    "v_pk_fma_f32 v[68:69], v[56:57], s[92:93], s[92:93] op_sel_hi:[1,0,0] neg_lo:[0,0,1] neg_hi:[0,0,1]\n\t"
    "v_pk_fma_f32 v[70:71], v[58:59], s[92:93], s[92:93] op_sel_hi:[1,0,0] neg_lo:[0,0,1] neg_hi:[0,0,1]\n\t"
    "v_exp_f32 v64, v64\n\t"
    "buffer_load_dwordx4 v213, %[srdb], %[tmp] offen offset:1024 lds\n\t"
    "v_exp_f32 v66, v66\n\t"
    "v_exp_f32 v68, v68\n\t"
    "v_exp_f32 v70, v70\n\t"
    "v_exp_f32 v65, v65\n\t"
    "v_exp_f32 v67, v67\n\t"
    "v_exp_f32 v69, v69\n\t"
    "v_exp_f32 v71, v71\n\t"
    "v_med3_f32 v24, v24, v64, 1.0\n\t"
    "v_med3_f32 v26, v26, v66, 1.0\n\t"
    "v_med3_f32 v56, v56, v68, 1.0\n\t"
    "v_med3_f32 v58, v58, v70, 1.0\n\t"
    "v_med3_f32 v25, v25, v65, 1.0\n\t"
    "s_add_u32 m0, %[m0b], 57344\n\t"
    "s_add_u32 %[tmp], %[kb3], 128\n\t"
    "buffer_load_dwordx4 v212, %[srdb], %[tmp] offen lds\n\t"
    "v_med3_f32 v27, v27, v67, 1.0\n\t"
    "v_med3_f32 v57, v57, v69, 1.0\n\t"
    "v_med3_f32 v59, v59, v71, 1.0\n\t"
    "v_pk_add_f32 v[24:25], v[24:25], s[94:95] op_sel_hi:[1,0]\n\t"
    "v_pk_add_f32 v[26:27], v[26:27], s[94:95] op_sel_hi:[1,0]\n\t"
    "v_pk_add_f32 v[56:57], v[56:57], s[94:95] op_sel_hi:[1,0]\n\t"
    "v_pk_add_f32 v[58:59], v[58:59], s[94:95] op_sel_hi:[1,0]\n\t"
    "v_cvt_pk_bf16_f32 v24, v24, v25\n\t"
    "v_cvt_pk_bf16_f32 v25, v26, v27\n\t"
    "v_cvt_pk_bf16_f32 v26, v56, v57\n\t"
    "v_cvt_pk_bf16_f32 v27, v58, v59\n\t"
    "v_mov_b32 v234, v157\n\t"
    "v_pk_fma_f32 v[28:29], v[156:157], v[160:161], v[28:29] op_sel_hi:[0,1,1]\n\t"
    "buffer_load_dwordx4 v213, %[srdb], %[tmp] offen offset:1024 lds\n\t"
    "v_pk_fma_f32 v[30:31], v[156:157], v[162:163], v[30:31] op_sel_hi:[0,1,1]\n\t"
    "v_pk_fma_f32 v[60:61], v[156:157], v[164:165], v[60:61] op_sel_hi:[0,1,1]\n\t"
    "v_pk_fma_f32 v[62:63], v[156:157], v[166:167], v[62:63] op_sel_hi:[0,1,1]\n\t"
    "v_pk_fma_f32 v[28:29], v[28:29], v[234:235], 1.0 op_sel_hi:[1,0,0]\n\t"
    "v_pk_fma_f32 v[30:31], v[30:31], v[234:235], 1.0 op_sel_hi:[1,0,0]\n\t"
    "v_pk_fma_f32 v[60:61], v[60:61], v[234:235], 1.0 op_sel_hi:[1,0,0]\n\t"
    "v_pk_fma_f32 v[62:63], v[62:63], v[234:235], 1.0 op_sel_hi:[1,0,0]\n\t"
    "v_pk_fma_f32 v[72:73], v[28:29], s[92:93], s[92:93] op_sel_hi:[1,0,0] neg_lo:[0,0,1] neg_hi:[0,0,1]\n\t"
    "v_pk_fma_f32 v[74:75], v[30:31], s[92:93], s[92:93] op_sel_hi:[1,0,0] neg_lo:[0,0,1] neg_hi:[0,0,1]\n\t"
    "v_pk_fma_f32 v[76:77], v[60:61], s[92:93], s[92:93] op_sel_hi:[1,0,0] neg_lo:[0,0,1] neg_hi:[0,0,1]\n\t"
    "v_pk_fma_f32 v[78:79], v[62:63], s[92:93], s[92:93] op_sel_hi:[1,0,0] neg_lo:[0,0,1] neg_hi:[0,0,1]\n\t"
    "v_exp_f32 v72, v72\n\t"
    "v_exp_f32 v74, v74\n\t"
    "v_exp_f32 v76, v76\n\t"
    "v_exp_f32 v78, v78\n\t"
    "v_exp_f32 v73, v73\n\t"
    "v_exp_f32 v75, v75\n\t"
    "v_exp_f32 v77, v77\n\t"
    "v_exp_f32 v79, v79\n\t"
    "v_med3_f32 v28, v28, v72, 1.0\n\t"
    "v_med3_f32 v30, v30, v74, 1.0\n\t"
    "v_med3_f32 v60, v60, v76, 1.0\n\t"
    "v_med3_f32 v62, v62, v78, 1.0\n\t"
    "v_med3_f32 v29, v29, v73, 1.0\n\t"
    "v_med3_f32 v31, v31, v75, 1.0\n\t"
    "v_med3_f32 v61, v61, v77, 1.0\n\t"
    "v_med3_f32 v63, v63, v79, 1.0\n\t"
    "v_pk_add_f32 v[28:29], v[28:29], s[94:95] op_sel_hi:[1,0]\n\t"
    "v_pk_add_f32 v[30:31], v[30:31], s[94:95] op_sel_hi:[1,0]\n\t"
    "v_pk_add_f32 v[60:61], v[60:61], s[94:95] op_sel_hi:[1,0]\n\t"
    "v_pk_add_f32 v[62:63], v[62:63], s[94:95] op_sel_hi:[1,0]\n\t"
    "v_cvt_pk_bf16_f32 v28, v28, v29\n\t"
    "v_cvt_pk_bf16_f32 v29, v30, v31\n\t"
    "v_cvt_pk_bf16_f32 v30, v60, v61\n\t"
    "v_cvt_pk_bf16_f32 v31, v62, v63\n\t"
    "buffer_load_dwordx2 v[230:231], v229, %[srdw], 0 offen sc1\n\t"
    "v_accvgpr_read_b32 v32, a160\n\t"
    "v_accvgpr_read_b32 v33, a161\n\t"
    "v_accvgpr_read_b32 v34, a162\n\t"
    "v_accvgpr_read_b32 v35, a163\n\t"
    "v_accvgpr_read_b32 v36, a192\n\t"
    "v_accvgpr_read_b32 v37, a193\n\t"
    "v_accvgpr_read_b32 v38, a194\n\t"
    "v_accvgpr_read_b32 v39, a195\n\t"
    "v_accvgpr_read_b32 v40, a224\n\t"
    "v_accvgpr_read_b32 v41, a225\n\t"
    "v_accvgpr_read_b32 v42, a226\n\t"
    "v_accvgpr_read_b32 v43, a227\n\t"
    "v_mov_b32 v232, v131\n\t"
    "v_pk_fma_f32 v[192:193], v[128:129], v[168:169], v[192:193] op_sel_hi:[0,1,1]\n\t"
    "v_pk_fma_f32 v[194:195], v[128:129], v[170:171], v[194:195] op_sel_hi:[0,1,1]\n\t"
    "v_pk_fma_f32 v[32:33], v[128:129], v[172:173], v[32:33] op_sel_hi:[0,1,1]\n\t"
    "v_pk_fma_f32 v[34:35], v[128:129], v[174:175], v[34:35] op_sel_hi:[0,1,1]\n\t"
    "v_pk_fma_f32 v[36:37], v[128:129], v[176:177], v[36:37] op_sel_hi:[0,1,1]\n\t"
    "v_pk_fma_f32 v[38:39], v[128:129], v[178:179], v[38:39] op_sel_hi:[0,1,1]\n\t"
    "v_pk_fma_f32 v[40:41], v[128:129], v[180:181], v[40:41] op_sel_hi:[0,1,1]\n\t"
    "v_pk_fma_f32 v[42:43], v[128:129], v[182:183], v[42:43] op_sel_hi:[0,1,1]\n\t"
    "v_pk_mul_f32 v[36:37], v[192:193], v[36:37]\n\t"
    "v_pk_mul_f32 v[38:39], v[194:195], v[38:39]\n\t"
    "v_pk_mul_f32 v[40:41], v[32:33], v[40:41]\n\t"
    "v_pk_mul_f32 v[42:43], v[34:35], v[42:43]\n\t"
    "v_pk_mul_f32 v[192:193], v[192:193], v[232:233] op_sel_hi:[1,0]\n\t"
    "v_pk_mul_f32 v[194:195], v[194:195], v[232:233] op_sel_hi:[1,0]\n\t"
    "v_pk_mul_f32 v[32:33], v[32:33], v[232:233] op_sel_hi:[1,0]\n\t"
    "v_pk_mul_f32 v[34:35], v[34:35], v[232:233] op_sel_hi:[1,0]\n\t"
    "v_exp_f32 v192, v192\n\t"
    "v_exp_f32 v194, v194\n\t"
    "v_exp_f32 v32, v32\n\t"
    "v_exp_f32 v34, v34\n\t"
    "v_exp_f32 v193, v193\n\t"
    "v_exp_f32 v195, v195\n\t"
    "v_exp_f32 v33, v33\n\t"
    "v_exp_f32 v35, v35\n\t"
    "v_pk_fma_f32 v[192:193], v[192:193], v[130:131], v[130:131] op_sel_hi:[1,0,0]\n\t"
    "v_pk_fma_f32 v[194:195], v[194:195], v[130:131], v[130:131] op_sel_hi:[1,0,0]\n\t"
    "v_pk_fma_f32 v[32:33], v[32:33], v[130:131], v[130:131] op_sel_hi:[1,0,0]\n\t"
    "v_pk_fma_f32 v[34:35], v[34:35], v[130:131], v[130:131] op_sel_hi:[1,0,0]\n\t"
    "v_rcp_f32 v192, v192\n\t"
    "v_rcp_f32 v194, v194\n\t"
    "v_rcp_f32 v32, v32\n\t"
    "v_rcp_f32 v34, v34\n\t"
    "v_rcp_f32 v193, v193\n\t"
    "v_rcp_f32 v195, v195\n\t"
    "v_rcp_f32 v33, v33\n\t"
    "v_rcp_f32 v35, v35\n\t"
    "v_pk_mul_f32 v[36:37], v[36:37], v[192:193]\n\t"
    "v_pk_mul_f32 v[38:39], v[38:39], v[194:195]\n\t"
    "v_pk_mul_f32 v[40:41], v[40:41], v[32:33]\n\t"
    "v_pk_mul_f32 v[42:43], v[42:43], v[34:35]\n\t"
    "v_cvt_pk_bf16_f32 v192, v36, v37\n\t"
    "v_cvt_pk_bf16_f32 v193, v38, v39\n\t"
    "v_cvt_pk_bf16_f32 v194, v40, v41\n\t"
    "v_cvt_pk_bf16_f32 v195, v42, v43\n\t"
    "s_mov_b32 %[tmp2], 0\n\t"
    "buffer_store_dwordx4 v[192:195], v227, %[srdw], %[tmp2] offen sc1\n\t"
    "s_waitcnt vmcnt(1)\n\t"
    "v_cmp_ne_u64 vcc, s[90:91], v[230:231]\n\t"
    "s_cbranch_vccz 62f\n\t"
    "s_mov_b32 %[tmp2], 0x10000\n\t"
    "63:\n\t"
    "s_sleep 2\n\t"
    "buffer_load_dwordx2 v[230:231], v229, %[srdw], 0 offen sc1\n\t"
    "s_sub_u32 %[tmp2], %[tmp2], 1\n\t"
    "s_waitcnt vmcnt(0)\n\t"
    "s_cbranch_scc1 62f\n\t"
    "v_cmp_ne_u64 vcc, s[90:91], v[230:231]\n\t"
    "s_cbranch_vccnz 63b\n\t"
    "62:\n\t"
    "v_accvgpr_read_b32 v48, a164\n\t"
    "v_accvgpr_read_b32 v49, a165\n\t"
    "buffer_load_dwordx4 v[184:187], v252, %[srdw], 0 offen sc1\n\t"
    "v_accvgpr_read_b32 v50, a166\n\t"
    "v_accvgpr_read_b32 v51, a167\n\t"
    "v_accvgpr_read_b32 v52, a196\n\t"
    "v_accvgpr_read_b32 v53, a197\n\t"
    "v_accvgpr_read_b32 v54, a198\n\t"
    "v_accvgpr_read_b32 v55, a199\n\t"
    "v_accvgpr_read_b32 v56, a228\n\t"
    "v_accvgpr_read_b32 v57, a229\n\t"
    "v_accvgpr_read_b32 v58, a230\n\t"
    "s_mov_b32 %[tmp], 0\n\t"
    "buffer_load_dwordx4 v[64:67], v228, %[srdw], %[tmp] offen sc1\n\t"
    "v_accvgpr_read_b32 v59, a231\n\t"
    "v_mov_b32 v234, v135\n\t"
    "v_pk_fma_f32 v[196:197], v[132:133], v[168:169], v[196:197] op_sel_hi:[0,1,1]\n\t"
    "v_pk_fma_f32 v[198:199], v[132:133], v[170:171], v[198:199] op_sel_hi:[0,1,1]\n\t"
    "v_pk_fma_f32 v[48:49], v[132:133], v[172:173], v[48:49] op_sel_hi:[0,1,1]\n\t"
    "v_pk_fma_f32 v[50:51], v[132:133], v[174:175], v[50:51] op_sel_hi:[0,1,1]\n\t"
    "v_pk_fma_f32 v[52:53], v[132:133], v[176:177], v[52:53] op_sel_hi:[0,1,1]\n\t"
    "v_pk_fma_f32 v[54:55], v[132:133], v[178:179], v[54:55] op_sel_hi:[0,1,1]\n\t"
    "v_pk_fma_f32 v[56:57], v[132:133], v[180:181], v[56:57] op_sel_hi:[0,1,1]\n\t"
    "v_pk_fma_f32 v[58:59], v[132:133], v[182:183], v[58:59] op_sel_hi:[0,1,1]\n\t"
    "buffer_load_dwordx4 v[96:99], v228, %[srdw], %[tmp] offen offset:1024 sc1\n\t"
    "v_pk_mul_f32 v[52:53], v[196:197], v[52:53]\n\t"
    "v_pk_mul_f32 v[54:55], v[198:199], v[54:55]\n\t"
    "v_pk_mul_f32 v[56:57], v[48:49], v[56:57]\n\t"
    "v_pk_mul_f32 v[58:59], v[50:51], v[58:59]\n\t"
    "v_pk_mul_f32 v[196:197], v[196:197], v[234:235] op_sel_hi:[1,0]\n\t"
    "v_pk_mul_f32 v[198:199], v[198:199], v[234:235] op_sel_hi:[1,0]\n\t"
    "v_pk_mul_f32 v[48:49], v[48:49], v[234:235] op_sel_hi:[1,0]\n\t"
    "v_pk_mul_f32 v[50:51], v[50:51], v[234:235] op_sel_hi:[1,0]\n\t"
    "v_exp_f32 v196, v196\n\t"
    "s_mov_b32 %[tmp], 8192\n\t"
    "buffer_load_dwordx4 v[68:71], v228, %[srdw], %[tmp] offen sc1\n\t"
    "v_exp_f32 v198, v198\n\t"
    "v_exp_f32 v48, v48\n\t"
    "v_exp_f32 v50, v50\n\t"
    "v_exp_f32 v197, v197\n\t"
    "v_exp_f32 v199, v199\n\t"
    "v_exp_f32 v49, v49\n\t"
    "v_exp_f32 v51, v51\n\t"
    "v_pk_fma_f32 v[196:197], v[196:197], v[134:135], v[134:135] op_sel_hi:[1,0,0]\n\t"
    "v_pk_fma_f32 v[198:199], v[198:199], v[134:135], v[134:135] op_sel_hi:[1,0,0]\n\t"
    "v_pk_fma_f32 v[48:49], v[48:49], v[134:135], v[134:135] op_sel_hi:[1,0,0]\n\t"
    "buffer_load_dwordx4 v[100:103], v228, %[srdw], %[tmp] offen offset:1024 sc1\n\t"
    "v_pk_fma_f32 v[50:51], v[50:51], v[134:135], v[134:135] op_sel_hi:[1,0,0]\n\t"
    "v_rcp_f32 v196, v196\n\t"
    "v_rcp_f32 v198, v198\n\t"
    "v_rcp_f32 v48, v48\n\t"
    "v_rcp_f32 v50, v50\n\t"
    "v_rcp_f32 v197, v197\n\t"
    "v_rcp_f32 v199, v199\n\t"
    "v_rcp_f32 v49, v49\n\t"
    "v_rcp_f32 v51, v51\n\t"
    "s_mov_b32 %[tmp], 16384\n\t"
    "buffer_load_dwordx4 v[72:75], v228, %[srdw], %[tmp] offen sc1\n\t"
    "v_pk_mul_f32 v[52:53], v[52:53], v[196:197]\n\t"
    "v_pk_mul_f32 v[54:55], v[54:55], v[198:199]\n\t"
    "v_pk_mul_f32 v[56:57], v[56:57], v[48:49]\n\t"
    "v_pk_mul_f32 v[58:59], v[58:59], v[50:51]\n\t"
    "v_cvt_pk_bf16_f32 v196, v52, v53\n\t"
    "v_cvt_pk_bf16_f32 v197, v54, v55\n\t"
    "v_cvt_pk_bf16_f32 v198, v56, v57\n\t"
    "v_cvt_pk_bf16_f32 v199, v58, v59\n\t"
    "s_mov_b32 %[tmp2], 147456\n\t"
    "buffer_store_dwordx4 v[196:199], v227, %[srdw], %[tmp2] offen sc1\n\t"
    "v_accvgpr_read_b32 v32, a168\n\t"
    "v_accvgpr_read_b32 v33, a169\n\t"
    "buffer_load_dwordx4 v[104:107], v228, %[srdw], %[tmp] offen offset:1024 sc1\n\t"
    "v_accvgpr_read_b32 v34, a170\n\t"
    "v_accvgpr_read_b32 v35, a171\n\t"
    "v_accvgpr_read_b32 v36, a200\n\t"
    "v_accvgpr_read_b32 v37, a201\n\t"
    "v_accvgpr_read_b32 v38, a202\n\t"
    "v_accvgpr_read_b32 v39, a203\n\t"
    "v_accvgpr_read_b32 v40, a232\n\t"
    "v_accvgpr_read_b32 v41, a233\n\t"
    "v_accvgpr_read_b32 v42, a234\n\t"
    "s_mov_b32 %[tmp], 24576\n\t"
    "buffer_load_dwordx4 v[76:79], v228, %[srdw], %[tmp] offen sc1\n\t"
    "v_accvgpr_read_b32 v43, a235\n\t"
    "v_mov_b32 v232, v139\n\t"
    "v_pk_fma_f32 v[200:201], v[136:137], v[168:169], v[200:201] op_sel_hi:[0,1,1]\n\t"
    "v_pk_fma_f32 v[202:203], v[136:137], v[170:171], v[202:203] op_sel_hi:[0,1,1]\n\t"
    "v_pk_fma_f32 v[32:33], v[136:137], v[172:173], v[32:33] op_sel_hi:[0,1,1]\n\t"
    "v_pk_fma_f32 v[34:35], v[136:137], v[174:175], v[34:35] op_sel_hi:[0,1,1]\n\t"
    "v_pk_fma_f32 v[36:37], v[136:137], v[176:177], v[36:37] op_sel_hi:[0,1,1]\n\t"
    "v_pk_fma_f32 v[38:39], v[136:137], v[178:179], v[38:39] op_sel_hi:[0,1,1]\n\t"
    "v_pk_fma_f32 v[40:41], v[136:137], v[180:181], v[40:41] op_sel_hi:[0,1,1]\n\t"
    "v_pk_fma_f32 v[42:43], v[136:137], v[182:183], v[42:43] op_sel_hi:[0,1,1]\n\t"
    "buffer_load_dwordx4 v[108:111], v228, %[srdw], %[tmp] offen offset:1024 sc1\n\t"
    "v_pk_mul_f32 v[36:37], v[200:201], v[36:37]\n\t"
    "v_pk_mul_f32 v[38:39], v[202:203], v[38:39]\n\t"
    "v_pk_mul_f32 v[40:41], v[32:33], v[40:41]\n\t"
    "v_pk_mul_f32 v[42:43], v[34:35], v[42:43]\n\t"
    "v_pk_mul_f32 v[200:201], v[200:201], v[232:233] op_sel_hi:[1,0]\n\t"
    "v_pk_mul_f32 v[202:203], v[202:203], v[232:233] op_sel_hi:[1,0]\n\t"
    "v_pk_mul_f32 v[32:33], v[32:33], v[232:233] op_sel_hi:[1,0]\n\t"
    "v_pk_mul_f32 v[34:35], v[34:35], v[232:233] op_sel_hi:[1,0]\n\t"
    "v_exp_f32 v200, v200\n\t"
    "s_mov_b32 %[tmp], 32768\n\t"
    "buffer_load_dwordx4 v[80:83], v228, %[srdw], %[tmp] offen sc1\n\t"
    "v_exp_f32 v202, v202\n\t"
    "v_exp_f32 v32, v32\n\t"
    "v_exp_f32 v34, v34\n\t"
    "v_exp_f32 v201, v201\n\t"
    "v_exp_f32 v203, v203\n\t"
    "v_exp_f32 v33, v33\n\t"
    "v_exp_f32 v35, v35\n\t"
    "v_pk_fma_f32 v[200:201], v[200:201], v[138:139], v[138:139] op_sel_hi:[1,0,0]\n\t"
    "v_pk_fma_f32 v[202:203], v[202:203], v[138:139], v[138:139] op_sel_hi:[1,0,0]\n\t"
    "v_pk_fma_f32 v[32:33], v[32:33], v[138:139], v[138:139] op_sel_hi:[1,0,0]\n\t"
    "buffer_load_dwordx4 v[112:115], v228, %[srdw], %[tmp] offen offset:1024 sc1\n\t"
    "v_pk_fma_f32 v[34:35], v[34:35], v[138:139], v[138:139] op_sel_hi:[1,0,0]\n\t"
    "v_rcp_f32 v200, v200\n\t"
    "v_rcp_f32 v202, v202\n\t"
    "v_rcp_f32 v32, v32\n\t"
    "v_rcp_f32 v34, v34\n\t"
    "v_rcp_f32 v201, v201\n\t"
    "v_rcp_f32 v203, v203\n\t"
    "v_rcp_f32 v33, v33\n\t"
    "v_rcp_f32 v35, v35\n\t"
    "s_mov_b32 %[tmp], 40960\n\t"
    "buffer_load_dwordx4 v[84:87], v228, %[srdw], %[tmp] offen sc1\n\t"
    "v_pk_mul_f32 v[36:37], v[36:37], v[200:201]\n\t"
    "v_pk_mul_f32 v[38:39], v[38:39], v[202:203]\n\t"
    "v_pk_mul_f32 v[40:41], v[40:41], v[32:33]\n\t"
    "v_pk_mul_f32 v[42:43], v[42:43], v[34:35]\n\t"
    "v_cvt_pk_bf16_f32 v200, v36, v37\n\t"
    "v_cvt_pk_bf16_f32 v201, v38, v39\n\t"
    "v_cvt_pk_bf16_f32 v202, v40, v41\n\t"
    "v_cvt_pk_bf16_f32 v203, v42, v43\n\t"
    "s_mov_b32 %[tmp2], 294912\n\t"
    "buffer_store_dwordx4 v[200:203], v227, %[srdw], %[tmp2] offen sc1\n\t"
    "v_accvgpr_read_b32 v48, a172\n\t"
    "v_accvgpr_read_b32 v49, a173\n\t"
    "buffer_load_dwordx4 v[116:119], v228, %[srdw], %[tmp] offen offset:1024 sc1\n\t"
    "v_accvgpr_read_b32 v50, a174\n\t"
    "v_accvgpr_read_b32 v51, a175\n\t"
    "v_accvgpr_read_b32 v52, a204\n\t"
    "v_accvgpr_read_b32 v53, a205\n\t"
    "v_accvgpr_read_b32 v54, a206\n\t"
    "v_accvgpr_read_b32 v55, a207\n\t"
    "v_accvgpr_read_b32 v56, a236\n\t"
    "v_accvgpr_read_b32 v57, a237\n\t"
    "v_accvgpr_read_b32 v58, a238\n\t"
    "v_accvgpr_read_b32 v59, a239\n\t"
    "v_mov_b32 v234, v143\n\t"
    "s_mov_b32 %[tmp], 49152\n\t"
    "buffer_load_dwordx4 v[88:91], v228, %[srdw], %[tmp] offen sc1\n\t"
    "v_pk_fma_f32 v[204:205], v[140:141], v[168:169], v[204:205] op_sel_hi:[0,1,1]\n\t"
    "v_pk_fma_f32 v[206:207], v[140:141], v[170:171], v[206:207] op_sel_hi:[0,1,1]\n\t"
    "v_pk_fma_f32 v[48:49], v[140:141], v[172:173], v[48:49] op_sel_hi:[0,1,1]\n\t"
    "v_pk_fma_f32 v[50:51], v[140:141], v[174:175], v[50:51] op_sel_hi:[0,1,1]\n\t"
    "v_pk_fma_f32 v[52:53], v[140:141], v[176:177], v[52:53] op_sel_hi:[0,1,1]\n\t"
    "v_pk_fma_f32 v[54:55], v[140:141], v[178:179], v[54:55] op_sel_hi:[0,1,1]\n\t"
    "v_pk_fma_f32 v[56:57], v[140:141], v[180:181], v[56:57] op_sel_hi:[0,1,1]\n\t"
    "v_pk_fma_f32 v[58:59], v[140:141], v[182:183], v[58:59] op_sel_hi:[0,1,1]\n\t"
    "v_pk_mul_f32 v[52:53], v[204:205], v[52:53]\n\t"
    "v_pk_mul_f32 v[54:55], v[206:207], v[54:55]\n\t"
    "v_pk_mul_f32 v[56:57], v[48:49], v[56:57]\n\t"
    "buffer_load_dwordx4 v[120:123], v228, %[srdw], %[tmp] offen offset:1024 sc1\n\t"
    "v_pk_mul_f32 v[58:59], v[50:51], v[58:59]\n\t"
    "v_pk_mul_f32 v[204:205], v[204:205], v[234:235] op_sel_hi:[1,0]\n\t"
    "v_pk_mul_f32 v[206:207], v[206:207], v[234:235] op_sel_hi:[1,0]\n\t"
    "v_pk_mul_f32 v[48:49], v[48:49], v[234:235] op_sel_hi:[1,0]\n\t"
    "v_pk_mul_f32 v[50:51], v[50:51], v[234:235] op_sel_hi:[1,0]\n\t"
    "v_exp_f32 v204, v204\n\t"
    "v_exp_f32 v206, v206\n\t"
    "v_exp_f32 v48, v48\n\t"
    "v_exp_f32 v50, v50\n\t"
    "v_exp_f32 v205, v205\n\t"
    "v_exp_f32 v207, v207\n\t"
    "v_exp_f32 v49, v49\n\t"
    "s_mov_b32 %[tmp], 57344\n\t"
    "buffer_load_dwordx4 v[92:95], v228, %[srdw], %[tmp] offen sc1\n\t"
    "v_exp_f32 v51, v51\n\t"
    "v_pk_fma_f32 v[204:205], v[204:205], v[142:143], v[142:143] op_sel_hi:[1,0,0]\n\t"
    "v_pk_fma_f32 v[206:207], v[206:207], v[142:143], v[142:143] op_sel_hi:[1,0,0]\n\t"
    "v_pk_fma_f32 v[48:49], v[48:49], v[142:143], v[142:143] op_sel_hi:[1,0,0]\n\t"
    "v_pk_fma_f32 v[50:51], v[50:51], v[142:143], v[142:143] op_sel_hi:[1,0,0]\n\t"
    "v_rcp_f32 v204, v204\n\t"
    "v_rcp_f32 v206, v206\n\t"
    "v_rcp_f32 v48, v48\n\t"
    "v_rcp_f32 v50, v50\n\t"
    "v_rcp_f32 v205, v205\n\t"
    "v_rcp_f32 v207, v207\n\t"
    "buffer_load_dwordx4 v[124:127], v228, %[srdw], %[tmp] offen offset:1024 sc1\n\t"
    "v_rcp_f32 v49, v49\n\t"
    "v_rcp_f32 v51, v51\n\t"
    "v_pk_mul_f32 v[52:53], v[52:53], v[204:205]\n\t"
    "v_pk_mul_f32 v[54:55], v[54:55], v[206:207]\n\t"
    "v_pk_mul_f32 v[56:57], v[56:57], v[48:49]\n\t"
    "v_pk_mul_f32 v[58:59], v[58:59], v[50:51]\n\t"
    "v_cvt_pk_bf16_f32 v204, v52, v53\n\t"
    "v_cvt_pk_bf16_f32 v205, v54, v55\n\t"
    "v_cvt_pk_bf16_f32 v206, v56, v57\n\t"
    "v_cvt_pk_bf16_f32 v207, v58, v59\n\t"
    "s_mov_b32 %[tmp2], 442368\n\t"
    "buffer_store_dwordx4 v[204:207], v227, %[srdw], %[tmp2] offen sc1\n\t"
    "v_accvgpr_read_b32 v44, a144\n\t"
    "v_accvgpr_read_b32 v45, a145\n\t"
    "v_accvgpr_read_b32 v46, a146\n\t"
    "v_accvgpr_read_b32 v47, a147\n\t"
    "v_accvgpr_read_b32 v32, a176\n\t"
    "v_accvgpr_read_b32 v33, a177\n\t"
    "v_accvgpr_read_b32 v34, a178\n\t"
    "v_accvgpr_read_b32 v35, a179\n\t"
    "v_accvgpr_read_b32 v36, a208\n\t"
    "v_accvgpr_read_b32 v37, a209\n\t"
    "v_accvgpr_read_b32 v38, a210\n\t"
    "v_accvgpr_read_b32 v39, a211\n\t"
    "v_accvgpr_read_b32 v40, a240\n\t"
    "v_accvgpr_read_b32 v41, a241\n\t"
    "v_accvgpr_read_b32 v42, a242\n\t"
    "v_accvgpr_read_b32 v43, a243\n\t"
    "v_mov_b32 v232, v147\n\t"
    "v_pk_fma_f32 v[44:45], v[144:145], v[168:169], v[44:45] op_sel_hi:[0,1,1]\n\t"
    "v_pk_fma_f32 v[46:47], v[144:145], v[170:171], v[46:47] op_sel_hi:[0,1,1]\n\t"
    "v_pk_fma_f32 v[32:33], v[144:145], v[172:173], v[32:33] op_sel_hi:[0,1,1]\n\t"
    "v_pk_fma_f32 v[34:35], v[144:145], v[174:175], v[34:35] op_sel_hi:[0,1,1]\n\t"
    "v_pk_fma_f32 v[36:37], v[144:145], v[176:177], v[36:37] op_sel_hi:[0,1,1]\n\t"
    "v_pk_fma_f32 v[38:39], v[144:145], v[178:179], v[38:39] op_sel_hi:[0,1,1]\n\t"
    "v_pk_fma_f32 v[40:41], v[144:145], v[180:181], v[40:41] op_sel_hi:[0,1,1]\n\t"
    "v_pk_fma_f32 v[42:43], v[144:145], v[182:183], v[42:43] op_sel_hi:[0,1,1]\n\t"
    "v_pk_mul_f32 v[36:37], v[44:45], v[36:37]\n\t"
    "v_pk_mul_f32 v[38:39], v[46:47], v[38:39]\n\t"
    "v_pk_mul_f32 v[40:41], v[32:33], v[40:41]\n\t"
    "v_pk_mul_f32 v[42:43], v[34:35], v[42:43]\n\t"
    "v_pk_mul_f32 v[44:45], v[44:45], v[232:233] op_sel_hi:[1,0]\n\t"
    "v_pk_mul_f32 v[46:47], v[46:47], v[232:233] op_sel_hi:[1,0]\n\t"
    "v_pk_mul_f32 v[32:33], v[32:33], v[232:233] op_sel_hi:[1,0]\n\t"
    "v_pk_mul_f32 v[34:35], v[34:35], v[232:233] op_sel_hi:[1,0]\n\t"
    "v_exp_f32 v44, v44\n\t"
    "v_exp_f32 v46, v46\n\t"
    "v_exp_f32 v32, v32\n\t"
    "v_exp_f32 v34, v34\n\t"
    "v_exp_f32 v45, v45\n\t"
    "v_exp_f32 v47, v47\n\t"
    "v_exp_f32 v33, v33\n\t"
    "v_exp_f32 v35, v35\n\t"
    "v_pk_fma_f32 v[44:45], v[44:45], v[146:147], v[146:147] op_sel_hi:[1,0,0]\n\t"
    "v_pk_fma_f32 v[46:47], v[46:47], v[146:147], v[146:147] op_sel_hi:[1,0,0]\n\t"
    "v_pk_fma_f32 v[32:33], v[32:33], v[146:147], v[146:147] op_sel_hi:[1,0,0]\n\t"
    "v_pk_fma_f32 v[34:35], v[34:35], v[146:147], v[146:147] op_sel_hi:[1,0,0]\n\t"
    "v_rcp_f32 v44, v44\n\t"
    "v_rcp_f32 v46, v46\n\t"
    "v_rcp_f32 v32, v32\n\t"
    "v_rcp_f32 v34, v34\n\t"
    "v_rcp_f32 v45, v45\n\t"
    "v_rcp_f32 v47, v47\n\t"
    "v_rcp_f32 v33, v33\n\t"
    "v_rcp_f32 v35, v35\n\t"
    "v_pk_mul_f32 v[36:37], v[36:37], v[44:45]\n\t"
    "v_pk_mul_f32 v[38:39], v[38:39], v[46:47]\n\t"
    "v_pk_mul_f32 v[40:41], v[40:41], v[32:33]\n\t"
    "v_pk_mul_f32 v[42:43], v[42:43], v[34:35]\n\t"
    "v_cvt_pk_bf16_f32 v44, v36, v37\n\t"
    "v_cvt_pk_bf16_f32 v45, v38, v39\n\t"
    "v_cvt_pk_bf16_f32 v46, v40, v41\n\t"
    "v_cvt_pk_bf16_f32 v47, v42, v43\n\t"
    "s_mov_b32 %[tmp2], 589824\n\t"
    "buffer_store_dwordx4 v[44:47], v227, %[srdw], %[tmp2] offen sc1\n\t"
    "v_and_b32 v223, 0x180, v223\n\t"
    "v_add_u32 v229, %[fl], v223\n\t"
    "s_waitcnt vmcnt(18)\n\t"
    "v_add_f32_dpp v184, v184, v184 row_ror:4 row_mask:0xf bank_mask:0xf\n\t"
    "v_add_f32_dpp v185, v185, v185 row_ror:4 row_mask:0xf bank_mask:0xf\n\t"
    "v_add_f32_dpp v186, v186, v186 row_ror:4 row_mask:0xf bank_mask:0xf\n\t"
    "v_add_f32_dpp v187, v187, v187 row_ror:4 row_mask:0xf bank_mask:0xf\n\t"
    "v_add_f32_dpp v184, v184, v184 row_ror:8 row_mask:0xf bank_mask:0xf\n\t"
    "v_add_f32_dpp v185, v185, v185 row_ror:8 row_mask:0xf bank_mask:0xf\n\t"
    "v_add_f32_dpp v186, v186, v186 row_ror:8 row_mask:0xf bank_mask:0xf\n\t"
    "v_add_f32_dpp v187, v187, v187 row_ror:8 row_mask:0xf bank_mask:0xf\n\t"
    "v_mov_b32 v188, v184\n\t"
    "v_mov_b32 v189, v185\n\t"
    "v_mov_b32 v190, v186\n\t"
    "v_mov_b32 v191, v187\n\t"
    "v_permlane16_swap_b32 v184, v188\n\t"
    "v_permlane16_swap_b32 v185, v189\n\t"
    "v_permlane16_swap_b32 v186, v190\n\t"
    "v_permlane16_swap_b32 v187, v191\n\t"
    "v_add_f32 v184, v184, v188\n\t"
    "v_add_f32 v185, v185, v189\n\t"
    "v_add_f32 v186, v186, v190\n\t"
    "v_add_f32 v187, v187, v191\n\t"
    "v_cvt_pk_bf16_f32 v184, v184, v185\n\t"
    "v_cvt_pk_bf16_f32 v185, v186, v187\n\t"
    "v_lshrrev_b32 v189, 1, v253\n\t"
    "v_add_u32 v189, %[x2o], v189\n\t"
    "ds_write_b64 v189, v[184:185] offset:1024\n\t"
    "v_add_u32 v188, %[x2o], v248\n\t"
    "v_add_u32 v190, %[x2r], v248\n\t"
    "v_lshrrev_b32 v191, 5, v248\n\t"
    "v_and_b32 v191, 0x18, v191\n\t"
    "v_add_u32 v191, %[x2r], v191\n\t"
    "s_waitcnt vmcnt(2)\n\t"
    "v_pk_add_f32 v[64:65], v[64:65], v[68:69]\n\t"
    "v_pk_add_f32 v[66:67], v[66:67], v[70:71]\n\t"
    "v_pk_add_f32 v[96:97], v[96:97], v[100:101]\n\t"
    "v_pk_add_f32 v[98:99], v[98:99], v[102:103]\n\t"
    "v_pk_add_f32 v[72:73], v[72:73], v[76:77]\n\t"
    "v_pk_add_f32 v[74:75], v[74:75], v[78:79]\n\t"
    "v_pk_add_f32 v[104:105], v[104:105], v[108:109]\n\t"
    "v_pk_add_f32 v[106:107], v[106:107], v[110:111]\n\t"
    "v_pk_add_f32 v[80:81], v[80:81], v[84:85]\n\t"
    "v_pk_add_f32 v[82:83], v[82:83], v[86:87]\n\t"
    "v_pk_add_f32 v[112:113], v[112:113], v[116:117]\n\t"
    "v_pk_add_f32 v[114:115], v[114:115], v[118:119]\n\t"
    "v_pk_add_f32 v[88:89], v[88:89], v[92:93]\n\t"
    "v_pk_add_f32 v[90:91], v[90:91], v[94:95]\n\t"
    "v_pk_add_f32 v[120:121], v[120:121], v[124:125]\n\t"
    "v_pk_add_f32 v[122:123], v[122:123], v[126:127]\n\t"
    "v_pk_add_f32 v[64:65], v[64:65], v[72:73]\n\t"
    "v_pk_add_f32 v[66:67], v[66:67], v[74:75]\n\t"
    "v_pk_add_f32 v[96:97], v[96:97], v[104:105]\n\t"
    "v_pk_add_f32 v[98:99], v[98:99], v[106:107]\n\t"
    "v_pk_add_f32 v[80:81], v[80:81], v[88:89]\n\t"
    "v_pk_add_f32 v[82:83], v[82:83], v[90:91]\n\t"
    "v_pk_add_f32 v[112:113], v[112:113], v[120:121]\n\t"
    "v_pk_add_f32 v[114:115], v[114:115], v[122:123]\n\t"
    "v_pk_add_f32 v[64:65], v[64:65], v[80:81]\n\t"
    "v_pk_add_f32 v[66:67], v[66:67], v[82:83]\n\t"
    "v_pk_add_f32 v[96:97], v[96:97], v[112:113]\n\t"
    "v_pk_add_f32 v[98:99], v[98:99], v[114:115]\n\t"
    "v_cvt_pk_bf16_f32 v64, v64, v65\n\t"
    "v_cvt_pk_bf16_f32 v65, v66, v67\n\t"
    "v_cvt_pk_bf16_f32 v66, v96, v97\n\t"
    "v_cvt_pk_bf16_f32 v67, v98, v99\n\t"
    "ds_write_b128 v188, v[64:67]\n\t"
    "v_add_u32 v84, 32768, v227\n\t"
    "s_waitcnt lgkmcnt(0)\n\t"
    "s_barrier\n\t"
    "ds_read_b64 v[80:81], v191 offset:1024\n\t"
    "ds_read_b64 v[82:83], v191 offset:2112\n\t"
    "ds_read_b128 v[72:75], v190\n\t"
    "ds_read_b128 v[76:79], v190 offset:1088\n\t"
    "s_waitcnt lgkmcnt(2)\n\t"
    "v_mfma_f32_16x16x32_bf16 v[32:35], v[80:83], v[0:3], 0\n\t"
    "v_mfma_f32_16x16x32_bf16 v[36:39], v[80:83], v[4:7], 0\n\t"
    "v_mfma_f32_16x16x32_bf16 v[40:43], v[80:83], v[8:11], 0\n\t"
    "v_mfma_f32_16x16x32_bf16 v[44:47], v[80:83], v[12:15], 0\n\t"
    "v_mfma_f32_16x16x32_bf16 v[48:51], v[80:83], v[16:19], 0\n\t"
    "v_mfma_f32_16x16x32_bf16 v[52:55], v[80:83], v[20:23], 0\n\t"
    "v_mfma_f32_16x16x32_bf16 v[56:59], v[80:83], v[24:27], 0\n\t"
    "v_mfma_f32_16x16x32_bf16 v[60:63], v[80:83], v[28:31], 0\n\t"
    "s_waitcnt lgkmcnt(0)\n\t"
    "v_mfma_f32_16x16x32_bf16 v[96:99], v[72:75], v[0:3], 0\n\t"
    "v_add_f32 v32, s94, v32\n\t"
    "v_mfma_f32_16x16x32_bf16 v[100:103], v[76:79], v[0:3], 0\n\t"
    "v_rcp_f32 v32, v32\n\t"
    "v_mfma_f32_16x16x32_bf16 v[104:107], v[72:75], v[4:7], 0\n\t"
    "v_add_f32 v36, s94, v36\n\t"
    "v_mfma_f32_16x16x32_bf16 v[108:111], v[76:79], v[4:7], 0\n\t"
    "v_rcp_f32 v36, v36\n\t"
    "v_mfma_f32_16x16x32_bf16 v[112:115], v[72:75], v[8:11], 0\n\t"
    "v_add_f32 v40, s94, v40\n\t"
    "v_mfma_f32_16x16x32_bf16 v[116:119], v[76:79], v[8:11], 0\n\t"
    "v_rcp_f32 v40, v40\n\t"
    "v_mfma_f32_16x16x32_bf16 v[120:123], v[72:75], v[12:15], 0\n\t"
    "v_add_f32 v44, s94, v44\n\t"
    "v_mfma_f32_16x16x32_bf16 v[124:127], v[76:79], v[12:15], 0\n\t"
    "v_rcp_f32 v44, v44\n\t"
    "v_mfma_f32_16x16x32_bf16 v[192:195], v[72:75], v[16:19], 0\n\t"
    "v_add_f32 v48, s94, v48\n\t"
    "v_mfma_f32_16x16x32_bf16 v[196:199], v[76:79], v[16:19], 0\n\t"
    "v_rcp_f32 v48, v48\n\t"
    "v_mfma_f32_16x16x32_bf16 v[200:203], v[72:75], v[20:23], 0\n\t"
    "v_add_f32 v52, s94, v52\n\t"
    "v_mfma_f32_16x16x32_bf16 v[204:207], v[76:79], v[20:23], 0\n\t"
    "v_rcp_f32 v52, v52\n\t"
    "v_mfma_f32_16x16x32_bf16 v[184:187], v[72:75], v[24:27], 0\n\t"
    "v_add_f32 v56, s94, v56\n\t"
    "v_mfma_f32_16x16x32_bf16 v[188:191], v[76:79], v[24:27], 0\n\t"
    "v_rcp_f32 v56, v56\n\t"
    "v_mfma_f32_16x16x32_bf16 v[88:91], v[72:75], v[28:31], 0\n\t"
    "v_add_f32 v60, s94, v60\n\t"
    "v_mfma_f32_16x16x32_bf16 v[92:95], v[76:79], v[28:31], 0\n\t"
    "v_rcp_f32 v60, v60\n\t"
    "v_pk_mul_f32 v[96:97], v[96:97], v[32:33] op_sel_hi:[1,0]\n\t"
    "v_pk_mul_f32 v[98:99], v[98:99], v[32:33] op_sel_hi:[1,0]\n\t"
    "v_pk_mul_f32 v[100:101], v[100:101], v[32:33] op_sel_hi:[1,0]\n\t"
    "v_pk_mul_f32 v[102:103], v[102:103], v[32:33] op_sel_hi:[1,0]\n\t"
    "v_cvt_pk_bf16_f32 v96, v96, v97\n\t"
    "v_cvt_pk_bf16_f32 v97, v98, v99\n\t"
    "v_cvt_pk_bf16_f32 v98, v100, v101\n\t"
    "v_cvt_pk_bf16_f32 v99, v102, v103\n\t"
    "s_mov_b32 %[tmp2], 0\n\t"
    "buffer_store_dwordx4 v[96:99], v84, %[srdw], %[tmp2] offen sc1\n\t"
    "v_accvgpr_read_b32 v12, a148\n\t"
    "v_accvgpr_read_b32 v13, a149\n\t"
    "v_accvgpr_read_b32 v14, a150\n\t"
    "v_accvgpr_read_b32 v15, a151\n\t"
    "v_pk_mul_f32 v[104:105], v[104:105], v[36:37] op_sel_hi:[1,0]\n\t"
    "v_pk_mul_f32 v[106:107], v[106:107], v[36:37] op_sel_hi:[1,0]\n\t"
    "v_pk_mul_f32 v[108:109], v[108:109], v[36:37] op_sel_hi:[1,0]\n\t"
    "v_pk_mul_f32 v[110:111], v[110:111], v[36:37] op_sel_hi:[1,0]\n\t"
    "v_cvt_pk_bf16_f32 v104, v104, v105\n\t"
    "v_cvt_pk_bf16_f32 v105, v106, v107\n\t"
    "v_cvt_pk_bf16_f32 v106, v108, v109\n\t"
    "v_cvt_pk_bf16_f32 v107, v110, v111\n\t"
    "s_mov_b32 %[tmp2], 147456\n\t"
    "buffer_store_dwordx4 v[104:107], v84, %[srdw], %[tmp2] offen sc1\n\t"
    "v_accvgpr_read_b32 v0, a180\n\t"
    "v_accvgpr_read_b32 v1, a181\n\t"
    "v_accvgpr_read_b32 v2, a182\n\t"
    "v_accvgpr_read_b32 v3, a183\n\t"
    "v_pk_mul_f32 v[112:113], v[112:113], v[40:41] op_sel_hi:[1,0]\n\t"
    "v_pk_mul_f32 v[114:115], v[114:115], v[40:41] op_sel_hi:[1,0]\n\t"
    "v_pk_mul_f32 v[116:117], v[116:117], v[40:41] op_sel_hi:[1,0]\n\t"
    "v_pk_mul_f32 v[118:119], v[118:119], v[40:41] op_sel_hi:[1,0]\n\t"
    "v_cvt_pk_bf16_f32 v112, v112, v113\n\t"
    "v_cvt_pk_bf16_f32 v113, v114, v115\n\t"
    "v_cvt_pk_bf16_f32 v114, v116, v117\n\t"
    "v_cvt_pk_bf16_f32 v115, v118, v119\n\t"
    "s_mov_b32 %[tmp2], 294912\n\t"
    "buffer_store_dwordx4 v[112:115], v84, %[srdw], %[tmp2] offen sc1\n\t"
    "v_accvgpr_read_b32 v4, a212\n\t"
    "v_accvgpr_read_b32 v5, a213\n\t"
    "v_accvgpr_read_b32 v6, a214\n\t"
    "v_accvgpr_read_b32 v7, a215\n\t"
    "v_pk_mul_f32 v[120:121], v[120:121], v[44:45] op_sel_hi:[1,0]\n\t"
    "v_pk_mul_f32 v[122:123], v[122:123], v[44:45] op_sel_hi:[1,0]\n\t"
    "v_pk_mul_f32 v[124:125], v[124:125], v[44:45] op_sel_hi:[1,0]\n\t"
    "v_pk_mul_f32 v[126:127], v[126:127], v[44:45] op_sel_hi:[1,0]\n\t"
    "v_cvt_pk_bf16_f32 v120, v120, v121\n\t"
    "v_cvt_pk_bf16_f32 v121, v122, v123\n\t"
    "v_cvt_pk_bf16_f32 v122, v124, v125\n\t"
    "v_cvt_pk_bf16_f32 v123, v126, v127\n\t"
    "s_mov_b32 %[tmp2], 442368\n\t"
    "buffer_store_dwordx4 v[120:123], v84, %[srdw], %[tmp2] offen sc1\n\t"
    "v_accvgpr_read_b32 v8, a244\n\t"
    "v_accvgpr_read_b32 v9, a245\n\t"
    "v_accvgpr_read_b32 v10, a246\n\t"
    "v_accvgpr_read_b32 v11, a247\n\t"
    "v_pk_mul_f32 v[192:193], v[192:193], v[48:49] op_sel_hi:[1,0]\n\t"
    "v_pk_mul_f32 v[194:195], v[194:195], v[48:49] op_sel_hi:[1,0]\n\t"
    "v_pk_mul_f32 v[196:197], v[196:197], v[48:49] op_sel_hi:[1,0]\n\t"
    "v_pk_mul_f32 v[198:199], v[198:199], v[48:49] op_sel_hi:[1,0]\n\t"
    "v_cvt_pk_bf16_f32 v192, v192, v193\n\t"
    "v_cvt_pk_bf16_f32 v193, v194, v195\n\t"
    "v_cvt_pk_bf16_f32 v194, v196, v197\n\t"
    "v_cvt_pk_bf16_f32 v195, v198, v199\n\t"
    "s_mov_b32 %[tmp2], 589824\n\t"
    "buffer_store_dwordx4 v[192:195], v84, %[srdw], %[tmp2] offen sc1\n\t"
    "v_accvgpr_read_b32 v28, a152\n\t"
    "v_accvgpr_read_b32 v29, a153\n\t"
    "v_accvgpr_read_b32 v30, a154\n\t"
    "v_accvgpr_read_b32 v31, a155\n\t"
    "v_pk_mul_f32 v[200:201], v[200:201], v[52:53] op_sel_hi:[1,0]\n\t"
    "v_pk_mul_f32 v[202:203], v[202:203], v[52:53] op_sel_hi:[1,0]\n\t"
    "v_pk_mul_f32 v[204:205], v[204:205], v[52:53] op_sel_hi:[1,0]\n\t"
    "v_pk_mul_f32 v[206:207], v[206:207], v[52:53] op_sel_hi:[1,0]\n\t"
    "v_cvt_pk_bf16_f32 v200, v200, v201\n\t"
    "v_cvt_pk_bf16_f32 v201, v202, v203\n\t"
    "v_cvt_pk_bf16_f32 v202, v204, v205\n\t"
    "v_cvt_pk_bf16_f32 v203, v206, v207\n\t"
    "s_mov_b32 %[tmp2], 737280\n\t"
    "buffer_store_dwordx4 v[200:203], v84, %[srdw], %[tmp2] offen sc1\n\t"
    "v_accvgpr_read_b32 v16, a184\n\t"
    "v_accvgpr_read_b32 v17, a185\n\t"
    "v_accvgpr_read_b32 v18, a186\n\t"
    "v_accvgpr_read_b32 v19, a187\n\t"
    "v_pk_mul_f32 v[184:185], v[184:185], v[56:57] op_sel_hi:[1,0]\n\t"
    "v_pk_mul_f32 v[186:187], v[186:187], v[56:57] op_sel_hi:[1,0]\n\t"
    "v_pk_mul_f32 v[188:189], v[188:189], v[56:57] op_sel_hi:[1,0]\n\t"
    "v_pk_mul_f32 v[190:191], v[190:191], v[56:57] op_sel_hi:[1,0]\n\t"
    "v_cvt_pk_bf16_f32 v184, v184, v185\n\t"
    "v_cvt_pk_bf16_f32 v185, v186, v187\n\t"
    "v_cvt_pk_bf16_f32 v186, v188, v189\n\t"
    "v_cvt_pk_bf16_f32 v187, v190, v191\n\t"
    "s_mov_b32 %[tmp2], 884736\n\t"
    "buffer_store_dwordx4 v[184:187], v84, %[srdw], %[tmp2] offen sc1\n\t"
    "v_accvgpr_read_b32 v20, a216\n\t"
    "v_accvgpr_read_b32 v21, a217\n\t"
    "v_accvgpr_read_b32 v22, a218\n\t"
    "v_accvgpr_read_b32 v23, a219\n\t"
    "v_pk_mul_f32 v[88:89], v[88:89], v[60:61] op_sel_hi:[1,0]\n\t"
    "v_pk_mul_f32 v[90:91], v[90:91], v[60:61] op_sel_hi:[1,0]\n\t"
    "v_pk_mul_f32 v[92:93], v[92:93], v[60:61] op_sel_hi:[1,0]\n\t"
    "v_pk_mul_f32 v[94:95], v[94:95], v[60:61] op_sel_hi:[1,0]\n\t"
    "v_cvt_pk_bf16_f32 v88, v88, v89\n\t"
    "v_cvt_pk_bf16_f32 v89, v90, v91\n\t"
    "v_cvt_pk_bf16_f32 v90, v92, v93\n\t"
    "v_cvt_pk_bf16_f32 v91, v94, v95\n\t"
    "s_mov_b32 %[tmp2], 1032192\n\t"
    "buffer_store_dwordx4 v[88:91], v84, %[srdw], %[tmp2] offen sc1\n\t"
    "v_accvgpr_read_b32 v24, a248\n\t"
    "v_accvgpr_read_b32 v25, a249\n\t"
    "v_accvgpr_read_b32 v26, a250\n\t"
    "v_accvgpr_read_b32 v27, a251\n\t"
    "v_mov_b32 v234, v151\n\t"
    "v_pk_fma_f32 v[12:13], v[148:149], v[168:169], v[12:13] op_sel_hi:[0,1,1]\n\t"
    "v_pk_fma_f32 v[14:15], v[148:149], v[170:171], v[14:15] op_sel_hi:[0,1,1]\n\t"
    "v_pk_fma_f32 v[0:1], v[148:149], v[172:173], v[0:1] op_sel_hi:[0,1,1]\n\t"
    "v_pk_fma_f32 v[2:3], v[148:149], v[174:175], v[2:3] op_sel_hi:[0,1,1]\n\t"
    "v_pk_fma_f32 v[4:5], v[148:149], v[176:177], v[4:5] op_sel_hi:[0,1,1]\n\t"
    "v_pk_fma_f32 v[6:7], v[148:149], v[178:179], v[6:7] op_sel_hi:[0,1,1]\n\t"
    "v_pk_fma_f32 v[8:9], v[148:149], v[180:181], v[8:9] op_sel_hi:[0,1,1]\n\t"
    "v_pk_fma_f32 v[10:11], v[148:149], v[182:183], v[10:11] op_sel_hi:[0,1,1]\n\t"
    "v_pk_mul_f32 v[4:5], v[12:13], v[4:5]\n\t"
    "v_pk_mul_f32 v[6:7], v[14:15], v[6:7]\n\t"
    "v_pk_mul_f32 v[8:9], v[0:1], v[8:9]\n\t"
    "v_pk_mul_f32 v[10:11], v[2:3], v[10:11]\n\t"
    "v_pk_mul_f32 v[12:13], v[12:13], v[234:235] op_sel_hi:[1,0]\n\t"
    "v_pk_mul_f32 v[14:15], v[14:15], v[234:235] op_sel_hi:[1,0]\n\t"
    "v_pk_mul_f32 v[0:1], v[0:1], v[234:235] op_sel_hi:[1,0]\n\t"
    "v_pk_mul_f32 v[2:3], v[2:3], v[234:235] op_sel_hi:[1,0]\n\t"
    "v_exp_f32 v12, v12\n\t"
    "v_exp_f32 v14, v14\n\t"
    "v_exp_f32 v0, v0\n\t"
    "v_exp_f32 v2, v2\n\t"
    "v_exp_f32 v13, v13\n\t"
    "v_exp_f32 v15, v15\n\t"
    "v_exp_f32 v1, v1\n\t"
    "v_exp_f32 v3, v3\n\t"
    "v_pk_fma_f32 v[12:13], v[12:13], v[150:151], v[150:151] op_sel_hi:[1,0,0]\n\t"
    "v_pk_fma_f32 v[14:15], v[14:15], v[150:151], v[150:151] op_sel_hi:[1,0,0]\n\t"
    "v_pk_fma_f32 v[0:1], v[0:1], v[150:151], v[150:151] op_sel_hi:[1,0,0]\n\t"
    "v_pk_fma_f32 v[2:3], v[2:3], v[150:151], v[150:151] op_sel_hi:[1,0,0]\n\t"
    "v_rcp_f32 v12, v12\n\t"
    "v_rcp_f32 v14, v14\n\t"
    "v_rcp_f32 v0, v0\n\t"
    "v_rcp_f32 v2, v2\n\t"
    "v_rcp_f32 v13, v13\n\t"
    "v_rcp_f32 v15, v15\n\t"
    "v_rcp_f32 v1, v1\n\t"
    "v_rcp_f32 v3, v3\n\t"
    "v_pk_mul_f32 v[4:5], v[4:5], v[12:13]\n\t"
    "v_pk_mul_f32 v[6:7], v[6:7], v[14:15]\n\t"
    "v_pk_mul_f32 v[8:9], v[8:9], v[0:1]\n\t"
    "v_pk_mul_f32 v[10:11], v[10:11], v[2:3]\n\t"
    "v_cvt_pk_bf16_f32 v12, v4, v5\n\t"
    "v_cvt_pk_bf16_f32 v13, v6, v7\n\t"
    "v_cvt_pk_bf16_f32 v14, v8, v9\n\t"
    "v_cvt_pk_bf16_f32 v15, v10, v11\n\t"
    "s_mov_b32 %[tmp2], 737280\n\t"
    "buffer_store_dwordx4 v[12:15], v227, %[srdw], %[tmp2] offen sc1\n\t"
    "v_mov_b32 v232, v155\n\t"
    "v_pk_fma_f32 v[28:29], v[152:153], v[168:169], v[28:29] op_sel_hi:[0,1,1]\n\t"
    "v_pk_fma_f32 v[30:31], v[152:153], v[170:171], v[30:31] op_sel_hi:[0,1,1]\n\t"
    "v_pk_fma_f32 v[16:17], v[152:153], v[172:173], v[16:17] op_sel_hi:[0,1,1]\n\t"
    "v_pk_fma_f32 v[18:19], v[152:153], v[174:175], v[18:19] op_sel_hi:[0,1,1]\n\t"
    "v_pk_fma_f32 v[20:21], v[152:153], v[176:177], v[20:21] op_sel_hi:[0,1,1]\n\t"
    "v_pk_fma_f32 v[22:23], v[152:153], v[178:179], v[22:23] op_sel_hi:[0,1,1]\n\t"
    "v_pk_fma_f32 v[24:25], v[152:153], v[180:181], v[24:25] op_sel_hi:[0,1,1]\n\t"
    "v_pk_fma_f32 v[26:27], v[152:153], v[182:183], v[26:27] op_sel_hi:[0,1,1]\n\t"
    "v_pk_mul_f32 v[20:21], v[28:29], v[20:21]\n\t"
    "v_pk_mul_f32 v[22:23], v[30:31], v[22:23]\n\t"
    "v_pk_mul_f32 v[24:25], v[16:17], v[24:25]\n\t"
    "v_pk_mul_f32 v[26:27], v[18:19], v[26:27]\n\t"
    "v_pk_mul_f32 v[28:29], v[28:29], v[232:233] op_sel_hi:[1,0]\n\t"
    "v_pk_mul_f32 v[30:31], v[30:31], v[232:233] op_sel_hi:[1,0]\n\t"
    "v_pk_mul_f32 v[16:17], v[16:17], v[232:233] op_sel_hi:[1,0]\n\t"
    "v_pk_mul_f32 v[18:19], v[18:19], v[232:233] op_sel_hi:[1,0]\n\t"
    "v_exp_f32 v28, v28\n\t"
    "v_exp_f32 v30, v30\n\t"
    "v_exp_f32 v16, v16\n\t"
    "v_exp_f32 v18, v18\n\t"
    "v_exp_f32 v29, v29\n\t"
    "v_exp_f32 v31, v31\n\t"
    "v_exp_f32 v17, v17\n\t"
    "v_exp_f32 v19, v19\n\t"
    "v_pk_fma_f32 v[28:29], v[28:29], v[154:155], v[154:155] op_sel_hi:[1,0,0]\n\t"
    "v_pk_fma_f32 v[30:31], v[30:31], v[154:155], v[154:155] op_sel_hi:[1,0,0]\n\t"
    "v_pk_fma_f32 v[16:17], v[16:17], v[154:155], v[154:155] op_sel_hi:[1,0,0]\n\t"
    "v_pk_fma_f32 v[18:19], v[18:19], v[154:155], v[154:155] op_sel_hi:[1,0,0]\n\t"
    "v_rcp_f32 v28, v28\n\t"
    "v_rcp_f32 v30, v30\n\t"
    "v_rcp_f32 v16, v16\n\t"
    "v_rcp_f32 v18, v18\n\t"
    "v_rcp_f32 v29, v29\n\t"
    "v_rcp_f32 v31, v31\n\t"
    "v_rcp_f32 v17, v17\n\t"
    "v_rcp_f32 v19, v19\n\t"
    "v_pk_mul_f32 v[20:21], v[20:21], v[28:29]\n\t"
    "v_pk_mul_f32 v[22:23], v[22:23], v[30:31]\n\t"
    "v_pk_mul_f32 v[24:25], v[24:25], v[16:17]\n\t"
    "v_pk_mul_f32 v[26:27], v[26:27], v[18:19]\n\t"
    "v_cvt_pk_bf16_f32 v28, v20, v21\n\t"
    "v_cvt_pk_bf16_f32 v29, v22, v23\n\t"
    "v_cvt_pk_bf16_f32 v30, v24, v25\n\t"
    "v_cvt_pk_bf16_f32 v31, v26, v27\n\t"
    "s_mov_b32 %[tmp2], 884736\n\t"
    "buffer_store_dwordx4 v[28:31], v227, %[srdw], %[tmp2] offen sc1\n\t"
    "s_cmp_eq_u32 %[role], 3\n\t"
    "s_cbranch_scc0 66f\n\t"
    "s_waitcnt vmcnt(2)\n\t"
    "s_barrier\n\t"
    "s_cmp_lg_u32 %[w], 0\n\t"
    "s_cbranch_scc1 66f\n\t"
    "v_mov_b32 v250, s90\n\t"
    "v_mov_b32 v251, s91\n\t"
    "v_mov_b32 v249, %[fop]\n\t"
    "s_mov_b64 exec, 1\n\t"
    "buffer_store_dwordx2 v[250:251], v249, %[srdw], 0 offen sc1\n\t"
    "s_mov_b64 exec, -1\n\t"
    "66:\n\t"
    "v_accvgpr_read_b32 v12, a156\n\t"
    "v_accvgpr_read_b32 v13, a157\n\t"
    "v_lshrrev_b32 v255, 5, v248\n\t"
    "v_lshlrev_b32 v255, 11, v255\n\t"
    "v_and_b32 v254, 16, v248\n\t"
    "v_lshl_add_u32 v255, v254, 3, v255\n\t"
    "v_add_u32 v255, %[bvc], v255\n\t"
    "s_add_u32 m0, %[ldsb], 131072\n\t"
    "s_mov_b64 exec, 0xffffffff\n\t"
    "buffer_load_dword v255, %[srdb], %[kb4] offen lds\n\t"
    "s_mov_b64 exec, -1\n\t"
    "v_accvgpr_read_b32 v14, a158\n\t"
    "v_accvgpr_read_b32 v15, a159\n\t"
    "v_accvgpr_read_b32 v0, a188\n\t"
    "v_accvgpr_read_b32 v1, a189\n\t"
    "v_accvgpr_read_b32 v2, a190\n\t"
    "v_accvgpr_read_b32 v3, a191\n\t"
    "v_accvgpr_read_b32 v4, a220\n\t"
    "v_accvgpr_read_b32 v5, a221\n\t"
    "v_accvgpr_read_b32 v6, a222\n\t"
    "v_accvgpr_read_b32 v7, a223\n\t"
    "v_accvgpr_read_b32 v8, a252\n\t"
    "v_accvgpr_read_b32 v9, a253\n\t"
    "v_accvgpr_read_b32 v10, a254\n\t"
    "v_accvgpr_read_b32 v11, a255\n\t"
    "v_mov_b32 v234, v159\n\t"
    "v_pk_fma_f32 v[12:13], v[156:157], v[168:169], v[12:13] op_sel_hi:[0,1,1]\n\t"
    "v_pk_fma_f32 v[14:15], v[156:157], v[170:171], v[14:15] op_sel_hi:[0,1,1]\n\t"
    "v_pk_fma_f32 v[0:1], v[156:157], v[172:173], v[0:1] op_sel_hi:[0,1,1]\n\t"
    "v_pk_fma_f32 v[2:3], v[156:157], v[174:175], v[2:3] op_sel_hi:[0,1,1]\n\t"
    "v_pk_fma_f32 v[4:5], v[156:157], v[176:177], v[4:5] op_sel_hi:[0,1,1]\n\t"
    "v_pk_fma_f32 v[6:7], v[156:157], v[178:179], v[6:7] op_sel_hi:[0,1,1]\n\t"
    "v_pk_fma_f32 v[8:9], v[156:157], v[180:181], v[8:9] op_sel_hi:[0,1,1]\n\t"
    "v_pk_fma_f32 v[10:11], v[156:157], v[182:183], v[10:11] op_sel_hi:[0,1,1]\n\t"
    "v_pk_mul_f32 v[4:5], v[12:13], v[4:5]\n\t"
    "v_pk_mul_f32 v[6:7], v[14:15], v[6:7]\n\t"
    "v_pk_mul_f32 v[8:9], v[0:1], v[8:9]\n\t"
    "v_pk_mul_f32 v[10:11], v[2:3], v[10:11]\n\t"
    "v_pk_mul_f32 v[12:13], v[12:13], v[234:235] op_sel_hi:[1,0]\n\t"
    "v_pk_mul_f32 v[14:15], v[14:15], v[234:235] op_sel_hi:[1,0]\n\t"
    "v_pk_mul_f32 v[0:1], v[0:1], v[234:235] op_sel_hi:[1,0]\n\t"
    "buffer_load_dwordx2 v[230:231], v229, %[srdw], 0 offen sc1\n\t"
    "v_pk_mul_f32 v[2:3], v[2:3], v[234:235] op_sel_hi:[1,0]\n\t"
    "v_exp_f32 v12, v12\n\t"
    "v_exp_f32 v14, v14\n\t"
    "v_exp_f32 v0, v0\n\t"
    "v_exp_f32 v2, v2\n\t"
    "v_exp_f32 v13, v13\n\t"
    "v_exp_f32 v15, v15\n\t"
    "v_exp_f32 v1, v1\n\t"
    "v_exp_f32 v3, v3\n\t"
    "v_pk_fma_f32 v[12:13], v[12:13], v[158:159], v[158:159] op_sel_hi:[1,0,0]\n\t"
    "v_pk_fma_f32 v[14:15], v[14:15], v[158:159], v[158:159] op_sel_hi:[1,0,0]\n\t"
    "v_pk_fma_f32 v[0:1], v[0:1], v[158:159], v[158:159] op_sel_hi:[1,0,0]\n\t"
    "v_pk_fma_f32 v[2:3], v[2:3], v[158:159], v[158:159] op_sel_hi:[1,0,0]\n\t"
    "v_rcp_f32 v12, v12\n\t"
    "v_rcp_f32 v14, v14\n\t"
    "v_rcp_f32 v0, v0\n\t"
    "v_rcp_f32 v2, v2\n\t"
    "v_rcp_f32 v13, v13\n\t"
    "v_rcp_f32 v15, v15\n\t"
    "v_rcp_f32 v1, v1\n\t"
    "v_rcp_f32 v3, v3\n\t"
    "v_pk_mul_f32 v[4:5], v[4:5], v[12:13]\n\t"
    "v_pk_mul_f32 v[6:7], v[6:7], v[14:15]\n\t"
    "v_pk_mul_f32 v[8:9], v[8:9], v[0:1]\n\t"
    "v_pk_mul_f32 v[10:11], v[10:11], v[2:3]\n\t"
    "v_cvt_pk_bf16_f32 v12, v4, v5\n\t"
    "v_cvt_pk_bf16_f32 v13, v6, v7\n\t"
    "v_cvt_pk_bf16_f32 v14, v8, v9\n\t"
    "v_cvt_pk_bf16_f32 v15, v10, v11\n\t"
    "s_mov_b32 %[tmp2], 1032192\n\t"
    "buffer_store_dwordx4 v[12:15], v227, %[srdw], %[tmp2] offen sc1\n\t"
    "s_waitcnt vmcnt(1)\n\t"
    "v_cmp_ne_u64 vcc, s[90:91], v[230:231]\n\t"
    "s_cbranch_vccz 70f\n\t"
    "s_mov_b32 %[tmp2], 0x10000\n\t"
    "71:\n\t"
    "s_sleep 2\n\t"
    "buffer_load_dwordx2 v[230:231], v229, %[srdw], 0 offen sc1\n\t"
    "s_sub_u32 %[tmp2], %[tmp2], 1\n\t"
    "s_waitcnt vmcnt(0)\n\t"
    "s_cbranch_scc1 70f\n\t"
    "v_cmp_ne_u64 vcc, s[90:91], v[230:231]\n\t"
    "s_cbranch_vccnz 71b\n\t"
    "70:\n\t"
    "s_cmp_eq_u32 %[role], 3\n\t"
    "s_cbranch_scc0 72f\n\t"
    "s_waitcnt vmcnt(0)\n\t"
    "s_barrier\n\t"
    "s_cmp_lg_u32 %[w], 0\n\t"
    "s_cbranch_scc1 72f\n\t"
    "v_mov_b32 v250, s90\n\t"
    "v_mov_b32 v251, s91\n\t"
    "v_mov_b32 v249, %[fhp]\n\t"
    "s_mov_b64 exec, 1\n\t"
    "buffer_store_dwordx2 v[250:251], v249, %[srdw], 0 offen sc1\n\t"
    "s_mov_b64 exec, -1\n\t"
    "72:\n\t"
    "s_lshr_b32 %[tmpa], %[kb0], 6\n\t"
    "buffer_load_dwordx4 a[0:3], v208, %[srda], %[tmpa] offen offset:0 sc1\n\t"
    "buffer_load_dwordx4 a[16:19], v208, %[srda], %[tmpa] offen offset:1024 sc1\n\t"
    "buffer_load_dwordx4 a[4:7], v209, %[srda], %[tmpa] offen offset:0 sc1\n\t"
    "buffer_load_dwordx4 a[20:23], v209, %[srda], %[tmpa] offen offset:1024 sc1\n\t"
    "buffer_load_dwordx4 a[8:11], v210, %[srda], %[tmpa] offen offset:0 sc1\n\t"
    "buffer_load_dwordx4 a[24:27], v210, %[srda], %[tmpa] offen offset:1024 sc1\n\t"
    "buffer_load_dwordx4 a[12:15], v211, %[srda], %[tmpa] offen offset:0 sc1\n\t"
    "buffer_load_dwordx4 a[28:31], v211, %[srda], %[tmpa] offen offset:1024 sc1\n\t"
    "s_lshr_b32 %[tmpa], %[kb1], 6\n\t"
    "buffer_load_dwordx4 a[32:35], v208, %[srda], %[tmpa] offen offset:0 sc1\n\t"
    "buffer_load_dwordx4 a[48:51], v208, %[srda], %[tmpa] offen offset:1024 sc1\n\t"
    "buffer_load_dwordx4 a[36:39], v209, %[srda], %[tmpa] offen offset:0 sc1\n\t"
    "buffer_load_dwordx4 a[52:55], v209, %[srda], %[tmpa] offen offset:1024 sc1\n\t"
    "buffer_load_dwordx4 a[40:43], v210, %[srda], %[tmpa] offen offset:0 sc1\n\t"
    "buffer_load_dwordx4 a[56:59], v210, %[srda], %[tmpa] offen offset:1024 sc1\n\t"
    "buffer_load_dwordx4 a[44:47], v211, %[srda], %[tmpa] offen offset:0 sc1\n\t"
    "buffer_load_dwordx4 a[60:63], v211, %[srda], %[tmpa] offen offset:1024 sc1\n\t"
        : E_IO : E_IN, [aost] "s"(aob + (80 + 2 * col + wc) * 1024), [fkv] "s"((unsigned)(WS_FLAG + FL_KV) + col * 1024), [fkvp] "s"((unsigned)(WS_FLAG + FL_KV) + col * 1024 + p * 128), [kvld] "s"(kvo + wr * 2048), [kvz] "s"(kvo + 4096 + wr * 64),
          [x2o] "s"(lx + T_X + (wc * 2 + wr) * 1088), [x2r] "s"(lx + T_X + wc * 2176), [fl] "s"(fp1 + pm2 * 10240 + (role == 3 ? 4096 : 2048) + 1536), [fop] "s"(fp1 + p * 10240 + 4096 + col * 128), [fhp] "s"(fh) : E_CLOB);
    } else asm volatile(
    "v_add_u32 v227, %[aost], v248\n\t"
    "v_lshrrev_b32 v249, 4, v248\n\t"
    "v_add_u32 v250, %[rot], v249\n\t"
    "v_bfi_b32 v250, 3, v250, v249\n\t"
    "v_mul_u32_u24 v251, 5, v250\n\t"
    "v_add_u32 v252, %[kc1], v251\n\t"
    "v_add_u32 v251, %[kc0], v251\n\t"
    "v_subrev_u32 v253, 12, v250\n\t"
    "v_xor_b32 v253, 4, v253\n\t"
    "v_add_u32 v253, %[kc2], v253\n\t"
    "v_cmp_gt_u32 vcc, 12, v250\n\t"
    "s_nop 1\n\t"
    "v_cndmask_b32 v252, v253, v252, vcc\n\t"
    "v_cmp_gt_u32 vcc, 8, v250\n\t"
    "s_nop 1\n\t"
    "v_cndmask_b32 v251, v252, v251, vcc\n\t"
    "v_lshlrev_b32 v222, 17, v251\n\t"
    "v_cmp_gt_u32 vcc, 20, v249\n\t"
    "v_mov_b32 v253, 0x40000000\n\t"
    "s_nop 0\n\t"
    "v_cndmask_b32 v222, v253, v222, vcc\n\t"
    "v_and_b32 v212, 0x70, v248\n\t"
    "v_lshrrev_b32 v250, 2, v248\n\t"
    "v_and_b32 v250, 0x40, v250\n\t"
    "v_and_b32 v251, 0x20, v249\n\t"
    "v_xor_b32 v212, v212, v250\n\t"
    "v_xor_b32 v212, v212, v251\n\t"
    "v_and_b32 v250, 0x380, v248\n\t"
    "v_lshl_add_u32 v212, v250, 4, v212\n\t"
    "v_add_u32 v212, %[bvc], v212\n\t"
    "v_add_u32 v213, 0x3c00, v212\n\t"
    "v_readlane_b32 %[kb0], v222, 0\n\t"
    "v_readlane_b32 %[kb1], v222, 1\n\t"
    "v_readlane_b32 %[kb2], v222, 2\n\t"
    "v_readlane_b32 %[kb3], v222, 3\n\t"
    "v_readlane_b32 %[kb4], v222, 4\n\t"
    "v_add_u32 v208, %[av0], v248\n\t"
    "v_add_u32 v209, %[av1], v248\n\t"
    "v_add_u32 v210, %[av2], v248\n\t"
    "v_add_u32 v211, %[av3], v248\n\t"
    "v_lshrrev_b32 v250, 1, v248\n\t"
    "v_lshrrev_b32 v251, 3, v248\n\t"
    "v_and_b32 v252, 0x50, v250\n\t"
    "v_and_b32 v251, 0x20, v251\n\t"
    "v_or_b32 v252, v252, v251\n\t"
    "v_and_b32 v250, 8, v250\n\t"
    "v_and_b32 v251, 0x3c0, v248\n\t"
    "v_lshl_add_u32 v250, v251, 1, v250\n\t"
    "v_add_u32 v250, %[ldsb], v250\n\t"
    "v_add_u32 v214, v252, v250\n\t"
    "v_xad_u32 v215, v252, 32, v250\n\t"
    "v_xad_u32 v216, v252, 64, v250\n\t"
    "v_xor_b32 v251, 0x60, v252\n\t"
    "v_add_u32 v217, v251, v250\n\t"
    "v_and_b32 v223, 0xf0, v248\n\t"
    "v_lshlrev_b32 v223, 3, v223\n\t"
    "v_add_u32 v228, %[aom], v248\n\t"
    "v_add_u32 v229, %[fe], v223\n\t"
    "v_accvgpr_read_b32 v4, a160\n\t"
    "v_accvgpr_read_b32 v5, a161\n\t"
    "s_add_u32 m0, %[m0b], 0\n\t"
    "s_add_u32 %[tmp], %[kb0], 0\n\t"
    "buffer_load_dwordx4 v212, %[srdb], %[tmp] offen lds\n\t"
    "v_accvgpr_read_b32 v6, a162\n\t"
    "v_accvgpr_read_b32 v7, a163\n\t"
    "s_waitcnt lgkmcnt(9)\n\t"
    "v_mov_b32 v232, v131\n\t"
    "v_pk_fma_f32 v[64:65], v[128:129], v[160:161], v[64:65] op_sel_hi:[0,1,1]\n\t"
    "v_pk_fma_f32 v[66:67], v[128:129], v[162:163], v[66:67] op_sel_hi:[0,1,1]\n\t"
    "v_pk_fma_f32 v[96:97], v[128:129], v[164:165], v[96:97] op_sel_hi:[0,1,1]\n\t"
    "v_pk_fma_f32 v[98:99], v[128:129], v[166:167], v[98:99] op_sel_hi:[0,1,1]\n\t"
    "v_pk_fma_f32 v[192:193], v[128:129], v[168:169], v[192:193] op_sel_hi:[0,1,1]\n\t"
    "v_pk_fma_f32 v[194:195], v[128:129], v[170:171], v[194:195] op_sel_hi:[0,1,1]\n\t"
    "v_pk_fma_f32 v[4:5], v[128:129], v[172:173], v[4:5] op_sel_hi:[0,1,1]\n\t"
    "v_pk_fma_f32 v[6:7], v[128:129], v[174:175], v[6:7] op_sel_hi:[0,1,1]\n\t"
    "v_pk_mul_f32 v[192:193], v[64:65], v[192:193]\n\t"
    "v_pk_mul_f32 v[194:195], v[66:67], v[194:195]\n\t"
    "v_pk_mul_f32 v[4:5], v[96:97], v[4:5]\n\t"
    "v_pk_mul_f32 v[6:7], v[98:99], v[6:7]\n\t"
    "v_pk_mul_f32 v[64:65], v[64:65], v[232:233] op_sel_hi:[1,0]\n\t"
    "v_pk_mul_f32 v[66:67], v[66:67], v[232:233] op_sel_hi:[1,0]\n\t"
    "v_pk_mul_f32 v[96:97], v[96:97], v[232:233] op_sel_hi:[1,0]\n\t"
    "v_pk_mul_f32 v[98:99], v[98:99], v[232:233] op_sel_hi:[1,0]\n\t"
    "v_exp_f32 v64, v64\n\t"
    "v_exp_f32 v66, v66\n\t"
    "v_exp_f32 v96, v96\n\t"
    "v_exp_f32 v98, v98\n\t"
    "v_exp_f32 v65, v65\n\t"
    "buffer_load_dwordx4 v213, %[srdb], %[tmp] offen offset:1024 lds\n\t"
    "v_exp_f32 v67, v67\n\t"
    "v_exp_f32 v97, v97\n\t"
    "v_exp_f32 v99, v99\n\t"
    "v_pk_fma_f32 v[64:65], v[64:65], v[130:131], v[130:131] op_sel_hi:[1,0,0]\n\t"
    "v_pk_fma_f32 v[66:67], v[66:67], v[130:131], v[130:131] op_sel_hi:[1,0,0]\n\t"
    "v_pk_fma_f32 v[96:97], v[96:97], v[130:131], v[130:131] op_sel_hi:[1,0,0]\n\t"
    "v_pk_fma_f32 v[98:99], v[98:99], v[130:131], v[130:131] op_sel_hi:[1,0,0]\n\t"
    "v_rcp_f32 v64, v64\n\t"
    "v_rcp_f32 v66, v66\n\t"
    "v_rcp_f32 v96, v96\n\t"
    "v_rcp_f32 v98, v98\n\t"
    "v_rcp_f32 v65, v65\n\t"
    "v_rcp_f32 v67, v67\n\t"
    "v_rcp_f32 v97, v97\n\t"
    "v_rcp_f32 v99, v99\n\t"
    "v_pk_mul_f32 v[192:193], v[192:193], v[64:65]\n\t"
    "v_pk_mul_f32 v[194:195], v[194:195], v[66:67]\n\t"
    "v_pk_mul_f32 v[4:5], v[4:5], v[96:97]\n\t"
    "v_pk_mul_f32 v[6:7], v[6:7], v[98:99]\n\t"
    "v_cvt_pk_bf16_f32 v64, v192, v193\n\t"
    "v_cvt_pk_bf16_f32 v65, v194, v195\n\t"
    "v_cvt_pk_bf16_f32 v66, v4, v5\n\t"
    "v_cvt_pk_bf16_f32 v67, v6, v7\n\t"
    "s_mov_b32 %[tmp2], 0\n\t"
    "buffer_store_dwordx4 v[64:67], v227, %[srdw], %[tmp2] offen sc1\n\t"
    "v_accvgpr_read_b32 v12, a164\n\t"
    "v_accvgpr_read_b32 v13, a165\n\t"
    "s_add_u32 m0, %[m0b], 8192\n\t"
    "s_add_u32 %[tmp], %[kb0], 128\n\t"
    "buffer_load_dwordx4 v212, %[srdb], %[tmp] offen lds\n\t"
    "v_accvgpr_read_b32 v14, a166\n\t"
    "v_accvgpr_read_b32 v15, a167\n\t"
    "s_waitcnt lgkmcnt(0)\n\t"
    "v_mov_b32 v234, v135\n\t"
    "v_pk_fma_f32 v[68:69], v[132:133], v[160:161], v[68:69] op_sel_hi:[0,1,1]\n\t"
    "v_pk_fma_f32 v[70:71], v[132:133], v[162:163], v[70:71] op_sel_hi:[0,1,1]\n\t"
    "v_pk_fma_f32 v[100:101], v[132:133], v[164:165], v[100:101] op_sel_hi:[0,1,1]\n\t"
    "v_pk_fma_f32 v[102:103], v[132:133], v[166:167], v[102:103] op_sel_hi:[0,1,1]\n\t"
    "v_pk_fma_f32 v[196:197], v[132:133], v[168:169], v[196:197] op_sel_hi:[0,1,1]\n\t"
    "v_pk_fma_f32 v[198:199], v[132:133], v[170:171], v[198:199] op_sel_hi:[0,1,1]\n\t"
    "v_pk_fma_f32 v[12:13], v[132:133], v[172:173], v[12:13] op_sel_hi:[0,1,1]\n\t"
    "v_pk_fma_f32 v[14:15], v[132:133], v[174:175], v[14:15] op_sel_hi:[0,1,1]\n\t"
    "v_pk_mul_f32 v[196:197], v[68:69], v[196:197]\n\t"
    "v_pk_mul_f32 v[198:199], v[70:71], v[198:199]\n\t"
    "v_pk_mul_f32 v[12:13], v[100:101], v[12:13]\n\t"
    "v_pk_mul_f32 v[14:15], v[102:103], v[14:15]\n\t"
    "v_pk_mul_f32 v[68:69], v[68:69], v[234:235] op_sel_hi:[1,0]\n\t"
    "v_pk_mul_f32 v[70:71], v[70:71], v[234:235] op_sel_hi:[1,0]\n\t"
    "v_pk_mul_f32 v[100:101], v[100:101], v[234:235] op_sel_hi:[1,0]\n\t"
    "v_pk_mul_f32 v[102:103], v[102:103], v[234:235] op_sel_hi:[1,0]\n\t"
    "v_exp_f32 v68, v68\n\t"
    "v_exp_f32 v70, v70\n\t"
    "v_exp_f32 v100, v100\n\t"
    "v_exp_f32 v102, v102\n\t"
    "v_exp_f32 v69, v69\n\t"
    "buffer_load_dwordx4 v213, %[srdb], %[tmp] offen offset:1024 lds\n\t"
    "v_exp_f32 v71, v71\n\t"
    "v_exp_f32 v101, v101\n\t"
    "v_exp_f32 v103, v103\n\t"
    "v_pk_fma_f32 v[68:69], v[68:69], v[134:135], v[134:135] op_sel_hi:[1,0,0]\n\t"
    "v_pk_fma_f32 v[70:71], v[70:71], v[134:135], v[134:135] op_sel_hi:[1,0,0]\n\t"
    "v_pk_fma_f32 v[100:101], v[100:101], v[134:135], v[134:135] op_sel_hi:[1,0,0]\n\t"
    "v_pk_fma_f32 v[102:103], v[102:103], v[134:135], v[134:135] op_sel_hi:[1,0,0]\n\t"
    "v_rcp_f32 v68, v68\n\t"
    "v_rcp_f32 v70, v70\n\t"
    "v_rcp_f32 v100, v100\n\t"
    "v_rcp_f32 v102, v102\n\t"
    "v_rcp_f32 v69, v69\n\t"
    "v_rcp_f32 v71, v71\n\t"
    "v_rcp_f32 v101, v101\n\t"
    "v_rcp_f32 v103, v103\n\t"
    "v_pk_mul_f32 v[196:197], v[196:197], v[68:69]\n\t"
    "v_pk_mul_f32 v[198:199], v[198:199], v[70:71]\n\t"
    "v_pk_mul_f32 v[12:13], v[12:13], v[100:101]\n\t"
    "v_pk_mul_f32 v[14:15], v[14:15], v[102:103]\n\t"
    "v_cvt_pk_bf16_f32 v68, v196, v197\n\t"
    "v_cvt_pk_bf16_f32 v69, v198, v199\n\t"
    "v_cvt_pk_bf16_f32 v70, v12, v13\n\t"
    "v_cvt_pk_bf16_f32 v71, v14, v15\n\t"
    "s_mov_b32 %[tmp2], 147456\n\t"
    "buffer_store_dwordx4 v[68:71], v227, %[srdw], %[tmp2] offen sc1\n\t"
    "s_waitcnt vmcnt(6)\n\t"
    "s_barrier\n\t"
    "s_cmp_lg_u32 %[w], 0\n\t"
    "s_cbranch_scc1 61f\n\t"
    "v_mov_b32 v250, s90\n\t"
    "v_mov_b32 v251, s91\n\t"
    "v_mov_b32 v249, %[fep]\n\t"
    "s_mov_b64 exec, 1\n\t"
    "buffer_store_dwordx2 v[250:251], v249, %[srdw], 0 offen sc1\n\t"
    "s_mov_b64 exec, -1\n\t"
    "61:\n\t"
    "v_accvgpr_read_b32 v4, a168\n\t"
    "v_accvgpr_read_b32 v5, a169\n\t"
    "s_add_u32 m0, %[m0b], 16384\n\t"
    "s_add_u32 %[tmp], %[kb1], 0\n\t"
    "buffer_load_dwordx4 v212, %[srdb], %[tmp] offen lds\n\t"
    "v_accvgpr_read_b32 v6, a170\n\t"
    "v_accvgpr_read_b32 v7, a171\n\t"
    "v_mov_b32 v236, v139\n\t"
    "v_pk_fma_f32 v[72:73], v[136:137], v[160:161], v[72:73] op_sel_hi:[0,1,1]\n\t"
    "v_pk_fma_f32 v[74:75], v[136:137], v[162:163], v[74:75] op_sel_hi:[0,1,1]\n\t"
    "v_pk_fma_f32 v[104:105], v[136:137], v[164:165], v[104:105] op_sel_hi:[0,1,1]\n\t"
    "v_pk_fma_f32 v[106:107], v[136:137], v[166:167], v[106:107] op_sel_hi:[0,1,1]\n\t"
    "v_pk_fma_f32 v[200:201], v[136:137], v[168:169], v[200:201] op_sel_hi:[0,1,1]\n\t"
    "v_pk_fma_f32 v[202:203], v[136:137], v[170:171], v[202:203] op_sel_hi:[0,1,1]\n\t"
    "v_pk_fma_f32 v[4:5], v[136:137], v[172:173], v[4:5] op_sel_hi:[0,1,1]\n\t"
    "v_pk_fma_f32 v[6:7], v[136:137], v[174:175], v[6:7] op_sel_hi:[0,1,1]\n\t"
    "v_pk_mul_f32 v[200:201], v[72:73], v[200:201]\n\t"
    "v_pk_mul_f32 v[202:203], v[74:75], v[202:203]\n\t"
    "v_pk_mul_f32 v[4:5], v[104:105], v[4:5]\n\t"
    "v_pk_mul_f32 v[6:7], v[106:107], v[6:7]\n\t"
    "v_pk_mul_f32 v[72:73], v[72:73], v[236:237] op_sel_hi:[1,0]\n\t"
    "v_pk_mul_f32 v[74:75], v[74:75], v[236:237] op_sel_hi:[1,0]\n\t"
    "v_pk_mul_f32 v[104:105], v[104:105], v[236:237] op_sel_hi:[1,0]\n\t"
    "v_pk_mul_f32 v[106:107], v[106:107], v[236:237] op_sel_hi:[1,0]\n\t"
    "v_exp_f32 v72, v72\n\t"
    "v_exp_f32 v74, v74\n\t"
    "v_exp_f32 v104, v104\n\t"
    "v_exp_f32 v106, v106\n\t"
    "v_exp_f32 v73, v73\n\t"
    "buffer_load_dwordx4 v213, %[srdb], %[tmp] offen offset:1024 lds\n\t"
    "v_exp_f32 v75, v75\n\t"
    "v_exp_f32 v105, v105\n\t"
    "v_exp_f32 v107, v107\n\t"
    "v_pk_fma_f32 v[72:73], v[72:73], v[138:139], v[138:139] op_sel_hi:[1,0,0]\n\t"
    "v_pk_fma_f32 v[74:75], v[74:75], v[138:139], v[138:139] op_sel_hi:[1,0,0]\n\t"
    "v_pk_fma_f32 v[104:105], v[104:105], v[138:139], v[138:139] op_sel_hi:[1,0,0]\n\t"
    "v_pk_fma_f32 v[106:107], v[106:107], v[138:139], v[138:139] op_sel_hi:[1,0,0]\n\t"
    "v_rcp_f32 v72, v72\n\t"
    "v_rcp_f32 v74, v74\n\t"
    "v_rcp_f32 v104, v104\n\t"
    "v_rcp_f32 v106, v106\n\t"
    "v_rcp_f32 v73, v73\n\t"
    "v_rcp_f32 v75, v75\n\t"
    "v_rcp_f32 v105, v105\n\t"
    "v_rcp_f32 v107, v107\n\t"
    "v_pk_mul_f32 v[200:201], v[200:201], v[72:73]\n\t"
    "v_pk_mul_f32 v[202:203], v[202:203], v[74:75]\n\t"
    "v_pk_mul_f32 v[4:5], v[4:5], v[104:105]\n\t"
    "v_pk_mul_f32 v[6:7], v[6:7], v[106:107]\n\t"
    "v_cvt_pk_bf16_f32 v72, v200, v201\n\t"
    "v_cvt_pk_bf16_f32 v73, v202, v203\n\t"
    "v_cvt_pk_bf16_f32 v74, v4, v5\n\t"
    "v_cvt_pk_bf16_f32 v75, v6, v7\n\t"
    "s_mov_b32 %[tmp2], 294912\n\t"
    "buffer_store_dwordx4 v[72:75], v227, %[srdw], %[tmp2] offen sc1\n\t"
    "v_accvgpr_read_b32 v12, a172\n\t"
    "v_accvgpr_read_b32 v13, a173\n\t"
    "s_add_u32 m0, %[m0b], 24576\n\t"
    "s_add_u32 %[tmp], %[kb1], 128\n\t"
    "buffer_load_dwordx4 v212, %[srdb], %[tmp] offen lds\n\t"
    "v_accvgpr_read_b32 v14, a174\n\t"
    "v_accvgpr_read_b32 v15, a175\n\t"
    "v_mov_b32 v238, v143\n\t"
    "v_pk_fma_f32 v[76:77], v[140:141], v[160:161], v[76:77] op_sel_hi:[0,1,1]\n\t"
    "v_pk_fma_f32 v[78:79], v[140:141], v[162:163], v[78:79] op_sel_hi:[0,1,1]\n\t"
    "v_pk_fma_f32 v[108:109], v[140:141], v[164:165], v[108:109] op_sel_hi:[0,1,1]\n\t"
    "v_pk_fma_f32 v[110:111], v[140:141], v[166:167], v[110:111] op_sel_hi:[0,1,1]\n\t"
    "v_pk_fma_f32 v[204:205], v[140:141], v[168:169], v[204:205] op_sel_hi:[0,1,1]\n\t"
    "v_pk_fma_f32 v[206:207], v[140:141], v[170:171], v[206:207] op_sel_hi:[0,1,1]\n\t"
    "v_pk_fma_f32 v[12:13], v[140:141], v[172:173], v[12:13] op_sel_hi:[0,1,1]\n\t"
    "v_pk_fma_f32 v[14:15], v[140:141], v[174:175], v[14:15] op_sel_hi:[0,1,1]\n\t"
    "v_pk_mul_f32 v[204:205], v[76:77], v[204:205]\n\t"
    "v_pk_mul_f32 v[206:207], v[78:79], v[206:207]\n\t"
    "v_pk_mul_f32 v[12:13], v[108:109], v[12:13]\n\t"
    "v_pk_mul_f32 v[14:15], v[110:111], v[14:15]\n\t"
    "v_pk_mul_f32 v[76:77], v[76:77], v[238:239] op_sel_hi:[1,0]\n\t"
    "v_pk_mul_f32 v[78:79], v[78:79], v[238:239] op_sel_hi:[1,0]\n\t"
    "v_pk_mul_f32 v[108:109], v[108:109], v[238:239] op_sel_hi:[1,0]\n\t"
    "v_pk_mul_f32 v[110:111], v[110:111], v[238:239] op_sel_hi:[1,0]\n\t"
    "v_exp_f32 v76, v76\n\t"
    "v_exp_f32 v78, v78\n\t"
    "v_exp_f32 v108, v108\n\t"
    "v_exp_f32 v110, v110\n\t"
    "v_exp_f32 v77, v77\n\t"
    "buffer_load_dwordx4 v213, %[srdb], %[tmp] offen offset:1024 lds\n\t"
    "v_exp_f32 v79, v79\n\t"
    "v_exp_f32 v109, v109\n\t"
    "v_exp_f32 v111, v111\n\t"
    "v_pk_fma_f32 v[76:77], v[76:77], v[142:143], v[142:143] op_sel_hi:[1,0,0]\n\t"
    "v_pk_fma_f32 v[78:79], v[78:79], v[142:143], v[142:143] op_sel_hi:[1,0,0]\n\t"
    "v_pk_fma_f32 v[108:109], v[108:109], v[142:143], v[142:143] op_sel_hi:[1,0,0]\n\t"
    "v_pk_fma_f32 v[110:111], v[110:111], v[142:143], v[142:143] op_sel_hi:[1,0,0]\n\t"
    "v_rcp_f32 v76, v76\n\t"
    "v_rcp_f32 v78, v78\n\t"
    "v_rcp_f32 v108, v108\n\t"
    "v_rcp_f32 v110, v110\n\t"
    "v_rcp_f32 v77, v77\n\t"
    "v_rcp_f32 v79, v79\n\t"
    "v_rcp_f32 v109, v109\n\t"
    "v_rcp_f32 v111, v111\n\t"
    "v_pk_mul_f32 v[204:205], v[204:205], v[76:77]\n\t"
    "v_pk_mul_f32 v[206:207], v[206:207], v[78:79]\n\t"
    "v_pk_mul_f32 v[12:13], v[12:13], v[108:109]\n\t"
    "v_pk_mul_f32 v[14:15], v[14:15], v[110:111]\n\t"
    "v_cvt_pk_bf16_f32 v76, v204, v205\n\t"
    "v_cvt_pk_bf16_f32 v77, v206, v207\n\t"
    "v_cvt_pk_bf16_f32 v78, v12, v13\n\t"
    "v_cvt_pk_bf16_f32 v79, v14, v15\n\t"
    "s_mov_b32 %[tmp2], 442368\n\t"
    "buffer_store_dwordx4 v[76:79], v227, %[srdw], %[tmp2] offen sc1\n\t"
    "buffer_load_dwordx2 v[48:49], v229, %[srdw], 0 offen sc1\n\t"
    "v_accvgpr_read_b32 v0, a144\n\t"
    "v_accvgpr_read_b32 v1, a145\n\t"
    "s_add_u32 m0, %[m0b], 32768\n\t"
    "s_add_u32 %[tmp], %[kb2], 0\n\t"
    "buffer_load_dwordx4 v212, %[srdb], %[tmp] offen lds\n\t"
    "v_accvgpr_read_b32 v2, a146\n\t"
    "v_accvgpr_read_b32 v3, a147\n\t"
    "v_accvgpr_read_b32 v4, a176\n\t"
    "v_accvgpr_read_b32 v5, a177\n\t"
    "v_accvgpr_read_b32 v6, a178\n\t"
    "v_accvgpr_read_b32 v7, a179\n\t"
    "v_mov_b32 v240, v147\n\t"
    "v_pk_fma_f32 v[80:81], v[144:145], v[160:161], v[80:81] op_sel_hi:[0,1,1]\n\t"
    "v_pk_fma_f32 v[82:83], v[144:145], v[162:163], v[82:83] op_sel_hi:[0,1,1]\n\t"
    "v_pk_fma_f32 v[112:113], v[144:145], v[164:165], v[112:113] op_sel_hi:[0,1,1]\n\t"
    "v_pk_fma_f32 v[114:115], v[144:145], v[166:167], v[114:115] op_sel_hi:[0,1,1]\n\t"
    "v_pk_fma_f32 v[0:1], v[144:145], v[168:169], v[0:1] op_sel_hi:[0,1,1]\n\t"
    "v_pk_fma_f32 v[2:3], v[144:145], v[170:171], v[2:3] op_sel_hi:[0,1,1]\n\t"
    "v_pk_fma_f32 v[4:5], v[144:145], v[172:173], v[4:5] op_sel_hi:[0,1,1]\n\t"
    "v_pk_fma_f32 v[6:7], v[144:145], v[174:175], v[6:7] op_sel_hi:[0,1,1]\n\t"
    "v_pk_mul_f32 v[0:1], v[80:81], v[0:1]\n\t"
    "v_pk_mul_f32 v[2:3], v[82:83], v[2:3]\n\t"
    "v_pk_mul_f32 v[4:5], v[112:113], v[4:5]\n\t"
    "v_pk_mul_f32 v[6:7], v[114:115], v[6:7]\n\t"
    "v_pk_mul_f32 v[80:81], v[80:81], v[240:241] op_sel_hi:[1,0]\n\t"
    "v_pk_mul_f32 v[82:83], v[82:83], v[240:241] op_sel_hi:[1,0]\n\t"
    "v_pk_mul_f32 v[112:113], v[112:113], v[240:241] op_sel_hi:[1,0]\n\t"
    "v_pk_mul_f32 v[114:115], v[114:115], v[240:241] op_sel_hi:[1,0]\n\t"
    "v_exp_f32 v80, v80\n\t"
    "v_exp_f32 v82, v82\n\t"
    "v_exp_f32 v112, v112\n\t"
    "buffer_load_dwordx4 v213, %[srdb], %[tmp] offen offset:1024 lds\n\t"
    "v_exp_f32 v114, v114\n\t"
    "v_exp_f32 v81, v81\n\t"
    "v_exp_f32 v83, v83\n\t"
    "v_exp_f32 v113, v113\n\t"
    "v_exp_f32 v115, v115\n\t"
    "v_pk_fma_f32 v[80:81], v[80:81], v[146:147], v[146:147] op_sel_hi:[1,0,0]\n\t"
    "v_pk_fma_f32 v[82:83], v[82:83], v[146:147], v[146:147] op_sel_hi:[1,0,0]\n\t"
    "v_pk_fma_f32 v[112:113], v[112:113], v[146:147], v[146:147] op_sel_hi:[1,0,0]\n\t"
    "v_pk_fma_f32 v[114:115], v[114:115], v[146:147], v[146:147] op_sel_hi:[1,0,0]\n\t"
    "v_rcp_f32 v80, v80\n\t"
    "v_rcp_f32 v82, v82\n\t"
    "v_rcp_f32 v112, v112\n\t"
    "v_rcp_f32 v114, v114\n\t"
    "v_rcp_f32 v81, v81\n\t"
    "v_rcp_f32 v83, v83\n\t"
    "v_rcp_f32 v113, v113\n\t"
    "v_rcp_f32 v115, v115\n\t"
    "v_pk_mul_f32 v[0:1], v[0:1], v[80:81]\n\t"
    "v_pk_mul_f32 v[2:3], v[2:3], v[82:83]\n\t"
    "v_pk_mul_f32 v[4:5], v[4:5], v[112:113]\n\t"
    "v_pk_mul_f32 v[6:7], v[6:7], v[114:115]\n\t"
    "v_cvt_pk_bf16_f32 v80, v0, v1\n\t"
    "v_cvt_pk_bf16_f32 v81, v2, v3\n\t"
    "v_cvt_pk_bf16_f32 v82, v4, v5\n\t"
    "v_cvt_pk_bf16_f32 v83, v6, v7\n\t"
    "s_mov_b32 %[tmp2], 589824\n\t"
    "buffer_store_dwordx4 v[80:83], v227, %[srdw], %[tmp2] offen sc1\n\t"
    "s_waitcnt vmcnt(3)\n\t"
    "v_cmp_ne_u64 vcc, s[90:91], v[48:49]\n\t"
    "s_cbranch_vccz 62f\n\t"
    "s_mov_b32 %[tmp2], 0x10000\n\t"
    "63:\n\t"
    "s_sleep 2\n\t"
    "buffer_load_dwordx2 v[48:49], v229, %[srdw], 0 offen sc1\n\t"
    "s_sub_u32 %[tmp2], %[tmp2], 1\n\t"
    "s_waitcnt vmcnt(0)\n\t"
    "s_cbranch_scc1 62f\n\t"
    "v_cmp_ne_u64 vcc, s[90:91], v[48:49]\n\t"
    "s_cbranch_vccnz 63b\n\t"
    "62:\n\t"
    "v_accvgpr_read_b32 v8, a148\n\t"
    "v_accvgpr_read_b32 v9, a149\n\t"
    "s_add_u32 m0, %[m0b], 40960\n\t"
    "s_add_u32 %[tmp], %[kb2], 128\n\t"
    "buffer_load_dwordx4 v212, %[srdb], %[tmp] offen lds\n\t"
    "v_accvgpr_read_b32 v10, a150\n\t"
    "v_accvgpr_read_b32 v11, a151\n\t"
    "v_accvgpr_read_b32 v12, a180\n\t"
    "v_accvgpr_read_b32 v13, a181\n\t"
    "v_accvgpr_read_b32 v14, a182\n\t"
    "v_accvgpr_read_b32 v15, a183\n\t"
    "v_mov_b32 v242, v151\n\t"
    "v_pk_fma_f32 v[84:85], v[148:149], v[160:161], v[84:85] op_sel_hi:[0,1,1]\n\t"
    "v_pk_fma_f32 v[86:87], v[148:149], v[162:163], v[86:87] op_sel_hi:[0,1,1]\n\t"
    "v_pk_fma_f32 v[116:117], v[148:149], v[164:165], v[116:117] op_sel_hi:[0,1,1]\n\t"
    "buffer_load_dwordx4 v213, %[srdb], %[tmp] offen offset:1024 lds\n\t"
    "v_pk_fma_f32 v[118:119], v[148:149], v[166:167], v[118:119] op_sel_hi:[0,1,1]\n\t"
    "v_pk_fma_f32 v[8:9], v[148:149], v[168:169], v[8:9] op_sel_hi:[0,1,1]\n\t"
    "v_pk_fma_f32 v[10:11], v[148:149], v[170:171], v[10:11] op_sel_hi:[0,1,1]\n\t"
    "v_pk_fma_f32 v[12:13], v[148:149], v[172:173], v[12:13] op_sel_hi:[0,1,1]\n\t"
    "v_pk_fma_f32 v[14:15], v[148:149], v[174:175], v[14:15] op_sel_hi:[0,1,1]\n\t"
    "v_pk_mul_f32 v[8:9], v[84:85], v[8:9]\n\t"
    "v_pk_mul_f32 v[10:11], v[86:87], v[10:11]\n\t"
    "v_pk_mul_f32 v[12:13], v[116:117], v[12:13]\n\t"
    "v_pk_mul_f32 v[14:15], v[118:119], v[14:15]\n\t"
    "v_pk_mul_f32 v[84:85], v[84:85], v[242:243] op_sel_hi:[1,0]\n\t"
    "v_pk_mul_f32 v[86:87], v[86:87], v[242:243] op_sel_hi:[1,0]\n\t"
    "s_lshr_b32 %[tmpa], %[kb0], 6\n\t"
    "buffer_load_dwordx4 a[0:3], v208, %[srda], %[tmpa] offen offset:0 sc1\n\t"
    "v_pk_mul_f32 v[116:117], v[116:117], v[242:243] op_sel_hi:[1,0]\n\t"
    "v_pk_mul_f32 v[118:119], v[118:119], v[242:243] op_sel_hi:[1,0]\n\t"
    "v_exp_f32 v84, v84\n\t"
    "v_exp_f32 v86, v86\n\t"
    "v_exp_f32 v116, v116\n\t"
    "v_exp_f32 v118, v118\n\t"
    "v_exp_f32 v85, v85\n\t"
    "v_exp_f32 v87, v87\n\t"
    "v_exp_f32 v117, v117\n\t"
    "v_exp_f32 v119, v119\n\t"
    "buffer_load_dwordx4 a[16:19], v208, %[srda], %[tmpa] offen offset:1024 sc1\n\t"
    "v_pk_fma_f32 v[84:85], v[84:85], v[150:151], v[150:151] op_sel_hi:[1,0,0]\n\t"
    "v_pk_fma_f32 v[86:87], v[86:87], v[150:151], v[150:151] op_sel_hi:[1,0,0]\n\t"
    "v_pk_fma_f32 v[116:117], v[116:117], v[150:151], v[150:151] op_sel_hi:[1,0,0]\n\t"
    "v_pk_fma_f32 v[118:119], v[118:119], v[150:151], v[150:151] op_sel_hi:[1,0,0]\n\t"
    "v_rcp_f32 v84, v84\n\t"
    "v_rcp_f32 v86, v86\n\t"
    "v_rcp_f32 v116, v116\n\t"
    "v_rcp_f32 v118, v118\n\t"
    "v_rcp_f32 v85, v85\n\t"
    "v_rcp_f32 v87, v87\n\t"
    "v_rcp_f32 v117, v117\n\t"
    "buffer_load_dwordx4 a[4:7], v209, %[srda], %[tmpa] offen offset:0 sc1\n\t"
    "v_rcp_f32 v119, v119\n\t"
    "v_pk_mul_f32 v[8:9], v[8:9], v[84:85]\n\t"
    "v_pk_mul_f32 v[10:11], v[10:11], v[86:87]\n\t"
    "v_pk_mul_f32 v[12:13], v[12:13], v[116:117]\n\t"
    "v_pk_mul_f32 v[14:15], v[14:15], v[118:119]\n\t"
    "v_cvt_pk_bf16_f32 v84, v8, v9\n\t"
    "v_cvt_pk_bf16_f32 v85, v10, v11\n\t"
    "v_cvt_pk_bf16_f32 v86, v12, v13\n\t"
    "v_cvt_pk_bf16_f32 v87, v14, v15\n\t"
    "s_mov_b32 %[tmp2], 737280\n\t"
    "buffer_store_dwordx4 v[84:87], v227, %[srdw], %[tmp2] offen sc1\n\t"
    "v_accvgpr_read_b32 v0, a152\n\t"
    "v_accvgpr_read_b32 v1, a153\n\t"
    "s_add_u32 m0, %[m0b], 49152\n\t"
    "s_add_u32 %[tmp], %[kb3], 0\n\t"
    "buffer_load_dwordx4 v212, %[srdb], %[tmp] offen lds\n\t"
    "v_accvgpr_read_b32 v2, a154\n\t"
    "v_accvgpr_read_b32 v3, a155\n\t"
    "v_accvgpr_read_b32 v4, a184\n\t"
    "v_accvgpr_read_b32 v5, a185\n\t"
    "v_accvgpr_read_b32 v6, a186\n\t"
    "v_accvgpr_read_b32 v7, a187\n\t"
    "v_mov_b32 v244, v155\n\t"
    "v_pk_fma_f32 v[88:89], v[152:153], v[160:161], v[88:89] op_sel_hi:[0,1,1]\n\t"
    "v_pk_fma_f32 v[90:91], v[152:153], v[162:163], v[90:91] op_sel_hi:[0,1,1]\n\t"
    "v_pk_fma_f32 v[120:121], v[152:153], v[164:165], v[120:121] op_sel_hi:[0,1,1]\n\t"
    "buffer_load_dwordx4 v213, %[srdb], %[tmp] offen offset:1024 lds\n\t"
    "v_pk_fma_f32 v[122:123], v[152:153], v[166:167], v[122:123] op_sel_hi:[0,1,1]\n\t"
    "v_pk_fma_f32 v[0:1], v[152:153], v[168:169], v[0:1] op_sel_hi:[0,1,1]\n\t"
    "v_pk_fma_f32 v[2:3], v[152:153], v[170:171], v[2:3] op_sel_hi:[0,1,1]\n\t"
    "v_pk_fma_f32 v[4:5], v[152:153], v[172:173], v[4:5] op_sel_hi:[0,1,1]\n\t"
    "v_pk_fma_f32 v[6:7], v[152:153], v[174:175], v[6:7] op_sel_hi:[0,1,1]\n\t"
    "v_pk_mul_f32 v[0:1], v[88:89], v[0:1]\n\t"
    "v_pk_mul_f32 v[2:3], v[90:91], v[2:3]\n\t"
    "v_pk_mul_f32 v[4:5], v[120:121], v[4:5]\n\t"
    "v_pk_mul_f32 v[6:7], v[122:123], v[6:7]\n\t"
    "v_pk_mul_f32 v[88:89], v[88:89], v[244:245] op_sel_hi:[1,0]\n\t"
    "v_pk_mul_f32 v[90:91], v[90:91], v[244:245] op_sel_hi:[1,0]\n\t"
    "buffer_load_dwordx4 a[20:23], v209, %[srda], %[tmpa] offen offset:1024 sc1\n\t"
    "v_pk_mul_f32 v[120:121], v[120:121], v[244:245] op_sel_hi:[1,0]\n\t"
    "v_pk_mul_f32 v[122:123], v[122:123], v[244:245] op_sel_hi:[1,0]\n\t"
    "v_exp_f32 v88, v88\n\t"
    "v_exp_f32 v90, v90\n\t"
    "v_exp_f32 v120, v120\n\t"
    "v_exp_f32 v122, v122\n\t"
    "v_exp_f32 v89, v89\n\t"
    "v_exp_f32 v91, v91\n\t"
    "v_exp_f32 v121, v121\n\t"
    "v_exp_f32 v123, v123\n\t"
    "buffer_load_dwordx4 a[8:11], v210, %[srda], %[tmpa] offen offset:0 sc1\n\t"
    "v_pk_fma_f32 v[88:89], v[88:89], v[154:155], v[154:155] op_sel_hi:[1,0,0]\n\t"
    "v_pk_fma_f32 v[90:91], v[90:91], v[154:155], v[154:155] op_sel_hi:[1,0,0]\n\t"
    "v_pk_fma_f32 v[120:121], v[120:121], v[154:155], v[154:155] op_sel_hi:[1,0,0]\n\t"
    "v_pk_fma_f32 v[122:123], v[122:123], v[154:155], v[154:155] op_sel_hi:[1,0,0]\n\t"
    "v_rcp_f32 v88, v88\n\t"
    "v_rcp_f32 v90, v90\n\t"
    "v_rcp_f32 v120, v120\n\t"
    "v_rcp_f32 v122, v122\n\t"
    "v_rcp_f32 v89, v89\n\t"
    "v_rcp_f32 v91, v91\n\t"
    "v_rcp_f32 v121, v121\n\t"
    "buffer_load_dwordx4 a[24:27], v210, %[srda], %[tmpa] offen offset:1024 sc1\n\t"
    "v_rcp_f32 v123, v123\n\t"
    "v_pk_mul_f32 v[0:1], v[0:1], v[88:89]\n\t"
    "v_pk_mul_f32 v[2:3], v[2:3], v[90:91]\n\t"
    "v_pk_mul_f32 v[4:5], v[4:5], v[120:121]\n\t"
    "v_pk_mul_f32 v[6:7], v[6:7], v[122:123]\n\t"
    "v_cvt_pk_bf16_f32 v88, v0, v1\n\t"
    "v_cvt_pk_bf16_f32 v89, v2, v3\n\t"
    "v_cvt_pk_bf16_f32 v90, v4, v5\n\t"
    "v_cvt_pk_bf16_f32 v91, v6, v7\n\t"
    "s_mov_b32 %[tmp2], 884736\n\t"
    "buffer_store_dwordx4 v[88:91], v227, %[srdw], %[tmp2] offen sc1\n\t"
    "v_accvgpr_read_b32 v8, a156\n\t"
    "v_accvgpr_read_b32 v9, a157\n\t"
    "s_add_u32 m0, %[m0b], 57344\n\t"
    "s_add_u32 %[tmp], %[kb3], 128\n\t"
    "buffer_load_dwordx4 v212, %[srdb], %[tmp] offen lds\n\t"
    "v_accvgpr_read_b32 v10, a158\n\t"
    "v_accvgpr_read_b32 v11, a159\n\t"
    "v_accvgpr_read_b32 v12, a188\n\t"
    "v_accvgpr_read_b32 v13, a189\n\t"
    "v_accvgpr_read_b32 v14, a190\n\t"
    "v_accvgpr_read_b32 v15, a191\n\t"
    "v_mov_b32 v246, v159\n\t"
    "v_pk_fma_f32 v[92:93], v[156:157], v[160:161], v[92:93] op_sel_hi:[0,1,1]\n\t"
    "v_pk_fma_f32 v[94:95], v[156:157], v[162:163], v[94:95] op_sel_hi:[0,1,1]\n\t"
    "v_pk_fma_f32 v[124:125], v[156:157], v[164:165], v[124:125] op_sel_hi:[0,1,1]\n\t"
    "v_pk_fma_f32 v[126:127], v[156:157], v[166:167], v[126:127] op_sel_hi:[0,1,1]\n\t"
    "v_pk_fma_f32 v[8:9], v[156:157], v[168:169], v[8:9] op_sel_hi:[0,1,1]\n\t"
    "v_pk_fma_f32 v[10:11], v[156:157], v[170:171], v[10:11] op_sel_hi:[0,1,1]\n\t"
    "v_pk_fma_f32 v[12:13], v[156:157], v[172:173], v[12:13] op_sel_hi:[0,1,1]\n\t"
    "v_pk_fma_f32 v[14:15], v[156:157], v[174:175], v[14:15] op_sel_hi:[0,1,1]\n\t"
    "v_pk_mul_f32 v[8:9], v[92:93], v[8:9]\n\t"
    "v_pk_mul_f32 v[10:11], v[94:95], v[10:11]\n\t"
    "v_pk_mul_f32 v[12:13], v[124:125], v[12:13]\n\t"
    "v_pk_mul_f32 v[14:15], v[126:127], v[14:15]\n\t"
    "v_pk_mul_f32 v[92:93], v[92:93], v[246:247] op_sel_hi:[1,0]\n\t"
    "v_pk_mul_f32 v[94:95], v[94:95], v[246:247] op_sel_hi:[1,0]\n\t"
    "v_pk_mul_f32 v[124:125], v[124:125], v[246:247] op_sel_hi:[1,0]\n\t"
    "v_pk_mul_f32 v[126:127], v[126:127], v[246:247] op_sel_hi:[1,0]\n\t"
    "v_exp_f32 v92, v92\n\t"
    "v_exp_f32 v94, v94\n\t"
    "v_exp_f32 v124, v124\n\t"
    "buffer_load_dwordx4 v213, %[srdb], %[tmp] offen offset:1024 lds\n\t"
    "v_exp_f32 v126, v126\n\t"
    "v_exp_f32 v93, v93\n\t"
    "v_exp_f32 v95, v95\n\t"
    "v_exp_f32 v125, v125\n\t"
    "v_exp_f32 v127, v127\n\t"
    "v_pk_fma_f32 v[92:93], v[92:93], v[158:159], v[158:159] op_sel_hi:[1,0,0]\n\t"
    "v_pk_fma_f32 v[94:95], v[94:95], v[158:159], v[158:159] op_sel_hi:[1,0,0]\n\t"
    "v_pk_fma_f32 v[124:125], v[124:125], v[158:159], v[158:159] op_sel_hi:[1,0,0]\n\t"
    "v_pk_fma_f32 v[126:127], v[126:127], v[158:159], v[158:159] op_sel_hi:[1,0,0]\n\t"
    "v_rcp_f32 v92, v92\n\t"
    "v_rcp_f32 v94, v94\n\t"
    "v_rcp_f32 v124, v124\n\t"
    "v_rcp_f32 v126, v126\n\t"
    "v_rcp_f32 v93, v93\n\t"
    "v_rcp_f32 v95, v95\n\t"
    "v_rcp_f32 v125, v125\n\t"
    "v_rcp_f32 v127, v127\n\t"
    "v_pk_mul_f32 v[8:9], v[8:9], v[92:93]\n\t"
    "v_pk_mul_f32 v[10:11], v[10:11], v[94:95]\n\t"
    "v_pk_mul_f32 v[12:13], v[12:13], v[124:125]\n\t"
    "v_pk_mul_f32 v[14:15], v[14:15], v[126:127]\n\t"
    "v_cvt_pk_bf16_f32 v92, v8, v9\n\t"
    "v_cvt_pk_bf16_f32 v93, v10, v11\n\t"
    "v_cvt_pk_bf16_f32 v94, v12, v13\n\t"
    "v_cvt_pk_bf16_f32 v95, v14, v15\n\t"
    "s_mov_b32 %[tmp2], 1032192\n\t"
    "buffer_store_dwordx4 v[92:95], v227, %[srdw], %[tmp2] offen sc1\n\t"
    "v_lshrrev_b32 v230, 1, v248\n\t"
    "v_add_u32 v231, %[mxr], v230\n\t"
    "v_add_u32 v230, %[mxw], v230\n\t"
    "v_accvgpr_read_b32 v16, a192\n\t"
    "v_accvgpr_read_b32 v17, a193\n\t"
    "v_lshrrev_b32 v255, 5, v248\n\t"
    "v_lshlrev_b32 v255, 11, v255\n\t"
    "v_and_b32 v254, 16, v248\n\t"
    "v_lshl_add_u32 v255, v254, 3, v255\n\t"
    "v_add_u32 v255, %[bvc], v255\n\t"
    "s_add_u32 m0, %[ldsb], 131072\n\t"
    "s_mov_b64 exec, 0xffffffff\n\t"
    "buffer_load_dword v255, %[srdb], %[kb4] offen lds\n\t"
    "s_mov_b64 exec, -1\n\t"
    "v_accvgpr_read_b32 v18, a194\n\t"
    "v_accvgpr_read_b32 v19, a195\n\t"
    "v_accvgpr_read_b32 v20, a224\n\t"
    "v_accvgpr_read_b32 v21, a225\n\t"
    "v_accvgpr_read_b32 v22, a226\n\t"
    "v_accvgpr_read_b32 v23, a227\n\t"
    "v_pk_fma_f32 v[16:17], v[128:129], v[176:177], v[16:17] op_sel_hi:[0,1,1]\n\t"
    "buffer_load_dwordx4 a[12:15], v211, %[srda], %[tmpa] offen offset:0 sc1\n\t"
    "v_pk_fma_f32 v[18:19], v[128:129], v[178:179], v[18:19] op_sel_hi:[0,1,1]\n\t"
    "v_pk_fma_f32 v[20:21], v[128:129], v[180:181], v[20:21] op_sel_hi:[0,1,1]\n\t"
    "v_pk_fma_f32 v[22:23], v[128:129], v[182:183], v[22:23] op_sel_hi:[0,1,1]\n\t"
    "v_pk_mul_f32 v[20:21], v[16:17], v[20:21]\n\t"
    "v_pk_mul_f32 v[22:23], v[18:19], v[22:23]\n\t"
    "v_pk_mul_f32 v[16:17], v[16:17], v[232:233] op_sel_hi:[1,0]\n\t"
    "v_pk_mul_f32 v[18:19], v[18:19], v[232:233] op_sel_hi:[1,0]\n\t"
    "v_exp_f32 v16, v16\n\t"
    "buffer_load_dwordx4 a[28:31], v211, %[srda], %[tmpa] offen offset:1024 sc1\n\t"
    "v_exp_f32 v18, v18\n\t"
    "v_exp_f32 v17, v17\n\t"
    "v_exp_f32 v19, v19\n\t"
    "v_pk_fma_f32 v[16:17], v[16:17], v[130:131], v[130:131] op_sel_hi:[1,0,0]\n\t"
    "v_pk_fma_f32 v[18:19], v[18:19], v[130:131], v[130:131] op_sel_hi:[1,0,0]\n\t"
    "v_rcp_f32 v16, v16\n\t"
    "v_rcp_f32 v18, v18\n\t"
    "s_lshr_b32 %[tmpa], %[kb1], 6\n\t"
    "buffer_load_dwordx4 a[32:35], v208, %[srda], %[tmpa] offen offset:0 sc1\n\t"
    "v_rcp_f32 v17, v17\n\t"
    "v_rcp_f32 v19, v19\n\t"
    "v_pk_mul_f32 v[20:21], v[20:21], v[16:17]\n\t"
    "v_pk_mul_f32 v[22:23], v[22:23], v[18:19]\n\t"
    "v_cvt_pk_bf16_f32 v32, v20, v21\n\t"
    "v_cvt_pk_bf16_f32 v33, v22, v23\n\t"
    "v_accvgpr_read_b32 v24, a196\n\t"
    "v_accvgpr_read_b32 v25, a197\n\t"
    "buffer_load_dwordx4 a[48:51], v208, %[srda], %[tmpa] offen offset:1024 sc1\n\t"
    "v_accvgpr_read_b32 v26, a198\n\t"
    "v_accvgpr_read_b32 v27, a199\n\t"
    "v_accvgpr_read_b32 v28, a228\n\t"
    "v_accvgpr_read_b32 v29, a229\n\t"
    "v_accvgpr_read_b32 v30, a230\n\t"
    "v_accvgpr_read_b32 v31, a231\n\t"
    "v_pk_fma_f32 v[24:25], v[132:133], v[176:177], v[24:25] op_sel_hi:[0,1,1]\n\t"
    "v_pk_fma_f32 v[26:27], v[132:133], v[178:179], v[26:27] op_sel_hi:[0,1,1]\n\t"
    "v_pk_fma_f32 v[28:29], v[132:133], v[180:181], v[28:29] op_sel_hi:[0,1,1]\n\t"
    "v_pk_fma_f32 v[30:31], v[132:133], v[182:183], v[30:31] op_sel_hi:[0,1,1]\n\t"
    "buffer_load_dwordx4 a[36:39], v209, %[srda], %[tmpa] offen offset:0 sc1\n\t"
    "v_pk_mul_f32 v[28:29], v[24:25], v[28:29]\n\t"
    "v_pk_mul_f32 v[30:31], v[26:27], v[30:31]\n\t"
    "v_pk_mul_f32 v[24:25], v[24:25], v[234:235] op_sel_hi:[1,0]\n\t"
    "v_pk_mul_f32 v[26:27], v[26:27], v[234:235] op_sel_hi:[1,0]\n\t"
    "v_exp_f32 v24, v24\n\t"
    "v_exp_f32 v26, v26\n\t"
    "v_exp_f32 v25, v25\n\t"
    "v_exp_f32 v27, v27\n\t"
    "v_pk_fma_f32 v[24:25], v[24:25], v[134:135], v[134:135] op_sel_hi:[1,0,0]\n\t"
    "v_pk_fma_f32 v[26:27], v[26:27], v[134:135], v[134:135] op_sel_hi:[1,0,0]\n\t"
    "buffer_load_dwordx4 a[52:55], v209, %[srda], %[tmpa] offen offset:1024 sc1\n\t"
    "v_rcp_f32 v24, v24\n\t"
    "v_rcp_f32 v26, v26\n\t"
    "v_rcp_f32 v25, v25\n\t"
    "v_rcp_f32 v27, v27\n\t"
    "v_pk_mul_f32 v[28:29], v[28:29], v[24:25]\n\t"
    "v_pk_mul_f32 v[30:31], v[30:31], v[26:27]\n\t"
    "v_cvt_pk_bf16_f32 v34, v28, v29\n\t"
    "v_cvt_pk_bf16_f32 v35, v30, v31\n\t"
    "v_accvgpr_read_b32 v16, a200\n\t"
    "v_accvgpr_read_b32 v17, a201\n\t"
    "buffer_load_dwordx4 a[40:43], v210, %[srda], %[tmpa] offen offset:0 sc1\n\t"
    "v_accvgpr_read_b32 v18, a202\n\t"
    "v_accvgpr_read_b32 v19, a203\n\t"
    "v_accvgpr_read_b32 v20, a232\n\t"
    "v_accvgpr_read_b32 v21, a233\n\t"
    "v_accvgpr_read_b32 v22, a234\n\t"
    "v_accvgpr_read_b32 v23, a235\n\t"
    "v_pk_fma_f32 v[16:17], v[136:137], v[176:177], v[16:17] op_sel_hi:[0,1,1]\n\t"
    "v_pk_fma_f32 v[18:19], v[136:137], v[178:179], v[18:19] op_sel_hi:[0,1,1]\n\t"
    "v_pk_fma_f32 v[20:21], v[136:137], v[180:181], v[20:21] op_sel_hi:[0,1,1]\n\t"
    "v_pk_fma_f32 v[22:23], v[136:137], v[182:183], v[22:23] op_sel_hi:[0,1,1]\n\t"
    "v_pk_mul_f32 v[20:21], v[16:17], v[20:21]\n\t"
    "v_pk_mul_f32 v[22:23], v[18:19], v[22:23]\n\t"
    "v_pk_mul_f32 v[16:17], v[16:17], v[236:237] op_sel_hi:[1,0]\n\t"
    "v_pk_mul_f32 v[18:19], v[18:19], v[236:237] op_sel_hi:[1,0]\n\t"
    "v_exp_f32 v16, v16\n\t"
    "buffer_load_dwordx4 a[56:59], v210, %[srda], %[tmpa] offen offset:1024 sc1\n\t"
    "v_exp_f32 v18, v18\n\t"
    "v_exp_f32 v17, v17\n\t"
    "v_exp_f32 v19, v19\n\t"
    "v_pk_fma_f32 v[16:17], v[16:17], v[138:139], v[138:139] op_sel_hi:[1,0,0]\n\t"
    "v_pk_fma_f32 v[18:19], v[18:19], v[138:139], v[138:139] op_sel_hi:[1,0,0]\n\t"
    "v_rcp_f32 v16, v16\n\t"
    "v_rcp_f32 v18, v18\n\t"
    "v_rcp_f32 v17, v17\n\t"
    "v_rcp_f32 v19, v19\n\t"
    "v_pk_mul_f32 v[20:21], v[20:21], v[16:17]\n\t"
    "v_pk_mul_f32 v[22:23], v[22:23], v[18:19]\n\t"
    "v_cvt_pk_bf16_f32 v36, v20, v21\n\t"
    "v_cvt_pk_bf16_f32 v37, v22, v23\n\t"
    "v_accvgpr_read_b32 v24, a204\n\t"
    "v_accvgpr_read_b32 v25, a205\n\t"
    "buffer_load_dwordx4 a[44:47], v211, %[srda], %[tmpa] offen offset:0 sc1\n\t"
    "v_accvgpr_read_b32 v26, a206\n\t"
    "v_accvgpr_read_b32 v27, a207\n\t"
    "v_accvgpr_read_b32 v28, a236\n\t"
    "v_accvgpr_read_b32 v29, a237\n\t"
    "v_accvgpr_read_b32 v30, a238\n\t"
    "v_accvgpr_read_b32 v31, a239\n\t"
    "v_pk_fma_f32 v[24:25], v[140:141], v[176:177], v[24:25] op_sel_hi:[0,1,1]\n\t"
    "v_pk_fma_f32 v[26:27], v[140:141], v[178:179], v[26:27] op_sel_hi:[0,1,1]\n\t"
    "v_pk_fma_f32 v[28:29], v[140:141], v[180:181], v[28:29] op_sel_hi:[0,1,1]\n\t"
    "v_pk_fma_f32 v[30:31], v[140:141], v[182:183], v[30:31] op_sel_hi:[0,1,1]\n\t"
    "v_pk_mul_f32 v[28:29], v[24:25], v[28:29]\n\t"
    "v_pk_mul_f32 v[30:31], v[26:27], v[30:31]\n\t"
    "v_pk_mul_f32 v[24:25], v[24:25], v[238:239] op_sel_hi:[1,0]\n\t"
    "v_pk_mul_f32 v[26:27], v[26:27], v[238:239] op_sel_hi:[1,0]\n\t"
    "v_exp_f32 v24, v24\n\t"
    "buffer_load_dwordx4 a[60:63], v211, %[srda], %[tmpa] offen offset:1024 sc1\n\t"
    "v_exp_f32 v26, v26\n\t"
    "v_exp_f32 v25, v25\n\t"
    "v_exp_f32 v27, v27\n\t"
    "v_pk_fma_f32 v[24:25], v[24:25], v[142:143], v[142:143] op_sel_hi:[1,0,0]\n\t"
    "v_pk_fma_f32 v[26:27], v[26:27], v[142:143], v[142:143] op_sel_hi:[1,0,0]\n\t"
    "v_rcp_f32 v24, v24\n\t"
    "v_rcp_f32 v26, v26\n\t"
    "v_rcp_f32 v25, v25\n\t"
    "v_rcp_f32 v27, v27\n\t"
    "v_pk_mul_f32 v[28:29], v[28:29], v[24:25]\n\t"
    "v_pk_mul_f32 v[30:31], v[30:31], v[26:27]\n\t"
    "v_cvt_pk_bf16_f32 v38, v28, v29\n\t"
    "v_cvt_pk_bf16_f32 v39, v30, v31\n\t"
    "s_waitcnt vmcnt(11)\n\t"
    "s_barrier\n\t"
    "s_cmp_lg_u32 %[w], 0\n\t"
    "s_cbranch_scc1 64f\n\t"
    "v_mov_b32 v250, s90\n\t"
    "v_mov_b32 v251, s91\n\t"
    "v_mov_b32 v249, %[flp]\n\t"
    "s_mov_b64 exec, 1\n\t"
    "buffer_store_dwordx2 v[250:251], v249, %[srdw], 0 offen sc1\n\t"
    "s_mov_b64 exec, -1\n\t"
    "64:\n\t"
    "v_accvgpr_read_b32 v16, a208\n\t"
    "v_accvgpr_read_b32 v17, a209\n\t"
    "ds_read_b64_tr_b16 a[128:129], v214 offset:0\n\t"
    "v_accvgpr_read_b32 v18, a210\n\t"
    "v_accvgpr_read_b32 v19, a211\n\t"
    "v_accvgpr_read_b32 v20, a240\n\t"
    "ds_read_b64_tr_b16 a[130:131], v214 offset:2048\n\t"
    "v_accvgpr_read_b32 v21, a241\n\t"
    "v_accvgpr_read_b32 v22, a242\n\t"
    "v_accvgpr_read_b32 v23, a243\n\t"
    "v_pk_fma_f32 v[16:17], v[144:145], v[176:177], v[16:17] op_sel_hi:[0,1,1]\n\t"
    "ds_read_b64_tr_b16 a[132:133], v215 offset:0\n\t"
    "v_pk_fma_f32 v[18:19], v[144:145], v[178:179], v[18:19] op_sel_hi:[0,1,1]\n\t"
    "v_pk_fma_f32 v[20:21], v[144:145], v[180:181], v[20:21] op_sel_hi:[0,1,1]\n\t"
    "v_pk_fma_f32 v[22:23], v[144:145], v[182:183], v[22:23] op_sel_hi:[0,1,1]\n\t"
    "v_pk_mul_f32 v[20:21], v[16:17], v[20:21]\n\t"
    "ds_read_b64_tr_b16 a[134:135], v215 offset:2048\n\t"
    "v_pk_mul_f32 v[22:23], v[18:19], v[22:23]\n\t"
    "v_pk_mul_f32 v[16:17], v[16:17], v[240:241] op_sel_hi:[1,0]\n\t"
    "v_pk_mul_f32 v[18:19], v[18:19], v[240:241] op_sel_hi:[1,0]\n\t"
    "v_exp_f32 v16, v16\n\t"
    "ds_read_b64_tr_b16 a[136:137], v216 offset:0\n\t"
    "v_exp_f32 v18, v18\n\t"
    "v_exp_f32 v17, v17\n\t"
    "v_exp_f32 v19, v19\n\t"
    "ds_read_b64_tr_b16 a[138:139], v216 offset:2048\n\t"
    "v_pk_fma_f32 v[16:17], v[16:17], v[146:147], v[146:147] op_sel_hi:[1,0,0]\n\t"
    "v_pk_fma_f32 v[18:19], v[18:19], v[146:147], v[146:147] op_sel_hi:[1,0,0]\n\t"
    "v_rcp_f32 v16, v16\n\t"
    "v_rcp_f32 v18, v18\n\t"
    "ds_read_b64_tr_b16 a[140:141], v217 offset:0\n\t"
    "v_rcp_f32 v17, v17\n\t"
    "v_rcp_f32 v19, v19\n\t"
    "v_pk_mul_f32 v[20:21], v[20:21], v[16:17]\n\t"
    "v_pk_mul_f32 v[22:23], v[22:23], v[18:19]\n\t"
    "ds_read_b64_tr_b16 a[142:143], v217 offset:2048\n\t"
    "v_cvt_pk_bf16_f32 v40, v20, v21\n\t"
    "v_cvt_pk_bf16_f32 v41, v22, v23\n\t"
    "v_accvgpr_read_b32 v24, a212\n\t"
    "v_accvgpr_read_b32 v25, a213\n\t"
    "ds_read_b64_tr_b16 a[144:145], v214 offset:8192\n\t"
    "v_accvgpr_read_b32 v26, a214\n\t"
    "v_accvgpr_read_b32 v27, a215\n\t"
    "v_accvgpr_read_b32 v28, a244\n\t"
    "ds_read_b64_tr_b16 a[146:147], v214 offset:10240\n\t"
    "v_accvgpr_read_b32 v29, a245\n\t"
    "v_accvgpr_read_b32 v30, a246\n\t"
    "v_accvgpr_read_b32 v31, a247\n\t"
    "v_pk_fma_f32 v[24:25], v[148:149], v[176:177], v[24:25] op_sel_hi:[0,1,1]\n\t"
    "ds_read_b64_tr_b16 a[148:149], v215 offset:8192\n\t"
    "v_pk_fma_f32 v[26:27], v[148:149], v[178:179], v[26:27] op_sel_hi:[0,1,1]\n\t"
    "v_pk_fma_f32 v[28:29], v[148:149], v[180:181], v[28:29] op_sel_hi:[0,1,1]\n\t"
    "v_pk_fma_f32 v[30:31], v[148:149], v[182:183], v[30:31] op_sel_hi:[0,1,1]\n\t"
    "v_pk_mul_f32 v[28:29], v[24:25], v[28:29]\n\t"
    "ds_read_b64_tr_b16 a[150:151], v215 offset:10240\n\t"
    "v_pk_mul_f32 v[30:31], v[26:27], v[30:31]\n\t"
    "v_pk_mul_f32 v[24:25], v[24:25], v[242:243] op_sel_hi:[1,0]\n\t"
    "v_pk_mul_f32 v[26:27], v[26:27], v[242:243] op_sel_hi:[1,0]\n\t"
    "v_exp_f32 v24, v24\n\t"
    "ds_read_b64_tr_b16 a[152:153], v216 offset:8192\n\t"
    "v_exp_f32 v26, v26\n\t"
    "v_exp_f32 v25, v25\n\t"
    "v_exp_f32 v27, v27\n\t"
    "ds_read_b64_tr_b16 a[154:155], v216 offset:10240\n\t"
    "v_pk_fma_f32 v[24:25], v[24:25], v[150:151], v[150:151] op_sel_hi:[1,0,0]\n\t"
    "v_pk_fma_f32 v[26:27], v[26:27], v[150:151], v[150:151] op_sel_hi:[1,0,0]\n\t"
    "v_rcp_f32 v24, v24\n\t"
    "v_rcp_f32 v26, v26\n\t"
    "ds_read_b64_tr_b16 a[156:157], v217 offset:8192\n\t"
    "v_rcp_f32 v25, v25\n\t"
    "v_rcp_f32 v27, v27\n\t"
    "v_pk_mul_f32 v[28:29], v[28:29], v[24:25]\n\t"
    "v_pk_mul_f32 v[30:31], v[30:31], v[26:27]\n\t"
    "ds_read_b64_tr_b16 a[158:159], v217 offset:10240\n\t"
    "v_cvt_pk_bf16_f32 v42, v28, v29\n\t"
    "v_cvt_pk_bf16_f32 v43, v30, v31\n\t"
    "v_accvgpr_read_b32 v16, a216\n\t"
    "v_accvgpr_read_b32 v17, a217\n\t"
    "ds_read_b64_tr_b16 a[160:161], v214 offset:4096\n\t"
    "v_accvgpr_read_b32 v18, a218\n\t"
    "v_accvgpr_read_b32 v19, a219\n\t"
    "v_accvgpr_read_b32 v20, a248\n\t"
    "ds_read_b64_tr_b16 a[162:163], v214 offset:6144\n\t"
    "v_accvgpr_read_b32 v21, a249\n\t"
    "v_accvgpr_read_b32 v22, a250\n\t"
    "v_accvgpr_read_b32 v23, a251\n\t"
    "v_pk_fma_f32 v[16:17], v[152:153], v[176:177], v[16:17] op_sel_hi:[0,1,1]\n\t"
    "ds_read_b64_tr_b16 a[164:165], v215 offset:4096\n\t"
    "v_pk_fma_f32 v[18:19], v[152:153], v[178:179], v[18:19] op_sel_hi:[0,1,1]\n\t"
    "v_pk_fma_f32 v[20:21], v[152:153], v[180:181], v[20:21] op_sel_hi:[0,1,1]\n\t"
    "v_pk_fma_f32 v[22:23], v[152:153], v[182:183], v[22:23] op_sel_hi:[0,1,1]\n\t"
    "v_pk_mul_f32 v[20:21], v[16:17], v[20:21]\n\t"
    "ds_read_b64_tr_b16 a[166:167], v215 offset:6144\n\t"
    "v_pk_mul_f32 v[22:23], v[18:19], v[22:23]\n\t"
    "v_pk_mul_f32 v[16:17], v[16:17], v[244:245] op_sel_hi:[1,0]\n\t"
    "v_pk_mul_f32 v[18:19], v[18:19], v[244:245] op_sel_hi:[1,0]\n\t"
    "v_exp_f32 v16, v16\n\t"
    "ds_read_b64_tr_b16 a[168:169], v216 offset:4096\n\t"
    "v_exp_f32 v18, v18\n\t"
    "v_exp_f32 v17, v17\n\t"
    "v_exp_f32 v19, v19\n\t"
    "ds_read_b64_tr_b16 a[170:171], v216 offset:6144\n\t"
    "v_pk_fma_f32 v[16:17], v[16:17], v[154:155], v[154:155] op_sel_hi:[1,0,0]\n\t"
    "v_pk_fma_f32 v[18:19], v[18:19], v[154:155], v[154:155] op_sel_hi:[1,0,0]\n\t"
    "v_rcp_f32 v16, v16\n\t"
    "v_rcp_f32 v18, v18\n\t"
    "ds_read_b64_tr_b16 a[172:173], v217 offset:4096\n\t"
    "v_rcp_f32 v17, v17\n\t"
    "v_rcp_f32 v19, v19\n\t"
    "v_pk_mul_f32 v[20:21], v[20:21], v[16:17]\n\t"
    "v_pk_mul_f32 v[22:23], v[22:23], v[18:19]\n\t"
    "ds_read_b64_tr_b16 a[174:175], v217 offset:6144\n\t"
    "v_cvt_pk_bf16_f32 v44, v20, v21\n\t"
    "v_cvt_pk_bf16_f32 v45, v22, v23\n\t"
    "v_accvgpr_read_b32 v24, a220\n\t"
    "v_accvgpr_read_b32 v25, a221\n\t"
    "ds_read_b64_tr_b16 a[176:177], v214 offset:12288\n\t"
    "v_accvgpr_read_b32 v26, a222\n\t"
    "v_accvgpr_read_b32 v27, a223\n\t"
    "v_accvgpr_read_b32 v28, a252\n\t"
    "ds_read_b64_tr_b16 a[178:179], v214 offset:14336\n\t"
    "v_accvgpr_read_b32 v29, a253\n\t"
    "v_accvgpr_read_b32 v30, a254\n\t"
    "v_accvgpr_read_b32 v31, a255\n\t"
    "v_pk_fma_f32 v[24:25], v[156:157], v[176:177], v[24:25] op_sel_hi:[0,1,1]\n\t"
    "ds_read_b64_tr_b16 a[180:181], v215 offset:12288\n\t"
    "v_pk_fma_f32 v[26:27], v[156:157], v[178:179], v[26:27] op_sel_hi:[0,1,1]\n\t"
    "v_pk_fma_f32 v[28:29], v[156:157], v[180:181], v[28:29] op_sel_hi:[0,1,1]\n\t"
    "v_pk_fma_f32 v[30:31], v[156:157], v[182:183], v[30:31] op_sel_hi:[0,1,1]\n\t"
    "v_pk_mul_f32 v[28:29], v[24:25], v[28:29]\n\t"
    "ds_read_b64_tr_b16 a[182:183], v215 offset:14336\n\t"
    "v_pk_mul_f32 v[30:31], v[26:27], v[30:31]\n\t"
    "v_pk_mul_f32 v[24:25], v[24:25], v[246:247] op_sel_hi:[1,0]\n\t"
    "v_pk_mul_f32 v[26:27], v[26:27], v[246:247] op_sel_hi:[1,0]\n\t"
    "v_exp_f32 v24, v24\n\t"
    "ds_read_b64_tr_b16 a[184:185], v216 offset:12288\n\t"
    "v_exp_f32 v26, v26\n\t"
    "v_exp_f32 v25, v25\n\t"
    "v_exp_f32 v27, v27\n\t"
    "ds_read_b64_tr_b16 a[186:187], v216 offset:14336\n\t"
    "v_pk_fma_f32 v[24:25], v[24:25], v[158:159], v[158:159] op_sel_hi:[1,0,0]\n\t"
    "v_pk_fma_f32 v[26:27], v[26:27], v[158:159], v[158:159] op_sel_hi:[1,0,0]\n\t"
    "v_rcp_f32 v24, v24\n\t"
    "v_rcp_f32 v26, v26\n\t"
    "ds_read_b64_tr_b16 a[188:189], v217 offset:12288\n\t"
    "v_rcp_f32 v25, v25\n\t"
    "v_rcp_f32 v27, v27\n\t"
    "v_pk_mul_f32 v[28:29], v[28:29], v[24:25]\n\t"
    "v_pk_mul_f32 v[30:31], v[30:31], v[26:27]\n\t"
    "ds_read_b64_tr_b16 a[190:191], v217 offset:14336\n\t"
    "v_cvt_pk_bf16_f32 v46, v28, v29\n\t"
    "v_cvt_pk_bf16_f32 v47, v30, v31\n\t"
    "s_cmp_eq_u32 %[wc], 0\n\t"
    "s_cbranch_scc1 66f\n\t"
    "v_swap_b32 v32, v34\n\t"
    "v_swap_b32 v33, v35\n\t"
    "v_swap_b32 v36, v38\n\t"
    "v_swap_b32 v37, v39\n\t"
    "v_swap_b32 v40, v42\n\t"
    "v_swap_b32 v41, v43\n\t"
    "v_swap_b32 v44, v46\n\t"
    "v_swap_b32 v45, v47\n\t"
    "66:\n\t"
    "ds_write_b64 v230, v[34:35] offset:0\n\t"
    "ds_write_b64 v230, v[38:39] offset:512\n\t"
    "ds_write_b64 v230, v[42:43] offset:1024\n\t"
    "ds_write_b64 v230, v[46:47] offset:1536\n\t"
    "s_waitcnt lgkmcnt(0)\n\t"
    "s_barrier\n\t"
    "ds_read_b64 v[34:35], v231 offset:0\n\t"
    "ds_read_b64 v[38:39], v231 offset:512\n\t"
    "ds_read_b64 v[42:43], v231 offset:1024\n\t"
    "ds_read_b64 v[46:47], v231 offset:1536\n\t"
    "s_waitcnt lgkmcnt(0)\n\t"
    "s_cmp_eq_u32 %[wc], 0\n\t"
    "s_cbranch_scc1 67f\n\t"
    "v_swap_b32 v32, v34\n\t"
    "v_swap_b32 v33, v35\n\t"
    "v_swap_b32 v36, v38\n\t"
    "v_swap_b32 v37, v39\n\t"
    "v_swap_b32 v40, v42\n\t"
    "v_swap_b32 v41, v43\n\t"
    "v_swap_b32 v44, v46\n\t"
    "v_swap_b32 v45, v47\n\t"
    "67:\n\t"
    "s_mov_b32 %[tmp2], 0\n\t"
    "buffer_store_dwordx4 v[32:35], v228, %[srdw], %[tmp2] offen sc1\n\t"
    "s_mov_b32 %[tmp2], 294912\n\t"
    "buffer_store_dwordx4 v[36:39], v228, %[srdw], %[tmp2] offen sc1\n\t"
    "s_mov_b32 %[tmp2], 589824\n\t"
    "buffer_store_dwordx4 v[40:43], v228, %[srdw], %[tmp2] offen sc1\n\t"
    "s_mov_b32 %[tmp2], 884736\n\t"
    "buffer_store_dwordx4 v[44:47], v228, %[srdw], %[tmp2] offen sc1\n\t"
        : E_IO : E_IN, [aost] "s"(aob + (5 * col + odd + wc + 2) * 1024), [aom] "s"(aob + (5 * col + (odd ? 0 : 4)) * 1024 + wc * 147456), [wc] "s"(wc), [mxw] "s"(lx + T_X + (w ^ 1) * 2048), [mxr] "s"(lx + T_X + w * 2048),
          [kc0] "s"(3 * role), [kc1] "s"(20 * role - 38), [kc2] "s"(40 + 8 * role), [fe] "s"(fp1 + pm2 * 10240), [fep] "s"(fp1 + p * 10240 + col * 128), [flp] "s"(fp1 + p * 10240 + 2048 + col * 128) : E_CLOB);
  }
  { unsigned soa = kb[2] >> 6, nsa = kb[3] >> 6, sob = kb[RING], tt = RING + 1, tmp, tmp2;
    static_assert(WS_PART == 0x1800000 && WS_FLAG + FL_FP == 0x2418800, "the GEMM2 stream holds these offsets as literals");
#define G2_IO "+{v[208:223]}"(a0), "+{v[128:143]}"(c[8]), "+{v[144:159]}"(c[9]), "+{v[160:175]}"(c[10]), "+{v[176:191]}"(c[11]), "+{v[192:207]}"(c[12]), "+{v[224:239]}"(h0), "+{v[240:247]}"(h1), [soa] "+s"(soa), [nsa] "+s"(nsa), [sob] "+s"(sob), [t] "+s"(tt), [tmp] "=&s"(tmp), [tmp2] "=&s"(tmp2)
#define G2_IN "{v248}"(l16), "{s[90:91]}"(tag), "{s[92:95]}"(kc), [srdw] "s"(srdw), [srda] "s"(srda2), [srdb] "s"(srdb), [m0b] "s"(ldsb + w * 2048), [w] "s"(w), [role] "s"(role), [tile] "s"(tile), [ldsb] "s"(ldsb), [ka] "s"(__builtin_amdgcn_kernarg_segment_ptr()), [pub] "s"(TA ? fh : fp1 + p * 10240 + 6144 + col * 128)
#define G2_CLOB "s64", "s65", "s66", "s67", "s68", "s69", "s70", "s71", "s72", "s73", "s74", "s75", "s76", "s77", "s78", "s79", "s80", "s81", "s82", "memory", "scc", "vcc", "s96", "s97", "v0", "v1", "v2", "v3", "v4", "v5", "v6", "v7", "v8", "v9", "v10", "v11", "v12", "v13", "v14", "v15", "v16", "v17", "v18", "v19", "v20", "v21", "v22", "v23", "v24", "v25", "v26", "v27", "v28", "v29", "v30", "v31", "v32", "v33", "v34", "v35", "v36", "v37", "v38", "v39", "v40", "v41", "v42", "v43", "v44", "v45", "v46", "v47", "v48", "v49", "v50", "v51", "v52", "v53", "v54", "v55", "v56", "v57", "v58", "v59", "v60", "v61", "v62", "v63", "v64", "v65", "v66", "v67", "v68", "v69", "v70", "v71", "v72", "v73", "v74", "v75", "v76", "v77", "v78", "v79", "v80", "v81", "v82", "v83", "v84", "v85", "v86", "v87", "v88", "v89", "v90", "v91", "v92", "v93", "v94", "v95", "v96", "v97", "v98", "v99", "v100", "v101", "v102", "v103", "v104", "v105", "v106", "v107", "v108", "v109", "v110", "v111", "v112", "v113", "v114", "v115", "v116", "v117", "v118", "v119", "v120", "v121", "v122", "v123", "v124", "v125", "v126", "v127", "v249", "v250", "v251", "v252", "v253", "v254", "v255", "a0", "a1", "a2", "a3", "a4", "a5", "a6", "a7", "a8", "a9", "a10", "a11", "a12", "a13", "a14", "a15", "a16", "a17", "a18", "a19", "a20", "a21", "a22", "a23", "a24", "a25", "a26", "a27", "a28", "a29", "a30", "a31", "a32", "a33", "a34", "a35", "a36", "a37", "a38", "a39", "a40", "a41", "a42", "a43", "a44", "a45", "a46", "a47", "a48", "a49", "a50", "a51", "a52", "a53", "a54", "a55", "a56", "a57", "a58", "a59", "a60", "a61", "a62", "a63", "a64", "a65", "a66", "a67", "a68", "a69", "a70", "a71", "a72", "a73", "a74", "a75", "a76", "a77", "a78", "a79", "a80", "a81", "a82", "a83", "a84", "a85", "a86", "a87", "a88", "a89", "a90", "a91", "a92", "a93", "a94", "a95", "a96", "a97", "a98", "a99", "a100", "a101", "a102", "a103", "a104", "a105", "a106", "a107", "a108", "a109", "a110", "a111", "a112", "a113", "a114", "a115", "a116", "a117", "a118", "a119", "a120", "a121", "a122", "a123", "a124", "a125", "a126", "a127", "a128", "a129", "a130", "a131", "a132", "a133", "a134", "a135", "a136", "a137", "a138", "a139", "a140", "a141", "a142", "a143", "a144", "a145", "a146", "a147", "a148", "a149", "a150", "a151", "a152", "a153", "a154", "a155", "a156", "a157", "a158", "a159", "a160", "a161", "a162", "a163", "a164", "a165", "a166", "a167", "a168", "a169", "a170", "a171", "a172", "a173", "a174", "a175", "a176", "a177", "a178", "a179", "a180", "a181", "a182", "a183", "a184", "a185", "a186", "a187", "a188", "a189", "a190", "a191", "a192", "a193", "a194", "a195", "a196", "a197", "a198", "a199", "a200", "a201", "a202", "a203", "a204", "a205", "a206", "a207", "a208", "a209", "a210", "a211", "a212", "a213", "a214", "a215", "a216", "a217", "a218", "a219", "a220", "a221", "a222", "a223", "a224", "a225", "a226", "a227", "a228", "a229", "a230", "a231", "a232", "a233", "a234", "a235", "a236", "a237", "a238", "a239", "a240", "a241", "a242", "a243", "a244", "a245", "a246", "a247", "a248", "a249", "a250", "a251", "a252", "a253", "a254", "a255"
    if constexpr (TA) { unsigned flgo = fp1 + pm2 * 10240 + (role == 3 ? 4096 : 2048) + 512, dfo = 512, df2 = -1024;
      asm volatile(
    "ds_read_b64_tr_b16 a[128:129], v214 offset:0\n\t"
    "ds_read_b64_tr_b16 a[130:131], v214 offset:2048\n\t"
    "ds_read_b64_tr_b16 a[132:133], v215 offset:0\n\t"
    "ds_read_b64_tr_b16 a[134:135], v215 offset:2048\n\t"
    "ds_read_b64_tr_b16 a[136:137], v216 offset:0\n\t"
    "ds_read_b64_tr_b16 a[138:139], v216 offset:2048\n\t"
    "ds_read_b64_tr_b16 a[140:141], v217 offset:0\n\t"
    "ds_read_b64_tr_b16 a[142:143], v217 offset:2048\n\t"
    "ds_read_b64_tr_b16 a[144:145], v214 offset:8192\n\t"
    "ds_read_b64_tr_b16 a[146:147], v214 offset:10240\n\t"
    "ds_read_b64_tr_b16 a[148:149], v215 offset:8192\n\t"
    "ds_read_b64_tr_b16 a[150:151], v215 offset:10240\n\t"
    "ds_read_b64_tr_b16 a[152:153], v216 offset:8192\n\t"
    "ds_read_b64_tr_b16 a[154:155], v216 offset:10240\n\t"
    "ds_read_b64_tr_b16 a[156:157], v217 offset:8192\n\t"
    "ds_read_b64_tr_b16 a[158:159], v217 offset:10240\n\t"
    "ds_read_b64_tr_b16 a[160:161], v214 offset:4096\n\t"
    "ds_read_b64_tr_b16 a[162:163], v214 offset:6144\n\t"
    "ds_read_b64_tr_b16 a[164:165], v215 offset:4096\n\t"
    "ds_read_b64_tr_b16 a[166:167], v215 offset:6144\n\t"
    "ds_read_b64_tr_b16 a[168:169], v216 offset:4096\n\t"
    "ds_read_b64_tr_b16 a[170:171], v216 offset:6144\n\t"
    "ds_read_b64_tr_b16 a[172:173], v217 offset:4096\n\t"
    "ds_read_b64_tr_b16 a[174:175], v217 offset:6144\n\t"
    "ds_read_b64_tr_b16 a[176:177], v214 offset:12288\n\t"
    "ds_read_b64_tr_b16 a[178:179], v214 offset:14336\n\t"
    "ds_read_b64_tr_b16 a[180:181], v215 offset:12288\n\t"
    "ds_read_b64_tr_b16 a[182:183], v215 offset:14336\n\t"
    "ds_read_b64_tr_b16 a[184:185], v216 offset:12288\n\t"
    "ds_read_b64_tr_b16 a[186:187], v216 offset:14336\n\t"
    "ds_read_b64_tr_b16 a[188:189], v217 offset:12288\n\t"
    "ds_read_b64_tr_b16 a[190:191], v217 offset:14336\n\t"
    "v_mov_b32 v224, 0\n\t"
    "v_mov_b32 v225, 0\n\t"
    "v_mov_b32 v226, 0\n\t"
    "v_mov_b32 v227, 0\n\t"
    "s_nop 1\n\t"
    "v_mfma_f32_32x32x16_bf16 v[0:15], v[224:227], v[224:227], 0\n\t"
    "v_mfma_f32_32x32x16_bf16 v[16:31], v[224:227], v[224:227], 0\n\t"
    "v_mfma_f32_32x32x16_bf16 v[32:47], v[224:227], v[224:227], 0\n\t"
    "v_mfma_f32_32x32x16_bf16 v[48:63], v[224:227], v[224:227], 0\n\t"
    "v_mfma_f32_32x32x16_bf16 v[64:79], v[224:227], v[224:227], 0\n\t"
    "v_mfma_f32_32x32x16_bf16 v[80:95], v[224:227], v[224:227], 0\n\t"
    "v_mfma_f32_32x32x16_bf16 v[96:111], v[224:227], v[224:227], 0\n\t"
    "v_mfma_f32_32x32x16_bf16 v[112:127], v[224:227], v[224:227], 0\n\t"
    "s_waitcnt vmcnt(8)\n\t"
    "1:\n\t"
    "s_waitcnt vmcnt(12) lgkmcnt(0)\n\t"
    "s_barrier\n\t"
    "v_mfma_f32_16x16x32_bf16 v[0:3], a[128:131], a[0:3], v[0:3]\n\t"
    "buffer_load_dwordx2 v[224:225], v223, %[srdw], %[flgo] offen sc1\n\t"
    "v_mfma_f32_16x16x32_bf16 v[16:19], a[132:135], a[0:3], v[16:19]\n\t"
    "ds_read_b64_tr_b16 a[192:193], v214 offset:16384\n\t"
    "v_mfma_f32_16x16x32_bf16 v[20:23], a[132:135], a[4:7], v[20:23]\n\t"
    "s_add_u32 m0, %[m0b], 0\n\t"
    "ds_read_b64_tr_b16 a[194:195], v214 offset:18432\n\t"
    "v_mfma_f32_16x16x32_bf16 v[4:7], a[128:131], a[4:7], v[4:7]\n\t"
    "buffer_load_dwordx4 v212, %[srdb], %[sob] offen offset:0 lds\n\t"
    "v_mfma_f32_16x16x32_bf16 v[8:11], a[128:131], a[8:11], v[8:11]\n\t"
    "ds_read_b64_tr_b16 a[196:197], v215 offset:16384\n\t"
    "v_mfma_f32_16x16x32_bf16 v[24:27], a[132:135], a[8:11], v[24:27]\n\t"
    "v_mfma_f32_16x16x32_bf16 v[28:31], a[132:135], a[12:15], v[28:31]\n\t"
    "ds_read_b64_tr_b16 a[198:199], v215 offset:18432\n\t"
    "v_mfma_f32_16x16x32_bf16 v[12:15], a[128:131], a[12:15], v[12:15]\n\t"
    "ds_read_b64_tr_b16 a[200:201], v216 offset:16384\n\t"
    "v_mfma_f32_16x16x32_bf16 v[44:47], a[136:139], a[12:15], v[44:47]\n\t"
    "buffer_load_dwordx4 a[64:67], v208, %[srda], %[soa] offen offset:0 sc1\n\t"
    "v_mfma_f32_16x16x32_bf16 v[60:63], a[140:143], a[12:15], v[60:63]\n\t"
    "ds_read_b64_tr_b16 a[202:203], v216 offset:18432\n\t"
    "v_mfma_f32_16x16x32_bf16 v[56:59], a[140:143], a[8:11], v[56:59]\n\t"
    "v_mfma_f32_16x16x32_bf16 v[40:43], a[136:139], a[8:11], v[40:43]\n\t"
    "ds_read_b64_tr_b16 a[204:205], v217 offset:16384\n\t"
    "v_mfma_f32_16x16x32_bf16 v[36:39], a[136:139], a[4:7], v[36:39]\n\t"
    "ds_read_b64_tr_b16 a[206:207], v217 offset:18432\n\t"
    "v_mfma_f32_16x16x32_bf16 v[52:55], a[140:143], a[4:7], v[52:55]\n\t"
    "buffer_load_dwordx4 a[80:83], v208, %[srda], %[soa] offen offset:1024 sc1\n\t"
    "v_mfma_f32_16x16x32_bf16 v[48:51], a[140:143], a[0:3], v[48:51]\n\t"
    "v_mfma_f32_16x16x32_bf16 v[32:35], a[136:139], a[0:3], v[32:35]\n\t"
    "ds_read_b64_tr_b16 a[208:209], v214 offset:24576\n\t"
    "v_mfma_f32_16x16x32_bf16 v[64:67], a[144:147], a[0:3], v[64:67]\n\t"
    "ds_read_b64_tr_b16 a[210:211], v214 offset:26624\n\t"
    "v_mfma_f32_16x16x32_bf16 v[80:83], a[148:151], a[0:3], v[80:83]\n\t"
    "ds_read_b64_tr_b16 a[212:213], v215 offset:24576\n\t"
    "v_mfma_f32_16x16x32_bf16 v[84:87], a[148:151], a[4:7], v[84:87]\n\t"
    "buffer_load_dwordx4 v213, %[srdb], %[sob] offen offset:1024 lds\n\t"
    "v_mfma_f32_16x16x32_bf16 v[68:71], a[144:147], a[4:7], v[68:71]\n\t"
    "v_mfma_f32_16x16x32_bf16 v[72:75], a[144:147], a[8:11], v[72:75]\n\t"
    "ds_read_b64_tr_b16 a[214:215], v215 offset:26624\n\t"
    "v_mfma_f32_16x16x32_bf16 v[88:91], a[148:151], a[8:11], v[88:91]\n\t"
    "ds_read_b64_tr_b16 a[216:217], v216 offset:24576\n\t"
    "v_mfma_f32_16x16x32_bf16 v[92:95], a[148:151], a[12:15], v[92:95]\n\t"
    "ds_read_b64_tr_b16 a[218:219], v216 offset:26624\n\t"
    "v_mfma_f32_16x16x32_bf16 v[76:79], a[144:147], a[12:15], v[76:79]\n\t"
    "buffer_load_dwordx4 a[68:71], v209, %[srda], %[soa] offen offset:0 sc1\n\t"
    "v_mfma_f32_16x16x32_bf16 v[108:111], a[152:155], a[12:15], v[108:111]\n\t"
    "v_mfma_f32_16x16x32_bf16 v[124:127], a[156:159], a[12:15], v[124:127]\n\t"
    "ds_read_b64_tr_b16 a[220:221], v217 offset:24576\n\t"
    "v_mfma_f32_16x16x32_bf16 v[120:123], a[156:159], a[8:11], v[120:123]\n\t"
    "ds_read_b64_tr_b16 a[222:223], v217 offset:26624\n\t"
    "v_mfma_f32_16x16x32_bf16 v[104:107], a[152:155], a[8:11], v[104:107]\n\t"
    "v_mfma_f32_16x16x32_bf16 v[100:103], a[152:155], a[4:7], v[100:103]\n\t"
    "buffer_load_dwordx4 a[84:87], v209, %[srda], %[soa] offen offset:1024 sc1\n\t"
    "v_mfma_f32_16x16x32_bf16 v[116:119], a[156:159], a[4:7], v[116:119]\n\t"
    "ds_read_b64_tr_b16 a[224:225], v214 offset:20480\n\t"
    "v_mfma_f32_16x16x32_bf16 v[112:115], a[156:159], a[0:3], v[112:115]\n\t"
    "ds_read_b64_tr_b16 a[226:227], v214 offset:22528\n\t"
    "v_mfma_f32_16x16x32_bf16 v[96:99], a[152:155], a[0:3], v[96:99]\n\t"
    "s_add_u32 m0, %[m0b], 8192\n\t"
    "ds_read_b64_tr_b16 a[228:229], v215 offset:20480\n\t"
    "v_mfma_f32_16x16x32_bf16 v[0:3], a[160:163], a[16:19], v[0:3]\n\t"
    "s_add_u32 %[tmp], %[sob], 128\n\t"
    "v_mfma_f32_16x16x32_bf16 v[16:19], a[164:167], a[16:19], v[16:19]\n\t"
    "buffer_load_dwordx4 v212, %[srdb], %[tmp] offen offset:0 lds\n\t"
    "v_mfma_f32_16x16x32_bf16 v[20:23], a[164:167], a[20:23], v[20:23]\n\t"
    "ds_read_b64_tr_b16 a[230:231], v215 offset:22528\n\t"
    "v_mfma_f32_16x16x32_bf16 v[4:7], a[160:163], a[20:23], v[4:7]\n\t"
    "ds_read_b64_tr_b16 a[232:233], v216 offset:20480\n\t"
    "v_mfma_f32_16x16x32_bf16 v[8:11], a[160:163], a[24:27], v[8:11]\n\t"
    "ds_read_b64_tr_b16 a[234:235], v216 offset:22528\n\t"
    "v_mfma_f32_16x16x32_bf16 v[24:27], a[164:167], a[24:27], v[24:27]\n\t"
    "v_mfma_f32_16x16x32_bf16 v[28:31], a[164:167], a[28:31], v[28:31]\n\t"
    "buffer_load_dwordx4 a[72:75], v210, %[srda], %[soa] offen offset:0 sc1\n\t"
    "v_mfma_f32_16x16x32_bf16 v[12:15], a[160:163], a[28:31], v[12:15]\n\t"
    "ds_read_b64_tr_b16 a[236:237], v217 offset:20480\n\t"
    "v_mfma_f32_16x16x32_bf16 v[44:47], a[168:171], a[28:31], v[44:47]\n\t"
    "ds_read_b64_tr_b16 a[238:239], v217 offset:22528\n\t"
    "v_mfma_f32_16x16x32_bf16 v[60:63], a[172:175], a[28:31], v[60:63]\n\t"
    "v_mfma_f32_16x16x32_bf16 v[56:59], a[172:175], a[24:27], v[56:59]\n\t"
    "ds_read_b64_tr_b16 a[240:241], v214 offset:28672\n\t"
    "v_mfma_f32_16x16x32_bf16 v[40:43], a[168:171], a[24:27], v[40:43]\n\t"
    "buffer_load_dwordx4 a[88:91], v210, %[srda], %[soa] offen offset:1024 sc1\n\t"
    "v_mfma_f32_16x16x32_bf16 v[36:39], a[168:171], a[20:23], v[36:39]\n\t"
    "ds_read_b64_tr_b16 a[242:243], v214 offset:30720\n\t"
    "v_mfma_f32_16x16x32_bf16 v[52:55], a[172:175], a[20:23], v[52:55]\n\t"
    "ds_read_b64_tr_b16 a[244:245], v215 offset:28672\n\t"
    "v_mfma_f32_16x16x32_bf16 v[48:51], a[172:175], a[16:19], v[48:51]\n\t"
    "v_mfma_f32_16x16x32_bf16 v[32:35], a[168:171], a[16:19], v[32:35]\n\t"
    "ds_read_b64_tr_b16 a[246:247], v215 offset:30720\n\t"
    "v_mfma_f32_16x16x32_bf16 v[64:67], a[176:179], a[16:19], v[64:67]\n\t"
    "buffer_load_dwordx4 v213, %[srdb], %[tmp] offen offset:1024 lds\n\t"
    "v_mfma_f32_16x16x32_bf16 v[80:83], a[180:183], a[16:19], v[80:83]\n\t"
    "ds_read_b64_tr_b16 a[248:249], v216 offset:28672\n\t"
    "v_mfma_f32_16x16x32_bf16 v[84:87], a[180:183], a[20:23], v[84:87]\n\t"
    "ds_read_b64_tr_b16 a[250:251], v216 offset:30720\n\t"
    "v_mfma_f32_16x16x32_bf16 v[68:71], a[176:179], a[20:23], v[68:71]\n\t"
    "v_mfma_f32_16x16x32_bf16 v[72:75], a[176:179], a[24:27], v[72:75]\n\t"
    "ds_read_b64_tr_b16 a[252:253], v217 offset:28672\n\t"
    "v_mfma_f32_16x16x32_bf16 v[88:91], a[180:183], a[24:27], v[88:91]\n\t"
    "buffer_load_dwordx4 a[76:79], v211, %[srda], %[soa] offen offset:0 sc1\n\t"
    "v_mfma_f32_16x16x32_bf16 v[92:95], a[180:183], a[28:31], v[92:95]\n\t"
    "ds_read_b64_tr_b16 a[254:255], v217 offset:30720\n\t"
    "v_mfma_f32_16x16x32_bf16 v[76:79], a[176:179], a[28:31], v[76:79]\n\t"
    "v_mfma_f32_16x16x32_bf16 v[108:111], a[184:187], a[28:31], v[108:111]\n\t"
    "v_mfma_f32_16x16x32_bf16 v[124:127], a[188:191], a[28:31], v[124:127]\n\t"
    "v_mfma_f32_16x16x32_bf16 v[120:123], a[188:191], a[24:27], v[120:123]\n\t"
    "buffer_load_dwordx4 a[92:95], v211, %[srda], %[soa] offen offset:1024 sc1\n\t"
    "v_mfma_f32_16x16x32_bf16 v[104:107], a[184:187], a[24:27], v[104:107]\n\t"
    "s_mov_b32 %[soa], %[nsa]\n\t"
    "s_lshr_b32 %[nsa], %[sob], 6\n\t"
    "v_mfma_f32_16x16x32_bf16 v[100:103], a[184:187], a[20:23], v[100:103]\n\t"
    "v_readlane_b32 %[sob], v222, %[t]\n\t"
    "v_mfma_f32_16x16x32_bf16 v[116:119], a[188:191], a[20:23], v[116:119]\n\t"
    "s_add_u32 %[t], %[t], 1\n\t"
    "v_mfma_f32_16x16x32_bf16 v[112:115], a[188:191], a[16:19], v[112:115]\n\t"
    "v_mfma_f32_16x16x32_bf16 v[96:99], a[184:187], a[16:19], v[96:99]\n\t"
    "s_waitcnt vmcnt(12) lgkmcnt(0)\n\t"
    "s_barrier\n\t"
    "v_mfma_f32_16x16x32_bf16 v[0:3], a[192:195], a[32:35], v[0:3]\n\t"
    "ds_read_b64_tr_b16 a[128:129], v214 offset:32768\n\t"
    "v_mfma_f32_16x16x32_bf16 v[16:19], a[196:199], a[32:35], v[16:19]\n\t"
    "ds_read_b64_tr_b16 a[130:131], v214 offset:34816\n\t"
    "v_mfma_f32_16x16x32_bf16 v[20:23], a[196:199], a[36:39], v[20:23]\n\t"
    "s_add_u32 m0, %[m0b], 16384\n\t"
    "ds_read_b64_tr_b16 a[132:133], v215 offset:32768\n\t"
    "v_mfma_f32_16x16x32_bf16 v[4:7], a[192:195], a[36:39], v[4:7]\n\t"
    "buffer_load_dwordx4 v212, %[srdb], %[sob] offen offset:0 lds\n\t"
    "v_mfma_f32_16x16x32_bf16 v[8:11], a[192:195], a[40:43], v[8:11]\n\t"
    "v_mfma_f32_16x16x32_bf16 v[24:27], a[196:199], a[40:43], v[24:27]\n\t"
    "ds_read_b64_tr_b16 a[134:135], v215 offset:34816\n\t"
    "v_mfma_f32_16x16x32_bf16 v[28:31], a[196:199], a[44:47], v[28:31]\n\t"
    "ds_read_b64_tr_b16 a[136:137], v216 offset:32768\n\t"
    "v_mfma_f32_16x16x32_bf16 v[12:15], a[192:195], a[44:47], v[12:15]\n\t"
    "v_mfma_f32_16x16x32_bf16 v[44:47], a[200:203], a[44:47], v[44:47]\n\t"
    "buffer_load_dwordx4 a[96:99], v208, %[srda], %[soa] offen offset:0 sc1\n\t"
    "v_mfma_f32_16x16x32_bf16 v[60:63], a[204:207], a[44:47], v[60:63]\n\t"
    "ds_read_b64_tr_b16 a[138:139], v216 offset:34816\n\t"
    "v_mfma_f32_16x16x32_bf16 v[56:59], a[204:207], a[40:43], v[56:59]\n\t"
    "ds_read_b64_tr_b16 a[140:141], v217 offset:32768\n\t"
    "v_mfma_f32_16x16x32_bf16 v[40:43], a[200:203], a[40:43], v[40:43]\n\t"
    "ds_read_b64_tr_b16 a[142:143], v217 offset:34816\n\t"
    "v_mfma_f32_16x16x32_bf16 v[36:39], a[200:203], a[36:39], v[36:39]\n\t"
    "v_mfma_f32_16x16x32_bf16 v[52:55], a[204:207], a[36:39], v[52:55]\n\t"
    "buffer_load_dwordx4 a[112:115], v208, %[srda], %[soa] offen offset:1024 sc1\n\t"
    "v_mfma_f32_16x16x32_bf16 v[48:51], a[204:207], a[32:35], v[48:51]\n\t"
    "ds_read_b64_tr_b16 a[144:145], v214 offset:40960\n\t"
    "v_mfma_f32_16x16x32_bf16 v[32:35], a[200:203], a[32:35], v[32:35]\n\t"
    "ds_read_b64_tr_b16 a[146:147], v214 offset:43008\n\t"
    "v_mfma_f32_16x16x32_bf16 v[64:67], a[208:211], a[32:35], v[64:67]\n\t"
    "v_mfma_f32_16x16x32_bf16 v[80:83], a[212:215], a[32:35], v[80:83]\n\t"
    "ds_read_b64_tr_b16 a[148:149], v215 offset:40960\n\t"
    "v_mfma_f32_16x16x32_bf16 v[84:87], a[212:215], a[36:39], v[84:87]\n\t"
    "buffer_load_dwordx4 v213, %[srdb], %[sob] offen offset:1024 lds\n\t"
    "v_mfma_f32_16x16x32_bf16 v[68:71], a[208:211], a[36:39], v[68:71]\n\t"
    "ds_read_b64_tr_b16 a[150:151], v215 offset:43008\n\t"
    "v_mfma_f32_16x16x32_bf16 v[72:75], a[208:211], a[40:43], v[72:75]\n\t"
    "ds_read_b64_tr_b16 a[152:153], v216 offset:40960\n\t"
    "v_mfma_f32_16x16x32_bf16 v[88:91], a[212:215], a[40:43], v[88:91]\n\t"
    "v_mfma_f32_16x16x32_bf16 v[92:95], a[212:215], a[44:47], v[92:95]\n\t"
    "ds_read_b64_tr_b16 a[154:155], v216 offset:43008\n\t"
    "v_mfma_f32_16x16x32_bf16 v[76:79], a[208:211], a[44:47], v[76:79]\n\t"
    "buffer_load_dwordx4 a[100:103], v209, %[srda], %[soa] offen offset:0 sc1\n\t"
    "v_mfma_f32_16x16x32_bf16 v[108:111], a[216:219], a[44:47], v[108:111]\n\t"
    "ds_read_b64_tr_b16 a[156:157], v217 offset:40960\n\t"
    "v_mfma_f32_16x16x32_bf16 v[124:127], a[220:223], a[44:47], v[124:127]\n\t"
    "v_mfma_f32_16x16x32_bf16 v[120:123], a[220:223], a[40:43], v[120:123]\n\t"
    "ds_read_b64_tr_b16 a[158:159], v217 offset:43008\n\t"
    "v_mfma_f32_16x16x32_bf16 v[104:107], a[216:219], a[40:43], v[104:107]\n\t"
    "ds_read_b64_tr_b16 a[160:161], v214 offset:36864\n\t"
    "v_mfma_f32_16x16x32_bf16 v[100:103], a[216:219], a[36:39], v[100:103]\n\t"
    "buffer_load_dwordx4 a[116:119], v209, %[srda], %[soa] offen offset:1024 sc1\n\t"
    "v_mfma_f32_16x16x32_bf16 v[116:119], a[220:223], a[36:39], v[116:119]\n\t"
    "ds_read_b64_tr_b16 a[162:163], v214 offset:38912\n\t"
    "v_mfma_f32_16x16x32_bf16 v[112:115], a[220:223], a[32:35], v[112:115]\n\t"
    "v_mfma_f32_16x16x32_bf16 v[96:99], a[216:219], a[32:35], v[96:99]\n\t"
    "s_add_u32 m0, %[m0b], 24576\n\t"
    "ds_read_b64_tr_b16 a[164:165], v215 offset:36864\n\t"
    "v_mfma_f32_16x16x32_bf16 v[0:3], a[224:227], a[48:51], v[0:3]\n\t"
    "s_add_u32 %[tmp], %[sob], 128\n\t"
    "ds_read_b64_tr_b16 a[166:167], v215 offset:38912\n\t"
    "v_mfma_f32_16x16x32_bf16 v[16:19], a[228:231], a[48:51], v[16:19]\n\t"
    "buffer_load_dwordx4 v212, %[srdb], %[tmp] offen offset:0 lds\n\t"
    "v_mfma_f32_16x16x32_bf16 v[20:23], a[228:231], a[52:55], v[20:23]\n\t"
    "v_mfma_f32_16x16x32_bf16 v[4:7], a[224:227], a[52:55], v[4:7]\n\t"
    "ds_read_b64_tr_b16 a[168:169], v216 offset:36864\n\t"
    "v_mfma_f32_16x16x32_bf16 v[8:11], a[224:227], a[56:59], v[8:11]\n\t"
    "ds_read_b64_tr_b16 a[170:171], v216 offset:38912\n\t"
    "v_mfma_f32_16x16x32_bf16 v[24:27], a[228:231], a[56:59], v[24:27]\n\t"
    "ds_read_b64_tr_b16 a[172:173], v217 offset:36864\n\t"
    "v_mfma_f32_16x16x32_bf16 v[28:31], a[228:231], a[60:63], v[28:31]\n\t"
    "buffer_load_dwordx4 a[104:107], v210, %[srda], %[soa] offen offset:0 sc1\n\t"
    "v_mfma_f32_16x16x32_bf16 v[12:15], a[224:227], a[60:63], v[12:15]\n\t"
    "v_mfma_f32_16x16x32_bf16 v[44:47], a[232:235], a[60:63], v[44:47]\n\t"
    "ds_read_b64_tr_b16 a[174:175], v217 offset:38912\n\t"
    "v_mfma_f32_16x16x32_bf16 v[60:63], a[236:239], a[60:63], v[60:63]\n\t"
    "ds_read_b64_tr_b16 a[176:177], v214 offset:45056\n\t"
    "v_mfma_f32_16x16x32_bf16 v[56:59], a[236:239], a[56:59], v[56:59]\n\t"
    "v_mfma_f32_16x16x32_bf16 v[40:43], a[232:235], a[56:59], v[40:43]\n\t"
    "buffer_load_dwordx4 a[120:123], v210, %[srda], %[soa] offen offset:1024 sc1\n\t"
    "v_mfma_f32_16x16x32_bf16 v[36:39], a[232:235], a[52:55], v[36:39]\n\t"
    "ds_read_b64_tr_b16 a[178:179], v214 offset:47104\n\t"
    "v_mfma_f32_16x16x32_bf16 v[52:55], a[236:239], a[52:55], v[52:55]\n\t"
    "ds_read_b64_tr_b16 a[180:181], v215 offset:45056\n\t"
    "v_mfma_f32_16x16x32_bf16 v[48:51], a[236:239], a[48:51], v[48:51]\n\t"
    "ds_read_b64_tr_b16 a[182:183], v215 offset:47104\n\t"
    "v_mfma_f32_16x16x32_bf16 v[32:35], a[232:235], a[48:51], v[32:35]\n\t"
    "v_mfma_f32_16x16x32_bf16 v[64:67], a[240:243], a[48:51], v[64:67]\n\t"
    "buffer_load_dwordx4 v213, %[srdb], %[tmp] offen offset:1024 lds\n\t"
    "v_mfma_f32_16x16x32_bf16 v[80:83], a[244:247], a[48:51], v[80:83]\n\t"
    "ds_read_b64_tr_b16 a[184:185], v216 offset:45056\n\t"
    "v_mfma_f32_16x16x32_bf16 v[84:87], a[244:247], a[52:55], v[84:87]\n\t"
    "ds_read_b64_tr_b16 a[186:187], v216 offset:47104\n\t"
    "v_mfma_f32_16x16x32_bf16 v[68:71], a[240:243], a[52:55], v[68:71]\n\t"
    "v_mfma_f32_16x16x32_bf16 v[72:75], a[240:243], a[56:59], v[72:75]\n\t"
    "ds_read_b64_tr_b16 a[188:189], v217 offset:45056\n\t"
    "v_mfma_f32_16x16x32_bf16 v[88:91], a[244:247], a[56:59], v[88:91]\n\t"
    "buffer_load_dwordx4 a[108:111], v211, %[srda], %[soa] offen offset:0 sc1\n\t"
    "v_mfma_f32_16x16x32_bf16 v[92:95], a[244:247], a[60:63], v[92:95]\n\t"
    "ds_read_b64_tr_b16 a[190:191], v217 offset:47104\n\t"
    "v_mfma_f32_16x16x32_bf16 v[76:79], a[240:243], a[60:63], v[76:79]\n\t"
    "v_mfma_f32_16x16x32_bf16 v[108:111], a[248:251], a[60:63], v[108:111]\n\t"
    "v_mfma_f32_16x16x32_bf16 v[124:127], a[252:255], a[60:63], v[124:127]\n\t"
    "v_mfma_f32_16x16x32_bf16 v[120:123], a[252:255], a[56:59], v[120:123]\n\t"
    "buffer_load_dwordx4 a[124:127], v211, %[srda], %[soa] offen offset:1024 sc1\n\t"
    "v_mfma_f32_16x16x32_bf16 v[104:107], a[248:251], a[56:59], v[104:107]\n\t"
    "s_mov_b32 %[soa], %[nsa]\n\t"
    "s_lshr_b32 %[nsa], %[sob], 6\n\t"
    "v_mfma_f32_16x16x32_bf16 v[100:103], a[248:251], a[52:55], v[100:103]\n\t"
    "v_readlane_b32 %[sob], v222, %[t]\n\t"
    "v_mfma_f32_16x16x32_bf16 v[116:119], a[252:255], a[52:55], v[116:119]\n\t"
    "s_add_u32 %[t], %[t], 1\n\t"
    "v_mfma_f32_16x16x32_bf16 v[112:115], a[252:255], a[48:51], v[112:115]\n\t"
    "v_mfma_f32_16x16x32_bf16 v[96:99], a[248:251], a[48:51], v[96:99]\n\t"
    "2:\n\t"
    "s_waitcnt vmcnt(12) lgkmcnt(0)\n\t"
    "s_barrier\n\t"
    "s_cmp_eq_u32 %[t], 7\n\t"
    "s_cbranch_scc1 52f\n\t"
    "62:\n\t"
    "v_mfma_f32_16x16x32_bf16 v[0:3], a[128:131], a[64:67], v[0:3]\n\t"
    "v_cmp_ne_u64 vcc, s[90:91], v[224:225]\n\t"
    "v_mfma_f32_16x16x32_bf16 v[16:19], a[132:135], a[64:67], v[16:19]\n\t"
    "s_cbranch_vccnz 56f\n\t"
    "57:\n\t"
    "ds_read_b64_tr_b16 a[192:193], v214 offset:49152\n\t"
    "v_mfma_f32_16x16x32_bf16 v[20:23], a[132:135], a[68:71], v[20:23]\n\t"
    "s_add_u32 m0, %[m0b], 32768\n\t"
    "ds_read_b64_tr_b16 a[194:195], v214 offset:51200\n\t"
    "v_mfma_f32_16x16x32_bf16 v[4:7], a[128:131], a[68:71], v[4:7]\n\t"
    "buffer_load_dwordx4 v212, %[srdb], %[sob] offen offset:0 lds\n\t"
    "v_mfma_f32_16x16x32_bf16 v[8:11], a[128:131], a[72:75], v[8:11]\n\t"
    "s_add_u32 %[flgo], %[flgo], %[dfo]\n\t"
    "ds_read_b64_tr_b16 a[196:197], v215 offset:49152\n\t"
    "v_mfma_f32_16x16x32_bf16 v[24:27], a[132:135], a[72:75], v[24:27]\n\t"
    "s_mov_b32 %[dfo], %[df2]\n\t"
    "s_mov_b32 %[df2], %[df3]\n\t"
    "v_mfma_f32_16x16x32_bf16 v[28:31], a[132:135], a[76:79], v[28:31]\n\t"
    "ds_read_b64_tr_b16 a[198:199], v215 offset:51200\n\t"
    "v_mfma_f32_16x16x32_bf16 v[12:15], a[128:131], a[76:79], v[12:15]\n\t"
    "ds_read_b64_tr_b16 a[200:201], v216 offset:49152\n\t"
    "v_mfma_f32_16x16x32_bf16 v[44:47], a[136:139], a[76:79], v[44:47]\n\t"
    "buffer_load_dwordx4 a[0:3], v208, %[srda], %[soa] offen offset:0 sc1\n\t"
    "v_mfma_f32_16x16x32_bf16 v[60:63], a[140:143], a[76:79], v[60:63]\n\t"
    "ds_read_b64_tr_b16 a[202:203], v216 offset:51200\n\t"
    "v_mfma_f32_16x16x32_bf16 v[56:59], a[140:143], a[72:75], v[56:59]\n\t"
    "v_mfma_f32_16x16x32_bf16 v[40:43], a[136:139], a[72:75], v[40:43]\n\t"
    "ds_read_b64_tr_b16 a[204:205], v217 offset:49152\n\t"
    "v_mfma_f32_16x16x32_bf16 v[36:39], a[136:139], a[68:71], v[36:39]\n\t"
    "ds_read_b64_tr_b16 a[206:207], v217 offset:51200\n\t"
    "v_mfma_f32_16x16x32_bf16 v[52:55], a[140:143], a[68:71], v[52:55]\n\t"
    "buffer_load_dwordx4 a[16:19], v208, %[srda], %[soa] offen offset:1024 sc1\n\t"
    "v_mfma_f32_16x16x32_bf16 v[48:51], a[140:143], a[64:67], v[48:51]\n\t"
    "v_mfma_f32_16x16x32_bf16 v[32:35], a[136:139], a[64:67], v[32:35]\n\t"
    "ds_read_b64_tr_b16 a[208:209], v214 offset:57344\n\t"
    "v_mfma_f32_16x16x32_bf16 v[64:67], a[144:147], a[64:67], v[64:67]\n\t"
    "ds_read_b64_tr_b16 a[210:211], v214 offset:59392\n\t"
    "v_mfma_f32_16x16x32_bf16 v[80:83], a[148:151], a[64:67], v[80:83]\n\t"
    "ds_read_b64_tr_b16 a[212:213], v215 offset:57344\n\t"
    "v_mfma_f32_16x16x32_bf16 v[84:87], a[148:151], a[68:71], v[84:87]\n\t"
    "buffer_load_dwordx4 v213, %[srdb], %[sob] offen offset:1024 lds\n\t"
    "v_mfma_f32_16x16x32_bf16 v[68:71], a[144:147], a[68:71], v[68:71]\n\t"
    "v_mfma_f32_16x16x32_bf16 v[72:75], a[144:147], a[72:75], v[72:75]\n\t"
    "ds_read_b64_tr_b16 a[214:215], v215 offset:59392\n\t"
    "v_mfma_f32_16x16x32_bf16 v[88:91], a[148:151], a[72:75], v[88:91]\n\t"
    "ds_read_b64_tr_b16 a[216:217], v216 offset:57344\n\t"
    "v_mfma_f32_16x16x32_bf16 v[92:95], a[148:151], a[76:79], v[92:95]\n\t"
    "ds_read_b64_tr_b16 a[218:219], v216 offset:59392\n\t"
    "v_mfma_f32_16x16x32_bf16 v[76:79], a[144:147], a[76:79], v[76:79]\n\t"
    "buffer_load_dwordx4 a[4:7], v209, %[srda], %[soa] offen offset:0 sc1\n\t"
    "v_mfma_f32_16x16x32_bf16 v[108:111], a[152:155], a[76:79], v[108:111]\n\t"
    "v_mfma_f32_16x16x32_bf16 v[124:127], a[156:159], a[76:79], v[124:127]\n\t"
    "ds_read_b64_tr_b16 a[220:221], v217 offset:57344\n\t"
    "v_mfma_f32_16x16x32_bf16 v[120:123], a[156:159], a[72:75], v[120:123]\n\t"
    "ds_read_b64_tr_b16 a[222:223], v217 offset:59392\n\t"
    "v_mfma_f32_16x16x32_bf16 v[104:107], a[152:155], a[72:75], v[104:107]\n\t"
    "v_mfma_f32_16x16x32_bf16 v[100:103], a[152:155], a[68:71], v[100:103]\n\t"
    "buffer_load_dwordx4 a[20:23], v209, %[srda], %[soa] offen offset:1024 sc1\n\t"
    "v_mfma_f32_16x16x32_bf16 v[116:119], a[156:159], a[68:71], v[116:119]\n\t"
    "ds_read_b64_tr_b16 a[224:225], v214 offset:53248\n\t"
    "v_mfma_f32_16x16x32_bf16 v[112:115], a[156:159], a[64:67], v[112:115]\n\t"
    "ds_read_b64_tr_b16 a[226:227], v214 offset:55296\n\t"
    "v_mfma_f32_16x16x32_bf16 v[96:99], a[152:155], a[64:67], v[96:99]\n\t"
    "s_add_u32 m0, %[m0b], 40960\n\t"
    "ds_read_b64_tr_b16 a[228:229], v215 offset:53248\n\t"
    "v_mfma_f32_16x16x32_bf16 v[0:3], a[160:163], a[80:83], v[0:3]\n\t"
    "s_add_u32 %[tmp], %[sob], 128\n\t"
    "v_mfma_f32_16x16x32_bf16 v[16:19], a[164:167], a[80:83], v[16:19]\n\t"
    "buffer_load_dwordx4 v212, %[srdb], %[tmp] offen offset:0 lds\n\t"
    "v_mfma_f32_16x16x32_bf16 v[20:23], a[164:167], a[84:87], v[20:23]\n\t"
    "ds_read_b64_tr_b16 a[230:231], v215 offset:55296\n\t"
    "v_mfma_f32_16x16x32_bf16 v[4:7], a[160:163], a[84:87], v[4:7]\n\t"
    "ds_read_b64_tr_b16 a[232:233], v216 offset:53248\n\t"
    "v_mfma_f32_16x16x32_bf16 v[8:11], a[160:163], a[88:91], v[8:11]\n\t"
    "ds_read_b64_tr_b16 a[234:235], v216 offset:55296\n\t"
    "v_mfma_f32_16x16x32_bf16 v[24:27], a[164:167], a[88:91], v[24:27]\n\t"
    "v_mfma_f32_16x16x32_bf16 v[28:31], a[164:167], a[92:95], v[28:31]\n\t"
    "buffer_load_dwordx4 a[8:11], v210, %[srda], %[soa] offen offset:0 sc1\n\t"
    "v_mfma_f32_16x16x32_bf16 v[12:15], a[160:163], a[92:95], v[12:15]\n\t"
    "ds_read_b64_tr_b16 a[236:237], v217 offset:53248\n\t"
    "v_mfma_f32_16x16x32_bf16 v[44:47], a[168:171], a[92:95], v[44:47]\n\t"
    "ds_read_b64_tr_b16 a[238:239], v217 offset:55296\n\t"
    "v_mfma_f32_16x16x32_bf16 v[60:63], a[172:175], a[92:95], v[60:63]\n\t"
    "v_mfma_f32_16x16x32_bf16 v[56:59], a[172:175], a[88:91], v[56:59]\n\t"
    "ds_read_b64_tr_b16 a[240:241], v214 offset:61440\n\t"
    "v_mfma_f32_16x16x32_bf16 v[40:43], a[168:171], a[88:91], v[40:43]\n\t"
    "buffer_load_dwordx4 a[24:27], v210, %[srda], %[soa] offen offset:1024 sc1\n\t"
    "v_mfma_f32_16x16x32_bf16 v[36:39], a[168:171], a[84:87], v[36:39]\n\t"
    "ds_read_b64_tr_b16 a[242:243], v214 offset:63488\n\t"
    "v_mfma_f32_16x16x32_bf16 v[52:55], a[172:175], a[84:87], v[52:55]\n\t"
    "ds_read_b64_tr_b16 a[244:245], v215 offset:61440\n\t"
    "v_mfma_f32_16x16x32_bf16 v[48:51], a[172:175], a[80:83], v[48:51]\n\t"
    "v_mfma_f32_16x16x32_bf16 v[32:35], a[168:171], a[80:83], v[32:35]\n\t"
    "ds_read_b64_tr_b16 a[246:247], v215 offset:63488\n\t"
    "v_mfma_f32_16x16x32_bf16 v[64:67], a[176:179], a[80:83], v[64:67]\n\t"
    "buffer_load_dwordx4 v213, %[srdb], %[tmp] offen offset:1024 lds\n\t"
    "v_mfma_f32_16x16x32_bf16 v[80:83], a[180:183], a[80:83], v[80:83]\n\t"
    "ds_read_b64_tr_b16 a[248:249], v216 offset:61440\n\t"
    "v_mfma_f32_16x16x32_bf16 v[84:87], a[180:183], a[84:87], v[84:87]\n\t"
    "ds_read_b64_tr_b16 a[250:251], v216 offset:63488\n\t"
    "v_mfma_f32_16x16x32_bf16 v[68:71], a[176:179], a[84:87], v[68:71]\n\t"
    "v_mfma_f32_16x16x32_bf16 v[72:75], a[176:179], a[88:91], v[72:75]\n\t"
    "ds_read_b64_tr_b16 a[252:253], v217 offset:61440\n\t"
    "v_mfma_f32_16x16x32_bf16 v[88:91], a[180:183], a[88:91], v[88:91]\n\t"
    "buffer_load_dwordx4 a[12:15], v211, %[srda], %[soa] offen offset:0 sc1\n\t"
    "v_mfma_f32_16x16x32_bf16 v[92:95], a[180:183], a[92:95], v[92:95]\n\t"
    "ds_read_b64_tr_b16 a[254:255], v217 offset:63488\n\t"
    "v_mfma_f32_16x16x32_bf16 v[76:79], a[176:179], a[92:95], v[76:79]\n\t"
    "v_mfma_f32_16x16x32_bf16 v[108:111], a[184:187], a[92:95], v[108:111]\n\t"
    "v_mfma_f32_16x16x32_bf16 v[124:127], a[188:191], a[92:95], v[124:127]\n\t"
    "v_mfma_f32_16x16x32_bf16 v[120:123], a[188:191], a[88:91], v[120:123]\n\t"
    "buffer_load_dwordx4 a[28:31], v211, %[srda], %[soa] offen offset:1024 sc1\n\t"
    "v_mfma_f32_16x16x32_bf16 v[104:107], a[184:187], a[88:91], v[104:107]\n\t"
    "s_mov_b32 %[soa], %[nsa]\n\t"
    "s_lshr_b32 %[nsa], %[sob], 6\n\t"
    "v_mfma_f32_16x16x32_bf16 v[100:103], a[184:187], a[84:87], v[100:103]\n\t"
    "v_readlane_b32 %[sob], v222, %[t]\n\t"
    "v_mfma_f32_16x16x32_bf16 v[116:119], a[188:191], a[84:87], v[116:119]\n\t"
    "s_add_u32 %[t], %[t], 1\n\t"
    "v_mfma_f32_16x16x32_bf16 v[112:115], a[188:191], a[80:83], v[112:115]\n\t"
    "v_mfma_f32_16x16x32_bf16 v[96:99], a[184:187], a[80:83], v[96:99]\n\t"
    "s_waitcnt vmcnt(12) lgkmcnt(0)\n\t"
    "s_barrier\n\t"
    "v_mfma_f32_16x16x32_bf16 v[0:3], a[192:195], a[96:99], v[0:3]\n\t"
    "ds_read_b64_tr_b16 a[128:129], v214 offset:0\n\t"
    "v_mfma_f32_16x16x32_bf16 v[16:19], a[196:199], a[96:99], v[16:19]\n\t"
    "ds_read_b64_tr_b16 a[130:131], v214 offset:2048\n\t"
    "v_mfma_f32_16x16x32_bf16 v[20:23], a[196:199], a[100:103], v[20:23]\n\t"
    "s_add_u32 m0, %[m0b], 49152\n\t"
    "ds_read_b64_tr_b16 a[132:133], v215 offset:0\n\t"
    "v_mfma_f32_16x16x32_bf16 v[4:7], a[192:195], a[100:103], v[4:7]\n\t"
    "buffer_load_dwordx4 v212, %[srdb], %[sob] offen offset:0 lds\n\t"
    "v_mfma_f32_16x16x32_bf16 v[8:11], a[192:195], a[104:107], v[8:11]\n\t"
    "v_mfma_f32_16x16x32_bf16 v[24:27], a[196:199], a[104:107], v[24:27]\n\t"
    "ds_read_b64_tr_b16 a[134:135], v215 offset:2048\n\t"
    "v_mfma_f32_16x16x32_bf16 v[28:31], a[196:199], a[108:111], v[28:31]\n\t"
    "ds_read_b64_tr_b16 a[136:137], v216 offset:0\n\t"
    "v_mfma_f32_16x16x32_bf16 v[12:15], a[192:195], a[108:111], v[12:15]\n\t"
    "v_mfma_f32_16x16x32_bf16 v[44:47], a[200:203], a[108:111], v[44:47]\n\t"
    "buffer_load_dwordx4 a[32:35], v208, %[srda], %[soa] offen offset:0 sc1\n\t"
    "v_mfma_f32_16x16x32_bf16 v[60:63], a[204:207], a[108:111], v[60:63]\n\t"
    "ds_read_b64_tr_b16 a[138:139], v216 offset:2048\n\t"
    "v_mfma_f32_16x16x32_bf16 v[56:59], a[204:207], a[104:107], v[56:59]\n\t"
    "ds_read_b64_tr_b16 a[140:141], v217 offset:0\n\t"
    "v_mfma_f32_16x16x32_bf16 v[40:43], a[200:203], a[104:107], v[40:43]\n\t"
    "ds_read_b64_tr_b16 a[142:143], v217 offset:2048\n\t"
    "v_mfma_f32_16x16x32_bf16 v[36:39], a[200:203], a[100:103], v[36:39]\n\t"
    "v_mfma_f32_16x16x32_bf16 v[52:55], a[204:207], a[100:103], v[52:55]\n\t"
    "buffer_load_dwordx4 a[48:51], v208, %[srda], %[soa] offen offset:1024 sc1\n\t"
    "v_mfma_f32_16x16x32_bf16 v[48:51], a[204:207], a[96:99], v[48:51]\n\t"
    "ds_read_b64_tr_b16 a[144:145], v214 offset:8192\n\t"
    "v_mfma_f32_16x16x32_bf16 v[32:35], a[200:203], a[96:99], v[32:35]\n\t"
    "ds_read_b64_tr_b16 a[146:147], v214 offset:10240\n\t"
    "v_mfma_f32_16x16x32_bf16 v[64:67], a[208:211], a[96:99], v[64:67]\n\t"
    "v_mfma_f32_16x16x32_bf16 v[80:83], a[212:215], a[96:99], v[80:83]\n\t"
    "ds_read_b64_tr_b16 a[148:149], v215 offset:8192\n\t"
    "v_mfma_f32_16x16x32_bf16 v[84:87], a[212:215], a[100:103], v[84:87]\n\t"
    "buffer_load_dwordx4 v213, %[srdb], %[sob] offen offset:1024 lds\n\t"
    "v_mfma_f32_16x16x32_bf16 v[68:71], a[208:211], a[100:103], v[68:71]\n\t"
    "ds_read_b64_tr_b16 a[150:151], v215 offset:10240\n\t"
    "v_mfma_f32_16x16x32_bf16 v[72:75], a[208:211], a[104:107], v[72:75]\n\t"
    "ds_read_b64_tr_b16 a[152:153], v216 offset:8192\n\t"
    "v_mfma_f32_16x16x32_bf16 v[88:91], a[212:215], a[104:107], v[88:91]\n\t"
    "v_mfma_f32_16x16x32_bf16 v[92:95], a[212:215], a[108:111], v[92:95]\n\t"
    "ds_read_b64_tr_b16 a[154:155], v216 offset:10240\n\t"
    "v_mfma_f32_16x16x32_bf16 v[76:79], a[208:211], a[108:111], v[76:79]\n\t"
    "buffer_load_dwordx4 a[36:39], v209, %[srda], %[soa] offen offset:0 sc1\n\t"
    "v_mfma_f32_16x16x32_bf16 v[108:111], a[216:219], a[108:111], v[108:111]\n\t"
    "ds_read_b64_tr_b16 a[156:157], v217 offset:8192\n\t"
    "v_mfma_f32_16x16x32_bf16 v[124:127], a[220:223], a[108:111], v[124:127]\n\t"
    "v_mfma_f32_16x16x32_bf16 v[120:123], a[220:223], a[104:107], v[120:123]\n\t"
    "ds_read_b64_tr_b16 a[158:159], v217 offset:10240\n\t"
    "v_mfma_f32_16x16x32_bf16 v[104:107], a[216:219], a[104:107], v[104:107]\n\t"
    "ds_read_b64_tr_b16 a[160:161], v214 offset:4096\n\t"
    "v_mfma_f32_16x16x32_bf16 v[100:103], a[216:219], a[100:103], v[100:103]\n\t"
    "buffer_load_dwordx4 a[52:55], v209, %[srda], %[soa] offen offset:1024 sc1\n\t"
    "v_mfma_f32_16x16x32_bf16 v[116:119], a[220:223], a[100:103], v[116:119]\n\t"
    "ds_read_b64_tr_b16 a[162:163], v214 offset:6144\n\t"
    "v_mfma_f32_16x16x32_bf16 v[112:115], a[220:223], a[96:99], v[112:115]\n\t"
    "v_mfma_f32_16x16x32_bf16 v[96:99], a[216:219], a[96:99], v[96:99]\n\t"
    "s_add_u32 m0, %[m0b], 57344\n\t"
    "ds_read_b64_tr_b16 a[164:165], v215 offset:4096\n\t"
    "v_mfma_f32_16x16x32_bf16 v[0:3], a[224:227], a[112:115], v[0:3]\n\t"
    "s_add_u32 %[tmp], %[sob], 128\n\t"
    "ds_read_b64_tr_b16 a[166:167], v215 offset:6144\n\t"
    "v_mfma_f32_16x16x32_bf16 v[16:19], a[228:231], a[112:115], v[16:19]\n\t"
    "buffer_load_dwordx4 v212, %[srdb], %[tmp] offen offset:0 lds\n\t"
    "v_mfma_f32_16x16x32_bf16 v[20:23], a[228:231], a[116:119], v[20:23]\n\t"
    "v_mfma_f32_16x16x32_bf16 v[4:7], a[224:227], a[116:119], v[4:7]\n\t"
    "ds_read_b64_tr_b16 a[168:169], v216 offset:4096\n\t"
    "v_mfma_f32_16x16x32_bf16 v[8:11], a[224:227], a[120:123], v[8:11]\n\t"
    "ds_read_b64_tr_b16 a[170:171], v216 offset:6144\n\t"
    "v_mfma_f32_16x16x32_bf16 v[24:27], a[228:231], a[120:123], v[24:27]\n\t"
    "ds_read_b64_tr_b16 a[172:173], v217 offset:4096\n\t"
    "v_mfma_f32_16x16x32_bf16 v[28:31], a[228:231], a[124:127], v[28:31]\n\t"
    "buffer_load_dwordx4 a[40:43], v210, %[srda], %[soa] offen offset:0 sc1\n\t"
    "v_mfma_f32_16x16x32_bf16 v[12:15], a[224:227], a[124:127], v[12:15]\n\t"
    "v_mfma_f32_16x16x32_bf16 v[44:47], a[232:235], a[124:127], v[44:47]\n\t"
    "ds_read_b64_tr_b16 a[174:175], v217 offset:6144\n\t"
    "v_mfma_f32_16x16x32_bf16 v[60:63], a[236:239], a[124:127], v[60:63]\n\t"
    "ds_read_b64_tr_b16 a[176:177], v214 offset:12288\n\t"
    "v_mfma_f32_16x16x32_bf16 v[56:59], a[236:239], a[120:123], v[56:59]\n\t"
    "v_mfma_f32_16x16x32_bf16 v[40:43], a[232:235], a[120:123], v[40:43]\n\t"
    "buffer_load_dwordx4 a[56:59], v210, %[srda], %[soa] offen offset:1024 sc1\n\t"
    "v_mfma_f32_16x16x32_bf16 v[36:39], a[232:235], a[116:119], v[36:39]\n\t"
    "ds_read_b64_tr_b16 a[178:179], v214 offset:14336\n\t"
    "v_mfma_f32_16x16x32_bf16 v[52:55], a[236:239], a[116:119], v[52:55]\n\t"
    "ds_read_b64_tr_b16 a[180:181], v215 offset:12288\n\t"
    "v_mfma_f32_16x16x32_bf16 v[48:51], a[236:239], a[112:115], v[48:51]\n\t"
    "ds_read_b64_tr_b16 a[182:183], v215 offset:14336\n\t"
    "v_mfma_f32_16x16x32_bf16 v[32:35], a[232:235], a[112:115], v[32:35]\n\t"
    "v_mfma_f32_16x16x32_bf16 v[64:67], a[240:243], a[112:115], v[64:67]\n\t"
    "buffer_load_dwordx4 v213, %[srdb], %[tmp] offen offset:1024 lds\n\t"
    "v_mfma_f32_16x16x32_bf16 v[80:83], a[244:247], a[112:115], v[80:83]\n\t"
    "ds_read_b64_tr_b16 a[184:185], v216 offset:12288\n\t"
    "v_mfma_f32_16x16x32_bf16 v[84:87], a[244:247], a[116:119], v[84:87]\n\t"
    "ds_read_b64_tr_b16 a[186:187], v216 offset:14336\n\t"
    "v_mfma_f32_16x16x32_bf16 v[68:71], a[240:243], a[116:119], v[68:71]\n\t"
    "v_mfma_f32_16x16x32_bf16 v[72:75], a[240:243], a[120:123], v[72:75]\n\t"
    "ds_read_b64_tr_b16 a[188:189], v217 offset:12288\n\t"
    "v_mfma_f32_16x16x32_bf16 v[88:91], a[244:247], a[120:123], v[88:91]\n\t"
    "buffer_load_dwordx4 a[44:47], v211, %[srda], %[soa] offen offset:0 sc1\n\t"
    "v_mfma_f32_16x16x32_bf16 v[92:95], a[244:247], a[124:127], v[92:95]\n\t"
    "ds_read_b64_tr_b16 a[190:191], v217 offset:14336\n\t"
    "v_mfma_f32_16x16x32_bf16 v[76:79], a[240:243], a[124:127], v[76:79]\n\t"
    "v_mfma_f32_16x16x32_bf16 v[108:111], a[248:251], a[124:127], v[108:111]\n\t"
    "v_mfma_f32_16x16x32_bf16 v[124:127], a[252:255], a[124:127], v[124:127]\n\t"
    "v_mfma_f32_16x16x32_bf16 v[120:123], a[252:255], a[120:123], v[120:123]\n\t"
    "buffer_load_dwordx4 a[60:63], v211, %[srda], %[soa] offen offset:1024 sc1\n\t"
    "v_mfma_f32_16x16x32_bf16 v[104:107], a[248:251], a[120:123], v[104:107]\n\t"
    "s_mov_b32 %[soa], %[nsa]\n\t"
    "s_lshr_b32 %[nsa], %[sob], 6\n\t"
    "v_mfma_f32_16x16x32_bf16 v[100:103], a[248:251], a[116:119], v[100:103]\n\t"
    "v_readlane_b32 %[sob], v222, %[t]\n\t"
    "v_mfma_f32_16x16x32_bf16 v[116:119], a[252:255], a[116:119], v[116:119]\n\t"
    "s_add_u32 %[t], %[t], 1\n\t"
    "s_cmp_eq_u32 %[t], %[tend]\n\t"
    "v_mfma_f32_16x16x32_bf16 v[112:115], a[252:255], a[112:115], v[112:115]\n\t"
    "v_mfma_f32_16x16x32_bf16 v[96:99], a[248:251], a[112:115], v[96:99]\n\t"
    "s_cbranch_scc0 1b\n\t"
    "s_waitcnt vmcnt(12) lgkmcnt(0)\n\t"
    "s_barrier\n\t"
    "v_mfma_f32_16x16x32_bf16 v[4:7], a[128:131], a[4:7], v[4:7]\n\t"
    "ds_read_b64_tr_b16 a[192:193], v214 offset:16384\n\t"
    "v_mfma_f32_16x16x32_bf16 v[20:23], a[132:135], a[4:7], v[20:23]\n\t"
    "s_load_dwordx2 s[64:65], %[ka], 0x0\n\t"
    "ds_read_b64_tr_b16 a[194:195], v214 offset:18432\n\t"
    "v_lshrrev_b32 v220, 4, v248\n\t"
    "v_mfma_f32_16x16x32_bf16 v[24:27], a[132:135], a[8:11], v[24:27]\n\t"
    "s_load_dwordx2 s[68:69], %[ka], 0x30\n\t"
    "ds_read_b64_tr_b16 a[196:197], v215 offset:16384\n\t"
    "v_and_b32 v220, 0x30, v220\n\t"
    "v_mfma_f32_16x16x32_bf16 v[8:11], a[128:131], a[8:11], v[8:11]\n\t"
    "s_mul_i32 s81, %[tile], 196608\n\t"
    "buffer_load_dwordx4 a[64:67], v208, %[srda], %[soa] offen offset:0 sc1\n\t"
    "v_mfma_f32_16x16x32_bf16 v[12:15], a[128:131], a[12:15], v[12:15]\n\t"
    "s_mul_i32 s82, %[w], 12288\n\t"
    "ds_read_b64_tr_b16 a[198:199], v215 offset:18432\n\t"
    "v_mfma_f32_16x16x32_bf16 v[28:31], a[132:135], a[12:15], v[28:31]\n\t"
    "s_add_u32 s81, s81, s82\n\t"
    "ds_read_b64_tr_b16 a[200:201], v216 offset:16384\n\t"
    "v_and_b32 v219, 0xf0, v248\n\t"
    "v_mfma_f32_16x16x32_bf16 v[44:47], a[136:139], a[12:15], v[44:47]\n\t"
    "s_add_u32 s81, s81, 25165824\n\t"
    "ds_read_b64_tr_b16 a[202:203], v216 offset:18432\n\t"
    "v_xor_b32 v219, v219, v220\n\t"
    "v_mfma_f32_16x16x32_bf16 v[60:63], a[140:143], a[12:15], v[60:63]\n\t"
    "s_mul_i32 s75, %[role], 49152\n\t"
    "buffer_load_dwordx4 a[80:83], v208, %[srda], %[soa] offen offset:1024 sc1\n\t"
    "v_mfma_f32_16x16x32_bf16 v[56:59], a[140:143], a[8:11], v[56:59]\n\t"
    "s_add_u32 s75, s75, s81\n\t"
    "ds_read_b64_tr_b16 a[204:205], v217 offset:16384\n\t"
    "v_mfma_f32_16x16x32_bf16 v[40:43], a[136:139], a[8:11], v[40:43]\n\t"
    "s_add_u32 s76, s75, 4096\n\t"
    "ds_read_b64_tr_b16 a[206:207], v217 offset:18432\n\t"
    "v_lshl_add_u32 v219, v220, 7, v219\n\t"
    "v_mfma_f32_16x16x32_bf16 v[36:39], a[136:139], a[4:7], v[36:39]\n\t"
    "s_add_u32 s77, s75, 8192\n\t"
    "ds_read_b64_tr_b16 a[208:209], v214 offset:24576\n\t"
    "v_lshrrev_b32 v228, 8, v248\n\t"
    "v_mfma_f32_16x16x32_bf16 v[52:55], a[140:143], a[4:7], v[52:55]\n\t"
    "s_add_u32 s82, %[role], 1\n\t"
    "buffer_load_dwordx4 a[68:71], v209, %[srda], %[soa] offen offset:0 sc1\n\t"
    "v_mfma_f32_16x16x32_bf16 v[68:71], a[144:147], a[4:7], v[68:71]\n\t"
    "s_and_b32 s82, s82, 3\n\t"
    "ds_read_b64_tr_b16 a[210:211], v214 offset:26624\n\t"
    "v_mfma_f32_16x16x32_bf16 v[84:87], a[148:151], a[4:7], v[84:87]\n\t"
    "s_mul_i32 s72, s82, 49152\n\t"
    "ds_read_b64_tr_b16 a[212:213], v215 offset:24576\n\t"
    "v_bfe_u32 v229, v248, 6, 2\n\t"
    "v_mfma_f32_16x16x32_bf16 v[88:91], a[148:151], a[8:11], v[88:91]\n\t"
    "s_cmp_gt_u32 %[role], s82\n\t"
    "v_bfe_u32 v230, v248, 4, 2\n\t"
    "v_mfma_f32_16x16x32_bf16 v[72:75], a[144:147], a[8:11], v[72:75]\n\t"
    "s_subb_u32 s82, %[role], 0\n\t"
    "buffer_load_dwordx4 a[84:87], v209, %[srda], %[soa] offen offset:1024 sc1\n\t"
    "v_mfma_f32_16x16x32_bf16 v[76:79], a[144:147], a[12:15], v[76:79]\n\t"
    "s_lshl_b32 s82, s82, 12\n\t"
    "ds_read_b64_tr_b16 a[214:215], v215 offset:26624\n\t"
    "v_cmp_eq_u32 vcc, v228, v229\n\t"
    "v_mfma_f32_16x16x32_bf16 v[92:95], a[148:151], a[12:15], v[92:95]\n\t"
    "s_add_u32 s72, s72, s82\n\t"
    "ds_read_b64_tr_b16 a[216:217], v216 offset:24576\n\t"
    "v_mov_b32 v231, 4\n\t"
    "v_mfma_f32_16x16x32_bf16 v[108:111], a[152:155], a[12:15], v[108:111]\n\t"
    "s_add_u32 s72, s72, s81\n\t"
    "ds_read_b64_tr_b16 a[218:219], v216 offset:26624\n\t"
    "v_mfma_f32_16x16x32_bf16 v[124:127], a[156:159], a[12:15], v[124:127]\n\t"
    "s_add_u32 s82, %[role], 2\n\t"
    "buffer_load_dwordx4 a[72:75], v210, %[srda], %[soa] offen offset:0 sc1\n\t"
    "v_mfma_f32_16x16x32_bf16 v[120:123], a[156:159], a[8:11], v[120:123]\n\t"
    "s_and_b32 s82, s82, 3\n\t"
    "ds_read_b64_tr_b16 a[220:221], v217 offset:24576\n\t"
    "v_mov_b32 v228, 0x3f80\n\t"
    "v_mfma_f32_16x16x32_bf16 v[104:107], a[152:155], a[8:11], v[104:107]\n\t"
    "s_mul_i32 s73, s82, 49152\n\t"
    "ds_read_b64_tr_b16 a[222:223], v217 offset:26624\n\t"
    "v_cndmask_b32 v230, v231, v230, vcc\n\t"
    "v_mfma_f32_16x16x32_bf16 v[100:103], a[152:155], a[4:7], v[100:103]\n\t"
    "s_cmp_gt_u32 %[role], s82\n\t"
    "ds_read_b64_tr_b16 a[224:225], v214 offset:20480\n\t"
    "v_mfma_f32_16x16x32_bf16 v[116:119], a[156:159], a[4:7], v[116:119]\n\t"
    "s_subb_u32 s82, %[role], 0\n\t"
    "buffer_load_dwordx4 a[88:91], v210, %[srda], %[soa] offen offset:1024 sc1\n\t"
    "v_mfma_f32_16x16x32_bf16 v[4:7], a[160:163], a[20:23], v[4:7]\n\t"
    "s_lshl_b32 s82, s82, 12\n\t"
    "ds_read_b64_tr_b16 a[226:227], v214 offset:22528\n\t"
    "v_mov_b32 v229, 0x3f800000\n\t"
    "v_mfma_f32_16x16x32_bf16 v[20:23], a[164:167], a[20:23], v[20:23]\n\t"
    "s_add_u32 s73, s73, s82\n\t"
    "ds_read_b64_tr_b16 a[228:229], v215 offset:20480\n\t"
    "v_mov_b32 v226, 0\n\t"
    "v_mfma_f32_16x16x32_bf16 v[24:27], a[164:167], a[24:27], v[24:27]\n\t"
    "s_add_u32 s73, s73, s81\n\t"
    "ds_read_b64_tr_b16 a[230:231], v215 offset:22528\n\t"
    "v_mfma_f32_16x16x32_bf16 v[8:11], a[160:163], a[24:27], v[8:11]\n\t"
    "s_add_u32 s82, %[role], 3\n\t"
    "buffer_load_dwordx4 a[76:79], v211, %[srda], %[soa] offen offset:0 sc1\n\t"
    "v_mfma_f32_16x16x32_bf16 v[12:15], a[160:163], a[28:31], v[12:15]\n\t"
    "s_and_b32 s82, s82, 3\n\t"
    "ds_read_b64_tr_b16 a[232:233], v216 offset:20480\n\t"
    "v_cmp_eq_u32 vcc, 0, v230\n\t"
    "v_mfma_f32_16x16x32_bf16 v[28:31], a[164:167], a[28:31], v[28:31]\n\t"
    "s_mul_i32 s74, s82, 49152\n\t"
    "ds_read_b64_tr_b16 a[234:235], v216 offset:22528\n\t"
    "v_mov_b32 v227, 0\n\t"
    "v_mfma_f32_16x16x32_bf16 v[44:47], a[168:171], a[28:31], v[44:47]\n\t"
    "s_cmp_gt_u32 %[role], s82\n\t"
    "v_mov_b32 v231, 0\n\t"
    "v_mfma_f32_16x16x32_bf16 v[60:63], a[172:175], a[28:31], v[60:63]\n\t"
    "s_subb_u32 s82, %[role], 0\n\t"
    "buffer_load_dwordx4 a[92:95], v211, %[srda], %[soa] offen offset:1024 sc1\n\t"
    "v_mfma_f32_16x16x32_bf16 v[56:59], a[172:175], a[24:27], v[56:59]\n\t"
    "s_lshl_b32 s82, s82, 12\n\t"
    "ds_read_b64_tr_b16 a[236:237], v217 offset:20480\n\t"
    "v_cndmask_b32 v224, 0, v228, vcc\n\t"
    "v_mfma_f32_16x16x32_bf16 v[40:43], a[168:171], a[24:27], v[40:43]\n\t"
    "s_add_u32 s74, s74, s82\n\t"
    "ds_read_b64_tr_b16 a[238:239], v217 offset:22528\n\t"
    "v_mfma_f32_16x16x32_bf16 v[36:39], a[168:171], a[20:23], v[36:39]\n\t"
    "s_add_u32 s74, s74, s81\n\t"
    "ds_read_b64_tr_b16 a[240:241], v214 offset:28672\n\t"
    "v_cmp_eq_u32 vcc, 1, v230\n\t"
    "v_mfma_f32_16x16x32_bf16 v[52:55], a[172:175], a[20:23], v[52:55]\n\t"
    "ds_read_b64_tr_b16 a[242:243], v214 offset:30720\n\t"
    "s_nop 1\n\t"
    "v_mfma_f32_16x16x32_bf16 v[68:71], a[176:179], a[20:23], v[68:71]\n\t"
    "ds_read_b64_tr_b16 a[244:245], v215 offset:28672\n\t"
    "v_mfma_f32_16x16x32_bf16 v[84:87], a[180:183], a[20:23], v[84:87]\n\t"
    "ds_read_b64_tr_b16 a[246:247], v215 offset:30720\n\t"
    "v_cndmask_b32 v224, v224, v229, vcc\n\t"
    "v_mfma_f32_16x16x32_bf16 v[88:91], a[180:183], a[24:27], v[88:91]\n\t"
    "ds_read_b64_tr_b16 a[248:249], v216 offset:28672\n\t"
    "v_cmp_eq_u32 vcc, 2, v230\n\t"
    "v_mfma_f32_16x16x32_bf16 v[72:75], a[176:179], a[24:27], v[72:75]\n\t"
    "ds_read_b64_tr_b16 a[250:251], v216 offset:30720\n\t"
    "v_mfma_f32_16x16x32_bf16 v[76:79], a[176:179], a[28:31], v[76:79]\n\t"
    "ds_read_b64_tr_b16 a[252:253], v217 offset:28672\n\t"
    "s_nop 1\n\t"
    "v_mfma_f32_16x16x32_bf16 v[92:95], a[180:183], a[28:31], v[92:95]\n\t"
    "ds_read_b64_tr_b16 a[254:255], v217 offset:30720\n\t"
    "v_cndmask_b32 v225, 0, v228, vcc\n\t"
    "v_mfma_f32_16x16x32_bf16 v[108:111], a[184:187], a[28:31], v[108:111]\n\t"
    "v_cmp_eq_u32 vcc, 3, v230\n\t"
    "v_mfma_f32_16x16x32_bf16 v[124:127], a[188:191], a[28:31], v[124:127]\n\t"
    "s_nop 1\n\t"
    "v_mfma_f32_16x16x32_bf16 v[120:123], a[188:191], a[24:27], v[120:123]\n\t"
    "v_cndmask_b32 v225, v225, v229, vcc\n\t"
    "v_mov_b32 v228, 0\n\t"
    "v_mfma_f32_16x16x32_bf16 v[104:107], a[184:187], a[24:27], v[104:107]\n\t"
    "v_mov_b32 v229, 0\n\t"
    "v_mfma_f32_16x16x32_bf16 v[100:103], a[184:187], a[20:23], v[100:103]\n\t"
    "v_mov_b32 v230, v224\n\t"
    "v_mfma_f32_16x16x32_bf16 v[116:119], a[188:191], a[20:23], v[116:119]\n\t"
    "s_mov_b32 %[soa], %[nsa]\n\t"
    "v_mov_b32 v231, v225\n\t"
    "s_waitcnt vmcnt(8) lgkmcnt(0)\n\t"
    "s_barrier\n\t"
    "v_mfma_f32_16x16x32_bf16 v[4:7], a[192:195], a[36:39], v[4:7]\n\t"
    "s_lshl_b32 s78, %[tile], 13\n\t"
    "ds_read_b64_tr_b16 a[128:129], v214 offset:32768\n\t"
    "ds_read_b64_tr_b16 a[130:131], v214 offset:34816\n\t"
    "v_mfma_f32_16x16x32_bf16 v[20:23], a[196:199], a[36:39], v[20:23]\n\t"
    "s_lshl_b32 s82, %[w], 9\n\t"
    "ds_read_b64_tr_b16 a[132:133], v215 offset:32768\n\t"
    "v_mfma_f32_16x16x32_bf16 v[24:27], a[196:199], a[40:43], v[24:27]\n\t"
    "s_add_u32 s78, s78, s82\n\t"
    "ds_read_b64_tr_b16 a[134:135], v215 offset:34816\n\t"
    "v_mfma_f32_16x16x32_bf16 v[8:11], a[192:195], a[40:43], v[8:11]\n\t"
    "s_add_u32 s78, s78, 37849088\n\t"
    "buffer_load_dwordx4 a[96:99], v208, %[srda], %[soa] offen offset:0 sc1\n\t"
    "v_mfma_f32_16x16x32_bf16 v[12:15], a[192:195], a[44:47], v[12:15]\n\t"
    "s_lshl_b32 s82, %[role], 7\n\t"
    "ds_read_b64_tr_b16 a[136:137], v216 offset:32768\n\t"
    "v_mfma_f32_16x16x32_bf16 v[28:31], a[196:199], a[44:47], v[28:31]\n\t"
    "s_add_u32 s79, s78, s82\n\t"
    "ds_read_b64_tr_b16 a[138:139], v216 offset:34816\n\t"
    "v_mfma_f32_16x16x32_bf16 v[44:47], a[200:203], a[44:47], v[44:47]\n\t"
    "s_lshl_b32 s82, %[role], 11\n\t"
    "ds_read_b64_tr_b16 a[140:141], v217 offset:32768\n\t"
    "v_mfma_f32_16x16x32_bf16 v[60:63], a[204:207], a[44:47], v[60:63]\n\t"
    "s_add_u32 s78, s78, s82\n\t"
    "buffer_load_dwordx4 a[112:115], v208, %[srda], %[soa] offen offset:1024 sc1\n\t"
    "v_mfma_f32_16x16x32_bf16 v[56:59], a[204:207], a[40:43], v[56:59]\n\t"
    "s_lshl_b32 s80, %[m0b], 1\n\t"
    "ds_read_b64_tr_b16 a[142:143], v217 offset:34816\n\t"
    "v_mfma_f32_16x16x32_bf16 v[40:43], a[200:203], a[40:43], v[40:43]\n\t"
    "s_sub_u32 s80, s80, %[ldsb]\n\t"
    "ds_read_b64_tr_b16 a[144:145], v214 offset:40960\n\t"
    "v_mfma_f32_16x16x32_bf16 v[36:39], a[200:203], a[36:39], v[36:39]\n\t"
    "s_add_u32 s80, s80, 65536\n\t"
    "ds_read_b64_tr_b16 a[146:147], v214 offset:43008\n\t"
    "v_mfma_f32_16x16x32_bf16 v[52:55], a[204:207], a[36:39], v[52:55]\n\t"
    "s_lshr_b32 s81, %[tile], 3\n\t"
    "buffer_load_dwordx4 a[100:103], v209, %[srda], %[soa] offen offset:0 sc1\n\t"
    "v_mfma_f32_16x16x32_bf16 v[68:71], a[208:211], a[36:39], v[68:71]\n\t"
    "s_lshl_b32 s81, s81, 19\n\t"
    "ds_read_b64_tr_b16 a[148:149], v215 offset:40960\n\t"
    "v_mfma_f32_16x16x32_bf16 v[84:87], a[212:215], a[36:39], v[84:87]\n\t"
    "s_lshl_b32 s82, %[w], 17\n\t"
    "ds_read_b64_tr_b16 a[150:151], v215 offset:43008\n\t"
    "v_mfma_f32_16x16x32_bf16 v[88:91], a[212:215], a[40:43], v[88:91]\n\t"
    "s_add_u32 s81, s81, s82\n\t"
    "ds_read_b64_tr_b16 a[152:153], v216 offset:40960\n\t"
    "v_mfma_f32_16x16x32_bf16 v[72:75], a[208:211], a[40:43], v[72:75]\n\t"
    "s_lshl_b32 s82, %[role], 15\n\t"
    "buffer_load_dwordx4 a[116:119], v209, %[srda], %[soa] offen offset:1024 sc1\n\t"
    "v_mfma_f32_16x16x32_bf16 v[76:79], a[208:211], a[44:47], v[76:79]\n\t"
    "s_add_u32 s81, s81, s82\n\t"
    "ds_read_b64_tr_b16 a[154:155], v216 offset:43008\n\t"
    "v_mfma_f32_16x16x32_bf16 v[92:95], a[212:215], a[44:47], v[92:95]\n\t"
    "s_and_b32 s82, %[tile], 7\n\t"
    "ds_read_b64_tr_b16 a[156:157], v217 offset:40960\n\t"
    "v_mfma_f32_16x16x32_bf16 v[108:111], a[216:219], a[44:47], v[108:111]\n\t"
    "s_lshl_b32 s82, s82, 8\n\t"
    "ds_read_b64_tr_b16 a[158:159], v217 offset:43008\n\t"
    "v_mfma_f32_16x16x32_bf16 v[124:127], a[220:223], a[44:47], v[124:127]\n\t"
    "s_add_u32 s81, s81, s82\n\t"
    "buffer_load_dwordx4 a[104:107], v210, %[srda], %[soa] offen offset:0 sc1\n\t"
    "v_mfma_f32_16x16x32_bf16 v[120:123], a[220:223], a[40:43], v[120:123]\n\t"
    "s_sub_u32 s66, 32768, s82\n\t"
    "ds_read_b64_tr_b16 a[160:161], v214 offset:36864\n\t"
    "ds_read_b64_tr_b16 a[162:163], v214 offset:38912\n\t"
    "v_mfma_f32_16x16x32_bf16 v[104:107], a[216:219], a[40:43], v[104:107]\n\t"
    "s_mov_b32 s70, s66\n\t"
    "ds_read_b64_tr_b16 a[164:165], v215 offset:36864\n\t"
    "v_mfma_f32_16x16x32_bf16 v[100:103], a[216:219], a[36:39], v[100:103]\n\t"
    "s_mov_b32 s67, 0x20000\n\t"
    "ds_read_b64_tr_b16 a[166:167], v215 offset:38912\n\t"
    "v_mfma_f32_16x16x32_bf16 v[116:119], a[220:223], a[36:39], v[116:119]\n\t"
    "s_mov_b32 s71, 0x20000\n\t"
    "buffer_load_dwordx4 a[120:123], v210, %[srda], %[soa] offen offset:1024 sc1\n\t"
    "v_mfma_f32_16x16x32_bf16 v[4:7], a[224:227], a[52:55], v[4:7]\n\t"
    "s_add_u32 s64, s64, s81\n\t"
    "ds_read_b64_tr_b16 a[168:169], v216 offset:36864\n\t"
    "v_mfma_f32_16x16x32_bf16 v[20:23], a[228:231], a[52:55], v[20:23]\n\t"
    "s_addc_u32 s65, s65, 0\n\t"
    "ds_read_b64_tr_b16 a[170:171], v216 offset:38912\n\t"
    "v_mfma_f32_16x16x32_bf16 v[24:27], a[228:231], a[56:59], v[24:27]\n\t"
    "s_add_u32 s68, s68, s81\n\t"
    "ds_read_b64_tr_b16 a[172:173], v217 offset:36864\n\t"
    "v_mfma_f32_16x16x32_bf16 v[8:11], a[224:227], a[56:59], v[8:11]\n\t"
    "s_addc_u32 s69, s69, 0\n\t"
    "buffer_load_dwordx4 a[108:111], v211, %[srda], %[soa] offen offset:0 sc1\n\t"
    "v_mfma_f32_16x16x32_bf16 v[12:15], a[224:227], a[60:63], v[12:15]\n\t"
    "ds_read_b64_tr_b16 a[174:175], v217 offset:38912\n\t"
    "v_mfma_f32_16x16x32_bf16 v[28:31], a[228:231], a[60:63], v[28:31]\n\t"
    "ds_read_b64_tr_b16 a[176:177], v214 offset:45056\n\t"
    "v_mfma_f32_16x16x32_bf16 v[44:47], a[232:235], a[60:63], v[44:47]\n\t"
    "ds_read_b64_tr_b16 a[178:179], v214 offset:47104\n\t"
    "v_mfma_f32_16x16x32_bf16 v[60:63], a[236:239], a[60:63], v[60:63]\n\t"
    "buffer_load_dwordx4 a[124:127], v211, %[srda], %[soa] offen offset:1024 sc1\n\t"
    "v_mfma_f32_16x16x32_bf16 v[56:59], a[236:239], a[56:59], v[56:59]\n\t"
    "v_xor_b32 v212, 0, v219\n\t"
    "v_mfma_f32_16x16x32_bf16 v[40:43], a[232:235], a[56:59], v[40:43]\n\t"
    "s_add_u32 m0, s80, 0\n\t"
    "ds_read_b64_tr_b16 a[180:181], v215 offset:45056\n\t"
    "v_mfma_f32_16x16x32_bf16 v[36:39], a[232:235], a[52:55], v[36:39]\n\t"
    "s_mov_b32 %[tmp2], 0\n\t"
    "ds_read_b64_tr_b16 a[182:183], v215 offset:47104\n\t"
    "v_mfma_f32_16x16x32_bf16 v[52:55], a[236:239], a[52:55], v[52:55]\n\t"
    "buffer_load_dwordx4 v212, s[64:67], %[tmp2] offen lds\n\t"
    "v_mfma_f32_16x16x32_bf16 v[68:71], a[240:243], a[52:55], v[68:71]\n\t"
    "v_xor_b32 v212, 64, v219\n\t"
    "v_mfma_f32_16x16x32_bf16 v[84:87], a[244:247], a[52:55], v[84:87]\n\t"
    "s_add_u32 m0, s80, 1024\n\t"
    "ds_read_b64_tr_b16 a[184:185], v216 offset:45056\n\t"
    "v_mfma_f32_16x16x32_bf16 v[88:91], a[244:247], a[56:59], v[88:91]\n\t"
    "s_mov_b32 %[tmp2], 8192\n\t"
    "ds_read_b64_tr_b16 a[186:187], v216 offset:47104\n\t"
    "v_mfma_f32_16x16x32_bf16 v[72:75], a[240:243], a[56:59], v[72:75]\n\t"
    "buffer_load_dwordx4 v212, s[64:67], %[tmp2] offen lds\n\t"
    "v_mfma_f32_16x16x32_bf16 v[76:79], a[240:243], a[60:63], v[76:79]\n\t"
    "v_xor_b32 v212, 128, v219\n\t"
    "v_mfma_f32_16x16x32_bf16 v[92:95], a[244:247], a[60:63], v[92:95]\n\t"
    "s_add_u32 m0, s80, 2048\n\t"
    "ds_read_b64_tr_b16 a[188:189], v217 offset:45056\n\t"
    "v_mfma_f32_16x16x32_bf16 v[108:111], a[248:251], a[60:63], v[108:111]\n\t"
    "s_mov_b32 %[tmp2], 16384\n\t"
    "ds_read_b64_tr_b16 a[190:191], v217 offset:47104\n\t"
    "v_mfma_f32_16x16x32_bf16 v[124:127], a[252:255], a[60:63], v[124:127]\n\t"
    "buffer_load_dwordx4 v212, s[64:67], %[tmp2] offen lds\n\t"
    "v_mfma_f32_16x16x32_bf16 v[120:123], a[252:255], a[56:59], v[120:123]\n\t"
    "v_xor_b32 v212, 192, v219\n\t"
    "v_mfma_f32_16x16x32_bf16 v[104:107], a[248:251], a[56:59], v[104:107]\n\t"
    "s_add_u32 m0, s80, 3072\n\t"
    "v_mfma_f32_16x16x32_bf16 v[100:103], a[248:251], a[52:55], v[100:103]\n\t"
    "s_mov_b32 %[tmp2], 24576\n\t"
    "v_mfma_f32_16x16x32_bf16 v[116:119], a[252:255], a[52:55], v[116:119]\n\t"
    "buffer_load_dwordx4 v212, s[64:67], %[tmp2] offen lds\n\t"
    "s_waitcnt vmcnt(12) lgkmcnt(0)\n\t"
    "s_barrier\n\t"
    "v_mfma_f32_16x16x32_bf16 v[4:7], a[128:131], a[68:71], v[4:7]\n\t"
    "ds_read_b64_tr_b16 a[192:193], v214 offset:49152\n\t"
    "v_mfma_f32_16x16x32_bf16 v[20:23], a[132:135], a[68:71], v[20:23]\n\t"
    "ds_read_b64_tr_b16 a[194:195], v214 offset:51200\n\t"
    "v_lshrrev_b32 v213, 5, v248\n\t"
    "v_mfma_f32_16x16x32_bf16 v[24:27], a[132:135], a[72:75], v[24:27]\n\t"
    "ds_read_b64_tr_b16 a[196:197], v215 offset:49152\n\t"
    "v_and_b32 v213, 16, v213\n\t"
    "v_mfma_f32_16x16x32_bf16 v[8:11], a[128:131], a[72:75], v[8:11]\n\t"
    "ds_read_b64_tr_b16 a[198:199], v215 offset:51200\n\t"
    "v_and_b32 v210, 0xf0, v248\n\t"
    "v_mfma_f32_16x16x32_bf16 v[12:15], a[128:131], a[76:79], v[12:15]\n\t"
    "ds_read_b64_tr_b16 a[224:225], v214 offset:53248\n\t"
    "v_xor_b32 v213, v213, v210\n\t"
    "v_mfma_f32_16x16x32_bf16 v[28:31], a[132:135], a[76:79], v[28:31]\n\t"
    "ds_read_b64_tr_b16 a[226:227], v214 offset:55296\n\t"
    "v_lshl_add_u32 v213, v210, 4, v213\n\t"
    "v_mfma_f32_16x16x32_bf16 v[4:7], a[160:163], a[84:87], v[4:7]\n\t"
    "ds_read_b64_tr_b16 a[228:229], v215 offset:53248\n\t"
    "v_lshrrev_b32 v210, 5, v248\n\t"
    "v_mfma_f32_16x16x32_bf16 v[20:23], a[164:167], a[84:87], v[20:23]\n\t"
    "ds_read_b64_tr_b16 a[230:231], v215 offset:55296\n\t"
    "v_and_b32 v210, 8, v210\n\t"
    "v_mfma_f32_16x16x32_bf16 v[24:27], a[164:167], a[88:91], v[24:27]\n\t"
    "v_add3_u32 v208, v213, v210, s80\n\t"
    "v_mfma_f32_16x16x32_bf16 v[8:11], a[160:163], a[88:91], v[8:11]\n\t"
    "v_xor_b32 v209, 32, v208\n\t"
    "v_mfma_f32_16x16x32_bf16 v[12:15], a[160:163], a[92:95], v[12:15]\n\t"
    "v_xor_b32 v210, 64, v208\n\t"
    "v_mfma_f32_16x16x32_bf16 v[28:31], a[164:167], a[92:95], v[28:31]\n\t"
    "s_waitcnt vmcnt(4) lgkmcnt(0)\n\t"
    "v_xor_b32 v211, 96, v208\n\t"
    "v_mfma_f32_16x16x32_bf16 v[4:7], a[192:195], a[100:103], v[4:7]\n\t"
    "ds_read_b64_tr_b16 a[200:201], v216 offset:49152\n\t"
    "v_add_u32 v213, s80, v248\n\t"
    "v_mfma_f32_16x16x32_bf16 v[20:23], a[196:199], a[100:103], v[20:23]\n\t"
    "ds_read_b64_tr_b16 a[202:203], v216 offset:51200\n\t"
    "v_bfe_u32 v223, v248, 4, 2\n\t"
    "v_mfma_f32_16x16x32_bf16 v[24:27], a[196:199], a[104:107], v[24:27]\n\t"
    "ds_read_b64_tr_b16 a[204:205], v217 offset:49152\n\t"
    "v_min_u32 v223, 2, v223\n\t"
    "v_mfma_f32_16x16x32_bf16 v[8:11], a[192:195], a[104:107], v[8:11]\n\t"
    "ds_read_b64_tr_b16 a[206:207], v217 offset:51200\n\t"
    "v_cmp_le_u32 vcc, %[role], v223\n\t"
    "v_mfma_f32_16x16x32_bf16 v[12:15], a[192:195], a[108:111], v[12:15]\n\t"
    "ds_read_b64_tr_b16 a[232:233], v216 offset:53248\n\t"
    "v_addc_co_u32 v223, vcc, 0, v223, vcc\n\t"
    "v_mfma_f32_16x16x32_bf16 v[28:31], a[196:199], a[108:111], v[28:31]\n\t"
    "ds_read_b64_tr_b16 a[234:235], v216 offset:55296\n\t"
    "v_lshlrev_b32 v223, 7, v223\n\t"
    "v_mfma_f32_16x16x32_bf16 v[4:7], a[224:227], a[116:119], v[4:7]\n\t"
    "ds_read_b64_tr_b16 a[236:237], v217 offset:53248\n\t"
    "v_add_u32 v223, s78, v223\n\t"
    "v_mfma_f32_16x16x32_bf16 v[20:23], a[228:231], a[116:119], v[20:23]\n\t"
    "ds_read_b64_tr_b16 a[238:239], v217 offset:55296\n\t"
    "v_lshrrev_b32 v249, 4, v248\n\t"
    "v_mfma_f32_16x16x32_bf16 v[24:27], a[228:231], a[120:123], v[24:27]\n\t"
    "v_add3_u32 v249, v249, %[role], 1\n\t"
    "v_mfma_f32_16x16x32_bf16 v[8:11], a[224:227], a[120:123], v[8:11]\n\t"
    "v_and_b32 v249, 3, v249\n\t"
    "v_mfma_f32_16x16x32_bf16 v[12:15], a[224:227], a[124:127], v[12:15]\n\t"
    "v_lshlrev_b32 v249, 11, v249\n\t"
    "v_mfma_f32_16x16x32_bf16 v[28:31], a[228:231], a[124:127], v[28:31]\n\t"
    "v_add_u32 v249, s79, v249\n\t"
    "v_mfma_f32_16x16x32_bf16 v[36:39], a[136:139], a[68:71], v[36:39]\n\t"
    "v_mfma_f32_16x16x32_bf16 v[52:55], a[140:143], a[68:71], v[52:55]\n\t"
    "v_cvt_pk_bf16_f32 v4, v4, v5\n\t"
    "v_cvt_pk_bf16_f32 v5, v6, v7\n\t"
    "v_mfma_f32_16x16x32_bf16 v[56:59], a[140:143], a[72:75], v[56:59]\n\t"
    "v_cvt_pk_bf16_f32 v6, v20, v21\n\t"
    "v_mfma_f32_16x16x32_bf16 v[40:43], a[136:139], a[72:75], v[40:43]\n\t"
    "v_cvt_pk_bf16_f32 v7, v22, v23\n\t"
    "buffer_store_dwordx4 v[4:7], v248, %[srdw], s72 offen offset:0 sc1\n\t"
    "v_mfma_f32_16x16x32_bf16 v[44:47], a[136:139], a[76:79], v[44:47]\n\t"
    "v_cvt_pk_bf16_f32 v8, v8, v9\n\t"
    "v_mfma_f32_16x16x32_bf16 v[60:63], a[140:143], a[76:79], v[60:63]\n\t"
    "v_cvt_pk_bf16_f32 v9, v10, v11\n\t"
    "v_mfma_f32_16x16x32_bf16 v[36:39], a[168:171], a[84:87], v[36:39]\n\t"
    "v_cvt_pk_bf16_f32 v10, v24, v25\n\t"
    "v_cvt_pk_bf16_f32 v11, v26, v27\n\t"
    "v_mfma_f32_16x16x32_bf16 v[52:55], a[172:175], a[84:87], v[52:55]\n\t"
    "buffer_store_dwordx4 v[8:11], v248, %[srdw], s73 offen offset:0 sc1\n\t"
    "v_mfma_f32_16x16x32_bf16 v[56:59], a[172:175], a[88:91], v[56:59]\n\t"
    "v_cvt_pk_bf16_f32 v12, v12, v13\n\t"
    "v_mfma_f32_16x16x32_bf16 v[40:43], a[168:171], a[88:91], v[40:43]\n\t"
    "v_cvt_pk_bf16_f32 v13, v14, v15\n\t"
    "v_cvt_pk_bf16_f32 v14, v28, v29\n\t"
    "v_mfma_f32_16x16x32_bf16 v[44:47], a[168:171], a[92:95], v[44:47]\n\t"
    "v_cvt_pk_bf16_f32 v15, v30, v31\n\t"
    "v_mfma_f32_16x16x32_bf16 v[60:63], a[172:175], a[92:95], v[60:63]\n\t"
    "buffer_store_dwordx4 v[12:15], v248, %[srdw], s74 offen offset:0 sc1\n\t"
    "s_waitcnt lgkmcnt(0)\n\t"
    "v_mfma_f32_16x16x32_bf16 v[36:39], a[200:203], a[100:103], v[36:39]\n\t"
    "ds_read_b64_tr_b16 a[208:209], v214 offset:57344\n\t"
    "v_mfma_f32_16x16x32_bf16 v[52:55], a[204:207], a[100:103], v[52:55]\n\t"
    "ds_read_b64_tr_b16 a[210:211], v214 offset:59392\n\t"
    "v_mfma_f32_16x16x32_bf16 v[56:59], a[204:207], a[104:107], v[56:59]\n\t"
    "ds_read_b64_tr_b16 a[212:213], v215 offset:57344\n\t"
    "v_mfma_f32_16x16x32_bf16 v[40:43], a[200:203], a[104:107], v[40:43]\n\t"
    "ds_read_b64_tr_b16 a[214:215], v215 offset:59392\n\t"
    "v_mfma_f32_16x16x32_bf16 v[44:47], a[200:203], a[108:111], v[44:47]\n\t"
    "ds_read_b64_tr_b16 a[240:241], v214 offset:61440\n\t"
    "v_mfma_f32_16x16x32_bf16 v[60:63], a[204:207], a[108:111], v[60:63]\n\t"
    "ds_read_b64_tr_b16 a[242:243], v214 offset:63488\n\t"
    "v_mfma_f32_16x16x32_bf16 v[36:39], a[232:235], a[116:119], v[36:39]\n\t"
    "ds_read_b64_tr_b16 a[244:245], v215 offset:61440\n\t"
    "v_mfma_f32_16x16x32_bf16 v[52:55], a[236:239], a[116:119], v[52:55]\n\t"
    "ds_read_b64_tr_b16 a[246:247], v215 offset:63488\n\t"
    "v_mfma_f32_16x16x32_bf16 v[56:59], a[236:239], a[120:123], v[56:59]\n\t"
    "v_mfma_f32_16x16x32_bf16 v[40:43], a[232:235], a[120:123], v[40:43]\n\t"
    "v_mfma_f32_16x16x32_bf16 v[44:47], a[232:235], a[124:127], v[44:47]\n\t"
    "v_mfma_f32_16x16x32_bf16 v[60:63], a[236:239], a[124:127], v[60:63]\n\t"
    "v_mfma_f32_16x16x32_bf16 v[68:71], a[144:147], a[68:71], v[68:71]\n\t"
    "v_mfma_f32_16x16x32_bf16 v[84:87], a[148:151], a[68:71], v[84:87]\n\t"
    "v_cvt_pk_bf16_f32 v36, v36, v37\n\t"
    "v_cvt_pk_bf16_f32 v37, v38, v39\n\t"
    "v_mfma_f32_16x16x32_bf16 v[88:91], a[148:151], a[72:75], v[88:91]\n\t"
    "v_cvt_pk_bf16_f32 v38, v52, v53\n\t"
    "v_mfma_f32_16x16x32_bf16 v[72:75], a[144:147], a[72:75], v[72:75]\n\t"
    "v_cvt_pk_bf16_f32 v39, v54, v55\n\t"
    "buffer_store_dwordx4 v[36:39], v248, %[srdw], s72 offen offset:1024 sc1\n\t"
    "v_mfma_f32_16x16x32_bf16 v[76:79], a[144:147], a[76:79], v[76:79]\n\t"
    "v_cvt_pk_bf16_f32 v40, v40, v41\n\t"
    "v_mfma_f32_16x16x32_bf16 v[92:95], a[148:151], a[76:79], v[92:95]\n\t"
    "v_cvt_pk_bf16_f32 v41, v42, v43\n\t"
    "v_mfma_f32_16x16x32_bf16 v[68:71], a[176:179], a[84:87], v[68:71]\n\t"
    "v_cvt_pk_bf16_f32 v42, v56, v57\n\t"
    "v_cvt_pk_bf16_f32 v43, v58, v59\n\t"
    "v_mfma_f32_16x16x32_bf16 v[84:87], a[180:183], a[84:87], v[84:87]\n\t"
    "buffer_store_dwordx4 v[40:43], v248, %[srdw], s73 offen offset:1024 sc1\n\t"
    "v_mfma_f32_16x16x32_bf16 v[88:91], a[180:183], a[88:91], v[88:91]\n\t"
    "v_cvt_pk_bf16_f32 v44, v44, v45\n\t"
    "v_mfma_f32_16x16x32_bf16 v[72:75], a[176:179], a[88:91], v[72:75]\n\t"
    "v_cvt_pk_bf16_f32 v45, v46, v47\n\t"
    "v_cvt_pk_bf16_f32 v46, v60, v61\n\t"
    "v_mfma_f32_16x16x32_bf16 v[76:79], a[176:179], a[92:95], v[76:79]\n\t"
    "v_cvt_pk_bf16_f32 v47, v62, v63\n\t"
    "v_mfma_f32_16x16x32_bf16 v[92:95], a[180:183], a[92:95], v[92:95]\n\t"
    "buffer_store_dwordx4 v[44:47], v248, %[srdw], s74 offen offset:1024 sc1\n\t"
    "s_waitcnt lgkmcnt(0)\n\t"
    "v_mfma_f32_16x16x32_bf16 v[68:71], a[208:211], a[100:103], v[68:71]\n\t"
    "ds_read_b64_tr_b16 a[216:217], v216 offset:57344\n\t"
    "v_mfma_f32_16x16x32_bf16 v[84:87], a[212:215], a[100:103], v[84:87]\n\t"
    "ds_read_b64_tr_b16 a[218:219], v216 offset:59392\n\t"
    "v_mfma_f32_16x16x32_bf16 v[88:91], a[212:215], a[104:107], v[88:91]\n\t"
    "ds_read_b64_tr_b16 a[220:221], v217 offset:57344\n\t"
    "v_mfma_f32_16x16x32_bf16 v[72:75], a[208:211], a[104:107], v[72:75]\n\t"
    "ds_read_b64_tr_b16 a[222:223], v217 offset:59392\n\t"
    "v_mfma_f32_16x16x32_bf16 v[76:79], a[208:211], a[108:111], v[76:79]\n\t"
    "ds_read_b64_tr_b16 a[248:249], v216 offset:61440\n\t"
    "v_mfma_f32_16x16x32_bf16 v[92:95], a[212:215], a[108:111], v[92:95]\n\t"
    "ds_read_b64_tr_b16 a[250:251], v216 offset:63488\n\t"
    "v_mfma_f32_16x16x32_bf16 v[68:71], a[240:243], a[116:119], v[68:71]\n\t"
    "ds_read_b64_tr_b16 a[252:253], v217 offset:61440\n\t"
    "v_mfma_f32_16x16x32_bf16 v[84:87], a[244:247], a[116:119], v[84:87]\n\t"
    "ds_read_b64_tr_b16 a[254:255], v217 offset:63488\n\t"
    "v_mfma_f32_16x16x32_bf16 v[88:91], a[244:247], a[120:123], v[88:91]\n\t"
    "v_mfma_f32_16x16x32_bf16 v[72:75], a[240:243], a[120:123], v[72:75]\n\t"
    "v_mfma_f32_16x16x32_bf16 v[76:79], a[240:243], a[124:127], v[76:79]\n\t"
    "v_mfma_f32_16x16x32_bf16 v[92:95], a[244:247], a[124:127], v[92:95]\n\t"
    "v_mfma_f32_16x16x32_bf16 v[100:103], a[152:155], a[68:71], v[100:103]\n\t"
    "v_mfma_f32_16x16x32_bf16 v[116:119], a[156:159], a[68:71], v[116:119]\n\t"
    "v_cvt_pk_bf16_f32 v68, v68, v69\n\t"
    "v_cvt_pk_bf16_f32 v69, v70, v71\n\t"
    "v_mfma_f32_16x16x32_bf16 v[120:123], a[156:159], a[72:75], v[120:123]\n\t"
    "v_cvt_pk_bf16_f32 v70, v84, v85\n\t"
    "v_mfma_f32_16x16x32_bf16 v[104:107], a[152:155], a[72:75], v[104:107]\n\t"
    "v_cvt_pk_bf16_f32 v71, v86, v87\n\t"
    "buffer_store_dwordx4 v[68:71], v248, %[srdw], s72 offen offset:2048 sc1\n\t"
    "v_mfma_f32_16x16x32_bf16 v[108:111], a[152:155], a[76:79], v[108:111]\n\t"
    "v_cvt_pk_bf16_f32 v72, v72, v73\n\t"
    "v_mfma_f32_16x16x32_bf16 v[124:127], a[156:159], a[76:79], v[124:127]\n\t"
    "v_cvt_pk_bf16_f32 v73, v74, v75\n\t"
    "v_mfma_f32_16x16x32_bf16 v[100:103], a[184:187], a[84:87], v[100:103]\n\t"
    "v_cvt_pk_bf16_f32 v74, v88, v89\n\t"
    "v_cvt_pk_bf16_f32 v75, v90, v91\n\t"
    "v_mfma_f32_16x16x32_bf16 v[116:119], a[188:191], a[84:87], v[116:119]\n\t"
    "buffer_store_dwordx4 v[72:75], v248, %[srdw], s73 offen offset:2048 sc1\n\t"
    "v_mfma_f32_16x16x32_bf16 v[120:123], a[188:191], a[88:91], v[120:123]\n\t"
    "v_cvt_pk_bf16_f32 v76, v76, v77\n\t"
    "v_mfma_f32_16x16x32_bf16 v[104:107], a[184:187], a[88:91], v[104:107]\n\t"
    "v_cvt_pk_bf16_f32 v77, v78, v79\n\t"
    "v_cvt_pk_bf16_f32 v78, v92, v93\n\t"
    "v_mfma_f32_16x16x32_bf16 v[108:111], a[184:187], a[92:95], v[108:111]\n\t"
    "v_cvt_pk_bf16_f32 v79, v94, v95\n\t"
    "v_mfma_f32_16x16x32_bf16 v[124:127], a[188:191], a[92:95], v[124:127]\n\t"
    "buffer_store_dwordx4 v[76:79], v248, %[srdw], s74 offen offset:2048 sc1\n\t"
    "s_waitcnt lgkmcnt(0)\n\t"
    "v_mfma_f32_16x16x32_bf16 v[100:103], a[216:219], a[100:103], v[100:103]\n\t"
    "v_mfma_f32_16x16x32_bf16 v[116:119], a[220:223], a[100:103], v[116:119]\n\t"
    "v_mfma_f32_16x16x32_bf16 v[120:123], a[220:223], a[104:107], v[120:123]\n\t"
    "v_mfma_f32_16x16x32_bf16 v[104:107], a[216:219], a[104:107], v[104:107]\n\t"
    "v_mfma_f32_16x16x32_bf16 v[108:111], a[216:219], a[108:111], v[108:111]\n\t"
    "v_mfma_f32_16x16x32_bf16 v[124:127], a[220:223], a[108:111], v[124:127]\n\t"
    "v_mfma_f32_16x16x32_bf16 v[100:103], a[248:251], a[116:119], v[100:103]\n\t"
    "v_mfma_f32_16x16x32_bf16 v[116:119], a[252:255], a[116:119], v[116:119]\n\t"
    "v_mfma_f32_16x16x32_bf16 v[120:123], a[252:255], a[120:123], v[120:123]\n\t"
    "v_mfma_f32_16x16x32_bf16 v[104:107], a[248:251], a[120:123], v[104:107]\n\t"
    "v_mfma_f32_16x16x32_bf16 v[108:111], a[248:251], a[124:127], v[108:111]\n\t"
    "v_mfma_f32_16x16x32_bf16 v[124:127], a[252:255], a[124:127], v[124:127]\n\t"
    "v_mfma_f32_16x16x32_bf16 v[0:3], a[128:131], a[64:67], v[0:3]\n\t"
    "v_mfma_f32_16x16x32_bf16 v[16:19], a[132:135], a[64:67], v[16:19]\n\t"
    "s_nop 0\n\t"
    "v_cvt_pk_bf16_f32 v100, v100, v101\n\t"
    "v_mfma_f32_16x16x32_bf16 v[32:35], a[136:139], a[64:67], v[32:35]\n\t"
    "v_cvt_pk_bf16_f32 v101, v102, v103\n\t"
    "v_mfma_f32_16x16x32_bf16 v[48:51], a[140:143], a[64:67], v[48:51]\n\t"
    "v_cvt_pk_bf16_f32 v102, v116, v117\n\t"
    "v_mfma_f32_16x16x32_bf16 v[64:67], a[144:147], a[64:67], v[64:67]\n\t"
    "v_cvt_pk_bf16_f32 v103, v118, v119\n\t"
    "v_mfma_f32_16x16x32_bf16 v[80:83], a[148:151], a[64:67], v[80:83]\n\t"
    "buffer_store_dwordx4 v[100:103], v248, %[srdw], s72 offen offset:3072 sc1\n\t"
    "v_mfma_f32_16x16x32_bf16 v[96:99], a[152:155], a[64:67], v[96:99]\n\t"
    "v_cvt_pk_bf16_f32 v104, v104, v105\n\t"
    "v_mfma_f32_16x16x32_bf16 v[112:115], a[156:159], a[64:67], v[112:115]\n\t"
    "v_cvt_pk_bf16_f32 v105, v106, v107\n\t"
    "v_mfma_f32_16x16x32_bf16 v[0:3], a[160:163], a[80:83], v[0:3]\n\t"
    "v_cvt_pk_bf16_f32 v106, v120, v121\n\t"
    "v_mfma_f32_16x16x32_bf16 v[16:19], a[164:167], a[80:83], v[16:19]\n\t"
    "v_cvt_pk_bf16_f32 v107, v122, v123\n\t"
    "v_mfma_f32_16x16x32_bf16 v[32:35], a[168:171], a[80:83], v[32:35]\n\t"
    "buffer_store_dwordx4 v[104:107], v248, %[srdw], s73 offen offset:3072 sc1\n\t"
    "v_mfma_f32_16x16x32_bf16 v[48:51], a[172:175], a[80:83], v[48:51]\n\t"
    "v_cvt_pk_bf16_f32 v108, v108, v109\n\t"
    "v_mfma_f32_16x16x32_bf16 v[64:67], a[176:179], a[80:83], v[64:67]\n\t"
    "v_cvt_pk_bf16_f32 v109, v110, v111\n\t"
    "v_mfma_f32_16x16x32_bf16 v[80:83], a[180:183], a[80:83], v[80:83]\n\t"
    "v_cvt_pk_bf16_f32 v110, v124, v125\n\t"
    "v_mfma_f32_16x16x32_bf16 v[96:99], a[184:187], a[80:83], v[96:99]\n\t"
    "v_cvt_pk_bf16_f32 v111, v126, v127\n\t"
    "v_mfma_f32_16x16x32_bf16 v[112:115], a[188:191], a[80:83], v[112:115]\n\t"
    "buffer_store_dwordx4 v[108:111], v248, %[srdw], s74 offen offset:3072 sc1\n\t"
    "v_mfma_f32_16x16x32_bf16 v[0:3], a[192:195], a[96:99], v[0:3]\n\t"
    "ds_read_b64_tr_b16 a[128:129], v214 offset:16384\n\t"
    "ds_read_b64_tr_b16 a[130:131], v214 offset:18432\n\t"
    "v_mfma_f32_16x16x32_bf16 v[16:19], a[196:199], a[96:99], v[16:19]\n\t"
    "ds_read_b64_tr_b16 a[132:133], v215 offset:16384\n\t"
    "ds_read_b64_tr_b16 a[134:135], v215 offset:18432\n\t"
    "v_mfma_f32_16x16x32_bf16 v[32:35], a[200:203], a[96:99], v[32:35]\n\t"
    "ds_read_b64_tr_b16 a[136:137], v216 offset:16384\n\t"
    "ds_read_b64_tr_b16 a[138:139], v216 offset:18432\n\t"
    "v_mfma_f32_16x16x32_bf16 v[48:51], a[204:207], a[96:99], v[48:51]\n\t"
    "ds_read_b64_tr_b16 a[140:141], v217 offset:16384\n\t"
    "ds_read_b64_tr_b16 a[142:143], v217 offset:18432\n\t"
    "v_mfma_f32_16x16x32_bf16 v[64:67], a[208:211], a[96:99], v[64:67]\n\t"
    "ds_read_b64_tr_b16 a[144:145], v214 offset:24576\n\t"
    "ds_read_b64_tr_b16 a[146:147], v214 offset:26624\n\t"
    "v_mfma_f32_16x16x32_bf16 v[80:83], a[212:215], a[96:99], v[80:83]\n\t"
    "ds_read_b64_tr_b16 a[148:149], v215 offset:24576\n\t"
    "ds_read_b64_tr_b16 a[150:151], v215 offset:26624\n\t"
    "v_mfma_f32_16x16x32_bf16 v[96:99], a[216:219], a[96:99], v[96:99]\n\t"
    "ds_read_b64_tr_b16 a[152:153], v216 offset:24576\n\t"
    "ds_read_b64_tr_b16 a[154:155], v216 offset:26624\n\t"
    "v_mfma_f32_16x16x32_bf16 v[112:115], a[220:223], a[96:99], v[112:115]\n\t"
    "ds_read_b64_tr_b16 a[156:157], v217 offset:24576\n\t"
    "ds_read_b64_tr_b16 a[158:159], v217 offset:26624\n\t"
    "v_mfma_f32_16x16x32_bf16 v[0:3], a[224:227], a[112:115], v[0:3]\n\t"
    "ds_read_b64_tr_b16 a[160:161], v214 offset:20480\n\t"
    "ds_read_b64_tr_b16 a[162:163], v214 offset:22528\n\t"
    "v_mfma_f32_16x16x32_bf16 v[16:19], a[228:231], a[112:115], v[16:19]\n\t"
    "ds_read_b64_tr_b16 a[164:165], v215 offset:20480\n\t"
    "ds_read_b64_tr_b16 a[166:167], v215 offset:22528\n\t"
    "v_mfma_f32_16x16x32_bf16 v[32:35], a[232:235], a[112:115], v[32:35]\n\t"
    "ds_read_b64_tr_b16 a[168:169], v216 offset:20480\n\t"
    "ds_read_b64_tr_b16 a[170:171], v216 offset:22528\n\t"
    "v_mfma_f32_16x16x32_bf16 v[48:51], a[236:239], a[112:115], v[48:51]\n\t"
    "ds_read_b64_tr_b16 a[172:173], v217 offset:20480\n\t"
    "ds_read_b64_tr_b16 a[174:175], v217 offset:22528\n\t"
    "v_mfma_f32_16x16x32_bf16 v[64:67], a[240:243], a[112:115], v[64:67]\n\t"
    "ds_read_b64_tr_b16 a[176:177], v214 offset:28672\n\t"
    "ds_read_b64_tr_b16 a[178:179], v214 offset:30720\n\t"
    "v_mfma_f32_16x16x32_bf16 v[80:83], a[244:247], a[112:115], v[80:83]\n\t"
    "ds_read_b64_tr_b16 a[180:181], v215 offset:28672\n\t"
    "ds_read_b64_tr_b16 a[182:183], v215 offset:30720\n\t"
    "v_mfma_f32_16x16x32_bf16 v[96:99], a[248:251], a[112:115], v[96:99]\n\t"
    "ds_read_b64_tr_b16 a[184:185], v216 offset:28672\n\t"
    "ds_read_b64_tr_b16 a[186:187], v216 offset:30720\n\t"
    "v_mfma_f32_16x16x32_bf16 v[112:115], a[252:255], a[112:115], v[112:115]\n\t"
    "ds_read_b64_tr_b16 a[188:189], v217 offset:28672\n\t"
    "ds_read_b64_tr_b16 a[190:191], v217 offset:30720\n\t"
    "s_waitcnt lgkmcnt(0)\n\t"
    "v_mfma_f32_16x16x32_bf16 v[0:3], a[128:131], a[32:35], v[0:3]\n\t"
    "ds_read_b64_tr_b16 a[192:193], v214 offset:0\n\t"
    "ds_read_b64_tr_b16 a[194:195], v214 offset:2048\n\t"
    "v_mfma_f32_16x16x32_bf16 v[16:19], a[132:135], a[32:35], v[16:19]\n\t"
    "ds_read_b64_tr_b16 a[196:197], v215 offset:0\n\t"
    "ds_read_b64_tr_b16 a[198:199], v215 offset:2048\n\t"
    "v_mfma_f32_16x16x32_bf16 v[32:35], a[136:139], a[32:35], v[32:35]\n\t"
    "ds_read_b64_tr_b16 a[200:201], v216 offset:0\n\t"
    "ds_read_b64_tr_b16 a[202:203], v216 offset:2048\n\t"
    "v_mfma_f32_16x16x32_bf16 v[48:51], a[140:143], a[32:35], v[48:51]\n\t"
    "ds_read_b64_tr_b16 a[204:205], v217 offset:0\n\t"
    "ds_read_b64_tr_b16 a[206:207], v217 offset:2048\n\t"
    "v_mfma_f32_16x16x32_bf16 v[64:67], a[144:147], a[32:35], v[64:67]\n\t"
    "ds_read_b64_tr_b16 a[208:209], v214 offset:8192\n\t"
    "ds_read_b64_tr_b16 a[210:211], v214 offset:10240\n\t"
    "v_mfma_f32_16x16x32_bf16 v[80:83], a[148:151], a[32:35], v[80:83]\n\t"
    "ds_read_b64_tr_b16 a[212:213], v215 offset:8192\n\t"
    "ds_read_b64_tr_b16 a[214:215], v215 offset:10240\n\t"
    "v_mfma_f32_16x16x32_bf16 v[96:99], a[152:155], a[32:35], v[96:99]\n\t"
    "ds_read_b64_tr_b16 a[216:217], v216 offset:8192\n\t"
    "ds_read_b64_tr_b16 a[218:219], v216 offset:10240\n\t"
    "v_mfma_f32_16x16x32_bf16 v[112:115], a[156:159], a[32:35], v[112:115]\n\t"
    "ds_read_b64_tr_b16 a[220:221], v217 offset:8192\n\t"
    "ds_read_b64_tr_b16 a[222:223], v217 offset:10240\n\t"
    "v_mfma_f32_16x16x32_bf16 v[0:3], a[160:163], a[48:51], v[0:3]\n\t"
    "ds_read_b64_tr_b16 a[224:225], v214 offset:4096\n\t"
    "ds_read_b64_tr_b16 a[226:227], v214 offset:6144\n\t"
    "v_mfma_f32_16x16x32_bf16 v[16:19], a[164:167], a[48:51], v[16:19]\n\t"
    "ds_read_b64_tr_b16 a[228:229], v215 offset:4096\n\t"
    "ds_read_b64_tr_b16 a[230:231], v215 offset:6144\n\t"
    "v_mfma_f32_16x16x32_bf16 v[32:35], a[168:171], a[48:51], v[32:35]\n\t"
    "ds_read_b64_tr_b16 a[232:233], v216 offset:4096\n\t"
    "ds_read_b64_tr_b16 a[234:235], v216 offset:6144\n\t"
    "v_mfma_f32_16x16x32_bf16 v[48:51], a[172:175], a[48:51], v[48:51]\n\t"
    "ds_read_b64_tr_b16 a[236:237], v217 offset:4096\n\t"
    "ds_read_b64_tr_b16 a[238:239], v217 offset:6144\n\t"
    "v_mfma_f32_16x16x32_bf16 v[64:67], a[176:179], a[48:51], v[64:67]\n\t"
    "ds_read_b64_tr_b16 a[240:241], v214 offset:12288\n\t"
    "ds_read_b64_tr_b16 a[242:243], v214 offset:14336\n\t"
    "v_mfma_f32_16x16x32_bf16 v[80:83], a[180:183], a[48:51], v[80:83]\n\t"
    "ds_read_b64_tr_b16 a[244:245], v215 offset:12288\n\t"
    "ds_read_b64_tr_b16 a[246:247], v215 offset:14336\n\t"
    "v_mfma_f32_16x16x32_bf16 v[96:99], a[184:187], a[48:51], v[96:99]\n\t"
    "ds_read_b64_tr_b16 a[248:249], v216 offset:12288\n\t"
    "ds_read_b64_tr_b16 a[250:251], v216 offset:14336\n\t"
    "v_mfma_f32_16x16x32_bf16 v[112:115], a[188:191], a[48:51], v[112:115]\n\t"
    "ds_read_b64_tr_b16 a[252:253], v217 offset:12288\n\t"
    "ds_read_b64_tr_b16 a[254:255], v217 offset:14336\n\t"
    "s_waitcnt lgkmcnt(0)\n\t"
    "v_mfma_f32_16x16x32_bf16 v[0:3], a[192:195], a[0:3], v[0:3]\n\t"
    "v_mfma_f32_16x16x32_bf16 v[16:19], a[196:199], a[0:3], v[16:19]\n\t"
    "v_mfma_f32_16x16x32_bf16 v[32:35], a[200:203], a[0:3], v[32:35]\n\t"
    "v_mfma_f32_16x16x32_bf16 v[48:51], a[204:207], a[0:3], v[48:51]\n\t"
    "v_mfma_f32_16x16x32_bf16 v[64:67], a[208:211], a[0:3], v[64:67]\n\t"
    "v_mfma_f32_16x16x32_bf16 v[80:83], a[212:215], a[0:3], v[80:83]\n\t"
    "v_mfma_f32_16x16x32_bf16 v[96:99], a[216:219], a[0:3], v[96:99]\n\t"
    "v_mfma_f32_16x16x32_bf16 v[112:115], a[220:223], a[0:3], v[112:115]\n\t"
    "v_mfma_f32_16x16x32_bf16 v[0:3], a[224:227], a[16:19], v[0:3]\n\t"
    "v_mfma_f32_16x16x32_bf16 v[16:19], a[228:231], a[16:19], v[16:19]\n\t"
    "v_mfma_f32_16x16x32_bf16 v[32:35], a[232:235], a[16:19], v[32:35]\n\t"
    "v_mfma_f32_16x16x32_bf16 v[48:51], a[236:239], a[16:19], v[48:51]\n\t"
    "v_mfma_f32_16x16x32_bf16 v[64:67], a[240:243], a[16:19], v[64:67]\n\t"
    "v_mfma_f32_16x16x32_bf16 v[80:83], a[244:247], a[16:19], v[80:83]\n\t"
    "v_mfma_f32_16x16x32_bf16 v[96:99], a[248:251], a[16:19], v[96:99]\n\t"
    "v_mfma_f32_16x16x32_bf16 v[112:115], a[252:255], a[16:19], v[112:115]\n\t"
    "v_xor_b32 v214, 128, v208\n\t"
    "v_xor_b32 v215, 160, v208\n\t"
    "v_xor_b32 v216, 192, v208\n\t"
    "v_xor_b32 v217, 224, v208\n\t"
    "v_mov_b32 v250, s90\n\t"
    "v_mov_b32 v251, s91\n\t"
    "s_waitcnt vmcnt(12)\n\t"
    "ds_read_b64 v[176:177], v208\n\t"
    "ds_read_b64 v[178:179], v209\n\t"
    "ds_read_b64 v[180:181], v210\n\t"
    "ds_read_b64 v[182:183], v211\n\t"
    "ds_read_b64 v[184:185], v214\n\t"
    "ds_read_b64 v[186:187], v215\n\t"
    "ds_read_b64 v[188:189], v216\n\t"
    "ds_read_b64 v[190:191], v217\n\t"
    "s_waitcnt vmcnt(0)\n\t"
    "s_mov_b64 exec, 7\n\t"
    "buffer_store_dwordx2 v[250:251], v249, %[srdw], 0 offen sc1\n\t"
    "s_mov_b64 exec, -1\n\t"
    "buffer_load_dwordx2 v[252:253], v223, %[srdw], 0 offen sc1\n\t"
    "s_waitcnt lgkmcnt(0)\n\t"
    "v_lshlrev_b32 v192, 16, v176\n\t"
    "v_and_b32 v193, 0xffff0000, v176\n\t"
    "v_lshlrev_b32 v194, 16, v177\n\t"
    "v_and_b32 v195, 0xffff0000, v177\n\t"
    "v_lshlrev_b32 v196, 16, v178\n\t"
    "v_and_b32 v197, 0xffff0000, v178\n\t"
    "v_lshlrev_b32 v198, 16, v179\n\t"
    "v_and_b32 v199, 0xffff0000, v179\n\t"
    "v_lshlrev_b32 v200, 16, v180\n\t"
    "v_and_b32 v201, 0xffff0000, v180\n\t"
    "v_lshlrev_b32 v202, 16, v181\n\t"
    "v_and_b32 v203, 0xffff0000, v181\n\t"
    "v_lshlrev_b32 v204, 16, v182\n\t"
    "v_and_b32 v205, 0xffff0000, v182\n\t"
    "v_lshlrev_b32 v206, 16, v183\n\t"
    "v_and_b32 v207, 0xffff0000, v183\n\t"
    "v_lshlrev_b32 v232, 16, v184\n\t"
    "v_and_b32 v233, 0xffff0000, v184\n\t"
    "v_lshlrev_b32 v234, 16, v185\n\t"
    "v_and_b32 v235, 0xffff0000, v185\n\t"
    "v_lshlrev_b32 v236, 16, v186\n\t"
    "v_and_b32 v237, 0xffff0000, v186\n\t"
    "v_lshlrev_b32 v238, 16, v187\n\t"
    "v_and_b32 v239, 0xffff0000, v187\n\t"
    "v_lshlrev_b32 v240, 16, v188\n\t"
    "v_and_b32 v241, 0xffff0000, v188\n\t"
    "v_lshlrev_b32 v242, 16, v189\n\t"
    "v_and_b32 v243, 0xffff0000, v189\n\t"
    "v_lshlrev_b32 v244, 16, v190\n\t"
    "v_and_b32 v245, 0xffff0000, v190\n\t"
    "v_lshlrev_b32 v246, 16, v191\n\t"
    "v_and_b32 v247, 0xffff0000, v191\n\t"
    "s_mov_b32 %[tmp2], 0x20000\n\t"
    "s_mov_b32 %[nsa], 7\n\t"
    "s_cmp_eq_u32 %[role], 3\n\t"
    "s_cbranch_scc1 80f\n\t"
    "88:\n\t"
    "s_waitcnt vmcnt(0)\n\t"
    "v_cmp_eq_u64 vcc, s[90:91], v[252:253]\n\t"
    "s_sub_u32 %[tmp2], %[tmp2], 1\n\t"
    "s_cselect_b32 %[tmp], 7, vcc_lo\n\t"
    "s_and_b32 %[tmp], %[tmp], %[nsa]\n\t"
    "s_andn2_b32 %[nsa], %[nsa], %[tmp]\n\t"
    "s_bitcmp1_b32 %[tmp], 0\n\t"
    "s_cbranch_scc0 89f\n\t"
    "buffer_load_dwordx4 v[128:131], v248, %[srdw], s75 offen offset:0 sc1\n\t"
    "buffer_load_dwordx4 v[132:135], v248, %[srdw], s75 offen offset:1024 sc1\n\t"
    "buffer_load_dwordx4 v[136:139], v248, %[srdw], s75 offen offset:2048 sc1\n\t"
    "buffer_load_dwordx4 v[140:143], v248, %[srdw], s75 offen offset:3072 sc1\n\t"
    "89:\n\t"
    "s_bitcmp1_b32 %[tmp], 1\n\t"
    "s_cbranch_scc0 90f\n\t"
    "buffer_load_dwordx4 v[144:147], v248, %[srdw], s76 offen offset:0 sc1\n\t"
    "buffer_load_dwordx4 v[148:151], v248, %[srdw], s76 offen offset:1024 sc1\n\t"
    "buffer_load_dwordx4 v[152:155], v248, %[srdw], s76 offen offset:2048 sc1\n\t"
    "buffer_load_dwordx4 v[156:159], v248, %[srdw], s76 offen offset:3072 sc1\n\t"
    "90:\n\t"
    "s_bitcmp1_b32 %[tmp], 2\n\t"
    "s_cbranch_scc0 91f\n\t"
    "buffer_load_dwordx4 v[160:163], v248, %[srdw], s77 offen offset:0 sc1\n\t"
    "buffer_load_dwordx4 v[164:167], v248, %[srdw], s77 offen offset:1024 sc1\n\t"
    "buffer_load_dwordx4 v[168:171], v248, %[srdw], s77 offen offset:2048 sc1\n\t"
    "buffer_load_dwordx4 v[172:175], v248, %[srdw], s77 offen offset:3072 sc1\n\t"
    "91:\n\t"
    "s_and_b32 %[tmp], %[nsa], 3\n\t"
    "s_cbranch_scc0 92f\n\t"
    "buffer_load_dwordx2 v[252:253], v223, %[srdw], 0 offen sc1\n\t"
    "s_branch 88b\n\t"
    "92:\n\t"
    "s_waitcnt vmcnt(0)\n\t"
    "v_mfma_f32_16x16x32_bf16 v[0:3], v[224:227], v[128:131], v[0:3]\n\t"
    "v_mfma_f32_16x16x32_bf16 v[16:19], v[228:231], v[128:131], v[16:19]\n\t"
    "v_mfma_f32_16x16x32_bf16 v[0:3], v[224:227], v[144:147], v[0:3]\n\t"
    "v_mfma_f32_16x16x32_bf16 v[16:19], v[228:231], v[144:147], v[16:19]\n\t"
    "v_mfma_f32_16x16x32_bf16 v[32:35], v[224:227], v[132:135], v[32:35]\n\t"
    "v_mfma_f32_16x16x32_bf16 v[48:51], v[228:231], v[132:135], v[48:51]\n\t"
    "v_mfma_f32_16x16x32_bf16 v[32:35], v[224:227], v[148:151], v[32:35]\n\t"
    "v_mfma_f32_16x16x32_bf16 v[48:51], v[228:231], v[148:151], v[48:51]\n\t"
    "v_mfma_f32_16x16x32_bf16 v[64:67], v[224:227], v[136:139], v[64:67]\n\t"
    "v_mfma_f32_16x16x32_bf16 v[80:83], v[228:231], v[136:139], v[80:83]\n\t"
    "v_mfma_f32_16x16x32_bf16 v[64:67], v[224:227], v[152:155], v[64:67]\n\t"
    "v_mfma_f32_16x16x32_bf16 v[80:83], v[228:231], v[152:155], v[80:83]\n\t"
    "v_mfma_f32_16x16x32_bf16 v[96:99], v[224:227], v[140:143], v[96:99]\n\t"
    "v_mfma_f32_16x16x32_bf16 v[112:115], v[228:231], v[140:143], v[112:115]\n\t"
    "v_mfma_f32_16x16x32_bf16 v[96:99], v[224:227], v[156:159], v[96:99]\n\t"
    "v_mfma_f32_16x16x32_bf16 v[112:115], v[228:231], v[156:159], v[112:115]\n\t"
    "buffer_load_dwordx2 v[252:253], v223, %[srdw], 0 offen sc1\n\t"
    "v_pk_mul_f32 v[4:5], v[0:1], s[92:93] op_sel_hi:[1,0] neg_lo:[0,1] neg_hi:[0,1]\n\t"
    "v_pk_mul_f32 v[6:7], v[2:3], s[92:93] op_sel_hi:[1,0] neg_lo:[0,1] neg_hi:[0,1]\n\t"
    "v_pk_mul_f32 v[20:21], v[16:17], s[92:93] op_sel_hi:[1,0] neg_lo:[0,1] neg_hi:[0,1]\n\t"
    "v_pk_mul_f32 v[22:23], v[18:19], s[92:93] op_sel_hi:[1,0] neg_lo:[0,1] neg_hi:[0,1]\n\t"
    "v_exp_f32 v4, v4\n\t"
    "v_exp_f32 v6, v6\n\t"
    "v_exp_f32 v20, v20\n\t"
    "v_exp_f32 v22, v22\n\t"
    "v_exp_f32 v5, v5\n\t"
    "v_exp_f32 v7, v7\n\t"
    "v_exp_f32 v21, v21\n\t"
    "v_exp_f32 v23, v23\n\t"
    "v_pk_add_f32 v[4:5], v[4:5], 1.0 op_sel_hi:[1,0]\n\t"
    "v_pk_add_f32 v[6:7], v[6:7], 1.0 op_sel_hi:[1,0]\n\t"
    "v_pk_add_f32 v[20:21], v[20:21], 1.0 op_sel_hi:[1,0]\n\t"
    "v_pk_add_f32 v[22:23], v[22:23], 1.0 op_sel_hi:[1,0]\n\t"
    "v_rcp_f32 v4, v4\n\t"
    "v_rcp_f32 v6, v6\n\t"
    "v_rcp_f32 v20, v20\n\t"
    "v_rcp_f32 v22, v22\n\t"
    "v_rcp_f32 v5, v5\n\t"
    "v_rcp_f32 v7, v7\n\t"
    "v_rcp_f32 v21, v21\n\t"
    "v_rcp_f32 v23, v23\n\t"
    "v_pk_mul_f32 v[0:1], v[0:1], v[4:5]\n\t"
    "v_pk_mul_f32 v[2:3], v[2:3], v[6:7]\n\t"
    "v_pk_mul_f32 v[16:17], v[16:17], v[20:21]\n\t"
    "v_pk_mul_f32 v[18:19], v[18:19], v[22:23]\n\t"
    "v_pk_mul_f32 v[36:37], v[32:33], s[92:93] op_sel_hi:[1,0] neg_lo:[0,1] neg_hi:[0,1]\n\t"
    "v_pk_mul_f32 v[38:39], v[34:35], s[92:93] op_sel_hi:[1,0] neg_lo:[0,1] neg_hi:[0,1]\n\t"
    "v_pk_mul_f32 v[52:53], v[48:49], s[92:93] op_sel_hi:[1,0] neg_lo:[0,1] neg_hi:[0,1]\n\t"
    "v_pk_mul_f32 v[54:55], v[50:51], s[92:93] op_sel_hi:[1,0] neg_lo:[0,1] neg_hi:[0,1]\n\t"
    "v_exp_f32 v36, v36\n\t"
    "v_exp_f32 v38, v38\n\t"
    "v_exp_f32 v52, v52\n\t"
    "v_exp_f32 v54, v54\n\t"
    "v_exp_f32 v37, v37\n\t"
    "v_exp_f32 v39, v39\n\t"
    "v_exp_f32 v53, v53\n\t"
    "v_exp_f32 v55, v55\n\t"
    "v_pk_add_f32 v[36:37], v[36:37], 1.0 op_sel_hi:[1,0]\n\t"
    "v_pk_add_f32 v[38:39], v[38:39], 1.0 op_sel_hi:[1,0]\n\t"
    "v_pk_add_f32 v[52:53], v[52:53], 1.0 op_sel_hi:[1,0]\n\t"
    "v_pk_add_f32 v[54:55], v[54:55], 1.0 op_sel_hi:[1,0]\n\t"
    "v_rcp_f32 v36, v36\n\t"
    "v_rcp_f32 v38, v38\n\t"
    "v_rcp_f32 v52, v52\n\t"
    "v_rcp_f32 v54, v54\n\t"
    "v_rcp_f32 v37, v37\n\t"
    "v_rcp_f32 v39, v39\n\t"
    "v_rcp_f32 v53, v53\n\t"
    "v_rcp_f32 v55, v55\n\t"
    "v_pk_mul_f32 v[32:33], v[32:33], v[36:37]\n\t"
    "v_pk_mul_f32 v[34:35], v[34:35], v[38:39]\n\t"
    "v_pk_mul_f32 v[48:49], v[48:49], v[52:53]\n\t"
    "v_pk_mul_f32 v[50:51], v[50:51], v[54:55]\n\t"
    "v_pk_mul_f32 v[68:69], v[64:65], s[92:93] op_sel_hi:[1,0] neg_lo:[0,1] neg_hi:[0,1]\n\t"
    "v_pk_mul_f32 v[70:71], v[66:67], s[92:93] op_sel_hi:[1,0] neg_lo:[0,1] neg_hi:[0,1]\n\t"
    "v_pk_mul_f32 v[84:85], v[80:81], s[92:93] op_sel_hi:[1,0] neg_lo:[0,1] neg_hi:[0,1]\n\t"
    "v_pk_mul_f32 v[86:87], v[82:83], s[92:93] op_sel_hi:[1,0] neg_lo:[0,1] neg_hi:[0,1]\n\t"
    "v_exp_f32 v68, v68\n\t"
    "v_exp_f32 v70, v70\n\t"
    "v_exp_f32 v84, v84\n\t"
    "v_exp_f32 v86, v86\n\t"
    "v_exp_f32 v69, v69\n\t"
    "v_exp_f32 v71, v71\n\t"
    "v_exp_f32 v85, v85\n\t"
    "v_exp_f32 v87, v87\n\t"
    "v_pk_add_f32 v[68:69], v[68:69], 1.0 op_sel_hi:[1,0]\n\t"
    "v_pk_add_f32 v[70:71], v[70:71], 1.0 op_sel_hi:[1,0]\n\t"
    "v_pk_add_f32 v[84:85], v[84:85], 1.0 op_sel_hi:[1,0]\n\t"
    "v_pk_add_f32 v[86:87], v[86:87], 1.0 op_sel_hi:[1,0]\n\t"
    "v_rcp_f32 v68, v68\n\t"
    "v_rcp_f32 v70, v70\n\t"
    "v_rcp_f32 v84, v84\n\t"
    "v_rcp_f32 v86, v86\n\t"
    "v_rcp_f32 v69, v69\n\t"
    "v_rcp_f32 v71, v71\n\t"
    "v_rcp_f32 v85, v85\n\t"
    "v_rcp_f32 v87, v87\n\t"
    "v_pk_mul_f32 v[64:65], v[64:65], v[68:69]\n\t"
    "v_pk_mul_f32 v[66:67], v[66:67], v[70:71]\n\t"
    "v_pk_mul_f32 v[80:81], v[80:81], v[84:85]\n\t"
    "v_pk_mul_f32 v[82:83], v[82:83], v[86:87]\n\t"
    "v_pk_mul_f32 v[100:101], v[96:97], s[92:93] op_sel_hi:[1,0] neg_lo:[0,1] neg_hi:[0,1]\n\t"
    "v_pk_mul_f32 v[102:103], v[98:99], s[92:93] op_sel_hi:[1,0] neg_lo:[0,1] neg_hi:[0,1]\n\t"
    "v_pk_mul_f32 v[116:117], v[112:113], s[92:93] op_sel_hi:[1,0] neg_lo:[0,1] neg_hi:[0,1]\n\t"
    "v_pk_mul_f32 v[118:119], v[114:115], s[92:93] op_sel_hi:[1,0] neg_lo:[0,1] neg_hi:[0,1]\n\t"
    "v_exp_f32 v100, v100\n\t"
    "v_exp_f32 v102, v102\n\t"
    "v_exp_f32 v116, v116\n\t"
    "v_exp_f32 v118, v118\n\t"
    "v_exp_f32 v101, v101\n\t"
    "v_exp_f32 v103, v103\n\t"
    "v_exp_f32 v117, v117\n\t"
    "v_exp_f32 v119, v119\n\t"
    "v_pk_add_f32 v[100:101], v[100:101], 1.0 op_sel_hi:[1,0]\n\t"
    "v_pk_add_f32 v[102:103], v[102:103], 1.0 op_sel_hi:[1,0]\n\t"
    "v_pk_add_f32 v[116:117], v[116:117], 1.0 op_sel_hi:[1,0]\n\t"
    "v_pk_add_f32 v[118:119], v[118:119], 1.0 op_sel_hi:[1,0]\n\t"
    "v_rcp_f32 v100, v100\n\t"
    "v_rcp_f32 v102, v102\n\t"
    "v_rcp_f32 v116, v116\n\t"
    "v_rcp_f32 v118, v118\n\t"
    "v_rcp_f32 v101, v101\n\t"
    "v_rcp_f32 v103, v103\n\t"
    "v_rcp_f32 v117, v117\n\t"
    "v_rcp_f32 v119, v119\n\t"
    "v_pk_mul_f32 v[96:97], v[96:97], v[100:101]\n\t"
    "v_pk_mul_f32 v[98:99], v[98:99], v[102:103]\n\t"
    "v_pk_mul_f32 v[112:113], v[112:113], v[116:117]\n\t"
    "v_pk_mul_f32 v[114:115], v[114:115], v[118:119]\n\t"
    "94:\n\t"
    "s_waitcnt vmcnt(0)\n\t"
    "v_cmp_eq_u64 vcc, s[90:91], v[252:253]\n\t"
    "s_sub_u32 %[tmp2], %[tmp2], 1\n\t"
    "s_cselect_b32 %[tmp], 7, vcc_lo\n\t"
    "s_and_b32 %[tmp], %[tmp], %[nsa]\n\t"
    "s_andn2_b32 %[nsa], %[nsa], %[tmp]\n\t"
    "s_bitcmp1_b32 %[tmp], 0\n\t"
    "s_cbranch_scc0 95f\n\t"
    "buffer_load_dwordx4 v[128:131], v248, %[srdw], s75 offen offset:0 sc1\n\t"
    "buffer_load_dwordx4 v[132:135], v248, %[srdw], s75 offen offset:1024 sc1\n\t"
    "buffer_load_dwordx4 v[136:139], v248, %[srdw], s75 offen offset:2048 sc1\n\t"
    "buffer_load_dwordx4 v[140:143], v248, %[srdw], s75 offen offset:3072 sc1\n\t"
    "95:\n\t"
    "s_bitcmp1_b32 %[tmp], 1\n\t"
    "s_cbranch_scc0 96f\n\t"
    "buffer_load_dwordx4 v[144:147], v248, %[srdw], s76 offen offset:0 sc1\n\t"
    "buffer_load_dwordx4 v[148:151], v248, %[srdw], s76 offen offset:1024 sc1\n\t"
    "buffer_load_dwordx4 v[152:155], v248, %[srdw], s76 offen offset:2048 sc1\n\t"
    "buffer_load_dwordx4 v[156:159], v248, %[srdw], s76 offen offset:3072 sc1\n\t"
    "96:\n\t"
    "s_bitcmp1_b32 %[tmp], 2\n\t"
    "s_cbranch_scc0 97f\n\t"
    "buffer_load_dwordx4 v[160:163], v248, %[srdw], s77 offen offset:0 sc1\n\t"
    "buffer_load_dwordx4 v[164:167], v248, %[srdw], s77 offen offset:1024 sc1\n\t"
    "buffer_load_dwordx4 v[168:171], v248, %[srdw], s77 offen offset:2048 sc1\n\t"
    "buffer_load_dwordx4 v[172:175], v248, %[srdw], s77 offen offset:3072 sc1\n\t"
    "97:\n\t"
    "s_and_b32 %[tmp], %[nsa], 4\n\t"
    "s_cbranch_scc0 98f\n\t"
    "buffer_load_dwordx2 v[252:253], v223, %[srdw], 0 offen sc1\n\t"
    "s_branch 94b\n\t"
    "98:\n\t"
    "s_waitcnt vmcnt(0)\n\t"
    "v_mfma_f32_16x16x32_bf16 v[8:11], v[224:227], v[160:163], 0\n\t"
    "v_mfma_f32_16x16x32_bf16 v[24:27], v[228:231], v[160:163], 0\n\t"
    "v_mfma_f32_16x16x32_bf16 v[40:43], v[224:227], v[164:167], 0\n\t"
    "v_mfma_f32_16x16x32_bf16 v[56:59], v[228:231], v[164:167], 0\n\t"
    "v_mfma_f32_16x16x32_bf16 v[72:75], v[224:227], v[168:171], 0\n\t"
    "v_mfma_f32_16x16x32_bf16 v[88:91], v[228:231], v[168:171], 0\n\t"
    "v_mfma_f32_16x16x32_bf16 v[104:107], v[224:227], v[172:175], 0\n\t"
    "v_mfma_f32_16x16x32_bf16 v[120:123], v[228:231], v[172:175], 0\n\t"
    "s_branch 81f\n\t"
    "80:\n\t"
    "82:\n\t"
    "s_waitcnt vmcnt(0)\n\t"
    "v_cmp_eq_u64 vcc, s[90:91], v[252:253]\n\t"
    "s_sub_u32 %[tmp2], %[tmp2], 1\n\t"
    "s_cselect_b32 %[tmp], 7, vcc_lo\n\t"
    "s_and_b32 %[tmp], %[tmp], %[nsa]\n\t"
    "s_andn2_b32 %[nsa], %[nsa], %[tmp]\n\t"
    "s_bitcmp1_b32 %[tmp], 0\n\t"
    "s_cbranch_scc0 83f\n\t"
    "buffer_load_dwordx4 v[128:131], v248, %[srdw], s75 offen offset:0 sc1\n\t"
    "buffer_load_dwordx4 v[132:135], v248, %[srdw], s75 offen offset:1024 sc1\n\t"
    "buffer_load_dwordx4 v[136:139], v248, %[srdw], s75 offen offset:2048 sc1\n\t"
    "buffer_load_dwordx4 v[140:143], v248, %[srdw], s75 offen offset:3072 sc1\n\t"
    "83:\n\t"
    "s_bitcmp1_b32 %[tmp], 1\n\t"
    "s_cbranch_scc0 84f\n\t"
    "buffer_load_dwordx4 v[144:147], v248, %[srdw], s76 offen offset:0 sc1\n\t"
    "buffer_load_dwordx4 v[148:151], v248, %[srdw], s76 offen offset:1024 sc1\n\t"
    "buffer_load_dwordx4 v[152:155], v248, %[srdw], s76 offen offset:2048 sc1\n\t"
    "buffer_load_dwordx4 v[156:159], v248, %[srdw], s76 offen offset:3072 sc1\n\t"
    "84:\n\t"
    "s_bitcmp1_b32 %[tmp], 2\n\t"
    "s_cbranch_scc0 85f\n\t"
    "buffer_load_dwordx4 v[160:163], v248, %[srdw], s77 offen offset:0 sc1\n\t"
    "buffer_load_dwordx4 v[164:167], v248, %[srdw], s77 offen offset:1024 sc1\n\t"
    "buffer_load_dwordx4 v[168:171], v248, %[srdw], s77 offen offset:2048 sc1\n\t"
    "buffer_load_dwordx4 v[172:175], v248, %[srdw], s77 offen offset:3072 sc1\n\t"
    "85:\n\t"
    "s_and_b32 %[tmp], %[nsa], 7\n\t"
    "s_cbranch_scc0 86f\n\t"
    "buffer_load_dwordx2 v[252:253], v223, %[srdw], 0 offen sc1\n\t"
    "s_branch 82b\n\t"
    "86:\n\t"
    "s_waitcnt vmcnt(0)\n\t"
    "v_mfma_f32_16x16x32_bf16 v[8:11], v[224:227], v[128:131], 0\n\t"
    "v_mfma_f32_16x16x32_bf16 v[24:27], v[228:231], v[128:131], 0\n\t"
    "v_mfma_f32_16x16x32_bf16 v[8:11], v[224:227], v[144:147], v[8:11]\n\t"
    "v_mfma_f32_16x16x32_bf16 v[24:27], v[228:231], v[144:147], v[24:27]\n\t"
    "v_mfma_f32_16x16x32_bf16 v[8:11], v[224:227], v[160:163], v[8:11]\n\t"
    "v_mfma_f32_16x16x32_bf16 v[24:27], v[228:231], v[160:163], v[24:27]\n\t"
    "v_mfma_f32_16x16x32_bf16 v[40:43], v[224:227], v[132:135], 0\n\t"
    "v_mfma_f32_16x16x32_bf16 v[56:59], v[228:231], v[132:135], 0\n\t"
    "v_mfma_f32_16x16x32_bf16 v[40:43], v[224:227], v[148:151], v[40:43]\n\t"
    "v_mfma_f32_16x16x32_bf16 v[56:59], v[228:231], v[148:151], v[56:59]\n\t"
    "v_mfma_f32_16x16x32_bf16 v[40:43], v[224:227], v[164:167], v[40:43]\n\t"
    "v_mfma_f32_16x16x32_bf16 v[56:59], v[228:231], v[164:167], v[56:59]\n\t"
    "v_mfma_f32_16x16x32_bf16 v[72:75], v[224:227], v[136:139], 0\n\t"
    "v_mfma_f32_16x16x32_bf16 v[88:91], v[228:231], v[136:139], 0\n\t"
    "v_mfma_f32_16x16x32_bf16 v[72:75], v[224:227], v[152:155], v[72:75]\n\t"
    "v_mfma_f32_16x16x32_bf16 v[88:91], v[228:231], v[152:155], v[88:91]\n\t"
    "v_mfma_f32_16x16x32_bf16 v[72:75], v[224:227], v[168:171], v[72:75]\n\t"
    "v_mfma_f32_16x16x32_bf16 v[88:91], v[228:231], v[168:171], v[88:91]\n\t"
    "v_mfma_f32_16x16x32_bf16 v[104:107], v[224:227], v[140:143], 0\n\t"
    "v_mfma_f32_16x16x32_bf16 v[120:123], v[228:231], v[140:143], 0\n\t"
    "v_mfma_f32_16x16x32_bf16 v[104:107], v[224:227], v[156:159], v[104:107]\n\t"
    "v_mfma_f32_16x16x32_bf16 v[120:123], v[228:231], v[156:159], v[120:123]\n\t"
    "v_mfma_f32_16x16x32_bf16 v[104:107], v[224:227], v[172:175], v[104:107]\n\t"
    "v_mfma_f32_16x16x32_bf16 v[120:123], v[228:231], v[172:175], v[120:123]\n\t"
    "v_pk_mul_f32 v[4:5], v[8:9], s[92:93] op_sel_hi:[1,0] neg_lo:[0,1] neg_hi:[0,1]\n\t"
    "v_pk_mul_f32 v[6:7], v[10:11], s[92:93] op_sel_hi:[1,0] neg_lo:[0,1] neg_hi:[0,1]\n\t"
    "v_pk_mul_f32 v[20:21], v[24:25], s[92:93] op_sel_hi:[1,0] neg_lo:[0,1] neg_hi:[0,1]\n\t"
    "v_pk_mul_f32 v[22:23], v[26:27], s[92:93] op_sel_hi:[1,0] neg_lo:[0,1] neg_hi:[0,1]\n\t"
    "v_exp_f32 v4, v4\n\t"
    "v_exp_f32 v6, v6\n\t"
    "v_exp_f32 v20, v20\n\t"
    "v_exp_f32 v22, v22\n\t"
    "v_exp_f32 v5, v5\n\t"
    "v_exp_f32 v7, v7\n\t"
    "v_exp_f32 v21, v21\n\t"
    "v_exp_f32 v23, v23\n\t"
    "v_pk_add_f32 v[4:5], v[4:5], 1.0 op_sel_hi:[1,0]\n\t"
    "v_pk_add_f32 v[6:7], v[6:7], 1.0 op_sel_hi:[1,0]\n\t"
    "v_pk_add_f32 v[20:21], v[20:21], 1.0 op_sel_hi:[1,0]\n\t"
    "v_pk_add_f32 v[22:23], v[22:23], 1.0 op_sel_hi:[1,0]\n\t"
    "v_rcp_f32 v4, v4\n\t"
    "v_rcp_f32 v6, v6\n\t"
    "v_rcp_f32 v20, v20\n\t"
    "v_rcp_f32 v22, v22\n\t"
    "v_rcp_f32 v5, v5\n\t"
    "v_rcp_f32 v7, v7\n\t"
    "v_rcp_f32 v21, v21\n\t"
    "v_rcp_f32 v23, v23\n\t"
    "v_pk_mul_f32 v[8:9], v[8:9], v[4:5]\n\t"
    "v_pk_mul_f32 v[10:11], v[10:11], v[6:7]\n\t"
    "v_pk_mul_f32 v[24:25], v[24:25], v[20:21]\n\t"
    "v_pk_mul_f32 v[26:27], v[26:27], v[22:23]\n\t"
    "v_pk_mul_f32 v[36:37], v[40:41], s[92:93] op_sel_hi:[1,0] neg_lo:[0,1] neg_hi:[0,1]\n\t"
    "v_pk_mul_f32 v[38:39], v[42:43], s[92:93] op_sel_hi:[1,0] neg_lo:[0,1] neg_hi:[0,1]\n\t"
    "v_pk_mul_f32 v[52:53], v[56:57], s[92:93] op_sel_hi:[1,0] neg_lo:[0,1] neg_hi:[0,1]\n\t"
    "v_pk_mul_f32 v[54:55], v[58:59], s[92:93] op_sel_hi:[1,0] neg_lo:[0,1] neg_hi:[0,1]\n\t"
    "v_exp_f32 v36, v36\n\t"
    "v_exp_f32 v38, v38\n\t"
    "v_exp_f32 v52, v52\n\t"
    "v_exp_f32 v54, v54\n\t"
    "v_exp_f32 v37, v37\n\t"
    "v_exp_f32 v39, v39\n\t"
    "v_exp_f32 v53, v53\n\t"
    "v_exp_f32 v55, v55\n\t"
    "v_pk_add_f32 v[36:37], v[36:37], 1.0 op_sel_hi:[1,0]\n\t"
    "v_pk_add_f32 v[38:39], v[38:39], 1.0 op_sel_hi:[1,0]\n\t"
    "v_pk_add_f32 v[52:53], v[52:53], 1.0 op_sel_hi:[1,0]\n\t"
    "v_pk_add_f32 v[54:55], v[54:55], 1.0 op_sel_hi:[1,0]\n\t"
    "v_rcp_f32 v36, v36\n\t"
    "v_rcp_f32 v38, v38\n\t"
    "v_rcp_f32 v52, v52\n\t"
    "v_rcp_f32 v54, v54\n\t"
    "v_rcp_f32 v37, v37\n\t"
    "v_rcp_f32 v39, v39\n\t"
    "v_rcp_f32 v53, v53\n\t"
    "v_rcp_f32 v55, v55\n\t"
    "v_pk_mul_f32 v[40:41], v[40:41], v[36:37]\n\t"
    "v_pk_mul_f32 v[42:43], v[42:43], v[38:39]\n\t"
    "v_pk_mul_f32 v[56:57], v[56:57], v[52:53]\n\t"
    "v_pk_mul_f32 v[58:59], v[58:59], v[54:55]\n\t"
    "v_pk_mul_f32 v[68:69], v[72:73], s[92:93] op_sel_hi:[1,0] neg_lo:[0,1] neg_hi:[0,1]\n\t"
    "v_pk_mul_f32 v[70:71], v[74:75], s[92:93] op_sel_hi:[1,0] neg_lo:[0,1] neg_hi:[0,1]\n\t"
    "v_pk_mul_f32 v[84:85], v[88:89], s[92:93] op_sel_hi:[1,0] neg_lo:[0,1] neg_hi:[0,1]\n\t"
    "v_pk_mul_f32 v[86:87], v[90:91], s[92:93] op_sel_hi:[1,0] neg_lo:[0,1] neg_hi:[0,1]\n\t"
    "v_exp_f32 v68, v68\n\t"
    "v_exp_f32 v70, v70\n\t"
    "v_exp_f32 v84, v84\n\t"
    "v_exp_f32 v86, v86\n\t"
    "v_exp_f32 v69, v69\n\t"
    "v_exp_f32 v71, v71\n\t"
    "v_exp_f32 v85, v85\n\t"
    "v_exp_f32 v87, v87\n\t"
    "v_pk_add_f32 v[68:69], v[68:69], 1.0 op_sel_hi:[1,0]\n\t"
    "v_pk_add_f32 v[70:71], v[70:71], 1.0 op_sel_hi:[1,0]\n\t"
    "v_pk_add_f32 v[84:85], v[84:85], 1.0 op_sel_hi:[1,0]\n\t"
    "v_pk_add_f32 v[86:87], v[86:87], 1.0 op_sel_hi:[1,0]\n\t"
    "v_rcp_f32 v68, v68\n\t"
    "v_rcp_f32 v70, v70\n\t"
    "v_rcp_f32 v84, v84\n\t"
    "v_rcp_f32 v86, v86\n\t"
    "v_rcp_f32 v69, v69\n\t"
    "v_rcp_f32 v71, v71\n\t"
    "v_rcp_f32 v85, v85\n\t"
    "v_rcp_f32 v87, v87\n\t"
    "v_pk_mul_f32 v[72:73], v[72:73], v[68:69]\n\t"
    "v_pk_mul_f32 v[74:75], v[74:75], v[70:71]\n\t"
    "v_pk_mul_f32 v[88:89], v[88:89], v[84:85]\n\t"
    "v_pk_mul_f32 v[90:91], v[90:91], v[86:87]\n\t"
    "v_pk_mul_f32 v[100:101], v[104:105], s[92:93] op_sel_hi:[1,0] neg_lo:[0,1] neg_hi:[0,1]\n\t"
    "v_pk_mul_f32 v[102:103], v[106:107], s[92:93] op_sel_hi:[1,0] neg_lo:[0,1] neg_hi:[0,1]\n\t"
    "v_pk_mul_f32 v[116:117], v[120:121], s[92:93] op_sel_hi:[1,0] neg_lo:[0,1] neg_hi:[0,1]\n\t"
    "v_pk_mul_f32 v[118:119], v[122:123], s[92:93] op_sel_hi:[1,0] neg_lo:[0,1] neg_hi:[0,1]\n\t"
    "v_exp_f32 v100, v100\n\t"
    "v_exp_f32 v102, v102\n\t"
    "v_exp_f32 v116, v116\n\t"
    "v_exp_f32 v118, v118\n\t"
    "v_exp_f32 v101, v101\n\t"
    "v_exp_f32 v103, v103\n\t"
    "v_exp_f32 v117, v117\n\t"
    "v_exp_f32 v119, v119\n\t"
    "v_pk_add_f32 v[100:101], v[100:101], 1.0 op_sel_hi:[1,0]\n\t"
    "v_pk_add_f32 v[102:103], v[102:103], 1.0 op_sel_hi:[1,0]\n\t"
    "v_pk_add_f32 v[116:117], v[116:117], 1.0 op_sel_hi:[1,0]\n\t"
    "v_pk_add_f32 v[118:119], v[118:119], 1.0 op_sel_hi:[1,0]\n\t"
    "v_rcp_f32 v100, v100\n\t"
    "v_rcp_f32 v102, v102\n\t"
    "v_rcp_f32 v116, v116\n\t"
    "v_rcp_f32 v118, v118\n\t"
    "v_rcp_f32 v101, v101\n\t"
    "v_rcp_f32 v103, v103\n\t"
    "v_rcp_f32 v117, v117\n\t"
    "v_rcp_f32 v119, v119\n\t"
    "v_pk_mul_f32 v[104:105], v[104:105], v[100:101]\n\t"
    "v_pk_mul_f32 v[106:107], v[106:107], v[102:103]\n\t"
    "v_pk_mul_f32 v[120:121], v[120:121], v[116:117]\n\t"
    "v_pk_mul_f32 v[122:123], v[122:123], v[118:119]\n\t"
    "81:\n\t"
    "v_pk_fma_f32 v[192:193], v[0:1], v[8:9], v[192:193]\n\t"
    "v_pk_fma_f32 v[194:195], v[2:3], v[10:11], v[194:195]\n\t"
    "v_cvt_pk_bf16_f32 v176, v192, v193\n\t"
    "v_cvt_pk_bf16_f32 v177, v194, v195\n\t"
    "ds_write_b64 v208, v[176:177]\n\t"
    "v_pk_fma_f32 v[196:197], v[16:17], v[24:25], v[196:197]\n\t"
    "v_pk_fma_f32 v[198:199], v[18:19], v[26:27], v[198:199]\n\t"
    "v_cvt_pk_bf16_f32 v178, v196, v197\n\t"
    "v_cvt_pk_bf16_f32 v179, v198, v199\n\t"
    "ds_write_b64 v209, v[178:179]\n\t"
    "v_pk_fma_f32 v[200:201], v[32:33], v[40:41], v[200:201]\n\t"
    "v_pk_fma_f32 v[202:203], v[34:35], v[42:43], v[202:203]\n\t"
    "v_cvt_pk_bf16_f32 v180, v200, v201\n\t"
    "v_cvt_pk_bf16_f32 v181, v202, v203\n\t"
    "ds_write_b64 v210, v[180:181]\n\t"
    "v_pk_fma_f32 v[204:205], v[48:49], v[56:57], v[204:205]\n\t"
    "v_pk_fma_f32 v[206:207], v[50:51], v[58:59], v[206:207]\n\t"
    "v_cvt_pk_bf16_f32 v182, v204, v205\n\t"
    "v_cvt_pk_bf16_f32 v183, v206, v207\n\t"
    "ds_write_b64 v211, v[182:183]\n\t"
    "v_pk_fma_f32 v[232:233], v[64:65], v[72:73], v[232:233]\n\t"
    "v_pk_fma_f32 v[234:235], v[66:67], v[74:75], v[234:235]\n\t"
    "v_cvt_pk_bf16_f32 v184, v232, v233\n\t"
    "v_cvt_pk_bf16_f32 v185, v234, v235\n\t"
    "ds_write_b64 v214, v[184:185]\n\t"
    "v_pk_fma_f32 v[236:237], v[80:81], v[88:89], v[236:237]\n\t"
    "v_pk_fma_f32 v[238:239], v[82:83], v[90:91], v[238:239]\n\t"
    "v_cvt_pk_bf16_f32 v186, v236, v237\n\t"
    "v_cvt_pk_bf16_f32 v187, v238, v239\n\t"
    "ds_write_b64 v215, v[186:187]\n\t"
    "v_pk_fma_f32 v[240:241], v[96:97], v[104:105], v[240:241]\n\t"
    "v_pk_fma_f32 v[242:243], v[98:99], v[106:107], v[242:243]\n\t"
    "v_cvt_pk_bf16_f32 v188, v240, v241\n\t"
    "v_cvt_pk_bf16_f32 v189, v242, v243\n\t"
    "ds_write_b64 v216, v[188:189]\n\t"
    "v_pk_fma_f32 v[244:245], v[112:113], v[120:121], v[244:245]\n\t"
    "v_pk_fma_f32 v[246:247], v[114:115], v[122:123], v[246:247]\n\t"
    "v_cvt_pk_bf16_f32 v190, v244, v245\n\t"
    "v_cvt_pk_bf16_f32 v191, v246, v247\n\t"
    "ds_write_b64 v217, v[190:191]\n\t"
    "s_waitcnt lgkmcnt(0)\n\t"
    "ds_read_b128 v[128:131], v213 offset:0\n\t"
    "ds_read_b128 v[132:135], v213 offset:1024\n\t"
    "ds_read_b128 v[136:139], v213 offset:2048\n\t"
    "ds_read_b128 v[140:143], v213 offset:3072\n\t"
    "s_mov_b32 %[tmp2], 0\n\t"
    "v_xor_b32 v212, 0, v219\n\t"
    "s_waitcnt lgkmcnt(3)\n\t"
    "buffer_store_dwordx4 v[128:131], v212, s[68:71], %[tmp2] offen sc1\n\t"
    "s_mov_b32 %[tmp2], 8192\n\t"
    "v_xor_b32 v212, 64, v219\n\t"
    "s_waitcnt lgkmcnt(2)\n\t"
    "buffer_store_dwordx4 v[132:135], v212, s[68:71], %[tmp2] offen sc1\n\t"
    "s_mov_b32 %[tmp2], 16384\n\t"
    "v_xor_b32 v212, 128, v219\n\t"
    "s_waitcnt lgkmcnt(1)\n\t"
    "buffer_store_dwordx4 v[136:139], v212, s[68:71], %[tmp2] offen sc1\n\t"
    "s_mov_b32 %[tmp2], 24576\n\t"
    "v_xor_b32 v212, 192, v219\n\t"
    "s_waitcnt lgkmcnt(0)\n\t"
    "buffer_store_dwordx4 v[140:143], v212, s[68:71], %[tmp2] offen sc1\n\t"
    "s_branch 3f\n\t"
    "52:\n\t"
    "s_cmp_eq_u32 %[role], 3\n\t"
    "s_cbranch_scc1 62b\n\t"
    "s_cmp_lg_u32 %[w], 0\n\t"
    "s_cbranch_scc1 54f\n\t"
    "v_mov_b32 v250, s90\n\t"
    "v_mov_b32 v251, s91\n\t"
    "v_mov_b32 v249, %[fop]\n\t"
    "s_mov_b64 exec, 1\n\t"
    "buffer_store_dwordx2 v[250:251], v249, %[srdw], 0 offen sc1\n\t"
    "s_mov_b64 exec, -1\n\t"
    "54:\n\t"
    "s_cmp_lg_u32 %[w], 0\n\t"
    "s_cbranch_scc1 53f\n\t"
    "v_mov_b32 v250, s90\n\t"
    "v_mov_b32 v251, s91\n\t"
    "v_mov_b32 v249, %[pub]\n\t"
    "s_mov_b64 exec, 1\n\t"
    "buffer_store_dwordx2 v[250:251], v249, %[srdw], 0 offen sc1\n\t"
    "s_mov_b64 exec, -1\n\t"
    "53:\n\t"
    "s_branch 62b\n\t"
    "56:\n\t"
    "s_mov_b32 %[tmp2], 0x10000\n\t"
    "58:\n\t"
    "s_sleep 2\n\t"
    "buffer_load_dwordx2 v[224:225], v223, %[srdw], %[flgo] offen sc1\n\t"
    "s_sub_u32 %[tmp2], %[tmp2], 1\n\t"
    "s_waitcnt vmcnt(0)\n\t"
    "s_cbranch_scc1 57b\n\t"
    "v_cmp_ne_u64 vcc, s[90:91], v[224:225]\n\t"
    "s_cbranch_vccnz 58b\n\t"
    "s_branch 57b\n\t"
    "3:\n\t"
    "blk_end_A:\n\t"
        : G2_IO, [flgo] "+s"(flgo), [dfo] "+s"(dfo), [df2] "+s"(df2) : G2_IN, [df3] "s"(0), [tend] "s"(16 - 4 + RING + 1), [fop] "s"(fp1 + p * 10240 + 4096 + col * 128) : G2_CLOB);
      if (bid == 0 && w == 0) __builtin_amdgcn_raw_buffer_store_b64((u32x2){(unsigned)tag, (unsigned)(tag >> 32)}, srdw, lane_id() == 0 ? (unsigned)(WS_FLAG + FL_EPOCH) : OOB, 0, 16);
    } else { unsigned flgo = fp1 + pm2 * 10240, dfo = 6144, df2 = role ? -6656 : 512;
      asm volatile(
    "s_waitcnt vmcnt(12) lgkmcnt(0)\n\t"
    "s_barrier\n\t"
    "v_mfma_f32_16x16x32_bf16 v[0:3], a[128:131], a[0:3], 0\n\t"
    "buffer_load_dwordx2 v[224:225], v223, %[srdw], %[flgo] offen sc1\n\t"
    "v_mfma_f32_16x16x32_bf16 v[16:19], a[132:135], a[0:3], 0\n\t"
    "ds_read_b64_tr_b16 a[192:193], v214 offset:16384\n\t"
    "v_mfma_f32_16x16x32_bf16 v[20:23], a[132:135], a[4:7], 0\n\t"
    "s_add_u32 m0, %[m0b], 0\n\t"
    "ds_read_b64_tr_b16 a[194:195], v214 offset:18432\n\t"
    "v_mfma_f32_16x16x32_bf16 v[4:7], a[128:131], a[4:7], 0\n\t"
    "buffer_load_dwordx4 v212, %[srdb], %[sob] offen offset:0 lds\n\t"
    "v_mfma_f32_16x16x32_bf16 v[8:11], a[128:131], a[8:11], 0\n\t"
    "ds_read_b64_tr_b16 a[196:197], v215 offset:16384\n\t"
    "v_mfma_f32_16x16x32_bf16 v[24:27], a[132:135], a[8:11], 0\n\t"
    "v_mfma_f32_16x16x32_bf16 v[28:31], a[132:135], a[12:15], 0\n\t"
    "ds_read_b64_tr_b16 a[198:199], v215 offset:18432\n\t"
    "v_mfma_f32_16x16x32_bf16 v[12:15], a[128:131], a[12:15], 0\n\t"
    "ds_read_b64_tr_b16 a[200:201], v216 offset:16384\n\t"
    "v_mfma_f32_16x16x32_bf16 v[44:47], a[136:139], a[12:15], 0\n\t"
    "buffer_load_dwordx4 a[64:67], v208, %[srda], %[soa] offen offset:0 sc1\n\t"
    "v_mfma_f32_16x16x32_bf16 v[60:63], a[140:143], a[12:15], 0\n\t"
    "ds_read_b64_tr_b16 a[202:203], v216 offset:18432\n\t"
    "v_mfma_f32_16x16x32_bf16 v[56:59], a[140:143], a[8:11], 0\n\t"
    "v_mfma_f32_16x16x32_bf16 v[40:43], a[136:139], a[8:11], 0\n\t"
    "ds_read_b64_tr_b16 a[204:205], v217 offset:16384\n\t"
    "v_mfma_f32_16x16x32_bf16 v[36:39], a[136:139], a[4:7], 0\n\t"
    "ds_read_b64_tr_b16 a[206:207], v217 offset:18432\n\t"
    "v_mfma_f32_16x16x32_bf16 v[52:55], a[140:143], a[4:7], 0\n\t"
    "buffer_load_dwordx4 a[80:83], v208, %[srda], %[soa] offen offset:1024 sc1\n\t"
    "v_mfma_f32_16x16x32_bf16 v[48:51], a[140:143], a[0:3], 0\n\t"
    "v_mfma_f32_16x16x32_bf16 v[32:35], a[136:139], a[0:3], 0\n\t"
    "ds_read_b64_tr_b16 a[208:209], v214 offset:24576\n\t"
    "v_mfma_f32_16x16x32_bf16 v[64:67], a[144:147], a[0:3], 0\n\t"
    "ds_read_b64_tr_b16 a[210:211], v214 offset:26624\n\t"
    "v_mfma_f32_16x16x32_bf16 v[80:83], a[148:151], a[0:3], 0\n\t"
    "ds_read_b64_tr_b16 a[212:213], v215 offset:24576\n\t"
    "v_mfma_f32_16x16x32_bf16 v[84:87], a[148:151], a[4:7], 0\n\t"
    "buffer_load_dwordx4 v213, %[srdb], %[sob] offen offset:1024 lds\n\t"
    "v_mfma_f32_16x16x32_bf16 v[68:71], a[144:147], a[4:7], 0\n\t"
    "v_mfma_f32_16x16x32_bf16 v[72:75], a[144:147], a[8:11], 0\n\t"
    "ds_read_b64_tr_b16 a[214:215], v215 offset:26624\n\t"
    "v_mfma_f32_16x16x32_bf16 v[88:91], a[148:151], a[8:11], 0\n\t"
    "ds_read_b64_tr_b16 a[216:217], v216 offset:24576\n\t"
    "v_mfma_f32_16x16x32_bf16 v[92:95], a[148:151], a[12:15], 0\n\t"
    "ds_read_b64_tr_b16 a[218:219], v216 offset:26624\n\t"
    "v_mfma_f32_16x16x32_bf16 v[76:79], a[144:147], a[12:15], 0\n\t"
    "buffer_load_dwordx4 a[68:71], v209, %[srda], %[soa] offen offset:0 sc1\n\t"
    "v_mfma_f32_16x16x32_bf16 v[108:111], a[152:155], a[12:15], 0\n\t"
    "v_mfma_f32_16x16x32_bf16 v[124:127], a[156:159], a[12:15], 0\n\t"
    "ds_read_b64_tr_b16 a[220:221], v217 offset:24576\n\t"
    "v_mfma_f32_16x16x32_bf16 v[120:123], a[156:159], a[8:11], 0\n\t"
    "ds_read_b64_tr_b16 a[222:223], v217 offset:26624\n\t"
    "v_mfma_f32_16x16x32_bf16 v[104:107], a[152:155], a[8:11], 0\n\t"
    "v_mfma_f32_16x16x32_bf16 v[100:103], a[152:155], a[4:7], 0\n\t"
    "buffer_load_dwordx4 a[84:87], v209, %[srda], %[soa] offen offset:1024 sc1\n\t"
    "v_mfma_f32_16x16x32_bf16 v[116:119], a[156:159], a[4:7], 0\n\t"
    "ds_read_b64_tr_b16 a[224:225], v214 offset:20480\n\t"
    "v_mfma_f32_16x16x32_bf16 v[112:115], a[156:159], a[0:3], 0\n\t"
    "ds_read_b64_tr_b16 a[226:227], v214 offset:22528\n\t"
    "v_mfma_f32_16x16x32_bf16 v[96:99], a[152:155], a[0:3], 0\n\t"
    "s_add_u32 m0, %[m0b], 8192\n\t"
    "ds_read_b64_tr_b16 a[228:229], v215 offset:20480\n\t"
    "v_mfma_f32_16x16x32_bf16 v[0:3], a[160:163], a[16:19], v[0:3]\n\t"
    "s_add_u32 %[tmp], %[sob], 128\n\t"
    "v_mfma_f32_16x16x32_bf16 v[16:19], a[164:167], a[16:19], v[16:19]\n\t"
    "buffer_load_dwordx4 v212, %[srdb], %[tmp] offen offset:0 lds\n\t"
    "v_mfma_f32_16x16x32_bf16 v[20:23], a[164:167], a[20:23], v[20:23]\n\t"
    "ds_read_b64_tr_b16 a[230:231], v215 offset:22528\n\t"
    "v_mfma_f32_16x16x32_bf16 v[4:7], a[160:163], a[20:23], v[4:7]\n\t"
    "ds_read_b64_tr_b16 a[232:233], v216 offset:20480\n\t"
    "v_mfma_f32_16x16x32_bf16 v[8:11], a[160:163], a[24:27], v[8:11]\n\t"
    "ds_read_b64_tr_b16 a[234:235], v216 offset:22528\n\t"
    "v_mfma_f32_16x16x32_bf16 v[24:27], a[164:167], a[24:27], v[24:27]\n\t"
    "v_mfma_f32_16x16x32_bf16 v[28:31], a[164:167], a[28:31], v[28:31]\n\t"
    "buffer_load_dwordx4 a[72:75], v210, %[srda], %[soa] offen offset:0 sc1\n\t"
    "v_mfma_f32_16x16x32_bf16 v[12:15], a[160:163], a[28:31], v[12:15]\n\t"
    "ds_read_b64_tr_b16 a[236:237], v217 offset:20480\n\t"
    "v_mfma_f32_16x16x32_bf16 v[44:47], a[168:171], a[28:31], v[44:47]\n\t"
    "ds_read_b64_tr_b16 a[238:239], v217 offset:22528\n\t"
    "v_mfma_f32_16x16x32_bf16 v[60:63], a[172:175], a[28:31], v[60:63]\n\t"
    "v_mfma_f32_16x16x32_bf16 v[56:59], a[172:175], a[24:27], v[56:59]\n\t"
    "ds_read_b64_tr_b16 a[240:241], v214 offset:28672\n\t"
    "v_mfma_f32_16x16x32_bf16 v[40:43], a[168:171], a[24:27], v[40:43]\n\t"
    "buffer_load_dwordx4 a[88:91], v210, %[srda], %[soa] offen offset:1024 sc1\n\t"
    "v_mfma_f32_16x16x32_bf16 v[36:39], a[168:171], a[20:23], v[36:39]\n\t"
    "ds_read_b64_tr_b16 a[242:243], v214 offset:30720\n\t"
    "v_mfma_f32_16x16x32_bf16 v[52:55], a[172:175], a[20:23], v[52:55]\n\t"
    "ds_read_b64_tr_b16 a[244:245], v215 offset:28672\n\t"
    "v_mfma_f32_16x16x32_bf16 v[48:51], a[172:175], a[16:19], v[48:51]\n\t"
    "v_mfma_f32_16x16x32_bf16 v[32:35], a[168:171], a[16:19], v[32:35]\n\t"
    "ds_read_b64_tr_b16 a[246:247], v215 offset:30720\n\t"
    "v_mfma_f32_16x16x32_bf16 v[64:67], a[176:179], a[16:19], v[64:67]\n\t"
    "buffer_load_dwordx4 v213, %[srdb], %[tmp] offen offset:1024 lds\n\t"
    "v_mfma_f32_16x16x32_bf16 v[80:83], a[180:183], a[16:19], v[80:83]\n\t"
    "ds_read_b64_tr_b16 a[248:249], v216 offset:28672\n\t"
    "v_mfma_f32_16x16x32_bf16 v[84:87], a[180:183], a[20:23], v[84:87]\n\t"
    "ds_read_b64_tr_b16 a[250:251], v216 offset:30720\n\t"
    "v_mfma_f32_16x16x32_bf16 v[68:71], a[176:179], a[20:23], v[68:71]\n\t"
    "v_mfma_f32_16x16x32_bf16 v[72:75], a[176:179], a[24:27], v[72:75]\n\t"
    "ds_read_b64_tr_b16 a[252:253], v217 offset:28672\n\t"
    "v_mfma_f32_16x16x32_bf16 v[88:91], a[180:183], a[24:27], v[88:91]\n\t"
    "buffer_load_dwordx4 a[76:79], v211, %[srda], %[soa] offen offset:0 sc1\n\t"
    "v_mfma_f32_16x16x32_bf16 v[92:95], a[180:183], a[28:31], v[92:95]\n\t"
    "ds_read_b64_tr_b16 a[254:255], v217 offset:30720\n\t"
    "v_mfma_f32_16x16x32_bf16 v[76:79], a[176:179], a[28:31], v[76:79]\n\t"
    "v_mfma_f32_16x16x32_bf16 v[108:111], a[184:187], a[28:31], v[108:111]\n\t"
    "v_mfma_f32_16x16x32_bf16 v[124:127], a[188:191], a[28:31], v[124:127]\n\t"
    "v_mfma_f32_16x16x32_bf16 v[120:123], a[188:191], a[24:27], v[120:123]\n\t"
    "buffer_load_dwordx4 a[92:95], v211, %[srda], %[soa] offen offset:1024 sc1\n\t"
    "v_mfma_f32_16x16x32_bf16 v[104:107], a[184:187], a[24:27], v[104:107]\n\t"
    "s_mov_b32 %[soa], %[nsa]\n\t"
    "s_lshr_b32 %[nsa], %[sob], 6\n\t"
    "v_mfma_f32_16x16x32_bf16 v[100:103], a[184:187], a[20:23], v[100:103]\n\t"
    "v_readlane_b32 %[sob], v222, %[t]\n\t"
    "v_mfma_f32_16x16x32_bf16 v[116:119], a[188:191], a[20:23], v[116:119]\n\t"
    "s_add_u32 %[t], %[t], 1\n\t"
    "v_mfma_f32_16x16x32_bf16 v[112:115], a[188:191], a[16:19], v[112:115]\n\t"
    "v_mfma_f32_16x16x32_bf16 v[96:99], a[184:187], a[16:19], v[96:99]\n\t"
    "s_waitcnt vmcnt(17) lgkmcnt(0)\n\t"
    "s_barrier\n\t"
    "v_mfma_f32_16x16x32_bf16 v[0:3], a[192:195], a[32:35], v[0:3]\n\t"
    "ds_read_b64_tr_b16 a[128:129], v214 offset:32768\n\t"
    "v_mfma_f32_16x16x32_bf16 v[16:19], a[196:199], a[32:35], v[16:19]\n\t"
    "ds_read_b64_tr_b16 a[130:131], v214 offset:34816\n\t"
    "v_mfma_f32_16x16x32_bf16 v[20:23], a[196:199], a[36:39], v[20:23]\n\t"
    "s_add_u32 m0, %[m0b], 16384\n\t"
    "ds_read_b64_tr_b16 a[132:133], v215 offset:32768\n\t"
    "v_mfma_f32_16x16x32_bf16 v[4:7], a[192:195], a[36:39], v[4:7]\n\t"
    "buffer_load_dwordx4 v212, %[srdb], %[sob] offen offset:0 lds\n\t"
    "v_mfma_f32_16x16x32_bf16 v[8:11], a[192:195], a[40:43], v[8:11]\n\t"
    "v_mfma_f32_16x16x32_bf16 v[24:27], a[196:199], a[40:43], v[24:27]\n\t"
    "ds_read_b64_tr_b16 a[134:135], v215 offset:34816\n\t"
    "v_mfma_f32_16x16x32_bf16 v[28:31], a[196:199], a[44:47], v[28:31]\n\t"
    "ds_read_b64_tr_b16 a[136:137], v216 offset:32768\n\t"
    "v_mfma_f32_16x16x32_bf16 v[12:15], a[192:195], a[44:47], v[12:15]\n\t"
    "v_mfma_f32_16x16x32_bf16 v[44:47], a[200:203], a[44:47], v[44:47]\n\t"
    "buffer_load_dwordx4 a[96:99], v208, %[srda], %[soa] offen offset:0 sc1\n\t"
    "v_mfma_f32_16x16x32_bf16 v[60:63], a[204:207], a[44:47], v[60:63]\n\t"
    "ds_read_b64_tr_b16 a[138:139], v216 offset:34816\n\t"
    "v_mfma_f32_16x16x32_bf16 v[56:59], a[204:207], a[40:43], v[56:59]\n\t"
    "ds_read_b64_tr_b16 a[140:141], v217 offset:32768\n\t"
    "v_mfma_f32_16x16x32_bf16 v[40:43], a[200:203], a[40:43], v[40:43]\n\t"
    "ds_read_b64_tr_b16 a[142:143], v217 offset:34816\n\t"
    "v_mfma_f32_16x16x32_bf16 v[36:39], a[200:203], a[36:39], v[36:39]\n\t"
    "v_mfma_f32_16x16x32_bf16 v[52:55], a[204:207], a[36:39], v[52:55]\n\t"
    "buffer_load_dwordx4 a[112:115], v208, %[srda], %[soa] offen offset:1024 sc1\n\t"
    "v_mfma_f32_16x16x32_bf16 v[48:51], a[204:207], a[32:35], v[48:51]\n\t"
    "ds_read_b64_tr_b16 a[144:145], v214 offset:40960\n\t"
    "v_mfma_f32_16x16x32_bf16 v[32:35], a[200:203], a[32:35], v[32:35]\n\t"
    "ds_read_b64_tr_b16 a[146:147], v214 offset:43008\n\t"
    "v_mfma_f32_16x16x32_bf16 v[64:67], a[208:211], a[32:35], v[64:67]\n\t"
    "v_mfma_f32_16x16x32_bf16 v[80:83], a[212:215], a[32:35], v[80:83]\n\t"
    "ds_read_b64_tr_b16 a[148:149], v215 offset:40960\n\t"
    "v_mfma_f32_16x16x32_bf16 v[84:87], a[212:215], a[36:39], v[84:87]\n\t"
    "buffer_load_dwordx4 v213, %[srdb], %[sob] offen offset:1024 lds\n\t"
    "v_mfma_f32_16x16x32_bf16 v[68:71], a[208:211], a[36:39], v[68:71]\n\t"
    "ds_read_b64_tr_b16 a[150:151], v215 offset:43008\n\t"
    "v_mfma_f32_16x16x32_bf16 v[72:75], a[208:211], a[40:43], v[72:75]\n\t"
    "ds_read_b64_tr_b16 a[152:153], v216 offset:40960\n\t"
    "v_mfma_f32_16x16x32_bf16 v[88:91], a[212:215], a[40:43], v[88:91]\n\t"
    "v_mfma_f32_16x16x32_bf16 v[92:95], a[212:215], a[44:47], v[92:95]\n\t"
    "ds_read_b64_tr_b16 a[154:155], v216 offset:43008\n\t"
    "v_mfma_f32_16x16x32_bf16 v[76:79], a[208:211], a[44:47], v[76:79]\n\t"
    "buffer_load_dwordx4 a[100:103], v209, %[srda], %[soa] offen offset:0 sc1\n\t"
    "v_mfma_f32_16x16x32_bf16 v[108:111], a[216:219], a[44:47], v[108:111]\n\t"
    "ds_read_b64_tr_b16 a[156:157], v217 offset:40960\n\t"
    "v_mfma_f32_16x16x32_bf16 v[124:127], a[220:223], a[44:47], v[124:127]\n\t"
    "v_mfma_f32_16x16x32_bf16 v[120:123], a[220:223], a[40:43], v[120:123]\n\t"
    "ds_read_b64_tr_b16 a[158:159], v217 offset:43008\n\t"
    "v_mfma_f32_16x16x32_bf16 v[104:107], a[216:219], a[40:43], v[104:107]\n\t"
    "ds_read_b64_tr_b16 a[160:161], v214 offset:36864\n\t"
    "v_mfma_f32_16x16x32_bf16 v[100:103], a[216:219], a[36:39], v[100:103]\n\t"
    "buffer_load_dwordx4 a[116:119], v209, %[srda], %[soa] offen offset:1024 sc1\n\t"
    "v_mfma_f32_16x16x32_bf16 v[116:119], a[220:223], a[36:39], v[116:119]\n\t"
    "ds_read_b64_tr_b16 a[162:163], v214 offset:38912\n\t"
    "v_mfma_f32_16x16x32_bf16 v[112:115], a[220:223], a[32:35], v[112:115]\n\t"
    "v_mfma_f32_16x16x32_bf16 v[96:99], a[216:219], a[32:35], v[96:99]\n\t"
    "s_add_u32 m0, %[m0b], 24576\n\t"
    "ds_read_b64_tr_b16 a[164:165], v215 offset:36864\n\t"
    "v_mfma_f32_16x16x32_bf16 v[0:3], a[224:227], a[48:51], v[0:3]\n\t"
    "s_add_u32 %[tmp], %[sob], 128\n\t"
    "ds_read_b64_tr_b16 a[166:167], v215 offset:38912\n\t"
    "v_mfma_f32_16x16x32_bf16 v[16:19], a[228:231], a[48:51], v[16:19]\n\t"
    "buffer_load_dwordx4 v212, %[srdb], %[tmp] offen offset:0 lds\n\t"
    "v_mfma_f32_16x16x32_bf16 v[20:23], a[228:231], a[52:55], v[20:23]\n\t"
    "v_mfma_f32_16x16x32_bf16 v[4:7], a[224:227], a[52:55], v[4:7]\n\t"
    "ds_read_b64_tr_b16 a[168:169], v216 offset:36864\n\t"
    "v_mfma_f32_16x16x32_bf16 v[8:11], a[224:227], a[56:59], v[8:11]\n\t"
    "ds_read_b64_tr_b16 a[170:171], v216 offset:38912\n\t"
    "v_mfma_f32_16x16x32_bf16 v[24:27], a[228:231], a[56:59], v[24:27]\n\t"
    "ds_read_b64_tr_b16 a[172:173], v217 offset:36864\n\t"
    "v_mfma_f32_16x16x32_bf16 v[28:31], a[228:231], a[60:63], v[28:31]\n\t"
    "buffer_load_dwordx4 a[104:107], v210, %[srda], %[soa] offen offset:0 sc1\n\t"
    "v_mfma_f32_16x16x32_bf16 v[12:15], a[224:227], a[60:63], v[12:15]\n\t"
    "v_mfma_f32_16x16x32_bf16 v[44:47], a[232:235], a[60:63], v[44:47]\n\t"
    "ds_read_b64_tr_b16 a[174:175], v217 offset:38912\n\t"
    "v_mfma_f32_16x16x32_bf16 v[60:63], a[236:239], a[60:63], v[60:63]\n\t"
    "ds_read_b64_tr_b16 a[176:177], v214 offset:45056\n\t"
    "v_mfma_f32_16x16x32_bf16 v[56:59], a[236:239], a[56:59], v[56:59]\n\t"
    "v_mfma_f32_16x16x32_bf16 v[40:43], a[232:235], a[56:59], v[40:43]\n\t"
    "buffer_load_dwordx4 a[120:123], v210, %[srda], %[soa] offen offset:1024 sc1\n\t"
    "v_mfma_f32_16x16x32_bf16 v[36:39], a[232:235], a[52:55], v[36:39]\n\t"
    "ds_read_b64_tr_b16 a[178:179], v214 offset:47104\n\t"
    "v_mfma_f32_16x16x32_bf16 v[52:55], a[236:239], a[52:55], v[52:55]\n\t"
    "ds_read_b64_tr_b16 a[180:181], v215 offset:45056\n\t"
    "v_mfma_f32_16x16x32_bf16 v[48:51], a[236:239], a[48:51], v[48:51]\n\t"
    "ds_read_b64_tr_b16 a[182:183], v215 offset:47104\n\t"
    "v_mfma_f32_16x16x32_bf16 v[32:35], a[232:235], a[48:51], v[32:35]\n\t"
    "v_mfma_f32_16x16x32_bf16 v[64:67], a[240:243], a[48:51], v[64:67]\n\t"
    "buffer_load_dwordx4 v213, %[srdb], %[tmp] offen offset:1024 lds\n\t"
    "v_mfma_f32_16x16x32_bf16 v[80:83], a[244:247], a[48:51], v[80:83]\n\t"
    "ds_read_b64_tr_b16 a[184:185], v216 offset:45056\n\t"
    "v_mfma_f32_16x16x32_bf16 v[84:87], a[244:247], a[52:55], v[84:87]\n\t"
    "ds_read_b64_tr_b16 a[186:187], v216 offset:47104\n\t"
    "v_mfma_f32_16x16x32_bf16 v[68:71], a[240:243], a[52:55], v[68:71]\n\t"
    "v_mfma_f32_16x16x32_bf16 v[72:75], a[240:243], a[56:59], v[72:75]\n\t"
    "ds_read_b64_tr_b16 a[188:189], v217 offset:45056\n\t"
    "v_mfma_f32_16x16x32_bf16 v[88:91], a[244:247], a[56:59], v[88:91]\n\t"
    "buffer_load_dwordx4 a[108:111], v211, %[srda], %[soa] offen offset:0 sc1\n\t"
    "v_mfma_f32_16x16x32_bf16 v[92:95], a[244:247], a[60:63], v[92:95]\n\t"
    "ds_read_b64_tr_b16 a[190:191], v217 offset:47104\n\t"
    "v_mfma_f32_16x16x32_bf16 v[76:79], a[240:243], a[60:63], v[76:79]\n\t"
    "v_mfma_f32_16x16x32_bf16 v[108:111], a[248:251], a[60:63], v[108:111]\n\t"
    "v_mfma_f32_16x16x32_bf16 v[124:127], a[252:255], a[60:63], v[124:127]\n\t"
    "v_mfma_f32_16x16x32_bf16 v[120:123], a[252:255], a[56:59], v[120:123]\n\t"
    "buffer_load_dwordx4 a[124:127], v211, %[srda], %[soa] offen offset:1024 sc1\n\t"
    "v_mfma_f32_16x16x32_bf16 v[104:107], a[248:251], a[56:59], v[104:107]\n\t"
    "s_mov_b32 %[soa], %[nsa]\n\t"
    "s_lshr_b32 %[nsa], %[sob], 6\n\t"
    "v_mfma_f32_16x16x32_bf16 v[100:103], a[248:251], a[52:55], v[100:103]\n\t"
    "v_readlane_b32 %[sob], v222, %[t]\n\t"
    "v_mfma_f32_16x16x32_bf16 v[116:119], a[252:255], a[52:55], v[116:119]\n\t"
    "s_add_u32 %[t], %[t], 1\n\t"
    "v_mfma_f32_16x16x32_bf16 v[112:115], a[252:255], a[48:51], v[112:115]\n\t"
    "v_mfma_f32_16x16x32_bf16 v[96:99], a[248:251], a[48:51], v[96:99]\n\t"
    "s_branch 2f\n\t"
    "1:\n\t"
    "s_waitcnt vmcnt(12) lgkmcnt(0)\n\t"
    "s_barrier\n\t"
    "v_mfma_f32_16x16x32_bf16 v[0:3], a[128:131], a[0:3], v[0:3]\n\t"
    "buffer_load_dwordx2 v[224:225], v223, %[srdw], %[flgo] offen sc1\n\t"
    "v_mfma_f32_16x16x32_bf16 v[16:19], a[132:135], a[0:3], v[16:19]\n\t"
    "ds_read_b64_tr_b16 a[192:193], v214 offset:16384\n\t"
    "v_mfma_f32_16x16x32_bf16 v[20:23], a[132:135], a[4:7], v[20:23]\n\t"
    "s_add_u32 m0, %[m0b], 0\n\t"
    "ds_read_b64_tr_b16 a[194:195], v214 offset:18432\n\t"
    "v_mfma_f32_16x16x32_bf16 v[4:7], a[128:131], a[4:7], v[4:7]\n\t"
    "buffer_load_dwordx4 v212, %[srdb], %[sob] offen offset:0 lds\n\t"
    "v_mfma_f32_16x16x32_bf16 v[8:11], a[128:131], a[8:11], v[8:11]\n\t"
    "ds_read_b64_tr_b16 a[196:197], v215 offset:16384\n\t"
    "v_mfma_f32_16x16x32_bf16 v[24:27], a[132:135], a[8:11], v[24:27]\n\t"
    "v_mfma_f32_16x16x32_bf16 v[28:31], a[132:135], a[12:15], v[28:31]\n\t"
    "ds_read_b64_tr_b16 a[198:199], v215 offset:18432\n\t"
    "v_mfma_f32_16x16x32_bf16 v[12:15], a[128:131], a[12:15], v[12:15]\n\t"
    "ds_read_b64_tr_b16 a[200:201], v216 offset:16384\n\t"
    "v_mfma_f32_16x16x32_bf16 v[44:47], a[136:139], a[12:15], v[44:47]\n\t"
    "buffer_load_dwordx4 a[64:67], v208, %[srda], %[soa] offen offset:0 sc1\n\t"
    "v_mfma_f32_16x16x32_bf16 v[60:63], a[140:143], a[12:15], v[60:63]\n\t"
    "ds_read_b64_tr_b16 a[202:203], v216 offset:18432\n\t"
    "v_mfma_f32_16x16x32_bf16 v[56:59], a[140:143], a[8:11], v[56:59]\n\t"
    "v_mfma_f32_16x16x32_bf16 v[40:43], a[136:139], a[8:11], v[40:43]\n\t"
    "ds_read_b64_tr_b16 a[204:205], v217 offset:16384\n\t"
    "v_mfma_f32_16x16x32_bf16 v[36:39], a[136:139], a[4:7], v[36:39]\n\t"
    "ds_read_b64_tr_b16 a[206:207], v217 offset:18432\n\t"
    "v_mfma_f32_16x16x32_bf16 v[52:55], a[140:143], a[4:7], v[52:55]\n\t"
    "buffer_load_dwordx4 a[80:83], v208, %[srda], %[soa] offen offset:1024 sc1\n\t"
    "v_mfma_f32_16x16x32_bf16 v[48:51], a[140:143], a[0:3], v[48:51]\n\t"
    "v_mfma_f32_16x16x32_bf16 v[32:35], a[136:139], a[0:3], v[32:35]\n\t"
    "ds_read_b64_tr_b16 a[208:209], v214 offset:24576\n\t"
    "v_mfma_f32_16x16x32_bf16 v[64:67], a[144:147], a[0:3], v[64:67]\n\t"
    "ds_read_b64_tr_b16 a[210:211], v214 offset:26624\n\t"
    "v_mfma_f32_16x16x32_bf16 v[80:83], a[148:151], a[0:3], v[80:83]\n\t"
    "ds_read_b64_tr_b16 a[212:213], v215 offset:24576\n\t"
    "v_mfma_f32_16x16x32_bf16 v[84:87], a[148:151], a[4:7], v[84:87]\n\t"
    "buffer_load_dwordx4 v213, %[srdb], %[sob] offen offset:1024 lds\n\t"
    "v_mfma_f32_16x16x32_bf16 v[68:71], a[144:147], a[4:7], v[68:71]\n\t"
    "v_mfma_f32_16x16x32_bf16 v[72:75], a[144:147], a[8:11], v[72:75]\n\t"
    "ds_read_b64_tr_b16 a[214:215], v215 offset:26624\n\t"
    "v_mfma_f32_16x16x32_bf16 v[88:91], a[148:151], a[8:11], v[88:91]\n\t"
    "ds_read_b64_tr_b16 a[216:217], v216 offset:24576\n\t"
    "v_mfma_f32_16x16x32_bf16 v[92:95], a[148:151], a[12:15], v[92:95]\n\t"
    "ds_read_b64_tr_b16 a[218:219], v216 offset:26624\n\t"
    "v_mfma_f32_16x16x32_bf16 v[76:79], a[144:147], a[12:15], v[76:79]\n\t"
    "buffer_load_dwordx4 a[68:71], v209, %[srda], %[soa] offen offset:0 sc1\n\t"
    "v_mfma_f32_16x16x32_bf16 v[108:111], a[152:155], a[12:15], v[108:111]\n\t"
    "v_mfma_f32_16x16x32_bf16 v[124:127], a[156:159], a[12:15], v[124:127]\n\t"
    "ds_read_b64_tr_b16 a[220:221], v217 offset:24576\n\t"
    "v_mfma_f32_16x16x32_bf16 v[120:123], a[156:159], a[8:11], v[120:123]\n\t"
    "ds_read_b64_tr_b16 a[222:223], v217 offset:26624\n\t"
    "v_mfma_f32_16x16x32_bf16 v[104:107], a[152:155], a[8:11], v[104:107]\n\t"
    "v_mfma_f32_16x16x32_bf16 v[100:103], a[152:155], a[4:7], v[100:103]\n\t"
    "buffer_load_dwordx4 a[84:87], v209, %[srda], %[soa] offen offset:1024 sc1\n\t"
    "v_mfma_f32_16x16x32_bf16 v[116:119], a[156:159], a[4:7], v[116:119]\n\t"
    "ds_read_b64_tr_b16 a[224:225], v214 offset:20480\n\t"
    "v_mfma_f32_16x16x32_bf16 v[112:115], a[156:159], a[0:3], v[112:115]\n\t"
    "ds_read_b64_tr_b16 a[226:227], v214 offset:22528\n\t"
    "v_mfma_f32_16x16x32_bf16 v[96:99], a[152:155], a[0:3], v[96:99]\n\t"
    "s_add_u32 m0, %[m0b], 8192\n\t"
    "ds_read_b64_tr_b16 a[228:229], v215 offset:20480\n\t"
    "v_mfma_f32_16x16x32_bf16 v[0:3], a[160:163], a[16:19], v[0:3]\n\t"
    "s_add_u32 %[tmp], %[sob], 128\n\t"
    "v_mfma_f32_16x16x32_bf16 v[16:19], a[164:167], a[16:19], v[16:19]\n\t"
    "buffer_load_dwordx4 v212, %[srdb], %[tmp] offen offset:0 lds\n\t"
    "v_mfma_f32_16x16x32_bf16 v[20:23], a[164:167], a[20:23], v[20:23]\n\t"
    "ds_read_b64_tr_b16 a[230:231], v215 offset:22528\n\t"
    "v_mfma_f32_16x16x32_bf16 v[4:7], a[160:163], a[20:23], v[4:7]\n\t"
    "ds_read_b64_tr_b16 a[232:233], v216 offset:20480\n\t"
    "v_mfma_f32_16x16x32_bf16 v[8:11], a[160:163], a[24:27], v[8:11]\n\t"
    "ds_read_b64_tr_b16 a[234:235], v216 offset:22528\n\t"
    "v_mfma_f32_16x16x32_bf16 v[24:27], a[164:167], a[24:27], v[24:27]\n\t"
    "v_mfma_f32_16x16x32_bf16 v[28:31], a[164:167], a[28:31], v[28:31]\n\t"
    "buffer_load_dwordx4 a[72:75], v210, %[srda], %[soa] offen offset:0 sc1\n\t"
    "v_mfma_f32_16x16x32_bf16 v[12:15], a[160:163], a[28:31], v[12:15]\n\t"
    "ds_read_b64_tr_b16 a[236:237], v217 offset:20480\n\t"
    "v_mfma_f32_16x16x32_bf16 v[44:47], a[168:171], a[28:31], v[44:47]\n\t"
    "ds_read_b64_tr_b16 a[238:239], v217 offset:22528\n\t"
    "v_mfma_f32_16x16x32_bf16 v[60:63], a[172:175], a[28:31], v[60:63]\n\t"
    "v_mfma_f32_16x16x32_bf16 v[56:59], a[172:175], a[24:27], v[56:59]\n\t"
    "ds_read_b64_tr_b16 a[240:241], v214 offset:28672\n\t"
    "v_mfma_f32_16x16x32_bf16 v[40:43], a[168:171], a[24:27], v[40:43]\n\t"
    "buffer_load_dwordx4 a[88:91], v210, %[srda], %[soa] offen offset:1024 sc1\n\t"
    "v_mfma_f32_16x16x32_bf16 v[36:39], a[168:171], a[20:23], v[36:39]\n\t"
    "ds_read_b64_tr_b16 a[242:243], v214 offset:30720\n\t"
    "v_mfma_f32_16x16x32_bf16 v[52:55], a[172:175], a[20:23], v[52:55]\n\t"
    "ds_read_b64_tr_b16 a[244:245], v215 offset:28672\n\t"
    "v_mfma_f32_16x16x32_bf16 v[48:51], a[172:175], a[16:19], v[48:51]\n\t"
    "v_mfma_f32_16x16x32_bf16 v[32:35], a[168:171], a[16:19], v[32:35]\n\t"
    "ds_read_b64_tr_b16 a[246:247], v215 offset:30720\n\t"
    "v_mfma_f32_16x16x32_bf16 v[64:67], a[176:179], a[16:19], v[64:67]\n\t"
    "buffer_load_dwordx4 v213, %[srdb], %[tmp] offen offset:1024 lds\n\t"
    "v_mfma_f32_16x16x32_bf16 v[80:83], a[180:183], a[16:19], v[80:83]\n\t"
    "ds_read_b64_tr_b16 a[248:249], v216 offset:28672\n\t"
    "v_mfma_f32_16x16x32_bf16 v[84:87], a[180:183], a[20:23], v[84:87]\n\t"
    "ds_read_b64_tr_b16 a[250:251], v216 offset:30720\n\t"
    "v_mfma_f32_16x16x32_bf16 v[68:71], a[176:179], a[20:23], v[68:71]\n\t"
    "v_mfma_f32_16x16x32_bf16 v[72:75], a[176:179], a[24:27], v[72:75]\n\t"
    "ds_read_b64_tr_b16 a[252:253], v217 offset:28672\n\t"
    "v_mfma_f32_16x16x32_bf16 v[88:91], a[180:183], a[24:27], v[88:91]\n\t"
    "buffer_load_dwordx4 a[76:79], v211, %[srda], %[soa] offen offset:0 sc1\n\t"
    "v_mfma_f32_16x16x32_bf16 v[92:95], a[180:183], a[28:31], v[92:95]\n\t"
    "ds_read_b64_tr_b16 a[254:255], v217 offset:30720\n\t"
    "v_mfma_f32_16x16x32_bf16 v[76:79], a[176:179], a[28:31], v[76:79]\n\t"
    "v_mfma_f32_16x16x32_bf16 v[108:111], a[184:187], a[28:31], v[108:111]\n\t"
    "v_mfma_f32_16x16x32_bf16 v[124:127], a[188:191], a[28:31], v[124:127]\n\t"
    "v_mfma_f32_16x16x32_bf16 v[120:123], a[188:191], a[24:27], v[120:123]\n\t"
    "buffer_load_dwordx4 a[92:95], v211, %[srda], %[soa] offen offset:1024 sc1\n\t"
    "v_mfma_f32_16x16x32_bf16 v[104:107], a[184:187], a[24:27], v[104:107]\n\t"
    "s_mov_b32 %[soa], %[nsa]\n\t"
    "s_lshr_b32 %[nsa], %[sob], 6\n\t"
    "v_mfma_f32_16x16x32_bf16 v[100:103], a[184:187], a[20:23], v[100:103]\n\t"
    "v_readlane_b32 %[sob], v222, %[t]\n\t"
    "v_mfma_f32_16x16x32_bf16 v[116:119], a[188:191], a[20:23], v[116:119]\n\t"
    "s_add_u32 %[t], %[t], 1\n\t"
    "v_mfma_f32_16x16x32_bf16 v[112:115], a[188:191], a[16:19], v[112:115]\n\t"
    "v_mfma_f32_16x16x32_bf16 v[96:99], a[184:187], a[16:19], v[96:99]\n\t"
    "s_waitcnt vmcnt(12) lgkmcnt(0)\n\t"
    "s_barrier\n\t"
    "v_mfma_f32_16x16x32_bf16 v[0:3], a[192:195], a[32:35], v[0:3]\n\t"
    "ds_read_b64_tr_b16 a[128:129], v214 offset:32768\n\t"
    "v_mfma_f32_16x16x32_bf16 v[16:19], a[196:199], a[32:35], v[16:19]\n\t"
    "ds_read_b64_tr_b16 a[130:131], v214 offset:34816\n\t"
    "v_mfma_f32_16x16x32_bf16 v[20:23], a[196:199], a[36:39], v[20:23]\n\t"
    "s_add_u32 m0, %[m0b], 16384\n\t"
    "ds_read_b64_tr_b16 a[132:133], v215 offset:32768\n\t"
    "v_mfma_f32_16x16x32_bf16 v[4:7], a[192:195], a[36:39], v[4:7]\n\t"
    "buffer_load_dwordx4 v212, %[srdb], %[sob] offen offset:0 lds\n\t"
    "v_mfma_f32_16x16x32_bf16 v[8:11], a[192:195], a[40:43], v[8:11]\n\t"
    "v_mfma_f32_16x16x32_bf16 v[24:27], a[196:199], a[40:43], v[24:27]\n\t"
    "ds_read_b64_tr_b16 a[134:135], v215 offset:34816\n\t"
    "v_mfma_f32_16x16x32_bf16 v[28:31], a[196:199], a[44:47], v[28:31]\n\t"
    "ds_read_b64_tr_b16 a[136:137], v216 offset:32768\n\t"
    "v_mfma_f32_16x16x32_bf16 v[12:15], a[192:195], a[44:47], v[12:15]\n\t"
    "v_mfma_f32_16x16x32_bf16 v[44:47], a[200:203], a[44:47], v[44:47]\n\t"
    "buffer_load_dwordx4 a[96:99], v208, %[srda], %[soa] offen offset:0 sc1\n\t"
    "v_mfma_f32_16x16x32_bf16 v[60:63], a[204:207], a[44:47], v[60:63]\n\t"
    "ds_read_b64_tr_b16 a[138:139], v216 offset:34816\n\t"
    "v_mfma_f32_16x16x32_bf16 v[56:59], a[204:207], a[40:43], v[56:59]\n\t"
    "ds_read_b64_tr_b16 a[140:141], v217 offset:32768\n\t"
    "v_mfma_f32_16x16x32_bf16 v[40:43], a[200:203], a[40:43], v[40:43]\n\t"
    "ds_read_b64_tr_b16 a[142:143], v217 offset:34816\n\t"
    "v_mfma_f32_16x16x32_bf16 v[36:39], a[200:203], a[36:39], v[36:39]\n\t"
    "v_mfma_f32_16x16x32_bf16 v[52:55], a[204:207], a[36:39], v[52:55]\n\t"
    "buffer_load_dwordx4 a[112:115], v208, %[srda], %[soa] offen offset:1024 sc1\n\t"
    "v_mfma_f32_16x16x32_bf16 v[48:51], a[204:207], a[32:35], v[48:51]\n\t"
    "ds_read_b64_tr_b16 a[144:145], v214 offset:40960\n\t"
    "v_mfma_f32_16x16x32_bf16 v[32:35], a[200:203], a[32:35], v[32:35]\n\t"
    "ds_read_b64_tr_b16 a[146:147], v214 offset:43008\n\t"
    "v_mfma_f32_16x16x32_bf16 v[64:67], a[208:211], a[32:35], v[64:67]\n\t"
    "v_mfma_f32_16x16x32_bf16 v[80:83], a[212:215], a[32:35], v[80:83]\n\t"
    "ds_read_b64_tr_b16 a[148:149], v215 offset:40960\n\t"
    "v_mfma_f32_16x16x32_bf16 v[84:87], a[212:215], a[36:39], v[84:87]\n\t"
    "buffer_load_dwordx4 v213, %[srdb], %[sob] offen offset:1024 lds\n\t"
    "v_mfma_f32_16x16x32_bf16 v[68:71], a[208:211], a[36:39], v[68:71]\n\t"
    "ds_read_b64_tr_b16 a[150:151], v215 offset:43008\n\t"
    "v_mfma_f32_16x16x32_bf16 v[72:75], a[208:211], a[40:43], v[72:75]\n\t"
    "ds_read_b64_tr_b16 a[152:153], v216 offset:40960\n\t"
    "v_mfma_f32_16x16x32_bf16 v[88:91], a[212:215], a[40:43], v[88:91]\n\t"
    "v_mfma_f32_16x16x32_bf16 v[92:95], a[212:215], a[44:47], v[92:95]\n\t"
    "ds_read_b64_tr_b16 a[154:155], v216 offset:43008\n\t"
    "v_mfma_f32_16x16x32_bf16 v[76:79], a[208:211], a[44:47], v[76:79]\n\t"
    "buffer_load_dwordx4 a[100:103], v209, %[srda], %[soa] offen offset:0 sc1\n\t"
    "v_mfma_f32_16x16x32_bf16 v[108:111], a[216:219], a[44:47], v[108:111]\n\t"
    "ds_read_b64_tr_b16 a[156:157], v217 offset:40960\n\t"
    "v_mfma_f32_16x16x32_bf16 v[124:127], a[220:223], a[44:47], v[124:127]\n\t"
    "v_mfma_f32_16x16x32_bf16 v[120:123], a[220:223], a[40:43], v[120:123]\n\t"
    "ds_read_b64_tr_b16 a[158:159], v217 offset:43008\n\t"
    "v_mfma_f32_16x16x32_bf16 v[104:107], a[216:219], a[40:43], v[104:107]\n\t"
    "ds_read_b64_tr_b16 a[160:161], v214 offset:36864\n\t"
    "v_mfma_f32_16x16x32_bf16 v[100:103], a[216:219], a[36:39], v[100:103]\n\t"
    "buffer_load_dwordx4 a[116:119], v209, %[srda], %[soa] offen offset:1024 sc1\n\t"
    "v_mfma_f32_16x16x32_bf16 v[116:119], a[220:223], a[36:39], v[116:119]\n\t"
    "ds_read_b64_tr_b16 a[162:163], v214 offset:38912\n\t"
    "v_mfma_f32_16x16x32_bf16 v[112:115], a[220:223], a[32:35], v[112:115]\n\t"
    "v_mfma_f32_16x16x32_bf16 v[96:99], a[216:219], a[32:35], v[96:99]\n\t"
    "s_add_u32 m0, %[m0b], 24576\n\t"
    "ds_read_b64_tr_b16 a[164:165], v215 offset:36864\n\t"
    "v_mfma_f32_16x16x32_bf16 v[0:3], a[224:227], a[48:51], v[0:3]\n\t"
    "s_add_u32 %[tmp], %[sob], 128\n\t"
    "ds_read_b64_tr_b16 a[166:167], v215 offset:38912\n\t"
    "v_mfma_f32_16x16x32_bf16 v[16:19], a[228:231], a[48:51], v[16:19]\n\t"
    "buffer_load_dwordx4 v212, %[srdb], %[tmp] offen offset:0 lds\n\t"
    "v_mfma_f32_16x16x32_bf16 v[20:23], a[228:231], a[52:55], v[20:23]\n\t"
    "v_mfma_f32_16x16x32_bf16 v[4:7], a[224:227], a[52:55], v[4:7]\n\t"
    "ds_read_b64_tr_b16 a[168:169], v216 offset:36864\n\t"
    "v_mfma_f32_16x16x32_bf16 v[8:11], a[224:227], a[56:59], v[8:11]\n\t"
    "ds_read_b64_tr_b16 a[170:171], v216 offset:38912\n\t"
    "v_mfma_f32_16x16x32_bf16 v[24:27], a[228:231], a[56:59], v[24:27]\n\t"
    "ds_read_b64_tr_b16 a[172:173], v217 offset:36864\n\t"
    "v_mfma_f32_16x16x32_bf16 v[28:31], a[228:231], a[60:63], v[28:31]\n\t"
    "buffer_load_dwordx4 a[104:107], v210, %[srda], %[soa] offen offset:0 sc1\n\t"
    "v_mfma_f32_16x16x32_bf16 v[12:15], a[224:227], a[60:63], v[12:15]\n\t"
    "v_mfma_f32_16x16x32_bf16 v[44:47], a[232:235], a[60:63], v[44:47]\n\t"
    "ds_read_b64_tr_b16 a[174:175], v217 offset:38912\n\t"
    "v_mfma_f32_16x16x32_bf16 v[60:63], a[236:239], a[60:63], v[60:63]\n\t"
    "ds_read_b64_tr_b16 a[176:177], v214 offset:45056\n\t"
    "v_mfma_f32_16x16x32_bf16 v[56:59], a[236:239], a[56:59], v[56:59]\n\t"
    "v_mfma_f32_16x16x32_bf16 v[40:43], a[232:235], a[56:59], v[40:43]\n\t"
    "buffer_load_dwordx4 a[120:123], v210, %[srda], %[soa] offen offset:1024 sc1\n\t"
    "v_mfma_f32_16x16x32_bf16 v[36:39], a[232:235], a[52:55], v[36:39]\n\t"
    "ds_read_b64_tr_b16 a[178:179], v214 offset:47104\n\t"
    "v_mfma_f32_16x16x32_bf16 v[52:55], a[236:239], a[52:55], v[52:55]\n\t"
    "ds_read_b64_tr_b16 a[180:181], v215 offset:45056\n\t"
    "v_mfma_f32_16x16x32_bf16 v[48:51], a[236:239], a[48:51], v[48:51]\n\t"
    "ds_read_b64_tr_b16 a[182:183], v215 offset:47104\n\t"
    "v_mfma_f32_16x16x32_bf16 v[32:35], a[232:235], a[48:51], v[32:35]\n\t"
    "v_mfma_f32_16x16x32_bf16 v[64:67], a[240:243], a[48:51], v[64:67]\n\t"
    "buffer_load_dwordx4 v213, %[srdb], %[tmp] offen offset:1024 lds\n\t"
    "v_mfma_f32_16x16x32_bf16 v[80:83], a[244:247], a[48:51], v[80:83]\n\t"
    "ds_read_b64_tr_b16 a[184:185], v216 offset:45056\n\t"
    "v_mfma_f32_16x16x32_bf16 v[84:87], a[244:247], a[52:55], v[84:87]\n\t"
    "ds_read_b64_tr_b16 a[186:187], v216 offset:47104\n\t"
    "v_mfma_f32_16x16x32_bf16 v[68:71], a[240:243], a[52:55], v[68:71]\n\t"
    "v_mfma_f32_16x16x32_bf16 v[72:75], a[240:243], a[56:59], v[72:75]\n\t"
    "ds_read_b64_tr_b16 a[188:189], v217 offset:45056\n\t"
    "v_mfma_f32_16x16x32_bf16 v[88:91], a[244:247], a[56:59], v[88:91]\n\t"
    "buffer_load_dwordx4 a[108:111], v211, %[srda], %[soa] offen offset:0 sc1\n\t"
    "v_mfma_f32_16x16x32_bf16 v[92:95], a[244:247], a[60:63], v[92:95]\n\t"
    "ds_read_b64_tr_b16 a[190:191], v217 offset:47104\n\t"
    "v_mfma_f32_16x16x32_bf16 v[76:79], a[240:243], a[60:63], v[76:79]\n\t"
    "v_mfma_f32_16x16x32_bf16 v[108:111], a[248:251], a[60:63], v[108:111]\n\t"
    "v_mfma_f32_16x16x32_bf16 v[124:127], a[252:255], a[60:63], v[124:127]\n\t"
    "v_mfma_f32_16x16x32_bf16 v[120:123], a[252:255], a[56:59], v[120:123]\n\t"
    "buffer_load_dwordx4 a[124:127], v211, %[srda], %[soa] offen offset:1024 sc1\n\t"
    "v_mfma_f32_16x16x32_bf16 v[104:107], a[248:251], a[56:59], v[104:107]\n\t"
    "s_mov_b32 %[soa], %[nsa]\n\t"
    "s_lshr_b32 %[nsa], %[sob], 6\n\t"
    "v_mfma_f32_16x16x32_bf16 v[100:103], a[248:251], a[52:55], v[100:103]\n\t"
    "v_readlane_b32 %[sob], v222, %[t]\n\t"
    "v_mfma_f32_16x16x32_bf16 v[116:119], a[252:255], a[52:55], v[116:119]\n\t"
    "s_add_u32 %[t], %[t], 1\n\t"
    "v_mfma_f32_16x16x32_bf16 v[112:115], a[252:255], a[48:51], v[112:115]\n\t"
    "v_mfma_f32_16x16x32_bf16 v[96:99], a[248:251], a[48:51], v[96:99]\n\t"
    "2:\n\t"
    "s_waitcnt vmcnt(12) lgkmcnt(0)\n\t"
    "s_barrier\n\t"
    "s_cmp_eq_u32 %[t], 7\n\t"
    "s_cbranch_scc1 52f\n\t"
    "62:\n\t"
    "v_mfma_f32_16x16x32_bf16 v[0:3], a[128:131], a[64:67], v[0:3]\n\t"
    "v_cmp_ne_u64 vcc, s[90:91], v[224:225]\n\t"
    "v_mfma_f32_16x16x32_bf16 v[16:19], a[132:135], a[64:67], v[16:19]\n\t"
    "s_cbranch_vccnz 56f\n\t"
    "57:\n\t"
    "ds_read_b64_tr_b16 a[192:193], v214 offset:49152\n\t"
    "v_mfma_f32_16x16x32_bf16 v[20:23], a[132:135], a[68:71], v[20:23]\n\t"
    "s_add_u32 m0, %[m0b], 32768\n\t"
    "ds_read_b64_tr_b16 a[194:195], v214 offset:51200\n\t"
    "v_mfma_f32_16x16x32_bf16 v[4:7], a[128:131], a[68:71], v[4:7]\n\t"
    "buffer_load_dwordx4 v212, %[srdb], %[sob] offen offset:0 lds\n\t"
    "v_mfma_f32_16x16x32_bf16 v[8:11], a[128:131], a[72:75], v[8:11]\n\t"
    "s_add_u32 %[flgo], %[flgo], %[dfo]\n\t"
    "ds_read_b64_tr_b16 a[196:197], v215 offset:49152\n\t"
    "v_mfma_f32_16x16x32_bf16 v[24:27], a[132:135], a[72:75], v[24:27]\n\t"
    "s_mov_b32 %[dfo], %[df2]\n\t"
    "s_mov_b32 %[df2], %[df3]\n\t"
    "v_mfma_f32_16x16x32_bf16 v[28:31], a[132:135], a[76:79], v[28:31]\n\t"
    "ds_read_b64_tr_b16 a[198:199], v215 offset:51200\n\t"
    "v_mfma_f32_16x16x32_bf16 v[12:15], a[128:131], a[76:79], v[12:15]\n\t"
    "ds_read_b64_tr_b16 a[200:201], v216 offset:49152\n\t"
    "v_mfma_f32_16x16x32_bf16 v[44:47], a[136:139], a[76:79], v[44:47]\n\t"
    "buffer_load_dwordx4 a[0:3], v208, %[srda], %[soa] offen offset:0 sc1\n\t"
    "v_mfma_f32_16x16x32_bf16 v[60:63], a[140:143], a[76:79], v[60:63]\n\t"
    "ds_read_b64_tr_b16 a[202:203], v216 offset:51200\n\t"
    "v_mfma_f32_16x16x32_bf16 v[56:59], a[140:143], a[72:75], v[56:59]\n\t"
    "v_mfma_f32_16x16x32_bf16 v[40:43], a[136:139], a[72:75], v[40:43]\n\t"
    "ds_read_b64_tr_b16 a[204:205], v217 offset:49152\n\t"
    "v_mfma_f32_16x16x32_bf16 v[36:39], a[136:139], a[68:71], v[36:39]\n\t"
    "ds_read_b64_tr_b16 a[206:207], v217 offset:51200\n\t"
    "v_mfma_f32_16x16x32_bf16 v[52:55], a[140:143], a[68:71], v[52:55]\n\t"
    "buffer_load_dwordx4 a[16:19], v208, %[srda], %[soa] offen offset:1024 sc1\n\t"
    "v_mfma_f32_16x16x32_bf16 v[48:51], a[140:143], a[64:67], v[48:51]\n\t"
    "v_mfma_f32_16x16x32_bf16 v[32:35], a[136:139], a[64:67], v[32:35]\n\t"
    "ds_read_b64_tr_b16 a[208:209], v214 offset:57344\n\t"
    "v_mfma_f32_16x16x32_bf16 v[64:67], a[144:147], a[64:67], v[64:67]\n\t"
    "ds_read_b64_tr_b16 a[210:211], v214 offset:59392\n\t"
    "v_mfma_f32_16x16x32_bf16 v[80:83], a[148:151], a[64:67], v[80:83]\n\t"
    "ds_read_b64_tr_b16 a[212:213], v215 offset:57344\n\t"
    "v_mfma_f32_16x16x32_bf16 v[84:87], a[148:151], a[68:71], v[84:87]\n\t"
    "buffer_load_dwordx4 v213, %[srdb], %[sob] offen offset:1024 lds\n\t"
    "v_mfma_f32_16x16x32_bf16 v[68:71], a[144:147], a[68:71], v[68:71]\n\t"
    "v_mfma_f32_16x16x32_bf16 v[72:75], a[144:147], a[72:75], v[72:75]\n\t"
    "ds_read_b64_tr_b16 a[214:215], v215 offset:59392\n\t"
    "v_mfma_f32_16x16x32_bf16 v[88:91], a[148:151], a[72:75], v[88:91]\n\t"
    "ds_read_b64_tr_b16 a[216:217], v216 offset:57344\n\t"
    "v_mfma_f32_16x16x32_bf16 v[92:95], a[148:151], a[76:79], v[92:95]\n\t"
    "ds_read_b64_tr_b16 a[218:219], v216 offset:59392\n\t"
    "v_mfma_f32_16x16x32_bf16 v[76:79], a[144:147], a[76:79], v[76:79]\n\t"
    "buffer_load_dwordx4 a[4:7], v209, %[srda], %[soa] offen offset:0 sc1\n\t"
    "v_mfma_f32_16x16x32_bf16 v[108:111], a[152:155], a[76:79], v[108:111]\n\t"
    "v_mfma_f32_16x16x32_bf16 v[124:127], a[156:159], a[76:79], v[124:127]\n\t"
    "ds_read_b64_tr_b16 a[220:221], v217 offset:57344\n\t"
    "v_mfma_f32_16x16x32_bf16 v[120:123], a[156:159], a[72:75], v[120:123]\n\t"
    "ds_read_b64_tr_b16 a[222:223], v217 offset:59392\n\t"
    "v_mfma_f32_16x16x32_bf16 v[104:107], a[152:155], a[72:75], v[104:107]\n\t"
    "v_mfma_f32_16x16x32_bf16 v[100:103], a[152:155], a[68:71], v[100:103]\n\t"
    "buffer_load_dwordx4 a[20:23], v209, %[srda], %[soa] offen offset:1024 sc1\n\t"
    "v_mfma_f32_16x16x32_bf16 v[116:119], a[156:159], a[68:71], v[116:119]\n\t"
    "ds_read_b64_tr_b16 a[224:225], v214 offset:53248\n\t"
    "v_mfma_f32_16x16x32_bf16 v[112:115], a[156:159], a[64:67], v[112:115]\n\t"
    "ds_read_b64_tr_b16 a[226:227], v214 offset:55296\n\t"
    "v_mfma_f32_16x16x32_bf16 v[96:99], a[152:155], a[64:67], v[96:99]\n\t"
    "s_add_u32 m0, %[m0b], 40960\n\t"
    "ds_read_b64_tr_b16 a[228:229], v215 offset:53248\n\t"
    "v_mfma_f32_16x16x32_bf16 v[0:3], a[160:163], a[80:83], v[0:3]\n\t"
    "s_add_u32 %[tmp], %[sob], 128\n\t"
    "v_mfma_f32_16x16x32_bf16 v[16:19], a[164:167], a[80:83], v[16:19]\n\t"
    "buffer_load_dwordx4 v212, %[srdb], %[tmp] offen offset:0 lds\n\t"
    "v_mfma_f32_16x16x32_bf16 v[20:23], a[164:167], a[84:87], v[20:23]\n\t"
    "ds_read_b64_tr_b16 a[230:231], v215 offset:55296\n\t"
    "v_mfma_f32_16x16x32_bf16 v[4:7], a[160:163], a[84:87], v[4:7]\n\t"
    "ds_read_b64_tr_b16 a[232:233], v216 offset:53248\n\t"
    "v_mfma_f32_16x16x32_bf16 v[8:11], a[160:163], a[88:91], v[8:11]\n\t"
    "ds_read_b64_tr_b16 a[234:235], v216 offset:55296\n\t"
    "v_mfma_f32_16x16x32_bf16 v[24:27], a[164:167], a[88:91], v[24:27]\n\t"
    "v_mfma_f32_16x16x32_bf16 v[28:31], a[164:167], a[92:95], v[28:31]\n\t"
    "buffer_load_dwordx4 a[8:11], v210, %[srda], %[soa] offen offset:0 sc1\n\t"
    "v_mfma_f32_16x16x32_bf16 v[12:15], a[160:163], a[92:95], v[12:15]\n\t"
    "ds_read_b64_tr_b16 a[236:237], v217 offset:53248\n\t"
    "v_mfma_f32_16x16x32_bf16 v[44:47], a[168:171], a[92:95], v[44:47]\n\t"
    "ds_read_b64_tr_b16 a[238:239], v217 offset:55296\n\t"
    "v_mfma_f32_16x16x32_bf16 v[60:63], a[172:175], a[92:95], v[60:63]\n\t"
    "v_mfma_f32_16x16x32_bf16 v[56:59], a[172:175], a[88:91], v[56:59]\n\t"
    "ds_read_b64_tr_b16 a[240:241], v214 offset:61440\n\t"
    "v_mfma_f32_16x16x32_bf16 v[40:43], a[168:171], a[88:91], v[40:43]\n\t"
    "buffer_load_dwordx4 a[24:27], v210, %[srda], %[soa] offen offset:1024 sc1\n\t"
    "v_mfma_f32_16x16x32_bf16 v[36:39], a[168:171], a[84:87], v[36:39]\n\t"
    "ds_read_b64_tr_b16 a[242:243], v214 offset:63488\n\t"
    "v_mfma_f32_16x16x32_bf16 v[52:55], a[172:175], a[84:87], v[52:55]\n\t"
    "ds_read_b64_tr_b16 a[244:245], v215 offset:61440\n\t"
    "v_mfma_f32_16x16x32_bf16 v[48:51], a[172:175], a[80:83], v[48:51]\n\t"
    "v_mfma_f32_16x16x32_bf16 v[32:35], a[168:171], a[80:83], v[32:35]\n\t"
    "ds_read_b64_tr_b16 a[246:247], v215 offset:63488\n\t"
    "v_mfma_f32_16x16x32_bf16 v[64:67], a[176:179], a[80:83], v[64:67]\n\t"
    "buffer_load_dwordx4 v213, %[srdb], %[tmp] offen offset:1024 lds\n\t"
    "v_mfma_f32_16x16x32_bf16 v[80:83], a[180:183], a[80:83], v[80:83]\n\t"
    "ds_read_b64_tr_b16 a[248:249], v216 offset:61440\n\t"
    "v_mfma_f32_16x16x32_bf16 v[84:87], a[180:183], a[84:87], v[84:87]\n\t"
    "ds_read_b64_tr_b16 a[250:251], v216 offset:63488\n\t"
    "v_mfma_f32_16x16x32_bf16 v[68:71], a[176:179], a[84:87], v[68:71]\n\t"
    "v_mfma_f32_16x16x32_bf16 v[72:75], a[176:179], a[88:91], v[72:75]\n\t"
    "ds_read_b64_tr_b16 a[252:253], v217 offset:61440\n\t"
    "v_mfma_f32_16x16x32_bf16 v[88:91], a[180:183], a[88:91], v[88:91]\n\t"
    "buffer_load_dwordx4 a[12:15], v211, %[srda], %[soa] offen offset:0 sc1\n\t"
    "v_mfma_f32_16x16x32_bf16 v[92:95], a[180:183], a[92:95], v[92:95]\n\t"
    "ds_read_b64_tr_b16 a[254:255], v217 offset:63488\n\t"
    "v_mfma_f32_16x16x32_bf16 v[76:79], a[176:179], a[92:95], v[76:79]\n\t"
    "v_mfma_f32_16x16x32_bf16 v[108:111], a[184:187], a[92:95], v[108:111]\n\t"
    "v_mfma_f32_16x16x32_bf16 v[124:127], a[188:191], a[92:95], v[124:127]\n\t"
    "v_mfma_f32_16x16x32_bf16 v[120:123], a[188:191], a[88:91], v[120:123]\n\t"
    "buffer_load_dwordx4 a[28:31], v211, %[srda], %[soa] offen offset:1024 sc1\n\t"
    "v_mfma_f32_16x16x32_bf16 v[104:107], a[184:187], a[88:91], v[104:107]\n\t"
    "s_mov_b32 %[soa], %[nsa]\n\t"
    "s_lshr_b32 %[nsa], %[sob], 6\n\t"
    "v_mfma_f32_16x16x32_bf16 v[100:103], a[184:187], a[84:87], v[100:103]\n\t"
    "v_readlane_b32 %[sob], v222, %[t]\n\t"
    "v_mfma_f32_16x16x32_bf16 v[116:119], a[188:191], a[84:87], v[116:119]\n\t"
    "s_add_u32 %[t], %[t], 1\n\t"
    "v_mfma_f32_16x16x32_bf16 v[112:115], a[188:191], a[80:83], v[112:115]\n\t"
    "v_mfma_f32_16x16x32_bf16 v[96:99], a[184:187], a[80:83], v[96:99]\n\t"
    "s_waitcnt vmcnt(12) lgkmcnt(0)\n\t"
    "s_barrier\n\t"
    "v_mfma_f32_16x16x32_bf16 v[0:3], a[192:195], a[96:99], v[0:3]\n\t"
    "ds_read_b64_tr_b16 a[128:129], v214 offset:0\n\t"
    "v_mfma_f32_16x16x32_bf16 v[16:19], a[196:199], a[96:99], v[16:19]\n\t"
    "ds_read_b64_tr_b16 a[130:131], v214 offset:2048\n\t"
    "v_mfma_f32_16x16x32_bf16 v[20:23], a[196:199], a[100:103], v[20:23]\n\t"
    "s_add_u32 m0, %[m0b], 49152\n\t"
    "ds_read_b64_tr_b16 a[132:133], v215 offset:0\n\t"
    "v_mfma_f32_16x16x32_bf16 v[4:7], a[192:195], a[100:103], v[4:7]\n\t"
    "buffer_load_dwordx4 v212, %[srdb], %[sob] offen offset:0 lds\n\t"
    "v_mfma_f32_16x16x32_bf16 v[8:11], a[192:195], a[104:107], v[8:11]\n\t"
    "v_mfma_f32_16x16x32_bf16 v[24:27], a[196:199], a[104:107], v[24:27]\n\t"
    "ds_read_b64_tr_b16 a[134:135], v215 offset:2048\n\t"
    "v_mfma_f32_16x16x32_bf16 v[28:31], a[196:199], a[108:111], v[28:31]\n\t"
    "ds_read_b64_tr_b16 a[136:137], v216 offset:0\n\t"
    "v_mfma_f32_16x16x32_bf16 v[12:15], a[192:195], a[108:111], v[12:15]\n\t"
    "v_mfma_f32_16x16x32_bf16 v[44:47], a[200:203], a[108:111], v[44:47]\n\t"
    "buffer_load_dwordx4 a[32:35], v208, %[srda], %[soa] offen offset:0 sc1\n\t"
    "v_mfma_f32_16x16x32_bf16 v[60:63], a[204:207], a[108:111], v[60:63]\n\t"
    "ds_read_b64_tr_b16 a[138:139], v216 offset:2048\n\t"
    "v_mfma_f32_16x16x32_bf16 v[56:59], a[204:207], a[104:107], v[56:59]\n\t"
    "ds_read_b64_tr_b16 a[140:141], v217 offset:0\n\t"
    "v_mfma_f32_16x16x32_bf16 v[40:43], a[200:203], a[104:107], v[40:43]\n\t"
    "ds_read_b64_tr_b16 a[142:143], v217 offset:2048\n\t"
    "v_mfma_f32_16x16x32_bf16 v[36:39], a[200:203], a[100:103], v[36:39]\n\t"
    "v_mfma_f32_16x16x32_bf16 v[52:55], a[204:207], a[100:103], v[52:55]\n\t"
    "buffer_load_dwordx4 a[48:51], v208, %[srda], %[soa] offen offset:1024 sc1\n\t"
    "v_mfma_f32_16x16x32_bf16 v[48:51], a[204:207], a[96:99], v[48:51]\n\t"
    "ds_read_b64_tr_b16 a[144:145], v214 offset:8192\n\t"
    "v_mfma_f32_16x16x32_bf16 v[32:35], a[200:203], a[96:99], v[32:35]\n\t"
    "ds_read_b64_tr_b16 a[146:147], v214 offset:10240\n\t"
    "v_mfma_f32_16x16x32_bf16 v[64:67], a[208:211], a[96:99], v[64:67]\n\t"
    "v_mfma_f32_16x16x32_bf16 v[80:83], a[212:215], a[96:99], v[80:83]\n\t"
    "ds_read_b64_tr_b16 a[148:149], v215 offset:8192\n\t"
    "v_mfma_f32_16x16x32_bf16 v[84:87], a[212:215], a[100:103], v[84:87]\n\t"
    "buffer_load_dwordx4 v213, %[srdb], %[sob] offen offset:1024 lds\n\t"
    "v_mfma_f32_16x16x32_bf16 v[68:71], a[208:211], a[100:103], v[68:71]\n\t"
    "ds_read_b64_tr_b16 a[150:151], v215 offset:10240\n\t"
    "v_mfma_f32_16x16x32_bf16 v[72:75], a[208:211], a[104:107], v[72:75]\n\t"
    "ds_read_b64_tr_b16 a[152:153], v216 offset:8192\n\t"
    "v_mfma_f32_16x16x32_bf16 v[88:91], a[212:215], a[104:107], v[88:91]\n\t"
    "v_mfma_f32_16x16x32_bf16 v[92:95], a[212:215], a[108:111], v[92:95]\n\t"
    "ds_read_b64_tr_b16 a[154:155], v216 offset:10240\n\t"
    "v_mfma_f32_16x16x32_bf16 v[76:79], a[208:211], a[108:111], v[76:79]\n\t"
    "buffer_load_dwordx4 a[36:39], v209, %[srda], %[soa] offen offset:0 sc1\n\t"
    "v_mfma_f32_16x16x32_bf16 v[108:111], a[216:219], a[108:111], v[108:111]\n\t"
    "ds_read_b64_tr_b16 a[156:157], v217 offset:8192\n\t"
    "v_mfma_f32_16x16x32_bf16 v[124:127], a[220:223], a[108:111], v[124:127]\n\t"
    "v_mfma_f32_16x16x32_bf16 v[120:123], a[220:223], a[104:107], v[120:123]\n\t"
    "ds_read_b64_tr_b16 a[158:159], v217 offset:10240\n\t"
    "v_mfma_f32_16x16x32_bf16 v[104:107], a[216:219], a[104:107], v[104:107]\n\t"
    "ds_read_b64_tr_b16 a[160:161], v214 offset:4096\n\t"
    "v_mfma_f32_16x16x32_bf16 v[100:103], a[216:219], a[100:103], v[100:103]\n\t"
    "buffer_load_dwordx4 a[52:55], v209, %[srda], %[soa] offen offset:1024 sc1\n\t"
    "v_mfma_f32_16x16x32_bf16 v[116:119], a[220:223], a[100:103], v[116:119]\n\t"
    "ds_read_b64_tr_b16 a[162:163], v214 offset:6144\n\t"
    "v_mfma_f32_16x16x32_bf16 v[112:115], a[220:223], a[96:99], v[112:115]\n\t"
    "v_mfma_f32_16x16x32_bf16 v[96:99], a[216:219], a[96:99], v[96:99]\n\t"
    "s_add_u32 m0, %[m0b], 57344\n\t"
    "ds_read_b64_tr_b16 a[164:165], v215 offset:4096\n\t"
    "v_mfma_f32_16x16x32_bf16 v[0:3], a[224:227], a[112:115], v[0:3]\n\t"
    "s_add_u32 %[tmp], %[sob], 128\n\t"
    "ds_read_b64_tr_b16 a[166:167], v215 offset:6144\n\t"
    "v_mfma_f32_16x16x32_bf16 v[16:19], a[228:231], a[112:115], v[16:19]\n\t"
    "buffer_load_dwordx4 v212, %[srdb], %[tmp] offen offset:0 lds\n\t"
    "v_mfma_f32_16x16x32_bf16 v[20:23], a[228:231], a[116:119], v[20:23]\n\t"
    "v_mfma_f32_16x16x32_bf16 v[4:7], a[224:227], a[116:119], v[4:7]\n\t"
    "ds_read_b64_tr_b16 a[168:169], v216 offset:4096\n\t"
    "v_mfma_f32_16x16x32_bf16 v[8:11], a[224:227], a[120:123], v[8:11]\n\t"
    "ds_read_b64_tr_b16 a[170:171], v216 offset:6144\n\t"
    "v_mfma_f32_16x16x32_bf16 v[24:27], a[228:231], a[120:123], v[24:27]\n\t"
    "ds_read_b64_tr_b16 a[172:173], v217 offset:4096\n\t"
    "v_mfma_f32_16x16x32_bf16 v[28:31], a[228:231], a[124:127], v[28:31]\n\t"
    "buffer_load_dwordx4 a[40:43], v210, %[srda], %[soa] offen offset:0 sc1\n\t"
    "v_mfma_f32_16x16x32_bf16 v[12:15], a[224:227], a[124:127], v[12:15]\n\t"
    "v_mfma_f32_16x16x32_bf16 v[44:47], a[232:235], a[124:127], v[44:47]\n\t"
    "ds_read_b64_tr_b16 a[174:175], v217 offset:6144\n\t"
    "v_mfma_f32_16x16x32_bf16 v[60:63], a[236:239], a[124:127], v[60:63]\n\t"
    "ds_read_b64_tr_b16 a[176:177], v214 offset:12288\n\t"
    "v_mfma_f32_16x16x32_bf16 v[56:59], a[236:239], a[120:123], v[56:59]\n\t"
    "v_mfma_f32_16x16x32_bf16 v[40:43], a[232:235], a[120:123], v[40:43]\n\t"
    "buffer_load_dwordx4 a[56:59], v210, %[srda], %[soa] offen offset:1024 sc1\n\t"
    "v_mfma_f32_16x16x32_bf16 v[36:39], a[232:235], a[116:119], v[36:39]\n\t"
    "ds_read_b64_tr_b16 a[178:179], v214 offset:14336\n\t"
    "v_mfma_f32_16x16x32_bf16 v[52:55], a[236:239], a[116:119], v[52:55]\n\t"
    "ds_read_b64_tr_b16 a[180:181], v215 offset:12288\n\t"
    "v_mfma_f32_16x16x32_bf16 v[48:51], a[236:239], a[112:115], v[48:51]\n\t"
    "ds_read_b64_tr_b16 a[182:183], v215 offset:14336\n\t"
    "v_mfma_f32_16x16x32_bf16 v[32:35], a[232:235], a[112:115], v[32:35]\n\t"
    "v_mfma_f32_16x16x32_bf16 v[64:67], a[240:243], a[112:115], v[64:67]\n\t"
    "buffer_load_dwordx4 v213, %[srdb], %[tmp] offen offset:1024 lds\n\t"
    "v_mfma_f32_16x16x32_bf16 v[80:83], a[244:247], a[112:115], v[80:83]\n\t"
    "ds_read_b64_tr_b16 a[184:185], v216 offset:12288\n\t"
    "v_mfma_f32_16x16x32_bf16 v[84:87], a[244:247], a[116:119], v[84:87]\n\t"
    "ds_read_b64_tr_b16 a[186:187], v216 offset:14336\n\t"
    "v_mfma_f32_16x16x32_bf16 v[68:71], a[240:243], a[116:119], v[68:71]\n\t"
    "v_mfma_f32_16x16x32_bf16 v[72:75], a[240:243], a[120:123], v[72:75]\n\t"
    "ds_read_b64_tr_b16 a[188:189], v217 offset:12288\n\t"
    "v_mfma_f32_16x16x32_bf16 v[88:91], a[244:247], a[120:123], v[88:91]\n\t"
    "buffer_load_dwordx4 a[44:47], v211, %[srda], %[soa] offen offset:0 sc1\n\t"
    "v_mfma_f32_16x16x32_bf16 v[92:95], a[244:247], a[124:127], v[92:95]\n\t"
    "ds_read_b64_tr_b16 a[190:191], v217 offset:14336\n\t"
    "v_mfma_f32_16x16x32_bf16 v[76:79], a[240:243], a[124:127], v[76:79]\n\t"
    "v_mfma_f32_16x16x32_bf16 v[108:111], a[248:251], a[124:127], v[108:111]\n\t"
    "v_mfma_f32_16x16x32_bf16 v[124:127], a[252:255], a[124:127], v[124:127]\n\t"
    "v_mfma_f32_16x16x32_bf16 v[120:123], a[252:255], a[120:123], v[120:123]\n\t"
    "buffer_load_dwordx4 a[60:63], v211, %[srda], %[soa] offen offset:1024 sc1\n\t"
    "v_mfma_f32_16x16x32_bf16 v[104:107], a[248:251], a[120:123], v[104:107]\n\t"
    "s_mov_b32 %[soa], %[nsa]\n\t"
    "s_lshr_b32 %[nsa], %[sob], 6\n\t"
    "v_mfma_f32_16x16x32_bf16 v[100:103], a[248:251], a[116:119], v[100:103]\n\t"
    "v_readlane_b32 %[sob], v222, %[t]\n\t"
    "v_mfma_f32_16x16x32_bf16 v[116:119], a[252:255], a[116:119], v[116:119]\n\t"
    "s_add_u32 %[t], %[t], 1\n\t"
    "s_cmp_eq_u32 %[t], %[tend]\n\t"
    "v_mfma_f32_16x16x32_bf16 v[112:115], a[252:255], a[112:115], v[112:115]\n\t"
    "v_mfma_f32_16x16x32_bf16 v[96:99], a[248:251], a[112:115], v[96:99]\n\t"
    "s_cbranch_scc0 1b\n\t"
    "s_waitcnt vmcnt(12) lgkmcnt(0)\n\t"
    "s_barrier\n\t"
    "v_mfma_f32_16x16x32_bf16 v[4:7], a[128:131], a[4:7], v[4:7]\n\t"
    "ds_read_b64_tr_b16 a[192:193], v214 offset:16384\n\t"
    "v_mfma_f32_16x16x32_bf16 v[20:23], a[132:135], a[4:7], v[20:23]\n\t"
    "s_load_dwordx2 s[64:65], %[ka], 0x0\n\t"
    "ds_read_b64_tr_b16 a[194:195], v214 offset:18432\n\t"
    "v_lshrrev_b32 v220, 4, v248\n\t"
    "v_mfma_f32_16x16x32_bf16 v[24:27], a[132:135], a[8:11], v[24:27]\n\t"
    "s_load_dwordx2 s[68:69], %[ka], 0x30\n\t"
    "ds_read_b64_tr_b16 a[196:197], v215 offset:16384\n\t"
    "v_and_b32 v220, 0x30, v220\n\t"
    "v_mfma_f32_16x16x32_bf16 v[8:11], a[128:131], a[8:11], v[8:11]\n\t"
    "s_mul_i32 s81, %[tile], 196608\n\t"
    "buffer_load_dwordx4 a[64:67], v208, %[srda], %[soa] offen offset:0 sc1\n\t"
    "v_mfma_f32_16x16x32_bf16 v[12:15], a[128:131], a[12:15], v[12:15]\n\t"
    "s_mul_i32 s82, %[w], 12288\n\t"
    "ds_read_b64_tr_b16 a[198:199], v215 offset:18432\n\t"
    "v_mfma_f32_16x16x32_bf16 v[28:31], a[132:135], a[12:15], v[28:31]\n\t"
    "s_add_u32 s81, s81, s82\n\t"
    "ds_read_b64_tr_b16 a[200:201], v216 offset:16384\n\t"
    "v_and_b32 v219, 0xf0, v248\n\t"
    "v_mfma_f32_16x16x32_bf16 v[44:47], a[136:139], a[12:15], v[44:47]\n\t"
    "s_add_u32 s81, s81, 25165824\n\t"
    "ds_read_b64_tr_b16 a[202:203], v216 offset:18432\n\t"
    "v_xor_b32 v219, v219, v220\n\t"
    "v_mfma_f32_16x16x32_bf16 v[60:63], a[140:143], a[12:15], v[60:63]\n\t"
    "s_mul_i32 s75, %[role], 49152\n\t"
    "buffer_load_dwordx4 a[80:83], v208, %[srda], %[soa] offen offset:1024 sc1\n\t"
    "v_mfma_f32_16x16x32_bf16 v[56:59], a[140:143], a[8:11], v[56:59]\n\t"
    "s_add_u32 s75, s75, s81\n\t"
    "ds_read_b64_tr_b16 a[204:205], v217 offset:16384\n\t"
    "v_mfma_f32_16x16x32_bf16 v[40:43], a[136:139], a[8:11], v[40:43]\n\t"
    "s_add_u32 s76, s75, 4096\n\t"
    "ds_read_b64_tr_b16 a[206:207], v217 offset:18432\n\t"
    "v_lshl_add_u32 v219, v220, 7, v219\n\t"
    "v_mfma_f32_16x16x32_bf16 v[36:39], a[136:139], a[4:7], v[36:39]\n\t"
    "s_add_u32 s77, s75, 8192\n\t"
    "ds_read_b64_tr_b16 a[208:209], v214 offset:24576\n\t"
    "v_lshrrev_b32 v228, 8, v248\n\t"
    "v_mfma_f32_16x16x32_bf16 v[52:55], a[140:143], a[4:7], v[52:55]\n\t"
    "s_add_u32 s82, %[role], 1\n\t"
    "buffer_load_dwordx4 a[68:71], v209, %[srda], %[soa] offen offset:0 sc1\n\t"
    "v_mfma_f32_16x16x32_bf16 v[68:71], a[144:147], a[4:7], v[68:71]\n\t"
    "s_and_b32 s82, s82, 3\n\t"
    "ds_read_b64_tr_b16 a[210:211], v214 offset:26624\n\t"
    "v_mfma_f32_16x16x32_bf16 v[84:87], a[148:151], a[4:7], v[84:87]\n\t"
    "s_mul_i32 s72, s82, 49152\n\t"
    "ds_read_b64_tr_b16 a[212:213], v215 offset:24576\n\t"
    "v_bfe_u32 v229, v248, 6, 2\n\t"
    "v_mfma_f32_16x16x32_bf16 v[88:91], a[148:151], a[8:11], v[88:91]\n\t"
    "s_cmp_gt_u32 %[role], s82\n\t"
    "v_bfe_u32 v230, v248, 4, 2\n\t"
    "v_mfma_f32_16x16x32_bf16 v[72:75], a[144:147], a[8:11], v[72:75]\n\t"
    "s_subb_u32 s82, %[role], 0\n\t"
    "buffer_load_dwordx4 a[84:87], v209, %[srda], %[soa] offen offset:1024 sc1\n\t"
    "v_mfma_f32_16x16x32_bf16 v[76:79], a[144:147], a[12:15], v[76:79]\n\t"
    "s_lshl_b32 s82, s82, 12\n\t"
    "ds_read_b64_tr_b16 a[214:215], v215 offset:26624\n\t"
    "v_cmp_eq_u32 vcc, v228, v229\n\t"
    "v_mfma_f32_16x16x32_bf16 v[92:95], a[148:151], a[12:15], v[92:95]\n\t"
    "s_add_u32 s72, s72, s82\n\t"
    "ds_read_b64_tr_b16 a[216:217], v216 offset:24576\n\t"
    "v_mov_b32 v231, 4\n\t"
    "v_mfma_f32_16x16x32_bf16 v[108:111], a[152:155], a[12:15], v[108:111]\n\t"
    "s_add_u32 s72, s72, s81\n\t"
    "ds_read_b64_tr_b16 a[218:219], v216 offset:26624\n\t"
    "v_mfma_f32_16x16x32_bf16 v[124:127], a[156:159], a[12:15], v[124:127]\n\t"
    "s_add_u32 s82, %[role], 2\n\t"
    "buffer_load_dwordx4 a[72:75], v210, %[srda], %[soa] offen offset:0 sc1\n\t"
    "v_mfma_f32_16x16x32_bf16 v[120:123], a[156:159], a[8:11], v[120:123]\n\t"
    "s_and_b32 s82, s82, 3\n\t"
    "ds_read_b64_tr_b16 a[220:221], v217 offset:24576\n\t"
    "v_mov_b32 v228, 0x3f80\n\t"
    "v_mfma_f32_16x16x32_bf16 v[104:107], a[152:155], a[8:11], v[104:107]\n\t"
    "s_mul_i32 s73, s82, 49152\n\t"
    "ds_read_b64_tr_b16 a[222:223], v217 offset:26624\n\t"
    "v_cndmask_b32 v230, v231, v230, vcc\n\t"
    "v_mfma_f32_16x16x32_bf16 v[100:103], a[152:155], a[4:7], v[100:103]\n\t"
    "s_cmp_gt_u32 %[role], s82\n\t"
    "ds_read_b64_tr_b16 a[224:225], v214 offset:20480\n\t"
    "v_mfma_f32_16x16x32_bf16 v[116:119], a[156:159], a[4:7], v[116:119]\n\t"
    "s_subb_u32 s82, %[role], 0\n\t"
    "buffer_load_dwordx4 a[88:91], v210, %[srda], %[soa] offen offset:1024 sc1\n\t"
    "v_mfma_f32_16x16x32_bf16 v[4:7], a[160:163], a[20:23], v[4:7]\n\t"
    "s_lshl_b32 s82, s82, 12\n\t"
    "ds_read_b64_tr_b16 a[226:227], v214 offset:22528\n\t"
    "v_mov_b32 v229, 0x3f800000\n\t"
    "v_mfma_f32_16x16x32_bf16 v[20:23], a[164:167], a[20:23], v[20:23]\n\t"
    "s_add_u32 s73, s73, s82\n\t"
    "ds_read_b64_tr_b16 a[228:229], v215 offset:20480\n\t"
    "v_mov_b32 v226, 0\n\t"
    "v_mfma_f32_16x16x32_bf16 v[24:27], a[164:167], a[24:27], v[24:27]\n\t"
    "s_add_u32 s73, s73, s81\n\t"
    "ds_read_b64_tr_b16 a[230:231], v215 offset:22528\n\t"
    "v_mfma_f32_16x16x32_bf16 v[8:11], a[160:163], a[24:27], v[8:11]\n\t"
    "s_add_u32 s82, %[role], 3\n\t"
    "buffer_load_dwordx4 a[76:79], v211, %[srda], %[soa] offen offset:0 sc1\n\t"
    "v_mfma_f32_16x16x32_bf16 v[12:15], a[160:163], a[28:31], v[12:15]\n\t"
    "s_and_b32 s82, s82, 3\n\t"
    "ds_read_b64_tr_b16 a[232:233], v216 offset:20480\n\t"
    "v_cmp_eq_u32 vcc, 0, v230\n\t"
    "v_mfma_f32_16x16x32_bf16 v[28:31], a[164:167], a[28:31], v[28:31]\n\t"
    "s_mul_i32 s74, s82, 49152\n\t"
    "ds_read_b64_tr_b16 a[234:235], v216 offset:22528\n\t"
    "v_mov_b32 v227, 0\n\t"
    "v_mfma_f32_16x16x32_bf16 v[44:47], a[168:171], a[28:31], v[44:47]\n\t"
    "s_cmp_gt_u32 %[role], s82\n\t"
    "v_mov_b32 v231, 0\n\t"
    "v_mfma_f32_16x16x32_bf16 v[60:63], a[172:175], a[28:31], v[60:63]\n\t"
    "s_subb_u32 s82, %[role], 0\n\t"
    "buffer_load_dwordx4 a[92:95], v211, %[srda], %[soa] offen offset:1024 sc1\n\t"
    "v_mfma_f32_16x16x32_bf16 v[56:59], a[172:175], a[24:27], v[56:59]\n\t"
    "s_lshl_b32 s82, s82, 12\n\t"
    "ds_read_b64_tr_b16 a[236:237], v217 offset:20480\n\t"
    "v_cndmask_b32 v224, 0, v228, vcc\n\t"
    "v_mfma_f32_16x16x32_bf16 v[40:43], a[168:171], a[24:27], v[40:43]\n\t"
    "s_add_u32 s74, s74, s82\n\t"
    "ds_read_b64_tr_b16 a[238:239], v217 offset:22528\n\t"
    "v_mfma_f32_16x16x32_bf16 v[36:39], a[168:171], a[20:23], v[36:39]\n\t"
    "s_add_u32 s74, s74, s81\n\t"
    "ds_read_b64_tr_b16 a[240:241], v214 offset:28672\n\t"
    "v_cmp_eq_u32 vcc, 1, v230\n\t"
    "v_mfma_f32_16x16x32_bf16 v[52:55], a[172:175], a[20:23], v[52:55]\n\t"
    "ds_read_b64_tr_b16 a[242:243], v214 offset:30720\n\t"
    "s_nop 1\n\t"
    "v_mfma_f32_16x16x32_bf16 v[68:71], a[176:179], a[20:23], v[68:71]\n\t"
    "ds_read_b64_tr_b16 a[244:245], v215 offset:28672\n\t"
    "v_mfma_f32_16x16x32_bf16 v[84:87], a[180:183], a[20:23], v[84:87]\n\t"
    "ds_read_b64_tr_b16 a[246:247], v215 offset:30720\n\t"
    "v_cndmask_b32 v224, v224, v229, vcc\n\t"
    "v_mfma_f32_16x16x32_bf16 v[88:91], a[180:183], a[24:27], v[88:91]\n\t"
    "ds_read_b64_tr_b16 a[248:249], v216 offset:28672\n\t"
    "v_cmp_eq_u32 vcc, 2, v230\n\t"
    "v_mfma_f32_16x16x32_bf16 v[72:75], a[176:179], a[24:27], v[72:75]\n\t"
    "ds_read_b64_tr_b16 a[250:251], v216 offset:30720\n\t"
    "v_mfma_f32_16x16x32_bf16 v[76:79], a[176:179], a[28:31], v[76:79]\n\t"
    "ds_read_b64_tr_b16 a[252:253], v217 offset:28672\n\t"
    "s_nop 1\n\t"
    "v_mfma_f32_16x16x32_bf16 v[92:95], a[180:183], a[28:31], v[92:95]\n\t"
    "ds_read_b64_tr_b16 a[254:255], v217 offset:30720\n\t"
    "v_cndmask_b32 v225, 0, v228, vcc\n\t"
    "v_mfma_f32_16x16x32_bf16 v[108:111], a[184:187], a[28:31], v[108:111]\n\t"
    "v_cmp_eq_u32 vcc, 3, v230\n\t"
    "v_mfma_f32_16x16x32_bf16 v[124:127], a[188:191], a[28:31], v[124:127]\n\t"
    "s_nop 1\n\t"
    "v_mfma_f32_16x16x32_bf16 v[120:123], a[188:191], a[24:27], v[120:123]\n\t"
    "v_cndmask_b32 v225, v225, v229, vcc\n\t"
    "v_mov_b32 v228, 0\n\t"
    "v_mfma_f32_16x16x32_bf16 v[104:107], a[184:187], a[24:27], v[104:107]\n\t"
    "v_mov_b32 v229, 0\n\t"
    "v_mfma_f32_16x16x32_bf16 v[100:103], a[184:187], a[20:23], v[100:103]\n\t"
    "v_mov_b32 v230, v224\n\t"
    "v_mfma_f32_16x16x32_bf16 v[116:119], a[188:191], a[20:23], v[116:119]\n\t"
    "s_mov_b32 %[soa], %[nsa]\n\t"
    "v_mov_b32 v231, v225\n\t"
    "s_waitcnt vmcnt(8) lgkmcnt(0)\n\t"
    "s_barrier\n\t"
    "v_mfma_f32_16x16x32_bf16 v[4:7], a[192:195], a[36:39], v[4:7]\n\t"
    "s_lshl_b32 s78, %[tile], 13\n\t"
    "ds_read_b64_tr_b16 a[128:129], v214 offset:32768\n\t"
    "ds_read_b64_tr_b16 a[130:131], v214 offset:34816\n\t"
    "v_mfma_f32_16x16x32_bf16 v[20:23], a[196:199], a[36:39], v[20:23]\n\t"
    "s_lshl_b32 s82, %[w], 9\n\t"
    "ds_read_b64_tr_b16 a[132:133], v215 offset:32768\n\t"
    "v_mfma_f32_16x16x32_bf16 v[24:27], a[196:199], a[40:43], v[24:27]\n\t"
    "s_add_u32 s78, s78, s82\n\t"
    "ds_read_b64_tr_b16 a[134:135], v215 offset:34816\n\t"
    "v_mfma_f32_16x16x32_bf16 v[8:11], a[192:195], a[40:43], v[8:11]\n\t"
    "s_add_u32 s78, s78, 37849088\n\t"
    "buffer_load_dwordx4 a[96:99], v208, %[srda], %[soa] offen offset:0 sc1\n\t"
    "v_mfma_f32_16x16x32_bf16 v[12:15], a[192:195], a[44:47], v[12:15]\n\t"
    "s_lshl_b32 s82, %[role], 7\n\t"
    "ds_read_b64_tr_b16 a[136:137], v216 offset:32768\n\t"
    "v_mfma_f32_16x16x32_bf16 v[28:31], a[196:199], a[44:47], v[28:31]\n\t"
    "s_add_u32 s79, s78, s82\n\t"
    "ds_read_b64_tr_b16 a[138:139], v216 offset:34816\n\t"
    "v_mfma_f32_16x16x32_bf16 v[44:47], a[200:203], a[44:47], v[44:47]\n\t"
    "s_lshl_b32 s82, %[role], 11\n\t"
    "ds_read_b64_tr_b16 a[140:141], v217 offset:32768\n\t"
    "v_mfma_f32_16x16x32_bf16 v[60:63], a[204:207], a[44:47], v[60:63]\n\t"
    "s_add_u32 s78, s78, s82\n\t"
    "buffer_load_dwordx4 a[112:115], v208, %[srda], %[soa] offen offset:1024 sc1\n\t"
    "v_mfma_f32_16x16x32_bf16 v[56:59], a[204:207], a[40:43], v[56:59]\n\t"
    "s_lshl_b32 s80, %[m0b], 1\n\t"
    "ds_read_b64_tr_b16 a[142:143], v217 offset:34816\n\t"
    "v_mfma_f32_16x16x32_bf16 v[40:43], a[200:203], a[40:43], v[40:43]\n\t"
    "s_sub_u32 s80, s80, %[ldsb]\n\t"
    "ds_read_b64_tr_b16 a[144:145], v214 offset:40960\n\t"
    "v_mfma_f32_16x16x32_bf16 v[36:39], a[200:203], a[36:39], v[36:39]\n\t"
    "s_add_u32 s80, s80, 65536\n\t"
    "ds_read_b64_tr_b16 a[146:147], v214 offset:43008\n\t"
    "v_mfma_f32_16x16x32_bf16 v[52:55], a[204:207], a[36:39], v[52:55]\n\t"
    "s_lshr_b32 s81, %[tile], 3\n\t"
    "buffer_load_dwordx4 a[100:103], v209, %[srda], %[soa] offen offset:0 sc1\n\t"
    "v_mfma_f32_16x16x32_bf16 v[68:71], a[208:211], a[36:39], v[68:71]\n\t"
    "s_lshl_b32 s81, s81, 19\n\t"
    "ds_read_b64_tr_b16 a[148:149], v215 offset:40960\n\t"
    "v_mfma_f32_16x16x32_bf16 v[84:87], a[212:215], a[36:39], v[84:87]\n\t"
    "s_lshl_b32 s82, %[w], 17\n\t"
    "ds_read_b64_tr_b16 a[150:151], v215 offset:43008\n\t"
    "v_mfma_f32_16x16x32_bf16 v[88:91], a[212:215], a[40:43], v[88:91]\n\t"
    "s_add_u32 s81, s81, s82\n\t"
    "ds_read_b64_tr_b16 a[152:153], v216 offset:40960\n\t"
    "v_mfma_f32_16x16x32_bf16 v[72:75], a[208:211], a[40:43], v[72:75]\n\t"
    "s_lshl_b32 s82, %[role], 15\n\t"
    "buffer_load_dwordx4 a[116:119], v209, %[srda], %[soa] offen offset:1024 sc1\n\t"
    "v_mfma_f32_16x16x32_bf16 v[76:79], a[208:211], a[44:47], v[76:79]\n\t"
    "s_add_u32 s81, s81, s82\n\t"
    "ds_read_b64_tr_b16 a[154:155], v216 offset:43008\n\t"
    "v_mfma_f32_16x16x32_bf16 v[92:95], a[212:215], a[44:47], v[92:95]\n\t"
    "s_and_b32 s82, %[tile], 7\n\t"
    "ds_read_b64_tr_b16 a[156:157], v217 offset:40960\n\t"
    "v_mfma_f32_16x16x32_bf16 v[108:111], a[216:219], a[44:47], v[108:111]\n\t"
    "s_lshl_b32 s82, s82, 8\n\t"
    "ds_read_b64_tr_b16 a[158:159], v217 offset:43008\n\t"
    "v_mfma_f32_16x16x32_bf16 v[124:127], a[220:223], a[44:47], v[124:127]\n\t"
    "s_add_u32 s81, s81, s82\n\t"
    "buffer_load_dwordx4 a[104:107], v210, %[srda], %[soa] offen offset:0 sc1\n\t"
    "v_mfma_f32_16x16x32_bf16 v[120:123], a[220:223], a[40:43], v[120:123]\n\t"
    "s_sub_u32 s66, 32768, s82\n\t"
    "ds_read_b64_tr_b16 a[160:161], v214 offset:36864\n\t"
    "ds_read_b64_tr_b16 a[162:163], v214 offset:38912\n\t"
    "v_mfma_f32_16x16x32_bf16 v[104:107], a[216:219], a[40:43], v[104:107]\n\t"
    "s_mov_b32 s70, s66\n\t"
    "ds_read_b64_tr_b16 a[164:165], v215 offset:36864\n\t"
    "v_mfma_f32_16x16x32_bf16 v[100:103], a[216:219], a[36:39], v[100:103]\n\t"
    "s_mov_b32 s67, 0x20000\n\t"
    "ds_read_b64_tr_b16 a[166:167], v215 offset:38912\n\t"
    "v_mfma_f32_16x16x32_bf16 v[116:119], a[220:223], a[36:39], v[116:119]\n\t"
    "s_mov_b32 s71, 0x20000\n\t"
    "buffer_load_dwordx4 a[120:123], v210, %[srda], %[soa] offen offset:1024 sc1\n\t"
    "v_mfma_f32_16x16x32_bf16 v[4:7], a[224:227], a[52:55], v[4:7]\n\t"
    "s_add_u32 s64, s64, s81\n\t"
    "ds_read_b64_tr_b16 a[168:169], v216 offset:36864\n\t"
    "v_mfma_f32_16x16x32_bf16 v[20:23], a[228:231], a[52:55], v[20:23]\n\t"
    "s_addc_u32 s65, s65, 0\n\t"
    "ds_read_b64_tr_b16 a[170:171], v216 offset:38912\n\t"
    "v_mfma_f32_16x16x32_bf16 v[24:27], a[228:231], a[56:59], v[24:27]\n\t"
    "s_add_u32 s68, s68, s81\n\t"
    "ds_read_b64_tr_b16 a[172:173], v217 offset:36864\n\t"
    "v_mfma_f32_16x16x32_bf16 v[8:11], a[224:227], a[56:59], v[8:11]\n\t"
    "s_addc_u32 s69, s69, 0\n\t"
    "buffer_load_dwordx4 a[108:111], v211, %[srda], %[soa] offen offset:0 sc1\n\t"
    "v_mfma_f32_16x16x32_bf16 v[12:15], a[224:227], a[60:63], v[12:15]\n\t"
    "ds_read_b64_tr_b16 a[174:175], v217 offset:38912\n\t"
    "v_mfma_f32_16x16x32_bf16 v[28:31], a[228:231], a[60:63], v[28:31]\n\t"
    "ds_read_b64_tr_b16 a[176:177], v214 offset:45056\n\t"
    "v_mfma_f32_16x16x32_bf16 v[44:47], a[232:235], a[60:63], v[44:47]\n\t"
    "ds_read_b64_tr_b16 a[178:179], v214 offset:47104\n\t"
    "v_mfma_f32_16x16x32_bf16 v[60:63], a[236:239], a[60:63], v[60:63]\n\t"
    "buffer_load_dwordx4 a[124:127], v211, %[srda], %[soa] offen offset:1024 sc1\n\t"
    "v_mfma_f32_16x16x32_bf16 v[56:59], a[236:239], a[56:59], v[56:59]\n\t"
    "v_xor_b32 v212, 0, v219\n\t"
    "v_mfma_f32_16x16x32_bf16 v[40:43], a[232:235], a[56:59], v[40:43]\n\t"
    "s_add_u32 m0, s80, 0\n\t"
    "ds_read_b64_tr_b16 a[180:181], v215 offset:45056\n\t"
    "v_mfma_f32_16x16x32_bf16 v[36:39], a[232:235], a[52:55], v[36:39]\n\t"
    "s_mov_b32 %[tmp2], 0\n\t"
    "ds_read_b64_tr_b16 a[182:183], v215 offset:47104\n\t"
    "v_mfma_f32_16x16x32_bf16 v[52:55], a[236:239], a[52:55], v[52:55]\n\t"
    "buffer_load_dwordx4 v212, s[64:67], %[tmp2] offen lds\n\t"
    "v_mfma_f32_16x16x32_bf16 v[68:71], a[240:243], a[52:55], v[68:71]\n\t"
    "v_xor_b32 v212, 64, v219\n\t"
    "v_mfma_f32_16x16x32_bf16 v[84:87], a[244:247], a[52:55], v[84:87]\n\t"
    "s_add_u32 m0, s80, 1024\n\t"
    "ds_read_b64_tr_b16 a[184:185], v216 offset:45056\n\t"
    "v_mfma_f32_16x16x32_bf16 v[88:91], a[244:247], a[56:59], v[88:91]\n\t"
    "s_mov_b32 %[tmp2], 8192\n\t"
    "ds_read_b64_tr_b16 a[186:187], v216 offset:47104\n\t"
    "v_mfma_f32_16x16x32_bf16 v[72:75], a[240:243], a[56:59], v[72:75]\n\t"
    "buffer_load_dwordx4 v212, s[64:67], %[tmp2] offen lds\n\t"
    "v_mfma_f32_16x16x32_bf16 v[76:79], a[240:243], a[60:63], v[76:79]\n\t"
    "v_xor_b32 v212, 128, v219\n\t"
    "v_mfma_f32_16x16x32_bf16 v[92:95], a[244:247], a[60:63], v[92:95]\n\t"
    "s_add_u32 m0, s80, 2048\n\t"
    "ds_read_b64_tr_b16 a[188:189], v217 offset:45056\n\t"
    "v_mfma_f32_16x16x32_bf16 v[108:111], a[248:251], a[60:63], v[108:111]\n\t"
    "s_mov_b32 %[tmp2], 16384\n\t"
    "ds_read_b64_tr_b16 a[190:191], v217 offset:47104\n\t"
    "v_mfma_f32_16x16x32_bf16 v[124:127], a[252:255], a[60:63], v[124:127]\n\t"
    "buffer_load_dwordx4 v212, s[64:67], %[tmp2] offen lds\n\t"
    "v_mfma_f32_16x16x32_bf16 v[120:123], a[252:255], a[56:59], v[120:123]\n\t"
    "v_xor_b32 v212, 192, v219\n\t"
    "v_mfma_f32_16x16x32_bf16 v[104:107], a[248:251], a[56:59], v[104:107]\n\t"
    "s_add_u32 m0, s80, 3072\n\t"
    "v_mfma_f32_16x16x32_bf16 v[100:103], a[248:251], a[52:55], v[100:103]\n\t"
    "s_mov_b32 %[tmp2], 24576\n\t"
    "v_mfma_f32_16x16x32_bf16 v[116:119], a[252:255], a[52:55], v[116:119]\n\t"
    "buffer_load_dwordx4 v212, s[64:67], %[tmp2] offen lds\n\t"
    "s_waitcnt vmcnt(12) lgkmcnt(0)\n\t"
    "s_barrier\n\t"
    "v_mfma_f32_16x16x32_bf16 v[4:7], a[128:131], a[68:71], v[4:7]\n\t"
    "ds_read_b64_tr_b16 a[192:193], v214 offset:49152\n\t"
    "v_mfma_f32_16x16x32_bf16 v[20:23], a[132:135], a[68:71], v[20:23]\n\t"
    "ds_read_b64_tr_b16 a[194:195], v214 offset:51200\n\t"
    "v_lshrrev_b32 v213, 5, v248\n\t"
    "v_mfma_f32_16x16x32_bf16 v[24:27], a[132:135], a[72:75], v[24:27]\n\t"
    "ds_read_b64_tr_b16 a[196:197], v215 offset:49152\n\t"
    "v_and_b32 v213, 16, v213\n\t"
    "v_mfma_f32_16x16x32_bf16 v[8:11], a[128:131], a[72:75], v[8:11]\n\t"
    "ds_read_b64_tr_b16 a[198:199], v215 offset:51200\n\t"
    "v_and_b32 v210, 0xf0, v248\n\t"
    "v_mfma_f32_16x16x32_bf16 v[12:15], a[128:131], a[76:79], v[12:15]\n\t"
    "ds_read_b64_tr_b16 a[224:225], v214 offset:53248\n\t"
    "v_xor_b32 v213, v213, v210\n\t"
    "v_mfma_f32_16x16x32_bf16 v[28:31], a[132:135], a[76:79], v[28:31]\n\t"
    "ds_read_b64_tr_b16 a[226:227], v214 offset:55296\n\t"
    "v_lshl_add_u32 v213, v210, 4, v213\n\t"
    "v_mfma_f32_16x16x32_bf16 v[4:7], a[160:163], a[84:87], v[4:7]\n\t"
    "ds_read_b64_tr_b16 a[228:229], v215 offset:53248\n\t"
    "v_lshrrev_b32 v210, 5, v248\n\t"
    "v_mfma_f32_16x16x32_bf16 v[20:23], a[164:167], a[84:87], v[20:23]\n\t"
    "ds_read_b64_tr_b16 a[230:231], v215 offset:55296\n\t"
    "v_and_b32 v210, 8, v210\n\t"
    "v_mfma_f32_16x16x32_bf16 v[24:27], a[164:167], a[88:91], v[24:27]\n\t"
    "v_add3_u32 v208, v213, v210, s80\n\t"
    "v_mfma_f32_16x16x32_bf16 v[8:11], a[160:163], a[88:91], v[8:11]\n\t"
    "v_xor_b32 v209, 32, v208\n\t"
    "v_mfma_f32_16x16x32_bf16 v[12:15], a[160:163], a[92:95], v[12:15]\n\t"
    "v_xor_b32 v210, 64, v208\n\t"
    "v_mfma_f32_16x16x32_bf16 v[28:31], a[164:167], a[92:95], v[28:31]\n\t"
    "s_waitcnt vmcnt(4) lgkmcnt(0)\n\t"
    "v_xor_b32 v211, 96, v208\n\t"
    "v_mfma_f32_16x16x32_bf16 v[4:7], a[192:195], a[100:103], v[4:7]\n\t"
    "ds_read_b64_tr_b16 a[200:201], v216 offset:49152\n\t"
    "v_add_u32 v213, s80, v248\n\t"
    "v_mfma_f32_16x16x32_bf16 v[20:23], a[196:199], a[100:103], v[20:23]\n\t"
    "ds_read_b64_tr_b16 a[202:203], v216 offset:51200\n\t"
    "v_bfe_u32 v223, v248, 4, 2\n\t"
    "v_mfma_f32_16x16x32_bf16 v[24:27], a[196:199], a[104:107], v[24:27]\n\t"
    "ds_read_b64_tr_b16 a[204:205], v217 offset:49152\n\t"
    "v_min_u32 v223, 2, v223\n\t"
    "v_mfma_f32_16x16x32_bf16 v[8:11], a[192:195], a[104:107], v[8:11]\n\t"
    "ds_read_b64_tr_b16 a[206:207], v217 offset:51200\n\t"
    "v_cmp_le_u32 vcc, %[role], v223\n\t"
    "v_mfma_f32_16x16x32_bf16 v[12:15], a[192:195], a[108:111], v[12:15]\n\t"
    "ds_read_b64_tr_b16 a[232:233], v216 offset:53248\n\t"
    "v_addc_co_u32 v223, vcc, 0, v223, vcc\n\t"
    "v_mfma_f32_16x16x32_bf16 v[28:31], a[196:199], a[108:111], v[28:31]\n\t"
    "ds_read_b64_tr_b16 a[234:235], v216 offset:55296\n\t"
    "v_lshlrev_b32 v223, 7, v223\n\t"
    "v_mfma_f32_16x16x32_bf16 v[4:7], a[224:227], a[116:119], v[4:7]\n\t"
    "ds_read_b64_tr_b16 a[236:237], v217 offset:53248\n\t"
    "v_add_u32 v223, s78, v223\n\t"
    "v_mfma_f32_16x16x32_bf16 v[20:23], a[228:231], a[116:119], v[20:23]\n\t"
    "ds_read_b64_tr_b16 a[238:239], v217 offset:55296\n\t"
    "v_lshrrev_b32 v249, 4, v248\n\t"
    "v_mfma_f32_16x16x32_bf16 v[24:27], a[228:231], a[120:123], v[24:27]\n\t"
    "v_add3_u32 v249, v249, %[role], 1\n\t"
    "v_mfma_f32_16x16x32_bf16 v[8:11], a[224:227], a[120:123], v[8:11]\n\t"
    "v_and_b32 v249, 3, v249\n\t"
    "v_mfma_f32_16x16x32_bf16 v[12:15], a[224:227], a[124:127], v[12:15]\n\t"
    "v_lshlrev_b32 v249, 11, v249\n\t"
    "v_mfma_f32_16x16x32_bf16 v[28:31], a[228:231], a[124:127], v[28:31]\n\t"
    "v_add_u32 v249, s79, v249\n\t"
    "v_mfma_f32_16x16x32_bf16 v[36:39], a[136:139], a[68:71], v[36:39]\n\t"
    "v_mfma_f32_16x16x32_bf16 v[52:55], a[140:143], a[68:71], v[52:55]\n\t"
    "v_cvt_pk_bf16_f32 v4, v4, v5\n\t"
    "v_cvt_pk_bf16_f32 v5, v6, v7\n\t"
    "v_mfma_f32_16x16x32_bf16 v[56:59], a[140:143], a[72:75], v[56:59]\n\t"
    "v_cvt_pk_bf16_f32 v6, v20, v21\n\t"
    "v_mfma_f32_16x16x32_bf16 v[40:43], a[136:139], a[72:75], v[40:43]\n\t"
    "v_cvt_pk_bf16_f32 v7, v22, v23\n\t"
    "buffer_store_dwordx4 v[4:7], v248, %[srdw], s72 offen offset:0 sc1\n\t"
    "v_mfma_f32_16x16x32_bf16 v[44:47], a[136:139], a[76:79], v[44:47]\n\t"
    "v_cvt_pk_bf16_f32 v8, v8, v9\n\t"
    "v_mfma_f32_16x16x32_bf16 v[60:63], a[140:143], a[76:79], v[60:63]\n\t"
    "v_cvt_pk_bf16_f32 v9, v10, v11\n\t"
    "v_mfma_f32_16x16x32_bf16 v[36:39], a[168:171], a[84:87], v[36:39]\n\t"
    "v_cvt_pk_bf16_f32 v10, v24, v25\n\t"
    "v_cvt_pk_bf16_f32 v11, v26, v27\n\t"
    "v_mfma_f32_16x16x32_bf16 v[52:55], a[172:175], a[84:87], v[52:55]\n\t"
    "buffer_store_dwordx4 v[8:11], v248, %[srdw], s73 offen offset:0 sc1\n\t"
    "v_mfma_f32_16x16x32_bf16 v[56:59], a[172:175], a[88:91], v[56:59]\n\t"
    "v_cvt_pk_bf16_f32 v12, v12, v13\n\t"
    "v_mfma_f32_16x16x32_bf16 v[40:43], a[168:171], a[88:91], v[40:43]\n\t"
    "v_cvt_pk_bf16_f32 v13, v14, v15\n\t"
    "v_cvt_pk_bf16_f32 v14, v28, v29\n\t"
    "v_mfma_f32_16x16x32_bf16 v[44:47], a[168:171], a[92:95], v[44:47]\n\t"
    "v_cvt_pk_bf16_f32 v15, v30, v31\n\t"
    "v_mfma_f32_16x16x32_bf16 v[60:63], a[172:175], a[92:95], v[60:63]\n\t"
    "buffer_store_dwordx4 v[12:15], v248, %[srdw], s74 offen offset:0 sc1\n\t"
    "s_waitcnt lgkmcnt(0)\n\t"
    "v_mfma_f32_16x16x32_bf16 v[36:39], a[200:203], a[100:103], v[36:39]\n\t"
    "ds_read_b64_tr_b16 a[208:209], v214 offset:57344\n\t"
    "v_mfma_f32_16x16x32_bf16 v[52:55], a[204:207], a[100:103], v[52:55]\n\t"
    "ds_read_b64_tr_b16 a[210:211], v214 offset:59392\n\t"
    "v_mfma_f32_16x16x32_bf16 v[56:59], a[204:207], a[104:107], v[56:59]\n\t"
    "ds_read_b64_tr_b16 a[212:213], v215 offset:57344\n\t"
    "v_mfma_f32_16x16x32_bf16 v[40:43], a[200:203], a[104:107], v[40:43]\n\t"
    "ds_read_b64_tr_b16 a[214:215], v215 offset:59392\n\t"
    "v_mfma_f32_16x16x32_bf16 v[44:47], a[200:203], a[108:111], v[44:47]\n\t"
    "ds_read_b64_tr_b16 a[240:241], v214 offset:61440\n\t"
    "v_mfma_f32_16x16x32_bf16 v[60:63], a[204:207], a[108:111], v[60:63]\n\t"
    "ds_read_b64_tr_b16 a[242:243], v214 offset:63488\n\t"
    "v_mfma_f32_16x16x32_bf16 v[36:39], a[232:235], a[116:119], v[36:39]\n\t"
    "ds_read_b64_tr_b16 a[244:245], v215 offset:61440\n\t"
    "v_mfma_f32_16x16x32_bf16 v[52:55], a[236:239], a[116:119], v[52:55]\n\t"
    "ds_read_b64_tr_b16 a[246:247], v215 offset:63488\n\t"
    "v_mfma_f32_16x16x32_bf16 v[56:59], a[236:239], a[120:123], v[56:59]\n\t"
    "v_mfma_f32_16x16x32_bf16 v[40:43], a[232:235], a[120:123], v[40:43]\n\t"
    "v_mfma_f32_16x16x32_bf16 v[44:47], a[232:235], a[124:127], v[44:47]\n\t"
    "v_mfma_f32_16x16x32_bf16 v[60:63], a[236:239], a[124:127], v[60:63]\n\t"
    "v_mfma_f32_16x16x32_bf16 v[68:71], a[144:147], a[68:71], v[68:71]\n\t"
    "v_mfma_f32_16x16x32_bf16 v[84:87], a[148:151], a[68:71], v[84:87]\n\t"
    "v_cvt_pk_bf16_f32 v36, v36, v37\n\t"
    "v_cvt_pk_bf16_f32 v37, v38, v39\n\t"
    "v_mfma_f32_16x16x32_bf16 v[88:91], a[148:151], a[72:75], v[88:91]\n\t"
    "v_cvt_pk_bf16_f32 v38, v52, v53\n\t"
    "v_mfma_f32_16x16x32_bf16 v[72:75], a[144:147], a[72:75], v[72:75]\n\t"
    "v_cvt_pk_bf16_f32 v39, v54, v55\n\t"
    "buffer_store_dwordx4 v[36:39], v248, %[srdw], s72 offen offset:1024 sc1\n\t"
    "v_mfma_f32_16x16x32_bf16 v[76:79], a[144:147], a[76:79], v[76:79]\n\t"
    "v_cvt_pk_bf16_f32 v40, v40, v41\n\t"
    "v_mfma_f32_16x16x32_bf16 v[92:95], a[148:151], a[76:79], v[92:95]\n\t"
    "v_cvt_pk_bf16_f32 v41, v42, v43\n\t"
    "v_mfma_f32_16x16x32_bf16 v[68:71], a[176:179], a[84:87], v[68:71]\n\t"
    "v_cvt_pk_bf16_f32 v42, v56, v57\n\t"
    "v_cvt_pk_bf16_f32 v43, v58, v59\n\t"
    "v_mfma_f32_16x16x32_bf16 v[84:87], a[180:183], a[84:87], v[84:87]\n\t"
    "buffer_store_dwordx4 v[40:43], v248, %[srdw], s73 offen offset:1024 sc1\n\t"
    "v_mfma_f32_16x16x32_bf16 v[88:91], a[180:183], a[88:91], v[88:91]\n\t"
    "v_cvt_pk_bf16_f32 v44, v44, v45\n\t"
    "v_mfma_f32_16x16x32_bf16 v[72:75], a[176:179], a[88:91], v[72:75]\n\t"
    "v_cvt_pk_bf16_f32 v45, v46, v47\n\t"
    "v_cvt_pk_bf16_f32 v46, v60, v61\n\t"
    "v_mfma_f32_16x16x32_bf16 v[76:79], a[176:179], a[92:95], v[76:79]\n\t"
    "v_cvt_pk_bf16_f32 v47, v62, v63\n\t"
    "v_mfma_f32_16x16x32_bf16 v[92:95], a[180:183], a[92:95], v[92:95]\n\t"
    "buffer_store_dwordx4 v[44:47], v248, %[srdw], s74 offen offset:1024 sc1\n\t"
    "s_waitcnt lgkmcnt(0)\n\t"
    "v_mfma_f32_16x16x32_bf16 v[68:71], a[208:211], a[100:103], v[68:71]\n\t"
    "ds_read_b64_tr_b16 a[216:217], v216 offset:57344\n\t"
    "v_mfma_f32_16x16x32_bf16 v[84:87], a[212:215], a[100:103], v[84:87]\n\t"
    "ds_read_b64_tr_b16 a[218:219], v216 offset:59392\n\t"
    "v_mfma_f32_16x16x32_bf16 v[88:91], a[212:215], a[104:107], v[88:91]\n\t"
    "ds_read_b64_tr_b16 a[220:221], v217 offset:57344\n\t"
    "v_mfma_f32_16x16x32_bf16 v[72:75], a[208:211], a[104:107], v[72:75]\n\t"
    "ds_read_b64_tr_b16 a[222:223], v217 offset:59392\n\t"
    "v_mfma_f32_16x16x32_bf16 v[76:79], a[208:211], a[108:111], v[76:79]\n\t"
    "ds_read_b64_tr_b16 a[248:249], v216 offset:61440\n\t"
    "v_mfma_f32_16x16x32_bf16 v[92:95], a[212:215], a[108:111], v[92:95]\n\t"
    "ds_read_b64_tr_b16 a[250:251], v216 offset:63488\n\t"
    "v_mfma_f32_16x16x32_bf16 v[68:71], a[240:243], a[116:119], v[68:71]\n\t"
    "ds_read_b64_tr_b16 a[252:253], v217 offset:61440\n\t"
    "v_mfma_f32_16x16x32_bf16 v[84:87], a[244:247], a[116:119], v[84:87]\n\t"
    "ds_read_b64_tr_b16 a[254:255], v217 offset:63488\n\t"
    "v_mfma_f32_16x16x32_bf16 v[88:91], a[244:247], a[120:123], v[88:91]\n\t"
    "v_mfma_f32_16x16x32_bf16 v[72:75], a[240:243], a[120:123], v[72:75]\n\t"
    "v_mfma_f32_16x16x32_bf16 v[76:79], a[240:243], a[124:127], v[76:79]\n\t"
    "v_mfma_f32_16x16x32_bf16 v[92:95], a[244:247], a[124:127], v[92:95]\n\t"
    "v_mfma_f32_16x16x32_bf16 v[100:103], a[152:155], a[68:71], v[100:103]\n\t"
    "v_mfma_f32_16x16x32_bf16 v[116:119], a[156:159], a[68:71], v[116:119]\n\t"
    "v_cvt_pk_bf16_f32 v68, v68, v69\n\t"
    "v_cvt_pk_bf16_f32 v69, v70, v71\n\t"
    "v_mfma_f32_16x16x32_bf16 v[120:123], a[156:159], a[72:75], v[120:123]\n\t"
    "v_cvt_pk_bf16_f32 v70, v84, v85\n\t"
    "v_mfma_f32_16x16x32_bf16 v[104:107], a[152:155], a[72:75], v[104:107]\n\t"
    "v_cvt_pk_bf16_f32 v71, v86, v87\n\t"
    "buffer_store_dwordx4 v[68:71], v248, %[srdw], s72 offen offset:2048 sc1\n\t"
    "v_mfma_f32_16x16x32_bf16 v[108:111], a[152:155], a[76:79], v[108:111]\n\t"
    "v_cvt_pk_bf16_f32 v72, v72, v73\n\t"
    "v_mfma_f32_16x16x32_bf16 v[124:127], a[156:159], a[76:79], v[124:127]\n\t"
    "v_cvt_pk_bf16_f32 v73, v74, v75\n\t"
    "v_mfma_f32_16x16x32_bf16 v[100:103], a[184:187], a[84:87], v[100:103]\n\t"
    "v_cvt_pk_bf16_f32 v74, v88, v89\n\t"
    "v_cvt_pk_bf16_f32 v75, v90, v91\n\t"
    "v_mfma_f32_16x16x32_bf16 v[116:119], a[188:191], a[84:87], v[116:119]\n\t"
    "buffer_store_dwordx4 v[72:75], v248, %[srdw], s73 offen offset:2048 sc1\n\t"
    "v_mfma_f32_16x16x32_bf16 v[120:123], a[188:191], a[88:91], v[120:123]\n\t"
    "v_cvt_pk_bf16_f32 v76, v76, v77\n\t"
    "v_mfma_f32_16x16x32_bf16 v[104:107], a[184:187], a[88:91], v[104:107]\n\t"
    "v_cvt_pk_bf16_f32 v77, v78, v79\n\t"
    "v_cvt_pk_bf16_f32 v78, v92, v93\n\t"
    "v_mfma_f32_16x16x32_bf16 v[108:111], a[184:187], a[92:95], v[108:111]\n\t"
    "v_cvt_pk_bf16_f32 v79, v94, v95\n\t"
    "v_mfma_f32_16x16x32_bf16 v[124:127], a[188:191], a[92:95], v[124:127]\n\t"
    "buffer_store_dwordx4 v[76:79], v248, %[srdw], s74 offen offset:2048 sc1\n\t"
    "s_waitcnt lgkmcnt(0)\n\t"
    "v_mfma_f32_16x16x32_bf16 v[100:103], a[216:219], a[100:103], v[100:103]\n\t"
    "v_mfma_f32_16x16x32_bf16 v[116:119], a[220:223], a[100:103], v[116:119]\n\t"
    "v_mfma_f32_16x16x32_bf16 v[120:123], a[220:223], a[104:107], v[120:123]\n\t"
    "v_mfma_f32_16x16x32_bf16 v[104:107], a[216:219], a[104:107], v[104:107]\n\t"
    "v_mfma_f32_16x16x32_bf16 v[108:111], a[216:219], a[108:111], v[108:111]\n\t"
    "v_mfma_f32_16x16x32_bf16 v[124:127], a[220:223], a[108:111], v[124:127]\n\t"
    "v_mfma_f32_16x16x32_bf16 v[100:103], a[248:251], a[116:119], v[100:103]\n\t"
    "v_mfma_f32_16x16x32_bf16 v[116:119], a[252:255], a[116:119], v[116:119]\n\t"
    "v_mfma_f32_16x16x32_bf16 v[120:123], a[252:255], a[120:123], v[120:123]\n\t"
    "v_mfma_f32_16x16x32_bf16 v[104:107], a[248:251], a[120:123], v[104:107]\n\t"
    "v_mfma_f32_16x16x32_bf16 v[108:111], a[248:251], a[124:127], v[108:111]\n\t"
    "v_mfma_f32_16x16x32_bf16 v[124:127], a[252:255], a[124:127], v[124:127]\n\t"
    "v_mfma_f32_16x16x32_bf16 v[0:3], a[128:131], a[64:67], v[0:3]\n\t"
    "v_mfma_f32_16x16x32_bf16 v[16:19], a[132:135], a[64:67], v[16:19]\n\t"
    "s_nop 0\n\t"
    "v_cvt_pk_bf16_f32 v100, v100, v101\n\t"
    "v_mfma_f32_16x16x32_bf16 v[32:35], a[136:139], a[64:67], v[32:35]\n\t"
    "v_cvt_pk_bf16_f32 v101, v102, v103\n\t"
    "v_mfma_f32_16x16x32_bf16 v[48:51], a[140:143], a[64:67], v[48:51]\n\t"
    "v_cvt_pk_bf16_f32 v102, v116, v117\n\t"
    "v_mfma_f32_16x16x32_bf16 v[64:67], a[144:147], a[64:67], v[64:67]\n\t"
    "v_cvt_pk_bf16_f32 v103, v118, v119\n\t"
    "v_mfma_f32_16x16x32_bf16 v[80:83], a[148:151], a[64:67], v[80:83]\n\t"
    "buffer_store_dwordx4 v[100:103], v248, %[srdw], s72 offen offset:3072 sc1\n\t"
    "v_mfma_f32_16x16x32_bf16 v[96:99], a[152:155], a[64:67], v[96:99]\n\t"
    "v_cvt_pk_bf16_f32 v104, v104, v105\n\t"
    "v_mfma_f32_16x16x32_bf16 v[112:115], a[156:159], a[64:67], v[112:115]\n\t"
    "v_cvt_pk_bf16_f32 v105, v106, v107\n\t"
    "v_mfma_f32_16x16x32_bf16 v[0:3], a[160:163], a[80:83], v[0:3]\n\t"
    "v_cvt_pk_bf16_f32 v106, v120, v121\n\t"
    "v_mfma_f32_16x16x32_bf16 v[16:19], a[164:167], a[80:83], v[16:19]\n\t"
    "v_cvt_pk_bf16_f32 v107, v122, v123\n\t"
    "v_mfma_f32_16x16x32_bf16 v[32:35], a[168:171], a[80:83], v[32:35]\n\t"
    "buffer_store_dwordx4 v[104:107], v248, %[srdw], s73 offen offset:3072 sc1\n\t"
    "v_mfma_f32_16x16x32_bf16 v[48:51], a[172:175], a[80:83], v[48:51]\n\t"
    "v_cvt_pk_bf16_f32 v108, v108, v109\n\t"
    "v_mfma_f32_16x16x32_bf16 v[64:67], a[176:179], a[80:83], v[64:67]\n\t"
    "v_cvt_pk_bf16_f32 v109, v110, v111\n\t"
    "v_mfma_f32_16x16x32_bf16 v[80:83], a[180:183], a[80:83], v[80:83]\n\t"
    "v_cvt_pk_bf16_f32 v110, v124, v125\n\t"
    "v_mfma_f32_16x16x32_bf16 v[96:99], a[184:187], a[80:83], v[96:99]\n\t"
    "v_cvt_pk_bf16_f32 v111, v126, v127\n\t"
    "v_mfma_f32_16x16x32_bf16 v[112:115], a[188:191], a[80:83], v[112:115]\n\t"
    "buffer_store_dwordx4 v[108:111], v248, %[srdw], s74 offen offset:3072 sc1\n\t"
    "v_mfma_f32_16x16x32_bf16 v[0:3], a[192:195], a[96:99], v[0:3]\n\t"
    "ds_read_b64_tr_b16 a[128:129], v214 offset:16384\n\t"
    "ds_read_b64_tr_b16 a[130:131], v214 offset:18432\n\t"
    "v_mfma_f32_16x16x32_bf16 v[16:19], a[196:199], a[96:99], v[16:19]\n\t"
    "ds_read_b64_tr_b16 a[132:133], v215 offset:16384\n\t"
    "ds_read_b64_tr_b16 a[134:135], v215 offset:18432\n\t"
    "v_mfma_f32_16x16x32_bf16 v[32:35], a[200:203], a[96:99], v[32:35]\n\t"
    "ds_read_b64_tr_b16 a[136:137], v216 offset:16384\n\t"
    "ds_read_b64_tr_b16 a[138:139], v216 offset:18432\n\t"
    "v_mfma_f32_16x16x32_bf16 v[48:51], a[204:207], a[96:99], v[48:51]\n\t"
    "ds_read_b64_tr_b16 a[140:141], v217 offset:16384\n\t"
    "ds_read_b64_tr_b16 a[142:143], v217 offset:18432\n\t"
    "v_mfma_f32_16x16x32_bf16 v[64:67], a[208:211], a[96:99], v[64:67]\n\t"
    "ds_read_b64_tr_b16 a[144:145], v214 offset:24576\n\t"
    "ds_read_b64_tr_b16 a[146:147], v214 offset:26624\n\t"
    "v_mfma_f32_16x16x32_bf16 v[80:83], a[212:215], a[96:99], v[80:83]\n\t"
    "ds_read_b64_tr_b16 a[148:149], v215 offset:24576\n\t"
    "ds_read_b64_tr_b16 a[150:151], v215 offset:26624\n\t"
    "v_mfma_f32_16x16x32_bf16 v[96:99], a[216:219], a[96:99], v[96:99]\n\t"
    "ds_read_b64_tr_b16 a[152:153], v216 offset:24576\n\t"
    "ds_read_b64_tr_b16 a[154:155], v216 offset:26624\n\t"
    "v_mfma_f32_16x16x32_bf16 v[112:115], a[220:223], a[96:99], v[112:115]\n\t"
    "ds_read_b64_tr_b16 a[156:157], v217 offset:24576\n\t"
    "ds_read_b64_tr_b16 a[158:159], v217 offset:26624\n\t"
    "v_mfma_f32_16x16x32_bf16 v[0:3], a[224:227], a[112:115], v[0:3]\n\t"
    "ds_read_b64_tr_b16 a[160:161], v214 offset:20480\n\t"
    "ds_read_b64_tr_b16 a[162:163], v214 offset:22528\n\t"
    "v_mfma_f32_16x16x32_bf16 v[16:19], a[228:231], a[112:115], v[16:19]\n\t"
    "ds_read_b64_tr_b16 a[164:165], v215 offset:20480\n\t"
    "ds_read_b64_tr_b16 a[166:167], v215 offset:22528\n\t"
    "v_mfma_f32_16x16x32_bf16 v[32:35], a[232:235], a[112:115], v[32:35]\n\t"
    "ds_read_b64_tr_b16 a[168:169], v216 offset:20480\n\t"
    "ds_read_b64_tr_b16 a[170:171], v216 offset:22528\n\t"
    "v_mfma_f32_16x16x32_bf16 v[48:51], a[236:239], a[112:115], v[48:51]\n\t"
    "ds_read_b64_tr_b16 a[172:173], v217 offset:20480\n\t"
    "ds_read_b64_tr_b16 a[174:175], v217 offset:22528\n\t"
    "v_mfma_f32_16x16x32_bf16 v[64:67], a[240:243], a[112:115], v[64:67]\n\t"
    "ds_read_b64_tr_b16 a[176:177], v214 offset:28672\n\t"
    "ds_read_b64_tr_b16 a[178:179], v214 offset:30720\n\t"
    "v_mfma_f32_16x16x32_bf16 v[80:83], a[244:247], a[112:115], v[80:83]\n\t"
    "ds_read_b64_tr_b16 a[180:181], v215 offset:28672\n\t"
    "ds_read_b64_tr_b16 a[182:183], v215 offset:30720\n\t"
    "v_mfma_f32_16x16x32_bf16 v[96:99], a[248:251], a[112:115], v[96:99]\n\t"
    "ds_read_b64_tr_b16 a[184:185], v216 offset:28672\n\t"
    "ds_read_b64_tr_b16 a[186:187], v216 offset:30720\n\t"
    "v_mfma_f32_16x16x32_bf16 v[112:115], a[252:255], a[112:115], v[112:115]\n\t"
    "ds_read_b64_tr_b16 a[188:189], v217 offset:28672\n\t"
    "ds_read_b64_tr_b16 a[190:191], v217 offset:30720\n\t"
    "s_waitcnt lgkmcnt(0)\n\t"
    "v_mfma_f32_16x16x32_bf16 v[0:3], a[128:131], a[32:35], v[0:3]\n\t"
    "ds_read_b64_tr_b16 a[192:193], v214 offset:0\n\t"
    "ds_read_b64_tr_b16 a[194:195], v214 offset:2048\n\t"
    "v_mfma_f32_16x16x32_bf16 v[16:19], a[132:135], a[32:35], v[16:19]\n\t"
    "ds_read_b64_tr_b16 a[196:197], v215 offset:0\n\t"
    "ds_read_b64_tr_b16 a[198:199], v215 offset:2048\n\t"
    "v_mfma_f32_16x16x32_bf16 v[32:35], a[136:139], a[32:35], v[32:35]\n\t"
    "ds_read_b64_tr_b16 a[200:201], v216 offset:0\n\t"
    "ds_read_b64_tr_b16 a[202:203], v216 offset:2048\n\t"
    "v_mfma_f32_16x16x32_bf16 v[48:51], a[140:143], a[32:35], v[48:51]\n\t"
    "ds_read_b64_tr_b16 a[204:205], v217 offset:0\n\t"
    "ds_read_b64_tr_b16 a[206:207], v217 offset:2048\n\t"
    "v_mfma_f32_16x16x32_bf16 v[64:67], a[144:147], a[32:35], v[64:67]\n\t"
    "ds_read_b64_tr_b16 a[208:209], v214 offset:8192\n\t"
    "ds_read_b64_tr_b16 a[210:211], v214 offset:10240\n\t"
    "v_mfma_f32_16x16x32_bf16 v[80:83], a[148:151], a[32:35], v[80:83]\n\t"
    "ds_read_b64_tr_b16 a[212:213], v215 offset:8192\n\t"
    "ds_read_b64_tr_b16 a[214:215], v215 offset:10240\n\t"
    "v_mfma_f32_16x16x32_bf16 v[96:99], a[152:155], a[32:35], v[96:99]\n\t"
    "ds_read_b64_tr_b16 a[216:217], v216 offset:8192\n\t"
    "ds_read_b64_tr_b16 a[218:219], v216 offset:10240\n\t"
    "v_mfma_f32_16x16x32_bf16 v[112:115], a[156:159], a[32:35], v[112:115]\n\t"
    "ds_read_b64_tr_b16 a[220:221], v217 offset:8192\n\t"
    "ds_read_b64_tr_b16 a[222:223], v217 offset:10240\n\t"
    "v_mfma_f32_16x16x32_bf16 v[0:3], a[160:163], a[48:51], v[0:3]\n\t"
    "ds_read_b64_tr_b16 a[224:225], v214 offset:4096\n\t"
    "ds_read_b64_tr_b16 a[226:227], v214 offset:6144\n\t"
    "v_mfma_f32_16x16x32_bf16 v[16:19], a[164:167], a[48:51], v[16:19]\n\t"
    "ds_read_b64_tr_b16 a[228:229], v215 offset:4096\n\t"
    "ds_read_b64_tr_b16 a[230:231], v215 offset:6144\n\t"
    "v_mfma_f32_16x16x32_bf16 v[32:35], a[168:171], a[48:51], v[32:35]\n\t"
    "ds_read_b64_tr_b16 a[232:233], v216 offset:4096\n\t"
    "ds_read_b64_tr_b16 a[234:235], v216 offset:6144\n\t"
    "v_mfma_f32_16x16x32_bf16 v[48:51], a[172:175], a[48:51], v[48:51]\n\t"
    "ds_read_b64_tr_b16 a[236:237], v217 offset:4096\n\t"
    "ds_read_b64_tr_b16 a[238:239], v217 offset:6144\n\t"
    "v_mfma_f32_16x16x32_bf16 v[64:67], a[176:179], a[48:51], v[64:67]\n\t"
    "ds_read_b64_tr_b16 a[240:241], v214 offset:12288\n\t"
    "ds_read_b64_tr_b16 a[242:243], v214 offset:14336\n\t"
    "v_mfma_f32_16x16x32_bf16 v[80:83], a[180:183], a[48:51], v[80:83]\n\t"
    "ds_read_b64_tr_b16 a[244:245], v215 offset:12288\n\t"
    "ds_read_b64_tr_b16 a[246:247], v215 offset:14336\n\t"
    "v_mfma_f32_16x16x32_bf16 v[96:99], a[184:187], a[48:51], v[96:99]\n\t"
    "ds_read_b64_tr_b16 a[248:249], v216 offset:12288\n\t"
    "ds_read_b64_tr_b16 a[250:251], v216 offset:14336\n\t"
    "v_mfma_f32_16x16x32_bf16 v[112:115], a[188:191], a[48:51], v[112:115]\n\t"
    "ds_read_b64_tr_b16 a[252:253], v217 offset:12288\n\t"
    "ds_read_b64_tr_b16 a[254:255], v217 offset:14336\n\t"
    "s_waitcnt lgkmcnt(0)\n\t"
    "v_mfma_f32_16x16x32_bf16 v[0:3], a[192:195], a[0:3], v[0:3]\n\t"
    "v_mfma_f32_16x16x32_bf16 v[16:19], a[196:199], a[0:3], v[16:19]\n\t"
    "v_mfma_f32_16x16x32_bf16 v[32:35], a[200:203], a[0:3], v[32:35]\n\t"
    "v_mfma_f32_16x16x32_bf16 v[48:51], a[204:207], a[0:3], v[48:51]\n\t"
    "v_mfma_f32_16x16x32_bf16 v[64:67], a[208:211], a[0:3], v[64:67]\n\t"
    "v_mfma_f32_16x16x32_bf16 v[80:83], a[212:215], a[0:3], v[80:83]\n\t"
    "v_mfma_f32_16x16x32_bf16 v[96:99], a[216:219], a[0:3], v[96:99]\n\t"
    "v_mfma_f32_16x16x32_bf16 v[112:115], a[220:223], a[0:3], v[112:115]\n\t"
    "v_mfma_f32_16x16x32_bf16 v[0:3], a[224:227], a[16:19], v[0:3]\n\t"
    "v_mfma_f32_16x16x32_bf16 v[16:19], a[228:231], a[16:19], v[16:19]\n\t"
    "v_mfma_f32_16x16x32_bf16 v[32:35], a[232:235], a[16:19], v[32:35]\n\t"
    "v_mfma_f32_16x16x32_bf16 v[48:51], a[236:239], a[16:19], v[48:51]\n\t"
    "v_mfma_f32_16x16x32_bf16 v[64:67], a[240:243], a[16:19], v[64:67]\n\t"
    "v_mfma_f32_16x16x32_bf16 v[80:83], a[244:247], a[16:19], v[80:83]\n\t"
    "v_mfma_f32_16x16x32_bf16 v[96:99], a[248:251], a[16:19], v[96:99]\n\t"
    "v_mfma_f32_16x16x32_bf16 v[112:115], a[252:255], a[16:19], v[112:115]\n\t"
    "v_xor_b32 v214, 128, v208\n\t"
    "v_xor_b32 v215, 160, v208\n\t"
    "v_xor_b32 v216, 192, v208\n\t"
    "v_xor_b32 v217, 224, v208\n\t"
    "v_mov_b32 v250, s90\n\t"
    "v_mov_b32 v251, s91\n\t"
    "s_waitcnt vmcnt(12)\n\t"
    "ds_read_b64 v[176:177], v208\n\t"
    "ds_read_b64 v[178:179], v209\n\t"
    "ds_read_b64 v[180:181], v210\n\t"
    "ds_read_b64 v[182:183], v211\n\t"
    "ds_read_b64 v[184:185], v214\n\t"
    "ds_read_b64 v[186:187], v215\n\t"
    "ds_read_b64 v[188:189], v216\n\t"
    "ds_read_b64 v[190:191], v217\n\t"
    "s_waitcnt vmcnt(0)\n\t"
    "s_mov_b64 exec, 7\n\t"
    "buffer_store_dwordx2 v[250:251], v249, %[srdw], 0 offen sc1\n\t"
    "s_mov_b64 exec, -1\n\t"
    "buffer_load_dwordx2 v[252:253], v223, %[srdw], 0 offen sc1\n\t"
    "s_waitcnt lgkmcnt(0)\n\t"
    "v_lshlrev_b32 v192, 16, v176\n\t"
    "v_and_b32 v193, 0xffff0000, v176\n\t"
    "v_lshlrev_b32 v194, 16, v177\n\t"
    "v_and_b32 v195, 0xffff0000, v177\n\t"
    "v_lshlrev_b32 v196, 16, v178\n\t"
    "v_and_b32 v197, 0xffff0000, v178\n\t"
    "v_lshlrev_b32 v198, 16, v179\n\t"
    "v_and_b32 v199, 0xffff0000, v179\n\t"
    "v_lshlrev_b32 v200, 16, v180\n\t"
    "v_and_b32 v201, 0xffff0000, v180\n\t"
    "v_lshlrev_b32 v202, 16, v181\n\t"
    "v_and_b32 v203, 0xffff0000, v181\n\t"
    "v_lshlrev_b32 v204, 16, v182\n\t"
    "v_and_b32 v205, 0xffff0000, v182\n\t"
    "v_lshlrev_b32 v206, 16, v183\n\t"
    "v_and_b32 v207, 0xffff0000, v183\n\t"
    "v_lshlrev_b32 v232, 16, v184\n\t"
    "v_and_b32 v233, 0xffff0000, v184\n\t"
    "v_lshlrev_b32 v234, 16, v185\n\t"
    "v_and_b32 v235, 0xffff0000, v185\n\t"
    "v_lshlrev_b32 v236, 16, v186\n\t"
    "v_and_b32 v237, 0xffff0000, v186\n\t"
    "v_lshlrev_b32 v238, 16, v187\n\t"
    "v_and_b32 v239, 0xffff0000, v187\n\t"
    "v_lshlrev_b32 v240, 16, v188\n\t"
    "v_and_b32 v241, 0xffff0000, v188\n\t"
    "v_lshlrev_b32 v242, 16, v189\n\t"
    "v_and_b32 v243, 0xffff0000, v189\n\t"
    "v_lshlrev_b32 v244, 16, v190\n\t"
    "v_and_b32 v245, 0xffff0000, v190\n\t"
    "v_lshlrev_b32 v246, 16, v191\n\t"
    "v_and_b32 v247, 0xffff0000, v191\n\t"
    "s_mov_b32 %[tmp2], 0x20000\n\t"
    "s_mov_b32 %[nsa], 7\n\t"
    "88:\n\t"
    "s_waitcnt vmcnt(0)\n\t"
    "v_cmp_eq_u64 vcc, s[90:91], v[252:253]\n\t"
    "s_sub_u32 %[tmp2], %[tmp2], 1\n\t"
    "s_cselect_b32 %[tmp], 7, vcc_lo\n\t"
    "s_and_b32 %[tmp], %[tmp], %[nsa]\n\t"
    "s_andn2_b32 %[nsa], %[nsa], %[tmp]\n\t"
    "s_bitcmp1_b32 %[tmp], 0\n\t"
    "s_cbranch_scc0 89f\n\t"
    "buffer_load_dwordx4 v[128:131], v248, %[srdw], s75 offen offset:0 sc1\n\t"
    "buffer_load_dwordx4 v[132:135], v248, %[srdw], s75 offen offset:1024 sc1\n\t"
    "buffer_load_dwordx4 v[136:139], v248, %[srdw], s75 offen offset:2048 sc1\n\t"
    "buffer_load_dwordx4 v[140:143], v248, %[srdw], s75 offen offset:3072 sc1\n\t"
    "89:\n\t"
    "s_bitcmp1_b32 %[tmp], 1\n\t"
    "s_cbranch_scc0 90f\n\t"
    "buffer_load_dwordx4 v[144:147], v248, %[srdw], s76 offen offset:0 sc1\n\t"
    "buffer_load_dwordx4 v[148:151], v248, %[srdw], s76 offen offset:1024 sc1\n\t"
    "buffer_load_dwordx4 v[152:155], v248, %[srdw], s76 offen offset:2048 sc1\n\t"
    "buffer_load_dwordx4 v[156:159], v248, %[srdw], s76 offen offset:3072 sc1\n\t"
    "90:\n\t"
    "s_bitcmp1_b32 %[tmp], 2\n\t"
    "s_cbranch_scc0 91f\n\t"
    "buffer_load_dwordx4 v[160:163], v248, %[srdw], s77 offen offset:0 sc1\n\t"
    "buffer_load_dwordx4 v[164:167], v248, %[srdw], s77 offen offset:1024 sc1\n\t"
    "buffer_load_dwordx4 v[168:171], v248, %[srdw], s77 offen offset:2048 sc1\n\t"
    "buffer_load_dwordx4 v[172:175], v248, %[srdw], s77 offen offset:3072 sc1\n\t"
    "91:\n\t"
    "s_and_b32 %[tmp], %[nsa], 3\n\t"
    "s_cbranch_scc0 92f\n\t"
    "buffer_load_dwordx2 v[252:253], v223, %[srdw], 0 offen sc1\n\t"
    "s_branch 88b\n\t"
    "92:\n\t"
    "s_waitcnt vmcnt(0)\n\t"
    "v_mfma_f32_16x16x32_bf16 v[0:3], v[224:227], v[128:131], v[0:3]\n\t"
    "v_mfma_f32_16x16x32_bf16 v[16:19], v[228:231], v[128:131], v[16:19]\n\t"
    "v_mfma_f32_16x16x32_bf16 v[0:3], v[224:227], v[144:147], v[0:3]\n\t"
    "v_mfma_f32_16x16x32_bf16 v[16:19], v[228:231], v[144:147], v[16:19]\n\t"
    "v_mfma_f32_16x16x32_bf16 v[32:35], v[224:227], v[132:135], v[32:35]\n\t"
    "v_mfma_f32_16x16x32_bf16 v[48:51], v[228:231], v[132:135], v[48:51]\n\t"
    "v_mfma_f32_16x16x32_bf16 v[32:35], v[224:227], v[148:151], v[32:35]\n\t"
    "v_mfma_f32_16x16x32_bf16 v[48:51], v[228:231], v[148:151], v[48:51]\n\t"
    "v_mfma_f32_16x16x32_bf16 v[64:67], v[224:227], v[136:139], v[64:67]\n\t"
    "v_mfma_f32_16x16x32_bf16 v[80:83], v[228:231], v[136:139], v[80:83]\n\t"
    "v_mfma_f32_16x16x32_bf16 v[64:67], v[224:227], v[152:155], v[64:67]\n\t"
    "v_mfma_f32_16x16x32_bf16 v[80:83], v[228:231], v[152:155], v[80:83]\n\t"
    "v_mfma_f32_16x16x32_bf16 v[96:99], v[224:227], v[140:143], v[96:99]\n\t"
    "v_mfma_f32_16x16x32_bf16 v[112:115], v[228:231], v[140:143], v[112:115]\n\t"
    "v_mfma_f32_16x16x32_bf16 v[96:99], v[224:227], v[156:159], v[96:99]\n\t"
    "v_mfma_f32_16x16x32_bf16 v[112:115], v[228:231], v[156:159], v[112:115]\n\t"
    "buffer_load_dwordx2 v[252:253], v223, %[srdw], 0 offen sc1\n\t"
    "v_pk_mul_f32 v[4:5], v[0:1], s[92:93] op_sel_hi:[1,0] neg_lo:[0,1] neg_hi:[0,1]\n\t"
    "v_pk_mul_f32 v[6:7], v[2:3], s[92:93] op_sel_hi:[1,0] neg_lo:[0,1] neg_hi:[0,1]\n\t"
    "v_pk_mul_f32 v[20:21], v[16:17], s[92:93] op_sel_hi:[1,0] neg_lo:[0,1] neg_hi:[0,1]\n\t"
    "v_pk_mul_f32 v[22:23], v[18:19], s[92:93] op_sel_hi:[1,0] neg_lo:[0,1] neg_hi:[0,1]\n\t"
    "v_exp_f32 v4, v4\n\t"
    "v_exp_f32 v6, v6\n\t"
    "v_exp_f32 v20, v20\n\t"
    "v_exp_f32 v22, v22\n\t"
    "v_exp_f32 v5, v5\n\t"
    "v_exp_f32 v7, v7\n\t"
    "v_exp_f32 v21, v21\n\t"
    "v_exp_f32 v23, v23\n\t"
    "v_pk_add_f32 v[4:5], v[4:5], 1.0 op_sel_hi:[1,0]\n\t"
    "v_pk_add_f32 v[6:7], v[6:7], 1.0 op_sel_hi:[1,0]\n\t"
    "v_pk_add_f32 v[20:21], v[20:21], 1.0 op_sel_hi:[1,0]\n\t"
    "v_pk_add_f32 v[22:23], v[22:23], 1.0 op_sel_hi:[1,0]\n\t"
    "v_rcp_f32 v4, v4\n\t"
    "v_rcp_f32 v6, v6\n\t"
    "v_rcp_f32 v20, v20\n\t"
    "v_rcp_f32 v22, v22\n\t"
    "v_rcp_f32 v5, v5\n\t"
    "v_rcp_f32 v7, v7\n\t"
    "v_rcp_f32 v21, v21\n\t"
    "v_rcp_f32 v23, v23\n\t"
    "v_pk_mul_f32 v[0:1], v[0:1], v[4:5]\n\t"
    "v_pk_mul_f32 v[2:3], v[2:3], v[6:7]\n\t"
    "v_pk_mul_f32 v[16:17], v[16:17], v[20:21]\n\t"
    "v_pk_mul_f32 v[18:19], v[18:19], v[22:23]\n\t"
    "v_pk_mul_f32 v[36:37], v[32:33], s[92:93] op_sel_hi:[1,0] neg_lo:[0,1] neg_hi:[0,1]\n\t"
    "v_pk_mul_f32 v[38:39], v[34:35], s[92:93] op_sel_hi:[1,0] neg_lo:[0,1] neg_hi:[0,1]\n\t"
    "v_pk_mul_f32 v[52:53], v[48:49], s[92:93] op_sel_hi:[1,0] neg_lo:[0,1] neg_hi:[0,1]\n\t"
    "v_pk_mul_f32 v[54:55], v[50:51], s[92:93] op_sel_hi:[1,0] neg_lo:[0,1] neg_hi:[0,1]\n\t"
    "v_exp_f32 v36, v36\n\t"
    "v_exp_f32 v38, v38\n\t"
    "v_exp_f32 v52, v52\n\t"
    "v_exp_f32 v54, v54\n\t"
    "v_exp_f32 v37, v37\n\t"
    "v_exp_f32 v39, v39\n\t"
    "v_exp_f32 v53, v53\n\t"
    "v_exp_f32 v55, v55\n\t"
    "v_pk_add_f32 v[36:37], v[36:37], 1.0 op_sel_hi:[1,0]\n\t"
    "v_pk_add_f32 v[38:39], v[38:39], 1.0 op_sel_hi:[1,0]\n\t"
    "v_pk_add_f32 v[52:53], v[52:53], 1.0 op_sel_hi:[1,0]\n\t"
    "v_pk_add_f32 v[54:55], v[54:55], 1.0 op_sel_hi:[1,0]\n\t"
    "v_rcp_f32 v36, v36\n\t"
    "v_rcp_f32 v38, v38\n\t"
    "v_rcp_f32 v52, v52\n\t"
    "v_rcp_f32 v54, v54\n\t"
    "v_rcp_f32 v37, v37\n\t"
    "v_rcp_f32 v39, v39\n\t"
    "v_rcp_f32 v53, v53\n\t"
    "v_rcp_f32 v55, v55\n\t"
    "v_pk_mul_f32 v[32:33], v[32:33], v[36:37]\n\t"
    "v_pk_mul_f32 v[34:35], v[34:35], v[38:39]\n\t"
    "v_pk_mul_f32 v[48:49], v[48:49], v[52:53]\n\t"
    "v_pk_mul_f32 v[50:51], v[50:51], v[54:55]\n\t"
    "v_pk_mul_f32 v[68:69], v[64:65], s[92:93] op_sel_hi:[1,0] neg_lo:[0,1] neg_hi:[0,1]\n\t"
    "v_pk_mul_f32 v[70:71], v[66:67], s[92:93] op_sel_hi:[1,0] neg_lo:[0,1] neg_hi:[0,1]\n\t"
    "v_pk_mul_f32 v[84:85], v[80:81], s[92:93] op_sel_hi:[1,0] neg_lo:[0,1] neg_hi:[0,1]\n\t"
    "v_pk_mul_f32 v[86:87], v[82:83], s[92:93] op_sel_hi:[1,0] neg_lo:[0,1] neg_hi:[0,1]\n\t"
    "v_exp_f32 v68, v68\n\t"
    "v_exp_f32 v70, v70\n\t"
    "v_exp_f32 v84, v84\n\t"
    "v_exp_f32 v86, v86\n\t"
    "v_exp_f32 v69, v69\n\t"
    "v_exp_f32 v71, v71\n\t"
    "v_exp_f32 v85, v85\n\t"
    "v_exp_f32 v87, v87\n\t"
    "v_pk_add_f32 v[68:69], v[68:69], 1.0 op_sel_hi:[1,0]\n\t"
    "v_pk_add_f32 v[70:71], v[70:71], 1.0 op_sel_hi:[1,0]\n\t"
    "v_pk_add_f32 v[84:85], v[84:85], 1.0 op_sel_hi:[1,0]\n\t"
    "v_pk_add_f32 v[86:87], v[86:87], 1.0 op_sel_hi:[1,0]\n\t"
    "v_rcp_f32 v68, v68\n\t"
    "v_rcp_f32 v70, v70\n\t"
    "v_rcp_f32 v84, v84\n\t"
    "v_rcp_f32 v86, v86\n\t"
    "v_rcp_f32 v69, v69\n\t"
    "v_rcp_f32 v71, v71\n\t"
    "v_rcp_f32 v85, v85\n\t"
    "v_rcp_f32 v87, v87\n\t"
    "v_pk_mul_f32 v[64:65], v[64:65], v[68:69]\n\t"
    "v_pk_mul_f32 v[66:67], v[66:67], v[70:71]\n\t"
    "v_pk_mul_f32 v[80:81], v[80:81], v[84:85]\n\t"
    "v_pk_mul_f32 v[82:83], v[82:83], v[86:87]\n\t"
    "v_pk_mul_f32 v[100:101], v[96:97], s[92:93] op_sel_hi:[1,0] neg_lo:[0,1] neg_hi:[0,1]\n\t"
    "v_pk_mul_f32 v[102:103], v[98:99], s[92:93] op_sel_hi:[1,0] neg_lo:[0,1] neg_hi:[0,1]\n\t"
    "v_pk_mul_f32 v[116:117], v[112:113], s[92:93] op_sel_hi:[1,0] neg_lo:[0,1] neg_hi:[0,1]\n\t"
    "v_pk_mul_f32 v[118:119], v[114:115], s[92:93] op_sel_hi:[1,0] neg_lo:[0,1] neg_hi:[0,1]\n\t"
    "v_exp_f32 v100, v100\n\t"
    "v_exp_f32 v102, v102\n\t"
    "v_exp_f32 v116, v116\n\t"
    "v_exp_f32 v118, v118\n\t"
    "v_exp_f32 v101, v101\n\t"
    "v_exp_f32 v103, v103\n\t"
    "v_exp_f32 v117, v117\n\t"
    "v_exp_f32 v119, v119\n\t"
    "v_pk_add_f32 v[100:101], v[100:101], 1.0 op_sel_hi:[1,0]\n\t"
    "v_pk_add_f32 v[102:103], v[102:103], 1.0 op_sel_hi:[1,0]\n\t"
    "v_pk_add_f32 v[116:117], v[116:117], 1.0 op_sel_hi:[1,0]\n\t"
    "v_pk_add_f32 v[118:119], v[118:119], 1.0 op_sel_hi:[1,0]\n\t"
    "v_rcp_f32 v100, v100\n\t"
    "v_rcp_f32 v102, v102\n\t"
    "v_rcp_f32 v116, v116\n\t"
    "v_rcp_f32 v118, v118\n\t"
    "v_rcp_f32 v101, v101\n\t"
    "v_rcp_f32 v103, v103\n\t"
    "v_rcp_f32 v117, v117\n\t"
    "v_rcp_f32 v119, v119\n\t"
    "v_pk_mul_f32 v[96:97], v[96:97], v[100:101]\n\t"
    "v_pk_mul_f32 v[98:99], v[98:99], v[102:103]\n\t"
    "v_pk_mul_f32 v[112:113], v[112:113], v[116:117]\n\t"
    "v_pk_mul_f32 v[114:115], v[114:115], v[118:119]\n\t"
    "94:\n\t"
    "s_waitcnt vmcnt(0)\n\t"
    "v_cmp_eq_u64 vcc, s[90:91], v[252:253]\n\t"
    "s_sub_u32 %[tmp2], %[tmp2], 1\n\t"
    "s_cselect_b32 %[tmp], 7, vcc_lo\n\t"
    "s_and_b32 %[tmp], %[tmp], %[nsa]\n\t"
    "s_andn2_b32 %[nsa], %[nsa], %[tmp]\n\t"
    "s_bitcmp1_b32 %[tmp], 0\n\t"
    "s_cbranch_scc0 95f\n\t"
    "buffer_load_dwordx4 v[128:131], v248, %[srdw], s75 offen offset:0 sc1\n\t"
    "buffer_load_dwordx4 v[132:135], v248, %[srdw], s75 offen offset:1024 sc1\n\t"
    "buffer_load_dwordx4 v[136:139], v248, %[srdw], s75 offen offset:2048 sc1\n\t"
    "buffer_load_dwordx4 v[140:143], v248, %[srdw], s75 offen offset:3072 sc1\n\t"
    "95:\n\t"
    "s_bitcmp1_b32 %[tmp], 1\n\t"
    "s_cbranch_scc0 96f\n\t"
    "buffer_load_dwordx4 v[144:147], v248, %[srdw], s76 offen offset:0 sc1\n\t"
    "buffer_load_dwordx4 v[148:151], v248, %[srdw], s76 offen offset:1024 sc1\n\t"
    "buffer_load_dwordx4 v[152:155], v248, %[srdw], s76 offen offset:2048 sc1\n\t"
    "buffer_load_dwordx4 v[156:159], v248, %[srdw], s76 offen offset:3072 sc1\n\t"
    "96:\n\t"
    "s_bitcmp1_b32 %[tmp], 2\n\t"
    "s_cbranch_scc0 97f\n\t"
    "buffer_load_dwordx4 v[160:163], v248, %[srdw], s77 offen offset:0 sc1\n\t"
    "buffer_load_dwordx4 v[164:167], v248, %[srdw], s77 offen offset:1024 sc1\n\t"
    "buffer_load_dwordx4 v[168:171], v248, %[srdw], s77 offen offset:2048 sc1\n\t"
    "buffer_load_dwordx4 v[172:175], v248, %[srdw], s77 offen offset:3072 sc1\n\t"
    "97:\n\t"
    "s_and_b32 %[tmp], %[nsa], 4\n\t"
    "s_cbranch_scc0 98f\n\t"
    "buffer_load_dwordx2 v[252:253], v223, %[srdw], 0 offen sc1\n\t"
    "s_branch 94b\n\t"
    "98:\n\t"
    "s_waitcnt vmcnt(0)\n\t"
    "v_mfma_f32_16x16x32_bf16 v[8:11], v[224:227], v[160:163], 0\n\t"
    "v_mfma_f32_16x16x32_bf16 v[24:27], v[228:231], v[160:163], 0\n\t"
    "v_mfma_f32_16x16x32_bf16 v[40:43], v[224:227], v[164:167], 0\n\t"
    "v_mfma_f32_16x16x32_bf16 v[56:59], v[228:231], v[164:167], 0\n\t"
    "v_mfma_f32_16x16x32_bf16 v[72:75], v[224:227], v[168:171], 0\n\t"
    "v_mfma_f32_16x16x32_bf16 v[88:91], v[228:231], v[168:171], 0\n\t"
    "v_mfma_f32_16x16x32_bf16 v[104:107], v[224:227], v[172:175], 0\n\t"
    "v_mfma_f32_16x16x32_bf16 v[120:123], v[228:231], v[172:175], 0\n\t"
    "s_nop 0\n\t"
    "v_pk_fma_f32 v[192:193], v[0:1], v[8:9], v[192:193]\n\t"
    "v_pk_fma_f32 v[194:195], v[2:3], v[10:11], v[194:195]\n\t"
    "v_cvt_pk_bf16_f32 v176, v192, v193\n\t"
    "v_cvt_pk_bf16_f32 v177, v194, v195\n\t"
    "ds_write_b64 v208, v[176:177]\n\t"
    "v_pk_fma_f32 v[196:197], v[16:17], v[24:25], v[196:197]\n\t"
    "v_pk_fma_f32 v[198:199], v[18:19], v[26:27], v[198:199]\n\t"
    "v_cvt_pk_bf16_f32 v178, v196, v197\n\t"
    "v_cvt_pk_bf16_f32 v179, v198, v199\n\t"
    "ds_write_b64 v209, v[178:179]\n\t"
    "v_pk_fma_f32 v[200:201], v[32:33], v[40:41], v[200:201]\n\t"
    "v_pk_fma_f32 v[202:203], v[34:35], v[42:43], v[202:203]\n\t"
    "v_cvt_pk_bf16_f32 v180, v200, v201\n\t"
    "v_cvt_pk_bf16_f32 v181, v202, v203\n\t"
    "ds_write_b64 v210, v[180:181]\n\t"
    "v_pk_fma_f32 v[204:205], v[48:49], v[56:57], v[204:205]\n\t"
    "v_pk_fma_f32 v[206:207], v[50:51], v[58:59], v[206:207]\n\t"
    "v_cvt_pk_bf16_f32 v182, v204, v205\n\t"
    "v_cvt_pk_bf16_f32 v183, v206, v207\n\t"
    "ds_write_b64 v211, v[182:183]\n\t"
    "v_pk_fma_f32 v[232:233], v[64:65], v[72:73], v[232:233]\n\t"
    "v_pk_fma_f32 v[234:235], v[66:67], v[74:75], v[234:235]\n\t"
    "v_cvt_pk_bf16_f32 v184, v232, v233\n\t"
    "v_cvt_pk_bf16_f32 v185, v234, v235\n\t"
    "ds_write_b64 v214, v[184:185]\n\t"
    "v_pk_fma_f32 v[236:237], v[80:81], v[88:89], v[236:237]\n\t"
    "v_pk_fma_f32 v[238:239], v[82:83], v[90:91], v[238:239]\n\t"
    "v_cvt_pk_bf16_f32 v186, v236, v237\n\t"
    "v_cvt_pk_bf16_f32 v187, v238, v239\n\t"
    "ds_write_b64 v215, v[186:187]\n\t"
    "v_pk_fma_f32 v[240:241], v[96:97], v[104:105], v[240:241]\n\t"
    "v_pk_fma_f32 v[242:243], v[98:99], v[106:107], v[242:243]\n\t"
    "v_cvt_pk_bf16_f32 v188, v240, v241\n\t"
    "v_cvt_pk_bf16_f32 v189, v242, v243\n\t"
    "ds_write_b64 v216, v[188:189]\n\t"
    "v_pk_fma_f32 v[244:245], v[112:113], v[120:121], v[244:245]\n\t"
    "v_pk_fma_f32 v[246:247], v[114:115], v[122:123], v[246:247]\n\t"
    "v_cvt_pk_bf16_f32 v190, v244, v245\n\t"
    "v_cvt_pk_bf16_f32 v191, v246, v247\n\t"
    "ds_write_b64 v217, v[190:191]\n\t"
    "s_waitcnt lgkmcnt(0)\n\t"
    "ds_read_b128 v[128:131], v213 offset:0\n\t"
    "ds_read_b128 v[132:135], v213 offset:1024\n\t"
    "ds_read_b128 v[136:139], v213 offset:2048\n\t"
    "ds_read_b128 v[140:143], v213 offset:3072\n\t"
    "s_mov_b32 %[tmp2], 0\n\t"
    "v_xor_b32 v212, 0, v219\n\t"
    "s_waitcnt lgkmcnt(3)\n\t"
    "buffer_store_dwordx4 v[128:131], v212, s[68:71], %[tmp2] offen sc1\n\t"
    "s_mov_b32 %[tmp2], 8192\n\t"
    "v_xor_b32 v212, 64, v219\n\t"
    "s_waitcnt lgkmcnt(2)\n\t"
    "buffer_store_dwordx4 v[132:135], v212, s[68:71], %[tmp2] offen sc1\n\t"
    "s_mov_b32 %[tmp2], 16384\n\t"
    "v_xor_b32 v212, 128, v219\n\t"
    "s_waitcnt lgkmcnt(1)\n\t"
    "buffer_store_dwordx4 v[136:139], v212, s[68:71], %[tmp2] offen sc1\n\t"
    "s_mov_b32 %[tmp2], 24576\n\t"
    "v_xor_b32 v212, 192, v219\n\t"
    "s_waitcnt lgkmcnt(0)\n\t"
    "buffer_store_dwordx4 v[140:143], v212, s[68:71], %[tmp2] offen sc1\n\t"
    "s_branch 3f\n\t"
    "52:\n\t"
    "s_cmp_lg_u32 %[w], 0\n\t"
    "s_cbranch_scc1 53f\n\t"
    "v_mov_b32 v250, s90\n\t"
    "v_mov_b32 v251, s91\n\t"
    "v_mov_b32 v249, %[pub]\n\t"
    "s_mov_b64 exec, 1\n\t"
    "buffer_store_dwordx2 v[250:251], v249, %[srdw], 0 offen sc1\n\t"
    "s_mov_b64 exec, -1\n\t"
    "53:\n\t"
    "s_branch 62b\n\t"
    "56:\n\t"
    "s_mov_b32 %[tmp2], 0x10000\n\t"
    "58:\n\t"
    "s_sleep 2\n\t"
    "buffer_load_dwordx2 v[224:225], v223, %[srdw], %[flgo] offen sc1\n\t"
    "s_sub_u32 %[tmp2], %[tmp2], 1\n\t"
    "s_waitcnt vmcnt(0)\n\t"
    "s_cbranch_scc1 57b\n\t"
    "v_cmp_ne_u64 vcc, s[90:91], v[224:225]\n\t"
    "s_cbranch_vccnz 58b\n\t"
    "s_branch 57b\n\t"
    "3:\n\t"
    "blk_end_B:\n\t"
        : G2_IO, [flgo] "+s"(flgo), [dfo] "+s"(dfo), [df2] "+s"(df2) : G2_IN, [df3] "s"(role ? -512 : 512), [tend] "s"(20 - 4 + RING + 1) : G2_CLOB); }
  }
}

__global__ __launch_bounds__(256, 1) void block_kernel(Params P) {
  extern __shared__ __attribute__((aligned(16))) char smem[];
  const unsigned ldsb = (unsigned)(size_t)smem;
  int w = __builtin_amdgcn_readfirstlane(threadIdx.x >> 6);
  if (map_ta(blockIdx.x)) run<true>(ldsb, w); else run<false>(ldsb, w);
}

extern "C" void kernel_launch(void* const* d_in, const int* in_sizes, int n_in, void* d_out, int out_size, void* d_ws, size_t ws_size, hipStream_t stream) {
  Params P; P.x = (const u16*)d_in[0]; P.wqkv = (const u16*)d_in[1]; P.wo = (const u16*)d_in[2]; P.wg = (const u16*)d_in[4]; P.wu = (const u16*)d_in[5]; P.wd = (const u16*)d_in[6];
  P.out = (u16*)d_out; P.ws = (char*)d_ws;
  (void)hipFuncSetAttribute((const void*)block_kernel, hipFuncAttributeMaxDynamicSharedMemorySize, LDS_BYTES);
  block_kernel<<<256, 256, LDS_BYTES, stream>>>(P);
}
```
